```python
import torch

def _softmax(x, axis=-1):
    return torch.softmax(x, dim=axis)
def _gelu(x):
    return torch.nn.functional.gelu(x, approximate="tanh")
def _silu(x):
    return torch.nn.functional.silu(x)
def _sigmoid(x):
    return torch.sigmoid(x)
def _layernorm(x, axis=-1, eps=1e-5):
    m = x.mean(dim=axis, keepdim=True)
    v = x.var(dim=axis, keepdim=True, unbiased=False)
    return (x - m) / torch.sqrt(v + eps)
def _scatter_add(tgt, idx, src, axis=0):
    return tgt.clone().index_add_(axis, idx, src)
def _segment_sum(src, seg, n):
    out = torch.zeros((n,) + src.shape[1:], dtype=src.dtype)
    return out.index_add_(0, seg, src)
def _pad(x, pw):
    flat = tuple(v for pair in reversed(pw) for v in pair)
    return torch.nn.functional.pad(x, flat)

kernel_name = 'concat_heads__linear_attn_block__ssm_conv_s5__prompt'

def setup_inputs():

    _w = (lambda a: (lambda t: t.to(torch.bfloat16) if t.is_floating_point() else t)(torch.as_tensor(a)))
    def _A_setup():
        B, S, D = 1, 2048, 512
        return {
            "x": _w((lambda *s: torch.randn(*s, dtype=torch.bfloat16) * 0.05)(B, S, D)),
            "wqkv": _w((lambda *s: torch.randn(*s, dtype=torch.bfloat16) * 0.4)(D, 3 * D)),
            "wo": _w((lambda *s: torch.randn(*s, dtype=torch.bfloat16) * 0.05)(D, D)),
        }
    _A_in = _A_setup()

    def _B_setup():
        B, S, D = 1, 2048, 512
        P = D
        Lambda_re = -0.5 * (lambda _x: torch.exp(torch.as_tensor(_x)))((lambda *s: torch.randn(*s, dtype=torch.bfloat16) * 0.2)(P))
        Lambda_im = (lambda *s: torch.randn(*s, dtype=torch.bfloat16) * 0.8)(P) * 1.0
        B_re = (lambda *s: torch.randn(*s, dtype=torch.bfloat16) * 0.2)(P, D) * (D ** -0.5)
        B_im = (lambda *s: torch.randn(*s, dtype=torch.bfloat16) * 0.2)(P, D) * (D ** -0.5)
        C_re = (lambda *s: torch.randn(*s, dtype=torch.bfloat16) * 0.2)(D, P) * (P ** -0.5)
        C_im = (lambda *s: torch.randn(*s, dtype=torch.bfloat16) * 0.2)(D, P) * (P ** -0.5)
        log_dt = (lambda *s: torch.randn(*s, dtype=torch.bfloat16) * 0.2)(P) * 0.3 - 2.0
        Dskip = (lambda *s: torch.randn(*s, dtype=torch.bfloat16) * 0.05)(D)
        return {
            "x": _w((lambda *s: torch.randn(*s, dtype=torch.bfloat16) * 0.05)(B, S, D)),
            "Lambda_re": _w(Lambda_re),
            "Lambda_im": _w(Lambda_im),
            "B_re": _w(B_re),
            "B_im": _w(B_im),
            "C_re": _w(C_re),
            "C_im": _w(C_im),
            "log_dt": _w(log_dt),
            "Dskip": _w(Dskip),
        }
    _B_in = _B_setup()

    return {
        "A_x": _A_in["x"],
        "A_wqkv": _A_in["wqkv"],
        "A_wo": _A_in["wo"],
        "B_x": _B_in["x"],
        "B_Lambda_re": _B_in["Lambda_re"],
        "B_Lambda_im": _B_in["Lambda_im"],
        "B_B_re": _B_in["B_re"],
        "B_B_im": _B_in["B_im"],
        "B_C_re": _B_in["C_re"],
        "B_C_im": _B_in["C_im"],
        "B_log_dt": _B_in["log_dt"],
        "B_Dskip": _B_in["Dskip"],
    }

def reference(A_x, A_wqkv, A_wo, B_x, B_Lambda_re, B_Lambda_im, B_B_re, B_B_im, B_C_re, B_C_im, B_log_dt, B_Dskip):
    def _A_ref(x, wqkv, wo):
        B, S, D = x.shape
        H = 32; Dh = D // H
        qkv = (lambda x, *d: x.permute(*d))((x @ wqkv).reshape(B, S, 3, H, Dh), 2, 0, 3, 1, 4)
        q, k, v = qkv[0], qkv[1], qkv[2]
        phi = lambda t: torch.where(t > 0, t + 1, torch.exp(t)) + 1e-6
        pq, pk = phi(q), phi(k)
        kv = torch.einsum("bhsd,bhse->bhde", pk, v)
        z = torch.einsum("bhsd->bhd", pk)
        num = torch.einsum("bhqd,bhde->bhqe", pq, kv)
        den = torch.einsum("bhqd,bhd->bhq", pq, z)[..., None] + 1e-6
        o = num / den
        return (lambda x, *d: x.permute(*d))(o, 0, 2, 1, 3).reshape(B, S, D) @ wo

    def _B_ref(x, Lambda_re, Lambda_im, B_re, B_im, C_re, C_im, log_dt, Dskip):
        Bsz, L, H = x.shape
        Lam = Lambda_re + 1j * Lambda_im
        Bmat = B_re + 1j * B_im
        Cmat = C_re + 1j * C_im
        dt = torch.exp(log_dt)
        Lam_bar = torch.exp(dt * Lam)
        B_bar = ((Lam_bar - 1.0) / Lam)[:, None] * Bmat
        Bu = torch.einsum("bld,pd->blp", x + 0j, B_bar)
        h = torch.zeros_like(Bu[:, 0, :])
        hs = []
        for t in range(L):
            h = Lam_bar[None, :] * h + Bu[:, t, :]
            hs.append(h)
        hseq = torch.stack(hs, 1)
        y = 2.0 * torch.einsum("blp,dp->bld", hseq, Cmat).real + x * Dskip[None, None, :]
        return y

    a = _A_ref(A_x, A_wqkv, A_wo)
    b = _B_ref(B_x, B_Lambda_re, B_Lambda_im, B_B_re, B_B_im, B_C_re, B_C_im, B_log_dt, B_Dskip)
    return torch.concatenate([a, b], axis=-1)
```

```cpp
#include <hip/hip_runtime.h>
#include <hip/hip_bf16.h>
typedef unsigned short u16;
__device__ __forceinline__ float bf2f(u16 v){ return __uint_as_float(((unsigned)v)<<16); }
__device__ __forceinline__ float rbf(float f){ return __bfloat162float(__float2bfloat16(f)); }
constexpr int S=2048, D=512;
__global__ void k_qkv(const u16* x, const u16* w, float* qkv){
  int n = blockIdx.x*256+threadIdx.x; int s = blockIdx.y;
  float a=0; for(int k=0;k<D;k++) a += bf2f(x[s*D+k])*bf2f(w[k*1536+n]);
  a = rbf(a);
  if(n<1024){ a = a>0? rbf(a+1.f): rbf(__expf(a)); a = rbf(a+1e-6f);}
  qkv[s*1536+n]=a;
}
__global__ void k_kv(const float* qkv, float* kv, float* z){
  int h = blockIdx.x; int d = threadIdx.x>>4, e = threadIdx.x&15;
  float a=0, zz=0;
  for(int s=0;s<S;s++){ float pk = qkv[s*1536+512+h*16+d]; a += pk*qkv[s*1536+1024+h*16+e]; zz+=pk; }
  kv[h*256+d*16+e]=rbf(a); if(e==0) z[h*16+d]=rbf(zz);
}
__global__ void k_o(const float* qkv, const float* kv, const float* z, float* o){
  int c = blockIdx.x*256+threadIdx.x; int s = blockIdx.y; int h=c>>4, e=c&15;
  float num=0, den=0;
  for(int d=0;d<16;d++){ float pq=qkv[s*1536+h*16+d]; num+=pq*kv[h*256+d*16+e]; den+=pq*z[h*16+d]; }
  o[s*D+c]=rbf(rbf(num)/rbf(rbf(den)+1e-6f));
}
__global__ void k_outa(const float* o, const u16* wo, float* out){
  int n = blockIdx.x*256+threadIdx.x; int s = blockIdx.y;
  float a=0; for(int k=0;k<D;k++) a += o[s*D+k]*bf2f(wo[k*D+n]);
  out[s*1024+n]=rbf(a);
}
__global__ void k_bu(const u16* x, const u16* bre, const u16* bim, float2* bu){
  int p = blockIdx.x*256+threadIdx.x; int s = blockIdx.y;
  float a=0,b=0; for(int k=0;k<D;k++){ float xv=bf2f(x[s*D+k]); a += xv*bf2f(bre[p*D+k]); b+= xv*bf2f(bim[p*D+k]); }
  bu[s*D+p]=make_float2(a,b);
}
__global__ void k_scan(const u16* lre, const u16* lim, const u16* ldt, float2* bu){
  int p = blockIdx.x*64+threadIdx.x;
  float dt = rbf(expf(bf2f(ldt[p]))); float lr=bf2f(lre[p]), li=bf2f(lim[p]);
  float m = expf(dt*lr); float sn,cs; sincosf(dt*li,&sn,&cs);
  float ar=m*cs, ai=m*sn; float nr=ar-1.f, ni=ai; float inv=1.f/(lr*lr+li*li);
  float cr=(nr*lr+ni*li)*inv, ci=(ni*lr-nr*li)*inv;
  float hr=0,hi=0;
  for(int s=0;s<S;s++){ float2 u=bu[s*D+p]; float ur=u.x*cr-u.y*ci, ui=u.x*ci+u.y*cr;
    float nr2=ar*hr-ai*hi+ur, ni2=ar*hi+ai*hr+ui; hr=nr2; hi=ni2; bu[s*D+p]=make_float2(hr,hi);}
}
__global__ void k_y(const float2* h, const u16* cre, const u16* cim, const u16* x, const u16* dsk, float* out){
  int d = blockIdx.x*256+threadIdx.x; int s = blockIdx.y;
  float a=0; for(int p=0;p<D;p++){ float2 hv=h[s*D+p]; a += hv.x*bf2f(cre[d*D+p]) - hv.y*bf2f(cim[d*D+p]); }
  out[s*1024+512+d]=2.f*a + rbf(bf2f(x[s*D+d])*bf2f(dsk[d]));
}
extern "C" void kernel_launch(void* const* d_in, const int* in_sizes, int n_in, void* d_out, int out_size, void* d_ws, size_t ws_size, hipStream_t stream){
  const u16* xa=(const u16*)d_in[0]; const u16* wqkv=(const u16*)d_in[1]; const u16* wo=(const u16*)d_in[2];
  const u16* xb=(const u16*)d_in[3]; const u16* lre=(const u16*)d_in[4]; const u16* lim=(const u16*)d_in[5];
  const u16* bre=(const u16*)d_in[6]; const u16* bim=(const u16*)d_in[7]; const u16* cre=(const u16*)d_in[8]; const u16* cim=(const u16*)d_in[9];
  const u16* ldt=(const u16*)d_in[10]; const u16* dsk=(const u16*)d_in[11];
  float* out=(float*)d_out; float* ws=(float*)d_ws;
  float* qkv=ws; float* kv=qkv+S*1536; float* z=kv+32*256; float* o=z+512; float2* bu=(float2*)(o+S*D);
  k_qkv<<<dim3(6,S),256,0,stream>>>(xa,wqkv,qkv);
  k_kv<<<32,256,0,stream>>>(qkv,kv,z);
  k_o<<<dim3(2,S),256,0,stream>>>(qkv,kv,z,o);
  k_outa<<<dim3(2,S),256,0,stream>>>(o,wo,out);
  k_bu<<<dim3(2,S),256,0,stream>>>(xb,bre,bim,bu);
  k_scan<<<8,64,0,stream>>>(lre,lim,ldt,bu);
  k_y<<<dim3(2,S),256,0,stream>>>(bu,cre,cim,xb,dsk,out);
}
```

```cpp
#include <hip/hip_runtime.h>
#include <hip/hip_bf16.h>

#define DI __device__ __forceinline__
#define LAS __attribute__((address_space(3)))
#define GAS __attribute__((address_space(1)))
typedef unsigned short u16;
typedef unsigned long long u64;
typedef __bf16 bf8 __attribute__((ext_vector_type(8)));
typedef __bf16 bf2 __attribute__((ext_vector_type(2)));
typedef short s4 __attribute__((ext_vector_type(4)));
typedef short s8 __attribute__((ext_vector_type(8)));
typedef float f16v __attribute__((ext_vector_type(16)));
typedef float f4 __attribute__((ext_vector_type(4)));
typedef float f2 __attribute__((ext_vector_type(2)));
typedef unsigned u4 __attribute__((ext_vector_type(4)));
typedef unsigned u2 __attribute__((ext_vector_type(2)));

constexpr unsigned WS_EPOCH = 0;
constexpr unsigned WS_FA = 4096;
constexpr unsigned WS_FB = WS_FA + 65536;
constexpr unsigned WS_EST = WS_FB + 65536;
constexpr unsigned WS_Z = WS_EST + 131072;
constexpr unsigned WS_KV = WS_Z + 32768;
constexpr unsigned WS_O = 1u << 20;
constexpr unsigned WS_H = 3u << 20;
constexpr unsigned WS_END = 7u << 20;
constexpr unsigned WS_STAMP = 8u << 20;
constexpr int LDS_BYTES = 160 * 1024;
constexpr unsigned SPIN_CAP = 200000;
constexpr int SC1 = 16;

#define ROT 1

#define ST(i) do {} while (0)

DI unsigned pk2(float a, float b) { f2 v = {a, b}; return __builtin_bit_cast(unsigned, __builtin_convertvector(v, bf2)); }
DI float bflo(unsigned v) { return __uint_as_float(v << 16); }
DI float bfhi(unsigned v) { return __uint_as_float(v & 0xffff0000u); }
DI bf8 asbf8(u4 v) { return __builtin_bit_cast(bf8, v); }
DI f16v mfma32(bf8 a, bf8 b, f16v c) { return __builtin_amdgcn_mfma_f32_32x32x16_bf16(a, b, c, 0, 0, 0); }
DI f4 mfma16(bf8 a, bf8 b, f4 c) { return __builtin_amdgcn_mfma_f32_16x16x32_bf16(a, b, c, 0, 0, 0); }
DI f4 mfma16k16(u2 a, u2 b, f4 c) { return __builtin_amdgcn_mfma_f32_16x16x16bf16_1k(__builtin_bit_cast(s4, a), __builtin_bit_cast(s4, b), c, 0, 0, 0); }
DI bf8 ldsr(const char LAS* p) { return *(const LAS bf8*)p; }
DI bf8 ldstr(const char LAS* p0, const char LAS* p1) {
  s4 a = __builtin_amdgcn_ds_read_tr16_b64_v4i16((LAS s4*)p0), b = __builtin_amdgcn_ds_read_tr16_b64_v4i16((LAS s4*)p1);
  return __builtin_bit_cast(bf8, (s8)__builtin_shufflevector(a, b, 0, 1, 2, 3, 4, 5, 6, 7));
}
DI void dma4(unsigned lds, const char* sb, unsigned v0, unsigned v1, unsigned v2, unsigned v3) {
  asm volatile("s_mov_b32 m0, %0\n\ts_nop 0\n\tglobal_load_lds_dwordx4 %1, %5\n\tglobal_load_lds_dwordx4 %2, %5 offset:1024\n\t"
               "global_load_lds_dwordx4 %3, %5 offset:2048\n\tglobal_load_lds_dwordx4 %4, %5 offset:3072"
               :: "s"(lds), "v"(v0), "v"(v1), "v"(v2), "v"(v3), "s"(sb) : "memory");
}
DI void dma2(unsigned lds, const char* sb, unsigned v0, unsigned v1) {
  asm volatile("s_mov_b32 m0, %0\n\ts_nop 0\n\tglobal_load_lds_dwordx4 %1, %3\n\tglobal_load_lds_dwordx4 %2, %3 offset:1024"
               :: "s"(lds), "v"(v0), "v"(v1), "s"(sb) : "memory");
}
template <int N> DI void wait_bar() { asm volatile("s_waitcnt vmcnt(%0) lgkmcnt(0)\n\ts_barrier" :: "i"(N) : "memory"); }
DI void drain() { asm volatile("s_waitcnt vmcnt(0)" ::: "memory"); }
DI unsigned tagv(float x, unsigned t) { return (__float_as_uint(x) & 0xffffff00u) | t; }
DI f2 cmul(f2 a, f2 b) { return (f2){a.x * b.x - a.y * b.y, a.x * b.y + a.y * b.x}; }
DI f2 cfma(f2 a, f2 b, f2 c) { return (f2){fmaf(a.x, b.x, fmaf(-a.y, b.y, c.x)), fmaf(a.x, b.y, fmaf(a.y, b.x, c.y))}; }
DI f2 fma2(f2 a, f2 b, f2 c) { return __builtin_elementwise_fma(a, b, c); }
DI float phi(float t) { return fmaxf(t, 0.f) + __builtin_amdgcn_fmed3f(__builtin_amdgcn_exp2f(t * 1.44269504088896341f), 0.f, 1.f) + 1e-6f; }

DI void wait_flags(__amdgpu_buffer_rsrc_t ws, unsigned off, bool active, unsigned tag) {
  for (unsigned sp = 0; sp < SPIN_CAP; ++sp) {
    u2 f = __builtin_amdgcn_raw_buffer_load_b64(ws, off, 0, SC1);
    bool ok = !active || (f.x == tag && f.y == ~tag);
    if (__all(ok)) break;
    __builtin_amdgcn_s_sleep(1);
  }
  asm volatile("" ::: "memory");
}

__global__ void __launch_bounds__(256, 2)
fused_kernel(const u16* __restrict__ xa, const u16* __restrict__ wqkv, const u16* __restrict__ wo, const u16* __restrict__ xb,
             const u16* __restrict__ lre, const u16* __restrict__ lim, const u16* __restrict__ bre, const u16* __restrict__ bim,
             const u16* __restrict__ cre, const u16* __restrict__ cim, const u16* __restrict__ ldt, const u16* __restrict__ dsk,
             float* __restrict__ out, char* wsp) {
  extern __shared__ __attribute__((aligned(16))) char smem_[];
  const char LAS* L = (const char LAS*)smem_;
  const unsigned L0 = (unsigned)(size_t)L;
  ST(0);
  const int lane = threadIdx.x & 63;
  const int w = __builtin_amdgcn_readfirstlane(threadIdx.x >> 6);
  const int bid = blockIdx.x, xcd = bid & 7, jj = bid >> 3;
  const int tile = (xcd & 3) * 4 + (jj >> 3), cg = jj & 7;
  const int rot = ROT ? ((cg + 3 * (jj >> 3) + xcd) & 7) : 0;
  unsigned epoch;
  asm volatile("" :: "s"(xa), "s"(wqkv), "s"(wo), "s"(xb), "s"(lre), "s"(lim), "s"(bre), "s"(bim), "s"(cre), "s"(cim), "s"(ldt), "s"(dsk), "s"(out), "s"(wsp));
  const __amdgpu_buffer_rsrc_t ws = __builtin_amdgcn_make_buffer_rsrc(wsp, 0, WS_END, 0x00020000);
  const int r8 = lane >> 3, c8 = lane & 7;
  unsigned tag;

  if (xcd < 4) {
    constexpr unsigned SLOT = 40960;
    unsigned vx[4], vw[2];
#pragma unroll
    for (int i = 0; i < 4; ++i) vx[i] = (32 * w + 8 * i + r8) * 1024 + ((c8 ^ ((4 * i + (r8 >> 1)) & 7)) << 4) - 1024 * i;
#pragma unroll
    for (int i = 0; i < 2; ++i) vw[i] = (16 * w + 8 * i + r8) * 3072 + ((c8 ^ ((((r8 >> 1) & 1) | (i << 1)) << 1)) << 4) - 1024 * i;
    const char* xbase = (const char*)xa + (size_t)tile * 131072;
    const char* wbase = (const char*)wqkv + cg * 128;
    auto issue = [&](int kk) {
      const int kc = (kk + rot) & 7;
      const unsigned s = L0 + (kk & 3) * SLOT;
      dma4(s + w * 4096, xbase + kc * 128, vx[0], vx[1], vx[2], vx[3]);
      const char* wb = wbase + kc * (64 * 3072);
      dma2(s + 16384 + w * 2048, wb, vw[0], vw[1]);
      dma2(s + 24576 + w * 2048, wb + 1024, vw[0], vw[1]);
      dma2(s + 32768 + w * 2048, wb + 2048, vw[0], vw[1]);
    };
    issue(0); issue(1); issue(2); issue(3);
    asm volatile("s_load_dword %0, %1, 0x0" : "=s"(epoch) : "s"(wsp) : "memory");
    unsigned vo[4];
#pragma unroll
    for (int i = 0; i < 4; ++i) vo[i] = (64 * w + 8 * i + r8) * 1024 + ((c8 ^ (((r8 >> 1) & 3) << 1)) << 4) - 1024 * i;
    const char* obase = (const char*)wo + cg * 128;
    auto issue_wo = [&](int piece) {
      dma4(L0 + piece * SLOT + w * 8192, obase + piece * 262144, vo[0], vo[1], vo[2], vo[3]);
      dma4(L0 + piece * SLOT + w * 8192 + 4096, obase + piece * 262144 + 32768, vo[0], vo[1], vo[2], vo[3]);
    };
    const int q = lane >> 4, l15 = lane & 15, qq = (lane >> 2) & 3, pp = lane & 3;
    const unsigned ax0 = l15 * 128 + ((q ^ (l15 >> 1)) << 4), ax1 = l15 * 128 + (((4 + q) ^ (l15 >> 1)) << 4);
    const unsigned aw = 16384 + (8 * q + qq) * 128 + (((2 * w + (pp >> 1)) ^ ((((qq >> 1) & 1) | ((q & 1) << 1)) << 1)) << 4) + 8 * (pp & 1);
    f4 ka[8], va[8], qa[8];
#pragma unroll
    for (int i = 0; i < 8; ++i) { ka[i] = (f4){0, 0, 0, 0}; va[i] = ka[i]; qa[i] = ka[i]; }
    auto compute = [&](int kk) {
      const char LAS* s = L + (kk & 3) * SLOT;
#pragma unroll
      for (int ks = 0; ks < 2; ++ks) {
        const char LAS* wp_ = s + aw + ks * 4096;
        const bf8 fq = ldstr(wp_, wp_ + 512), fk = ldstr(wp_ + 8192, wp_ + 8192 + 512), fv = ldstr(wp_ + 16384, wp_ + 16384 + 512);
#pragma unroll
        for (int mt = 0; mt < 8; ++mt) {
          const bf8 fx = ldsr(s + (ks ? ax1 : ax0) + mt * 2048);
          ka[mt] = mfma16(fx, fk, ka[mt]);
          va[mt] = mfma16(fx, fv, va[mt]);
          qa[mt] = mfma16(fq, fx, qa[mt]);
        }
      }
    };
    asm volatile("s_waitcnt vmcnt(30) lgkmcnt(0)\n\ts_barrier" : "+s"(epoch) :: "memory");
    ST(1);
    tag = epoch + 1;
    compute(0);
    wait_bar<20>(); issue(4); compute(1);
    wait_bar<20>(); issue(5); compute(2);
    wait_bar<20>(); issue(6); compute(3);
    wait_bar<20>(); issue(7); compute(4);
    wait_bar<20>(); issue_wo(0); compute(5);
    wait_bar<18>(); issue_wo(1); compute(6);
    wait_bar<16>(); ST(2); compute(7);
    ST(3);
    const unsigned t0 = tag & 0xffu, t1 = (tag >> 8) & 0xffu;
    const int head = cg * 4 + w;
    {
      f4 kv = {0, 0, 0, 0}, zz = {0, 0, 0, 0};
      const u4 ones = {0x3f803f80u, 0x3f803f80u, 0x3f803f80u, 0x3f803f80u};
#pragma unroll
      for (int mm = 0; mm < 4; ++mm) {
        const f4 a = ka[2 * mm], b = ka[2 * mm + 1], c = va[2 * mm], d = va[2 * mm + 1];
        const u4 pa = {pk2(phi(a[0]), phi(a[1])), pk2(phi(a[2]), phi(a[3])), pk2(phi(b[0]), phi(b[1])), pk2(phi(b[2]), phi(b[3]))};
        const u4 pb = {pk2(c[0], c[1]), pk2(c[2], c[3]), pk2(d[0], d[1]), pk2(d[2], d[3])};
        kv = mfma16(asbf8(pa), asbf8(pb), kv);
        zz = mfma16(asbf8(pa), asbf8(ones), zz);
      }
      const u4 skv = {tagv(kv[0], t0), tagv(kv[1], t1), tagv(kv[2], t0), tagv(kv[3], t1)};
      __builtin_amdgcn_raw_buffer_store_b128(skv, ws, WS_KV + (head * 16 + tile) * 1024 + lane * 16, 0, SC1);
      const u4 sz = {tagv(zz[0], t0), tagv(zz[1], t1), tagv(zz[2], t0), tagv(zz[3], t1)};
      if (l15 == 0) __builtin_amdgcn_raw_buffer_store_b128(sz, ws, WS_Z + (head * 16 + tile) * 64 + q * 16, 0, SC1);
    }
    ST(4);
    u2 pq[8];
#pragma unroll
    for (int mt = 0; mt < 8; ++mt) pq[mt] = (u2){pk2(phi(qa[mt][0]), phi(qa[mt][1])), pk2(phi(qa[mt][2]), phi(qa[mt][3]))};
    f4 kv, zz;
    for (unsigned sp = 0; sp < SPIN_CAP; ++sp) {
      u4 g[16];
      const u4 gz = __builtin_amdgcn_raw_buffer_load_b128(ws, WS_Z + head * 1024 + l15 * 64 + q * 16, 0, SC1);
#pragma unroll
      for (int i = 0; i < 16; ++i) g[i] = __builtin_amdgcn_raw_buffer_load_b128(ws, WS_KV + head * 16384 + lane * 16, i * 1024, SC1);
      const u4 tt = {t0, t1, t0, t1};
      const u4 cz = gz ^ tt;
      unsigned bad = cz.x | cz.y | cz.z | cz.w;
      kv = (f4){0, 0, 0, 0};
#pragma unroll
      for (int i = 0; i < 16; ++i) {
        const u4 c = g[i] ^ tt;
        bad |= c.x | c.y | c.z | c.w;
        kv += __builtin_bit_cast(f4, c);
      }
      zz = __builtin_bit_cast(f4, cz);
      if (__all((bad & 0xffu) == 0)) break;
      __builtin_amdgcn_s_sleep(1);
    }
    ST(5);
#pragma unroll
    for (int r = 0; r < 4; ++r) {
      float v = zz[r];
      v += __builtin_bit_cast(float, __builtin_amdgcn_update_dpp(0, __builtin_bit_cast(int, v), 0x128, 0xf, 0xf, false));
      v += __builtin_bit_cast(float, __builtin_amdgcn_update_dpp(0, __builtin_bit_cast(int, v), 0x124, 0xf, 0xf, false));
      v += __builtin_bit_cast(float, __builtin_amdgcn_update_dpp(0, __builtin_bit_cast(int, v), 0x122, 0xf, 0xf, false));
      v += __builtin_bit_cast(float, __builtin_amdgcn_update_dpp(0, __builtin_bit_cast(int, v), 0x121, 0xf, 0xf, false));
      zz[r] = v;
    }
    const u2 kvb = {pk2(kv[0], kv[1]), pk2(kv[2], kv[3])}, zb = {pk2(zz[0], zz[1]), pk2(zz[2], zz[3])};
    const f4 zero4 = {0, 0, 0, 0};
#pragma unroll
    for (int u = 0; u < 4; ++u) {
      u4 so;
#pragma unroll
      for (int hh = 0; hh < 2; ++hh) {
        const f4 num = mfma16k16(kvb, pq[2 * u + hh], zero4), den = mfma16k16(zb, pq[2 * u + hh], zero4);
        const float rc = __builtin_amdgcn_rcpf(den[0] + 1e-6f);
        so[2 * hh] = pk2(num[0] * rc, num[1] * rc);
        so[2 * hh + 1] = pk2(num[2] * rc, num[3] * rc);
      }
      __builtin_amdgcn_raw_buffer_store_b128(so, ws, WS_O + ((tile * 4 + u) * 32 + head) * 1024 + lane * 16, 0, SC1);
    }
    ST(6);
    drain();
    if (lane == 0) __builtin_amdgcn_raw_buffer_store_b64((u2){tag, ~tag}, ws, WS_FA + (tile * 32 + head) * 128, 0, SC1);
    asm volatile("s_barrier" ::: "memory");
    ST(7);
    wait_flags(ws, WS_FA + (tile * 32 + (lane & 31)) * 128, true, tag);
    ST(8);
    unsigned ao[4];
#pragma unroll
    for (int nt = 0; nt < 4; ++nt) ao[nt] = (4 * q + qq) * 128 + ((((2 * nt) ^ (((2 * q + (qq >> 1)) & 3) << 1)) + (pp >> 1)) << 4) + 8 * (pp & 1);
    f4 acc[2][4];
#pragma unroll
    for (int i = 0; i < 8; ++i) acc[i >> 2][i & 3] = zero4;
    const unsigned ob = WS_O + (tile * 4 + w) * 32768 + lane * 16;
    u4 of[32];
#pragma unroll
    for (int i = 0; i < 32; ++i) of[i] = __builtin_amdgcn_raw_buffer_load_b128(ws, ob, i * 1024, SC1);
#pragma unroll
    for (int k = 0; k < 16; ++k) {
      const u4 a = of[2 * k], b = of[2 * k + 1];
      const bf8 f0 = asbf8((u4){a.x, a.y, b.x, b.y}), f1 = asbf8((u4){a.z, a.w, b.z, b.w});
      const char LAS* base = L + (k >> 3) * SLOT + (k & 7) * 4096;
#pragma unroll
      for (int nt = 0; nt < 4; ++nt) {
        const bf8 fw = ldstr(base + ao[nt], base + ao[nt] + 2048);
        acc[0][nt] = mfma16(fw, f0, acc[0][nt]);
        acc[1][nt] = mfma16(fw, f1, acc[1][nt]);
      }
    }
    ST(9);
#pragma unroll
    for (int mt = 0; mt < 2; ++mt)
#pragma unroll
      for (int nt = 0; nt < 4; ++nt) {
        const f4 v = acc[mt][nt];
        const unsigned p0 = pk2(v[0], v[1]), p1 = pk2(v[2], v[3]);
        *(f4*)(out + (size_t)(tile * 128 + w * 32 + mt * 16 + l15) * 1024 + cg * 64 + nt * 16 + q * 4) = (f4){bflo(p0), bfhi(p0), bflo(p1), bfhi(p1)};
      }
    ST(10);
    if (bid == 0 && w == 0) {
      wait_flags(ws, WS_FA + lane * 1024, true, tag);
      wait_flags(ws, WS_FA + 512 + lane * 1024, true, tag);
      wait_flags(ws, WS_FB + (lane >> 3) * 4096 + (lane & 7) * 256, true, tag);
      wait_flags(ws, WS_FB + 32768 + (lane >> 3) * 4096 + (lane & 7) * 256, true, tag);
      if (lane == 0) __builtin_amdgcn_raw_buffer_store_b32(tag, ws, WS_EPOCH, 0, SC1);
    }
  } else {
    constexpr unsigned SLOT = 32768;
    const int wp = w & 1, wt = w >> 1, l31 = lane & 31, hh = lane >> 5;
    const int pl = cg * 64 + wp * 32 + ((l31 & 0x13) | ((l31 & 4) << 1) | ((l31 & 8) >> 1));
    unsigned plr, pli, pdt;
    asm volatile("global_load_ushort %0, %3, %4\n\tglobal_load_ushort %1, %3, %5\n\tglobal_load_ushort %2, %3, %6"
                 : "=&v"(plr), "=&v"(pli), "=&v"(pdt) : "v"(pl * 2), "s"(lre), "s"(lim), "s"(ldt) : "memory");
    unsigned vx[4], vw[4];
#pragma unroll
    for (int i = 0; i < 4; ++i) {
      const unsigned sw = (c8 ^ ((4 * i + (r8 >> 1)) & 7)) << 4;
      vx[i] = (64 * (w >> 1) + 32 * (r8 >> 2) + 16 * (r8 & 1) + 8 * (w & 1) + ((r8 >> 1) & 1) + 2 * i) * 1024 + sw - 1024 * i;
      vw[i] = (16 * (i >> 1) + 8 * (r8 >> 2) + 4 * (i & 1) + (r8 & 3)) * 1024 + sw - 1024 * i;
    }
    const char* xbase = (const char*)xb + (size_t)tile * 131072;
    const char* wbase = (const char*)((w & 1) ? bim : bre) + (cg * 64 + (w >> 1) * 32) * 1024;
    auto issue = [&](int kk) {
      const int kc = (kk + rot) & 7;
      const unsigned s = L0 + (kk % 5) * SLOT + w * 4096;
      dma4(s, xbase + kc * 128, vx[0], vx[1], vx[2], vx[3]);
      dma4(s + 16384, wbase + kc * 128, vw[0], vw[1], vw[2], vw[3]);
    };
    issue(0); issue(1); issue(2); issue(3); issue(4);
    asm volatile("s_load_dword %0, %1, 0x0" : "=s"(epoch) : "s"(wsp) : "memory");
    unsigned vc[4];
#pragma unroll
    for (int i = 0; i < 4; ++i) vc[i] = (8 * i + r8) * 1024 + ((c8 ^ ((4 * i + (r8 >> 1)) & 7)) << 4) - 1024 * i;
    const char* cbase = (const char*)((w & 1) ? cim : cre) + cg * 65536;
    const int crot = cg + 2 * (jj >> 3);
    auto issue_c = [&](int piece) {
      const unsigned s = L0 + ((piece + 3) % 5) * SLOT + w * 8192;
      const char* cb = cbase + ((crot + 2 * piece + (w >> 1)) & 7) * 128;
      dma4(s, cb, vc[0], vc[1], vc[2], vc[3]);
      dma4(s + 4096, cb + 32768, vc[0], vc[1], vc[2], vc[3]);
    };
    asm volatile("s_waitcnt vmcnt(40)" : "+v"(plr), "+v"(pli), "+v"(pdt) :: "memory");
    f2 lam, kap, l16, l32, l64, l128;
    {
      const float lr = bflo(plr), li = bflo(pli);
      const float dt = bflo(pk2(expf(bflo(pdt)), 0.f));
      const float mag = expf(dt * lr);
      float sn, cs;
      sincosf(dt * li, &sn, &cs);
      lam = (f2){mag * cs, mag * sn};
      const float inv = 2.f / (lr * lr + li * li), nr = lam.x - 1.f, ni = lam.y;
      kap = (f2){(nr * lr + ni * li) * inv, (ni * lr - nr * li) * inv};
      f2 t = cmul(lam, lam); t = cmul(t, t); t = cmul(t, t);
      l16 = cmul(t, t); l32 = cmul(l16, l16); l64 = cmul(l32, l32); l128 = cmul(l64, l64);
    }
    unsigned ar[4];
#pragma unroll
    for (int s = 0; s < 4; ++s) ar[s] = l31 * 128 + (((2 * s + hh) ^ ((lane >> 1) & 7)) << 4);
    f16v ur[2], ui[2];
#pragma unroll
    for (int i = 0; i < 16; ++i) { ur[0][i] = 0; ur[1][i] = 0; ui[0][i] = 0; ui[1][i] = 0; }
    auto compute = [&](int kk) {
      const char LAS* sx = L + (kk % 5) * SLOT + wt * 8192;
      const char LAS* sw = L + (kk % 5) * SLOT + 16384 + wp * 8192;
#pragma unroll
      for (int s = 0; s < 4; ++s) {
        const bf8 x0 = ldsr(sx + ar[s]), x1 = ldsr(sx + ar[s] + 4096), b0 = ldsr(sw + ar[s]), b1 = ldsr(sw + ar[s] + 4096);
        ur[0] = mfma32(x0, b0, ur[0]); ui[0] = mfma32(x0, b1, ui[0]);
        ur[1] = mfma32(x1, b0, ur[1]); ui[1] = mfma32(x1, b1, ui[1]);
      }
    };
    asm volatile("s_waitcnt vmcnt(32) lgkmcnt(0)\n\ts_barrier" : "+s"(epoch) :: "memory");
    ST(1);
    tag = epoch + 1;
    compute(0);
    wait_bar<24>(); issue(5); compute(1);
    wait_bar<24>(); issue(6); compute(2);
    wait_bar<24>(); issue(7); compute(3);
    wait_bar<24>(); issue_c(0); compute(4);
    wait_bar<24>(); issue_c(1); compute(5);
    wait_bar<24>(); issue_c(2); compute(6);
    wait_bar<24>(); ST(2); issue_c(3); compute(7);
    ST(3);
    const unsigned t0 = tag & 0xffu, t1 = (tag >> 8) & 0xffu;
    const f2 LR = {lam.x, lam.x}, LI = {lam.y, lam.y};
    f2 er = {0, 0}, ei = {0, 0};
#pragma unroll
    for (int a = 0; a < 16; ++a) {
      const f2 xr = {ur[a >> 3][2 * (a & 7)], ur[a >> 3][2 * (a & 7) + 1]}, xi = {ui[a >> 3][2 * (a & 7)], ui[a >> 3][2 * (a & 7) + 1]};
      const f2 nr = fma2(LR, er, fma2(-LI, ei, xr)), ni = fma2(LR, ei, fma2(LI, er, xi));
      er = nr; ei = ni;
    }
    const f2 e0 = {er.x, ei.x};
    const f2 e32 = cfma(l16, e0, (f2){er.y, ei.y});
    const auto sx_ = __builtin_amdgcn_permlane32_swap(__float_as_uint(e32.x), __float_as_uint(e32.x), false, false);
    const auto sy_ = __builtin_amdgcn_permlane32_swap(__float_as_uint(e32.y), __float_as_uint(e32.y), false, false);
    const f2 elo = {__uint_as_float(sx_[0]), __uint_as_float(sy_[0])}, ehi = {__uint_as_float(sx_[1]), __uint_as_float(sy_[1])};
    const f2 e64 = cfma(l32, elo, ehi);
    const int J = tile * 2 + wt;
    const unsigned ecol = WS_EST + (cg * 64 + wp * 32 + l31) * 8;
    if (hh == 0) __builtin_amdgcn_raw_buffer_store_b64((u2){tagv(e64.x, t0), tagv(e64.y, t1)}, ws, ecol + J * 4096, 0, SC1);
    ST(4);
    {
      const f2 KR = {kap.x, kap.x}, KI = {kap.y, kap.y};
#pragma unroll
      for (int a = 0; a < 16; ++a) {
        const f2 xr = {ur[a >> 3][2 * (a & 7)], ur[a >> 3][2 * (a & 7) + 1]}, xi = {ui[a >> 3][2 * (a & 7)], ui[a >> 3][2 * (a & 7) + 1]};
        const f2 nr = fma2(KR, xr, -KI * xi), ni = fma2(-KR, xi, -KI * xr);
        ur[a >> 3][2 * (a & 7)] = nr.x; ur[a >> 3][2 * (a & 7) + 1] = nr.y;
        ui[a >> 3][2 * (a & 7)] = ni.x; ui[a >> 3][2 * (a & 7) + 1] = ni.y;
      }
    }
    f2 cin;
    for (unsigned sp = 0; sp < SPIN_CAP; ++sp) {
      u2 g[16];
#pragma unroll
      for (int m = 0; m < 16; ++m) {
        const int i = J - 1 - hh - 2 * m;
        g[m] = __builtin_amdgcn_raw_buffer_load_b64(ws, i >= 0 ? ecol + i * 4096 : 0xfffffff0u, 0, SC1);
      }
      unsigned bad = 0;
      cin = (f2){0, 0};
#pragma unroll
      for (int m = 15; m >= 0; --m) {
        const int i = J - 1 - hh - 2 * m;
        const unsigned cx = i >= 0 ? g[m].x ^ t0 : 0u, cy = i >= 0 ? g[m].y ^ t1 : 0u;
        bad |= cx | cy;
        cin = cfma(l128, cin, (f2){__uint_as_float(cx), __uint_as_float(cy)});
      }
      if (__all((bad & 0xffu) == 0)) break;
      __builtin_amdgcn_s_sleep(1);
    }
    ST(5);
    {
      const auto cx = __builtin_amdgcn_permlane32_swap(__float_as_uint(cin.x), __float_as_uint(cin.x), false, false);
      const auto cy = __builtin_amdgcn_permlane32_swap(__float_as_uint(cin.y), __float_as_uint(cin.y), false, false);
      cin = cfma(l64, (f2){__uint_as_float(cx[1]), __uint_as_float(cy[1])}, (f2){__uint_as_float(cx[0]), __uint_as_float(cy[0])});
    }
    const f2 up = cfma(l32, cin, elo);
    const f2 c0 = hh ? up : cin;
    const f2 c1 = cfma(l16, c0, e0);
    const f2 d0 = cmul(kap, c0), d1 = cmul(kap, c1);
    f2 hr = {d0.x, d1.x}, hi = {-d0.y, -d1.y};
    u4 idn[2];
#pragma unroll
    for (int s = 0; s < 2; ++s)
#pragma unroll
      for (int jq = 0; jq < 4; ++jq) {
        const int r0 = 16 * s + 8 * (jq >> 1) + 4 * hh + 2 * (jq & 1);
        idn[s][jq] = (l31 == r0 ? 0x3f80u : 0u) | (l31 == r0 + 1 ? 0x3f800000u : 0u);
      }
    f16v zero16;
#pragma unroll
    for (int i = 0; i < 16; ++i) zero16[i] = 0;
#pragma unroll
    for (int m = 0; m < 2; ++m) {
#pragma unroll
      for (int a = 0; a < 8; ++a) {
        const f2 xr = {ur[m][2 * a], ur[m][2 * a + 1]}, xi = {ui[m][2 * a], ui[m][2 * a + 1]};
        const f2 nr = fma2(LR, hr, fma2(LI, hi, xr)), ni = fma2(LR, hi, fma2(-LI, hr, xi));
        hr = nr; hi = ni;
        ur[m][2 * a] = nr.x; ur[m][2 * a + 1] = nr.y; ui[m][2 * a] = ni.x; ui[m][2 * a + 1] = ni.y;
      }
#pragma unroll
      for (int c = 0; c < 2; ++c) {
        const f16v X = c ? ui[m] : ur[m];
        const u4 a0 = {pk2(X[0], X[1]), pk2(X[2], X[3]), pk2(X[4], X[5]), pk2(X[6], X[7])};
        const u4 a1 = {pk2(X[8], X[9]), pk2(X[10], X[11]), pk2(X[12], X[13]), pk2(X[14], X[15])};
        f16v T = mfma32(asbf8(a0), asbf8(idn[0]), zero16);
        T = mfma32(asbf8(a1), asbf8(idn[1]), T);
        const u4 o0 = {pk2(T[0], T[1]), pk2(T[2], T[3]), pk2(T[4], T[5]), pk2(T[6], T[7])};
        const u4 o1 = {pk2(T[8], T[9]), pk2(T[10], T[11]), pk2(T[12], T[13]), pk2(T[14], T[15])};
        const unsigned ho = WS_H + (tile * 4 + wt * 2 + m) * 65536 + ((cg * 2 + wp) * 2 + c) * 2048 + lane * 16;
        __builtin_amdgcn_raw_buffer_store_b128(o0, ws, ho, 0, SC1);
        __builtin_amdgcn_raw_buffer_store_b128(o1, ws, ho + 1024, 0, SC1);
      }
    }
    ST(6);
    drain();
    if (lane == 0) __builtin_amdgcn_raw_buffer_store_b64((u2){tag, ~tag}, ws, WS_FB + ((tile * 2 + wt) * 16 + cg * 2 + wp) * 128, 0, SC1);
    asm volatile("s_barrier" ::: "memory");
    ST(7);
    const int tok = tile * 128 + 64 * (w >> 1) + 32 * ((l31 >> 2) & 1) + 16 * (l31 & 1) + 8 * (w & 1) + ((l31 >> 1) & 1) + 2 * (l31 >> 3);
    f16v acc[2];
#pragma unroll
    for (int nt = 0; nt < 2; ++nt)
#pragma unroll
      for (int g = 0; g < 4; ++g) {
        const int d = cg * 64 + nt * 32 + g * 8 + hh * 4;
        const u2 xv = *(const u2*)(xb + tok * 512 + d), dv = *(const u2*)(dsk + d);
        const unsigned p0 = pk2(bflo(xv.x) * bflo(dv.x), bfhi(xv.x) * bfhi(dv.x)), p1 = pk2(bflo(xv.y) * bflo(dv.y), bfhi(xv.y) * bfhi(dv.y));
        acc[nt][4 * g] = bflo(p0); acc[nt][4 * g + 1] = bfhi(p0); acc[nt][4 * g + 2] = bflo(p1); acc[nt][4 * g + 3] = bfhi(p1);
      }
    wait_flags(ws, WS_FB + ((tile * 2 + (w >> 1)) * 16 + (lane & 15)) * 128, true, tag);
    ST(8);
    const unsigned hb = WS_H + (tile * 4 + w) * 65536 + lane * 16;
    u4 hf[3][8];
    auto loadh = [&](int t) {
      const unsigned so = ((crot + t) & 7) * 8192;
#pragma unroll
      for (int i = 0; i < 8; ++i) hf[t % 3][i] = __builtin_amdgcn_raw_buffer_load_b128(ws, hb + i * 1024, so, SC1);
    };
    loadh(0); loadh(1);
#pragma unroll
    for (int t = 0; t < 8; ++t) {
      if (t + 2 < 8) loadh(t + 2);
      __builtin_amdgcn_sched_barrier(0);
      const char LAS* cb = L + (((t >> 1) + 3) % 5) * SLOT + (t & 1) * 16384;
#pragma unroll
      for (int i = 0; i < 8; ++i) {
        const char LAS* p = cb + ((i >> 1) & 1) * 8192 + ar[(i >> 2) * 2 + (i & 1)];
        acc[0] = mfma32(ldsr(p), asbf8(hf[t % 3][i]), acc[0]);
        acc[1] = mfma32(ldsr(p + 4096), asbf8(hf[t % 3][i]), acc[1]);
      }
      __builtin_amdgcn_sched_barrier(0);
    }
    ST(9);
#pragma unroll
    for (int nt = 0; nt < 2; ++nt)
#pragma unroll
      for (int g = 0; g < 4; ++g)
        *(f4*)(out + (size_t)tok * 1024 + 512 + cg * 64 + nt * 32 + g * 8 + hh * 4) = (f4){acc[nt][4 * g], acc[nt][4 * g + 1], acc[nt][4 * g + 2], acc[nt][4 * g + 3]};
    ST(10);
  }
}

extern "C" void kernel_launch(void* const* d_in, const int* in_sizes, int n_in, void* d_out, int out_size, void* d_ws, size_t ws_size,
                              hipStream_t stream) {
  (void)hipFuncSetAttribute((const void*)fused_kernel, hipFuncAttributeMaxDynamicSharedMemorySize, LDS_BYTES);
  fused_kernel<<<256, 256, LDS_BYTES, stream>>>((const u16*)d_in[0], (const u16*)d_in[1], (const u16*)d_in[2], (const u16*)d_in[3],
                                               (const u16*)d_in[4], (const u16*)d_in[5], (const u16*)d_in[6], (const u16*)d_in[7],
                                               (const u16*)d_in[8], (const u16*)d_in[9], (const u16*)d_in[10], (const u16*)d_in[11],
                                               (float*)d_out, (char*)d_ws);
}
```

```cpp
#define AMP_A 0
#define AMP_B 0
#define AMP_BID 0
#define AMP_K 10
#include <hip/hip_runtime.h>
#include <hip/hip_bf16.h>

#define DI __device__ __forceinline__
#define LAS __attribute__((address_space(3)))
#define GAS __attribute__((address_space(1)))
typedef unsigned short u16;
typedef unsigned long long u64;
typedef __bf16 bf8 __attribute__((ext_vector_type(8)));
typedef __bf16 bf2 __attribute__((ext_vector_type(2)));
typedef short s4 __attribute__((ext_vector_type(4)));
typedef short s8 __attribute__((ext_vector_type(8)));
typedef float f16v __attribute__((ext_vector_type(16)));
typedef float f4 __attribute__((ext_vector_type(4)));
typedef float f2 __attribute__((ext_vector_type(2)));
typedef unsigned u4 __attribute__((ext_vector_type(4)));
typedef unsigned u2 __attribute__((ext_vector_type(2)));

constexpr unsigned WS_EPOCH = 0;
constexpr unsigned WS_FA = 4096;
constexpr unsigned WS_FB = WS_FA + 65536;
constexpr unsigned WS_EST = WS_FB + 65536;
constexpr unsigned WS_Z = WS_EST + 131072;
constexpr unsigned WS_KV = WS_Z + 32768;
constexpr unsigned WS_O = 1u << 20;
constexpr unsigned WS_H = 3u << 20;
constexpr unsigned WS_END = 7u << 20;
constexpr unsigned WS_STAMP = 8u << 20;
constexpr int LDS_BYTES = 160 * 1024;
constexpr unsigned SPIN_CAP = 200000;
constexpr int SC1 = 16;

#define ROT 1

#define ST(i) do { __builtin_amdgcn_sched_barrier(0); asm volatile("s_memrealtime %0\n\ts_waitcnt lgkmcnt(0)" : "=s"(st[i]) :: "memory"); __builtin_amdgcn_sched_barrier(0); } while (0)

DI unsigned pk2(float a, float b) { f2 v = {a, b}; return __builtin_bit_cast(unsigned, __builtin_convertvector(v, bf2)); }
DI float bflo(unsigned v) { return __uint_as_float(v << 16); }
DI float bfhi(unsigned v) { return __uint_as_float(v & 0xffff0000u); }
DI bf8 asbf8(u4 v) { return __builtin_bit_cast(bf8, v); }
DI f16v mfma32(bf8 a, bf8 b, f16v c) { return __builtin_amdgcn_mfma_f32_32x32x16_bf16(a, b, c, 0, 0, 0); }
DI f4 mfma16(bf8 a, bf8 b, f4 c) { return __builtin_amdgcn_mfma_f32_16x16x32_bf16(a, b, c, 0, 0, 0); }
DI f4 mfma16k16(u2 a, u2 b, f4 c) { return __builtin_amdgcn_mfma_f32_16x16x16bf16_1k(__builtin_bit_cast(s4, a), __builtin_bit_cast(s4, b), c, 0, 0, 0); }
DI bf8 ldsr(const char LAS* p) { return *(const LAS bf8*)p; }
DI bf8 ldstr(const char LAS* p0, const char LAS* p1) {
  s4 a = __builtin_amdgcn_ds_read_tr16_b64_v4i16((LAS s4*)p0), b = __builtin_amdgcn_ds_read_tr16_b64_v4i16((LAS s4*)p1);
  return __builtin_bit_cast(bf8, (s8)__builtin_shufflevector(a, b, 0, 1, 2, 3, 4, 5, 6, 7));
}
DI void dma4(unsigned lds, const char* sb, unsigned v0, unsigned v1, unsigned v2, unsigned v3) {
  asm volatile("s_mov_b32 m0, %0\n\ts_nop 0\n\tglobal_load_lds_dwordx4 %1, %5\n\tglobal_load_lds_dwordx4 %2, %5 offset:1024\n\t"
               "global_load_lds_dwordx4 %3, %5 offset:2048\n\tglobal_load_lds_dwordx4 %4, %5 offset:3072"
               :: "s"(lds), "v"(v0), "v"(v1), "v"(v2), "v"(v3), "s"(sb) : "memory");
}
DI void dma2(unsigned lds, const char* sb, unsigned v0, unsigned v1) {
  asm volatile("s_mov_b32 m0, %0\n\ts_nop 0\n\tglobal_load_lds_dwordx4 %1, %3\n\tglobal_load_lds_dwordx4 %2, %3 offset:1024"
               :: "s"(lds), "v"(v0), "v"(v1), "s"(sb) : "memory");
}
template <int N> DI void wait_bar() { asm volatile("s_waitcnt vmcnt(%0) lgkmcnt(0)\n\ts_barrier" :: "i"(N) : "memory"); }
DI void drain() { asm volatile("s_waitcnt vmcnt(0)" ::: "memory"); }
DI unsigned tagv(float x, unsigned t) { return (__float_as_uint(x) & 0xffffff00u) | t; }
DI f2 cmul(f2 a, f2 b) { return (f2){a.x * b.x - a.y * b.y, a.x * b.y + a.y * b.x}; }
DI f2 cfma(f2 a, f2 b, f2 c) { return (f2){fmaf(a.x, b.x, fmaf(-a.y, b.y, c.x)), fmaf(a.x, b.y, fmaf(a.y, b.x, c.y))}; }
DI f2 fma2(f2 a, f2 b, f2 c) { return __builtin_elementwise_fma(a, b, c); }
DI float phi(float t) { return fmaxf(t, 0.f) + __builtin_amdgcn_fmed3f(__builtin_amdgcn_exp2f(t * 1.44269504088896341f), 0.f, 1.f) + 1e-6f; }

DI void wait_flags(__amdgpu_buffer_rsrc_t ws, unsigned off, bool active, unsigned tag) {
  for (unsigned sp = 0; sp < SPIN_CAP; ++sp) {
    u2 f = __builtin_amdgcn_raw_buffer_load_b64(ws, off, 0, SC1);
    bool ok = !active || (f.x == tag && f.y == ~tag);
    if (__all(ok)) break;
    __builtin_amdgcn_s_sleep(1);
  }
  asm volatile("" ::: "memory");
}

__global__ void __launch_bounds__(256, 2)
fused_kernel(const u16* __restrict__ xa, const u16* __restrict__ wqkv, const u16* __restrict__ wo, const u16* __restrict__ xb,
             const u16* __restrict__ lre, const u16* __restrict__ lim, const u16* __restrict__ bre, const u16* __restrict__ bim,
             const u16* __restrict__ cre, const u16* __restrict__ cim, const u16* __restrict__ ldt, const u16* __restrict__ dsk,
             float* __restrict__ out, char* wsp) {
  extern __shared__ __attribute__((aligned(16))) char smem_[];
  const char LAS* L = (const char LAS*)smem_;
  const unsigned L0 = (unsigned)(size_t)L;
  u64 st[12];
  for (int i = 0; i < 12; ++i) st[i] = 0;
  ST(0);
  const int lane = threadIdx.x & 63;
  const int w = __builtin_amdgcn_readfirstlane(threadIdx.x >> 6);
  const int bid = blockIdx.x, xcd = bid & 7, jj = bid >> 3;
  const int tile = (xcd & 3) * 4 + (jj >> 3), cg = jj & 7;
  const int rot = ROT ? ((cg + 3 * (jj >> 3) + xcd) & 7) : 0;
  unsigned epoch;
  asm volatile("" :: "s"(xa), "s"(wqkv), "s"(wo), "s"(xb), "s"(lre), "s"(lim), "s"(bre), "s"(bim), "s"(cre), "s"(cim), "s"(ldt), "s"(dsk), "s"(out), "s"(wsp));
  const __amdgpu_buffer_rsrc_t ws = __builtin_amdgcn_make_buffer_rsrc(wsp, 0, WS_END, 0x00020000);
  const int r8 = lane >> 3, c8 = lane & 7;
  unsigned tag;

  if (xcd < 4) {
    constexpr unsigned SLOT = 40960;
    unsigned vx[4], vw[2];
#pragma unroll
    for (int i = 0; i < 4; ++i) vx[i] = (32 * w + 8 * i + r8) * 1024 + ((c8 ^ ((4 * i + (r8 >> 1)) & 7)) << 4) - 1024 * i;
#pragma unroll
    for (int i = 0; i < 2; ++i) vw[i] = (16 * w + 8 * i + r8) * 3072 + ((c8 ^ ((((r8 >> 1) & 1) | (i << 1)) << 1)) << 4) - 1024 * i;
    const char* xbase = (const char*)xa + (size_t)tile * 131072;
    const char* wbase = (const char*)wqkv + cg * 128;
    auto issue = [&](int kk) {
      const int kc = (kk + rot) & 7;
      const unsigned s = L0 + (kk & 3) * SLOT;
      dma4(s + w * 4096, xbase + kc * 128, vx[0], vx[1], vx[2], vx[3]);
      const char* wb = wbase + kc * (64 * 3072);
      dma2(s + 16384 + w * 2048, wb, vw[0], vw[1]);
      dma2(s + 24576 + w * 2048, wb + 1024, vw[0], vw[1]);
      dma2(s + 32768 + w * 2048, wb + 2048, vw[0], vw[1]);
    };
    issue(0); issue(1); issue(2); issue(3);
    asm volatile("s_load_dword %0, %1, 0x0" : "=s"(epoch) : "s"(wsp) : "memory");
    unsigned vo[4];
#pragma unroll
    for (int i = 0; i < 4; ++i) vo[i] = (64 * w + 8 * i + r8) * 1024 + ((c8 ^ (((r8 >> 1) & 3) << 1)) << 4) - 1024 * i;
    const char* obase = (const char*)wo + cg * 128;
    auto issue_wo = [&](int piece) {
      dma4(L0 + piece * SLOT + w * 8192, obase + piece * 262144, vo[0], vo[1], vo[2], vo[3]);
      dma4(L0 + piece * SLOT + w * 8192 + 4096, obase + piece * 262144 + 32768, vo[0], vo[1], vo[2], vo[3]);
    };
    const int q = lane >> 4, l15 = lane & 15, qq = (lane >> 2) & 3, pp = lane & 3;
    const unsigned ax0 = l15 * 128 + ((q ^ (l15 >> 1)) << 4), ax1 = l15 * 128 + (((4 + q) ^ (l15 >> 1)) << 4);
    const unsigned aw = 16384 + (8 * q + qq) * 128 + (((2 * w + (pp >> 1)) ^ ((((qq >> 1) & 1) | ((q & 1) << 1)) << 1)) << 4) + 8 * (pp & 1);
    f4 ka[8], va[8], qa[8];
#pragma unroll
    for (int i = 0; i < 8; ++i) { ka[i] = (f4){0, 0, 0, 0}; va[i] = ka[i]; qa[i] = ka[i]; }
    auto compute = [&](int kk) {
      const char LAS* s = L + (kk & 3) * SLOT;
#pragma unroll
      for (int ks = 0; ks < 2; ++ks) {
        const char LAS* wp_ = s + aw + ks * 4096;
        const bf8 fq = ldstr(wp_, wp_ + 512), fk = ldstr(wp_ + 8192, wp_ + 8192 + 512), fv = ldstr(wp_ + 16384, wp_ + 16384 + 512);
#pragma unroll
        for (int mt = 0; mt < 8; ++mt) {
          const bf8 fx = ldsr(s + (ks ? ax1 : ax0) + mt * 2048);
          ka[mt] = mfma16(fx, fk, ka[mt]);
          va[mt] = mfma16(fx, fv, va[mt]);
          qa[mt] = mfma16(fq, fx, qa[mt]);
        }
      }
    };
    asm volatile("s_waitcnt vmcnt(30) lgkmcnt(0)\n\ts_barrier" : "+s"(epoch) :: "memory");
    ST(1);
    tag = epoch + 1;
    compute(0);
    wait_bar<20>(); issue(4); compute(1);
    wait_bar<20>(); issue(5); compute(2);
    wait_bar<20>(); issue(6); compute(3);
    wait_bar<20>(); issue(7); compute(4);
    wait_bar<20>(); issue_wo(0); compute(5);
    wait_bar<18>(); issue_wo(1); compute(6);
    wait_bar<16>(); ST(2); compute(7);
    ST(3);
    const unsigned t0 = tag & 0xffu, t1 = (tag >> 8) & 0xffu;
    const int head = cg * 4 + w;
    {
      f4 kv = {0, 0, 0, 0}, zz = {0, 0, 0, 0};
      const u4 ones = {0x3f803f80u, 0x3f803f80u, 0x3f803f80u, 0x3f803f80u};
#pragma unroll
      for (int mm = 0; mm < 4; ++mm) {
        const f4 a = ka[2 * mm], b = ka[2 * mm + 1], c = va[2 * mm], d = va[2 * mm + 1];
        const u4 pa = {pk2(phi(a[0]), phi(a[1])), pk2(phi(a[2]), phi(a[3])), pk2(phi(b[0]), phi(b[1])), pk2(phi(b[2]), phi(b[3]))};
        const u4 pb = {pk2(c[0], c[1]), pk2(c[2], c[3]), pk2(d[0], d[1]), pk2(d[2], d[3])};
        kv = mfma16(asbf8(pa), asbf8(pb), kv);
        zz = mfma16(asbf8(pa), asbf8(ones), zz);
      }
      const u4 skv = {tagv(kv[0], t0), tagv(kv[1], t1), tagv(kv[2], t0), tagv(kv[3], t1)};
      __builtin_amdgcn_raw_buffer_store_b128(skv, ws, WS_KV + (head * 16 + tile) * 1024 + lane * 16, 0, SC1);
      const u4 sz = {tagv(zz[0], t0), tagv(zz[1], t1), tagv(zz[2], t0), tagv(zz[3], t1)};
      if (l15 == 0) __builtin_amdgcn_raw_buffer_store_b128(sz, ws, WS_Z + (head * 16 + tile) * 64 + q * 16, 0, SC1);
    }
    ST(4);
    u2 pq[8];
#pragma unroll
    for (int mt = 0; mt < 8; ++mt) pq[mt] = (u2){pk2(phi(qa[mt][0]), phi(qa[mt][1])), pk2(phi(qa[mt][2]), phi(qa[mt][3]))};
    f4 kv, zz;
    for (unsigned sp = 0; sp < SPIN_CAP; ++sp) {
      u4 g[16];
      const u4 gz = __builtin_amdgcn_raw_buffer_load_b128(ws, WS_Z + head * 1024 + l15 * 64 + q * 16, 0, SC1);
#pragma unroll
      for (int i = 0; i < 16; ++i) g[i] = __builtin_amdgcn_raw_buffer_load_b128(ws, WS_KV + head * 16384 + lane * 16, i * 1024, SC1);
      const u4 tt = {t0, t1, t0, t1};
      const u4 cz = gz ^ tt;
      unsigned bad = cz.x | cz.y | cz.z | cz.w;
      kv = (f4){0, 0, 0, 0};
#pragma unroll
      for (int i = 0; i < 16; ++i) {
        const u4 c = g[i] ^ tt;
        bad |= c.x | c.y | c.z | c.w;
        kv += __builtin_bit_cast(f4, c);
      }
      zz = __builtin_bit_cast(f4, cz);
      if (__all((bad & 0xffu) == 0)) break;
      __builtin_amdgcn_s_sleep(1);
    }
    ST(5);
#pragma unroll
    for (int r = 0; r < 4; ++r) {
      float v = zz[r];
      v += __builtin_bit_cast(float, __builtin_amdgcn_update_dpp(0, __builtin_bit_cast(int, v), 0x128, 0xf, 0xf, false));
      v += __builtin_bit_cast(float, __builtin_amdgcn_update_dpp(0, __builtin_bit_cast(int, v), 0x124, 0xf, 0xf, false));
      v += __builtin_bit_cast(float, __builtin_amdgcn_update_dpp(0, __builtin_bit_cast(int, v), 0x122, 0xf, 0xf, false));
      v += __builtin_bit_cast(float, __builtin_amdgcn_update_dpp(0, __builtin_bit_cast(int, v), 0x121, 0xf, 0xf, false));
      zz[r] = v;
    }
    const u2 kvb = {pk2(kv[0], kv[1]), pk2(kv[2], kv[3])}, zb = {pk2(zz[0], zz[1]), pk2(zz[2], zz[3])};
    const f4 zero4 = {0, 0, 0, 0};
#pragma unroll
    for (int u = 0; u < 4; ++u) {
      u4 so;
#pragma unroll
      for (int hh = 0; hh < 2; ++hh) {
        const f4 num = mfma16k16(kvb, pq[2 * u + hh], zero4), den = mfma16k16(zb, pq[2 * u + hh], zero4);
        const float rc = __builtin_amdgcn_rcpf(den[0] + 1e-6f);
        so[2 * hh] = pk2(num[0] * rc, num[1] * rc);
        so[2 * hh + 1] = pk2(num[2] * rc, num[3] * rc);
      }
      __builtin_amdgcn_raw_buffer_store_b128(so, ws, WS_O + ((tile * 4 + u) * 32 + head) * 1024 + lane * 16, 0, SC1);
    }
    ST(6);
    drain();
    if (lane == 0) __builtin_amdgcn_raw_buffer_store_b64((u2){tag, ~tag}, ws, WS_FA + (tile * 32 + head) * 128, 0, SC1);
    asm volatile("s_barrier" ::: "memory");
    ST(7);
    wait_flags(ws, WS_FA + (tile * 32 + (lane & 31)) * 128, true, tag);
    ST(8);
    unsigned ao[4];
#pragma unroll
    for (int nt = 0; nt < 4; ++nt) ao[nt] = (4 * q + qq) * 128 + ((((2 * nt) ^ (((2 * q + (qq >> 1)) & 3) << 1)) + (pp >> 1)) << 4) + 8 * (pp & 1);
    f4 acc[2][4];
#pragma unroll
    for (int i = 0; i < 8; ++i) acc[i >> 2][i & 3] = zero4;
    const unsigned ob = WS_O + (tile * 4 + w) * 32768 + lane * 16;
    u4 of[32];
#pragma unroll
    for (int i = 0; i < 32; ++i) of[i] = __builtin_amdgcn_raw_buffer_load_b128(ws, ob, i * 1024, SC1);
#pragma unroll
    for (int k = 0; k < 16; ++k) {
      const u4 a = of[2 * k], b = of[2 * k + 1];
      const bf8 f0 = asbf8((u4){a.x, a.y, b.x, b.y}), f1 = asbf8((u4){a.z, a.w, b.z, b.w});
      const char LAS* base = L + (k >> 3) * SLOT + (k & 7) * 4096;
#pragma unroll
      for (int nt = 0; nt < 4; ++nt) {
        const bf8 fw = ldstr(base + ao[nt], base + ao[nt] + 2048);
        acc[0][nt] = mfma16(fw, f0, acc[0][nt]);
        acc[1][nt] = mfma16(fw, f1, acc[1][nt]);
      }
    }
    ST(9);
#pragma unroll
    for (int mt = 0; mt < 2; ++mt)
#pragma unroll
      for (int nt = 0; nt < 4; ++nt) {
        const f4 v = acc[mt][nt];
        const unsigned p0 = pk2(v[0], v[1]), p1 = pk2(v[2], v[3]);
        *(f4*)(out + (size_t)(tile * 128 + w * 32 + mt * 16 + l15) * 1024 + cg * 64 + nt * 16 + q * 4) = (f4){bflo(p0), bfhi(p0), bflo(p1), bfhi(p1)};
      }
    ST(10);
    if (bid == 0 && w == 0) {
      wait_flags(ws, WS_FA + lane * 1024, true, tag);
      wait_flags(ws, WS_FA + 512 + lane * 1024, true, tag);
      wait_flags(ws, WS_FB + (lane >> 3) * 4096 + (lane & 7) * 256, true, tag);
      wait_flags(ws, WS_FB + 32768 + (lane >> 3) * 4096 + (lane & 7) * 256, true, tag);
      if (lane == 0) __builtin_amdgcn_raw_buffer_store_b32(tag, ws, WS_EPOCH, 0, SC1);
    }
  } else {
    constexpr unsigned SLOT = 32768;
    const int wp = w & 1, wt = w >> 1, l31 = lane & 31, hh = lane >> 5;
    const int pl = cg * 64 + wp * 32 + ((l31 & 0x13) | ((l31 & 4) << 1) | ((l31 & 8) >> 1));
    unsigned plr, pli, pdt;
    asm volatile("global_load_ushort %0, %3, %4\n\tglobal_load_ushort %1, %3, %5\n\tglobal_load_ushort %2, %3, %6"
                 : "=&v"(plr), "=&v"(pli), "=&v"(pdt) : "v"(pl * 2), "s"(lre), "s"(lim), "s"(ldt) : "memory");
    unsigned vx[4], vw[4];
#pragma unroll
    for (int i = 0; i < 4; ++i) {
      const unsigned sw = (c8 ^ ((4 * i + (r8 >> 1)) & 7)) << 4;
      vx[i] = (64 * (w >> 1) + 32 * (r8 >> 2) + 16 * (r8 & 1) + 8 * (w & 1) + ((r8 >> 1) & 1) + 2 * i) * 1024 + sw - 1024 * i;
      vw[i] = (16 * (i >> 1) + 8 * (r8 >> 2) + 4 * (i & 1) + (r8 & 3)) * 1024 + sw - 1024 * i;
    }
    const char* xbase = (const char*)xb + (size_t)tile * 131072;
    const char* wbase = (const char*)((w & 1) ? bim : bre) + (cg * 64 + (w >> 1) * 32) * 1024;
    auto issue = [&](int kk) {
      const int kc = (kk + rot) & 7;
      const unsigned s = L0 + (kk % 5) * SLOT + w * 4096;
      dma4(s, xbase + kc * 128, vx[0], vx[1], vx[2], vx[3]);
      dma4(s + 16384, wbase + kc * 128, vw[0], vw[1], vw[2], vw[3]);
    };
    issue(0); issue(1); issue(2); issue(3); issue(4);
    asm volatile("s_load_dword %0, %1, 0x0" : "=s"(epoch) : "s"(wsp) : "memory");
    unsigned vc[4];
#pragma unroll
    for (int i = 0; i < 4; ++i) vc[i] = (8 * i + r8) * 1024 + ((c8 ^ ((4 * i + (r8 >> 1)) & 7)) << 4) - 1024 * i;
    const char* cbase = (const char*)((w & 1) ? cim : cre) + cg * 65536;
    const int crot = cg + 2 * (jj >> 3);
    auto issue_c = [&](int piece) {
      const unsigned s = L0 + ((piece + 3) % 5) * SLOT + w * 8192;
      const char* cb = cbase + ((crot + 2 * piece + (w >> 1)) & 7) * 128;
      dma4(s, cb, vc[0], vc[1], vc[2], vc[3]);
      dma4(s + 4096, cb + 32768, vc[0], vc[1], vc[2], vc[3]);
    };
    asm volatile("s_waitcnt vmcnt(40)" : "+v"(plr), "+v"(pli), "+v"(pdt) :: "memory");
    f2 lam, kap, l16, l32, l64, l128;
    {
      const float lr = bflo(plr), li = bflo(pli);
      const float dt = bflo(pk2(expf(bflo(pdt)), 0.f));
      const float mag = expf(dt * lr);
      float sn, cs;
      sincosf(dt * li, &sn, &cs);
      lam = (f2){mag * cs, mag * sn};
      const float inv = 2.f / (lr * lr + li * li), nr = lam.x - 1.f, ni = lam.y;
      kap = (f2){(nr * lr + ni * li) * inv, (ni * lr - nr * li) * inv};
      f2 t = cmul(lam, lam); t = cmul(t, t); t = cmul(t, t);
      l16 = cmul(t, t); l32 = cmul(l16, l16); l64 = cmul(l32, l32); l128 = cmul(l64, l64);
    }
    unsigned ar[4];
#pragma unroll
    for (int s = 0; s < 4; ++s) ar[s] = l31 * 128 + (((2 * s + hh) ^ ((lane >> 1) & 7)) << 4);
    f16v ur[2], ui[2];
#pragma unroll
    for (int i = 0; i < 16; ++i) { ur[0][i] = 0; ur[1][i] = 0; ui[0][i] = 0; ui[1][i] = 0; }
    auto compute = [&](int kk) {
      const char LAS* sx = L + (kk % 5) * SLOT + wt * 8192;
      const char LAS* sw = L + (kk % 5) * SLOT + 16384 + wp * 8192;
#pragma unroll
      for (int s = 0; s < 4; ++s) {
        const bf8 x0 = ldsr(sx + ar[s]), x1 = ldsr(sx + ar[s] + 4096), b0 = ldsr(sw + ar[s]), b1 = ldsr(sw + ar[s] + 4096);
        ur[0] = mfma32(x0, b0, ur[0]); ui[0] = mfma32(x0, b1, ui[0]);
        ur[1] = mfma32(x1, b0, ur[1]); ui[1] = mfma32(x1, b1, ui[1]);
      }
    };
    asm volatile("s_waitcnt vmcnt(32) lgkmcnt(0)\n\ts_barrier" : "+s"(epoch) :: "memory");
    ST(1);
    tag = epoch + 1;
    compute(0);
    wait_bar<24>(); issue(5); compute(1);
    wait_bar<24>(); issue(6); compute(2);
    wait_bar<24>(); issue(7); compute(3);
    wait_bar<24>(); issue_c(0); compute(4);
    wait_bar<24>(); issue_c(1); compute(5);
    wait_bar<24>(); issue_c(2); compute(6);
    wait_bar<24>(); ST(2); issue_c(3); compute(7);
    ST(3);
    const unsigned t0 = tag & 0xffu, t1 = (tag >> 8) & 0xffu;
    const f2 LR = {lam.x, lam.x}, LI = {lam.y, lam.y};
    f2 er = {0, 0}, ei = {0, 0};
#pragma unroll
    for (int a = 0; a < 16; ++a) {
      const f2 xr = {ur[a >> 3][2 * (a & 7)], ur[a >> 3][2 * (a & 7) + 1]}, xi = {ui[a >> 3][2 * (a & 7)], ui[a >> 3][2 * (a & 7) + 1]};
      const f2 nr = fma2(LR, er, fma2(-LI, ei, xr)), ni = fma2(LR, ei, fma2(LI, er, xi));
      er = nr; ei = ni;
    }
    const f2 e0 = {er.x, ei.x};
    const f2 e32 = cfma(l16, e0, (f2){er.y, ei.y});
    const auto sx_ = __builtin_amdgcn_permlane32_swap(__float_as_uint(e32.x), __float_as_uint(e32.x), false, false);
    const auto sy_ = __builtin_amdgcn_permlane32_swap(__float_as_uint(e32.y), __float_as_uint(e32.y), false, false);
    const f2 elo = {__uint_as_float(sx_[0]), __uint_as_float(sy_[0])}, ehi = {__uint_as_float(sx_[1]), __uint_as_float(sy_[1])};
    const f2 e64 = cfma(l32, elo, ehi);
    const int J = tile * 2 + wt;
    const unsigned ecol = WS_EST + (cg * 64 + wp * 32 + l31) * 8;
    if (hh == 0) __builtin_amdgcn_raw_buffer_store_b64((u2){tagv(e64.x, t0), tagv(e64.y, t1)}, ws, ecol + J * 4096, 0, SC1);
    ST(4);
    {
      const f2 KR = {kap.x, kap.x}, KI = {kap.y, kap.y};
#pragma unroll
      for (int a = 0; a < 16; ++a) {
        const f2 xr = {ur[a >> 3][2 * (a & 7)], ur[a >> 3][2 * (a & 7) + 1]}, xi = {ui[a >> 3][2 * (a & 7)], ui[a >> 3][2 * (a & 7) + 1]};
        const f2 nr = fma2(KR, xr, -KI * xi), ni = fma2(-KR, xi, -KI * xr);
        ur[a >> 3][2 * (a & 7)] = nr.x; ur[a >> 3][2 * (a & 7) + 1] = nr.y;
        ui[a >> 3][2 * (a & 7)] = ni.x; ui[a >> 3][2 * (a & 7) + 1] = ni.y;
      }
    }
    f2 cin;
    for (unsigned sp = 0; sp < SPIN_CAP; ++sp) {
      u2 g[16];
#pragma unroll
      for (int m = 0; m < 16; ++m) {
        const int i = J - 1 - hh - 2 * m;
        g[m] = __builtin_amdgcn_raw_buffer_load_b64(ws, i >= 0 ? ecol + i * 4096 : 0xfffffff0u, 0, SC1);
      }
      unsigned bad = 0;
      cin = (f2){0, 0};
#pragma unroll
      for (int m = 15; m >= 0; --m) {
        const int i = J - 1 - hh - 2 * m;
        const unsigned cx = i >= 0 ? g[m].x ^ t0 : 0u, cy = i >= 0 ? g[m].y ^ t1 : 0u;
        bad |= cx | cy;
        cin = cfma(l128, cin, (f2){__uint_as_float(cx), __uint_as_float(cy)});
      }
      if (__all((bad & 0xffu) == 0)) break;
      __builtin_amdgcn_s_sleep(1);
    }
    ST(5);
    {
      const auto cx = __builtin_amdgcn_permlane32_swap(__float_as_uint(cin.x), __float_as_uint(cin.x), false, false);
      const auto cy = __builtin_amdgcn_permlane32_swap(__float_as_uint(cin.y), __float_as_uint(cin.y), false, false);
      cin = cfma(l64, (f2){__uint_as_float(cx[1]), __uint_as_float(cy[1])}, (f2){__uint_as_float(cx[0]), __uint_as_float(cy[0])});
    }
    const f2 up = cfma(l32, cin, elo);
    const f2 c0 = hh ? up : cin;
    const f2 c1 = cfma(l16, c0, e0);
    const f2 d0 = cmul(kap, c0), d1 = cmul(kap, c1);
    f2 hr = {d0.x, d1.x}, hi = {-d0.y, -d1.y};
    u4 idn[2];
#pragma unroll
    for (int s = 0; s < 2; ++s)
#pragma unroll
      for (int jq = 0; jq < 4; ++jq) {
        const int r0 = 16 * s + 8 * (jq >> 1) + 4 * hh + 2 * (jq & 1);
        idn[s][jq] = (l31 == r0 ? 0x3f80u : 0u) | (l31 == r0 + 1 ? 0x3f800000u : 0u);
      }
    f16v zero16;
#pragma unroll
    for (int i = 0; i < 16; ++i) zero16[i] = 0;
#pragma unroll
    for (int m = 0; m < 2; ++m) {
#pragma unroll
      for (int a = 0; a < 8; ++a) {
        const f2 xr = {ur[m][2 * a], ur[m][2 * a + 1]}, xi = {ui[m][2 * a], ui[m][2 * a + 1]};
        const f2 nr = fma2(LR, hr, fma2(LI, hi, xr)), ni = fma2(LR, hi, fma2(-LI, hr, xi));
        hr = nr; hi = ni;
        ur[m][2 * a] = nr.x; ur[m][2 * a + 1] = nr.y; ui[m][2 * a] = ni.x; ui[m][2 * a + 1] = ni.y;
      }
#pragma unroll
      for (int c = 0; c < 2; ++c) {
        const f16v X = c ? ui[m] : ur[m];
        const u4 a0 = {pk2(X[0], X[1]), pk2(X[2], X[3]), pk2(X[4], X[5]), pk2(X[6], X[7])};
        const u4 a1 = {pk2(X[8], X[9]), pk2(X[10], X[11]), pk2(X[12], X[13]), pk2(X[14], X[15])};
        f16v T = mfma32(asbf8(a0), asbf8(idn[0]), zero16);
        T = mfma32(asbf8(a1), asbf8(idn[1]), T);
        const u4 o0 = {pk2(T[0], T[1]), pk2(T[2], T[3]), pk2(T[4], T[5]), pk2(T[6], T[7])};
        const u4 o1 = {pk2(T[8], T[9]), pk2(T[10], T[11]), pk2(T[12], T[13]), pk2(T[14], T[15])};
        const unsigned ho = WS_H + (tile * 4 + wt * 2 + m) * 65536 + ((cg * 2 + wp) * 2 + c) * 2048 + lane * 16;
        __builtin_amdgcn_raw_buffer_store_b128(o0, ws, ho, 0, SC1);
        __builtin_amdgcn_raw_buffer_store_b128(o1, ws, ho + 1024, 0, SC1);
      }
    }
    ST(6);
    drain();
    if (lane == 0) __builtin_amdgcn_raw_buffer_store_b64((u2){tag, ~tag}, ws, WS_FB + ((tile * 2 + wt) * 16 + cg * 2 + wp) * 128, 0, SC1);
    asm volatile("s_barrier" ::: "memory");
    ST(7);
    const int tok = tile * 128 + 64 * (w >> 1) + 32 * ((l31 >> 2) & 1) + 16 * (l31 & 1) + 8 * (w & 1) + ((l31 >> 1) & 1) + 2 * (l31 >> 3);
    f16v acc[2];
#pragma unroll
    for (int nt = 0; nt < 2; ++nt)
#pragma unroll
      for (int g = 0; g < 4; ++g) {
        const int d = cg * 64 + nt * 32 + g * 8 + hh * 4;
        const u2 xv = *(const u2*)(xb + tok * 512 + d), dv = *(const u2*)(dsk + d);
        const unsigned p0 = pk2(bflo(xv.x) * bflo(dv.x), bfhi(xv.x) * bfhi(dv.x)), p1 = pk2(bflo(xv.y) * bflo(dv.y), bfhi(xv.y) * bfhi(dv.y));
        acc[nt][4 * g] = bflo(p0); acc[nt][4 * g + 1] = bfhi(p0); acc[nt][4 * g + 2] = bflo(p1); acc[nt][4 * g + 3] = bfhi(p1);
      }
    wait_flags(ws, WS_FB + ((tile * 2 + (w >> 1)) * 16 + (lane & 15)) * 128, true, tag);
    ST(8);
    const unsigned hb = WS_H + (tile * 4 + w) * 65536 + lane * 16;
    u4 hf[3][8];
    auto loadh = [&](int t) {
      const unsigned so = ((crot + t) & 7) * 8192;
#pragma unroll
      for (int i = 0; i < 8; ++i) hf[t % 3][i] = __builtin_amdgcn_raw_buffer_load_b128(ws, hb + i * 1024, so, SC1);
    };
    loadh(0); loadh(1);
#pragma unroll
    for (int t = 0; t < 8; ++t) {
      if (t + 2 < 8) loadh(t + 2);
      __builtin_amdgcn_sched_barrier(0);
      const char LAS* cb = L + (((t >> 1) + 3) % 5) * SLOT + (t & 1) * 16384;
#pragma unroll
      for (int i = 0; i < 8; ++i) {
        const char LAS* p = cb + ((i >> 1) & 1) * 8192 + ar[(i >> 2) * 2 + (i & 1)];
        acc[0] = mfma32(ldsr(p), asbf8(hf[t % 3][i]), acc[0]);
        acc[1] = mfma32(ldsr(p + 4096), asbf8(hf[t % 3][i]), acc[1]);
      }
      __builtin_amdgcn_sched_barrier(0);
    }
    ST(9);
#pragma unroll
    for (int nt = 0; nt < 2; ++nt)
#pragma unroll
      for (int g = 0; g < 4; ++g)
        *(f4*)(out + (size_t)tok * 1024 + 512 + cg * 64 + nt * 32 + g * 8 + hh * 4) = (f4){acc[nt][4 * g], acc[nt][4 * g + 1], acc[nt][4 * g + 2], acc[nt][4 * g + 3]};
    ST(10);
  }
  drain();
  ST(11);
  if (bid == AMP_BID && w == 0) {
    const u64 until = st[11] + AMP_K * (st[AMP_B] - st[AMP_A]);
    while (__builtin_amdgcn_s_memrealtime() < until) __builtin_amdgcn_s_sleep(2);
  }
}

extern "C" void kernel_launch(void* const* d_in, const int* in_sizes, int n_in, void* d_out, int out_size, void* d_ws, size_t ws_size,
                              hipStream_t stream) {
  (void)hipFuncSetAttribute((const void*)fused_kernel, hipFuncAttributeMaxDynamicSharedMemorySize, LDS_BYTES);
  fused_kernel<<<256, 256, LDS_BYTES, stream>>>((const u16*)d_in[0], (const u16*)d_in[1], (const u16*)d_in[2], (const u16*)d_in[3],
                                               (const u16*)d_in[4], (const u16*)d_in[5], (const u16*)d_in[6], (const u16*)d_in[7],
                                               (const u16*)d_in[8], (const u16*)d_in[9], (const u16*)d_in[10], (const u16*)d_in[11],
                                               (float*)d_out, (char*)d_ws);
}
```

```cpp
#define OUT_AUX 16
#include <hip/hip_runtime.h>
#include <hip/hip_bf16.h>

#define DI __device__ __forceinline__
#define LAS __attribute__((address_space(3)))
#define GAS __attribute__((address_space(1)))
typedef unsigned short u16;
typedef unsigned long long u64;
typedef __bf16 bf8 __attribute__((ext_vector_type(8)));
typedef __bf16 bf2 __attribute__((ext_vector_type(2)));
typedef short s4 __attribute__((ext_vector_type(4)));
typedef short s8 __attribute__((ext_vector_type(8)));
typedef float f16v __attribute__((ext_vector_type(16)));
typedef float f4 __attribute__((ext_vector_type(4)));
typedef float f2 __attribute__((ext_vector_type(2)));
typedef unsigned u4 __attribute__((ext_vector_type(4)));
typedef unsigned u2 __attribute__((ext_vector_type(2)));

constexpr unsigned WS_EPOCH = 0;
constexpr unsigned WS_FA = 4096;
constexpr unsigned WS_FB = WS_FA + 65536;
constexpr unsigned WS_EST = WS_FB + 65536;
constexpr unsigned WS_Z = WS_EST + 131072;
constexpr unsigned WS_KV = WS_Z + 32768;
constexpr unsigned WS_O = 1u << 20;
constexpr unsigned WS_H = 3u << 20;
constexpr unsigned WS_END = 7u << 20;
constexpr unsigned WS_STAMP = 8u << 20;
constexpr int LDS_BYTES = 160 * 1024;
constexpr unsigned SPIN_CAP = 200000;
constexpr int SC1 = 16;

#define ROT 1

#define ST(i) do {} while (0)

DI unsigned pk2(float a, float b) { f2 v = {a, b}; return __builtin_bit_cast(unsigned, __builtin_convertvector(v, bf2)); }
DI float bflo(unsigned v) { return __uint_as_float(v << 16); }
DI float bfhi(unsigned v) { return __uint_as_float(v & 0xffff0000u); }
DI bf8 asbf8(u4 v) { return __builtin_bit_cast(bf8, v); }
DI f16v mfma32(bf8 a, bf8 b, f16v c) { return __builtin_amdgcn_mfma_f32_32x32x16_bf16(a, b, c, 0, 0, 0); }
DI f4 mfma16(bf8 a, bf8 b, f4 c) { return __builtin_amdgcn_mfma_f32_16x16x32_bf16(a, b, c, 0, 0, 0); }
DI f4 mfma16k16(u2 a, u2 b, f4 c) { return __builtin_amdgcn_mfma_f32_16x16x16bf16_1k(__builtin_bit_cast(s4, a), __builtin_bit_cast(s4, b), c, 0, 0, 0); }
DI bf8 ldsr(const char LAS* p) { return *(const LAS bf8*)p; }
DI bf8 ldstr(const char LAS* p0, const char LAS* p1) {
  s4 a = __builtin_amdgcn_ds_read_tr16_b64_v4i16((LAS s4*)p0), b = __builtin_amdgcn_ds_read_tr16_b64_v4i16((LAS s4*)p1);
  return __builtin_bit_cast(bf8, (s8)__builtin_shufflevector(a, b, 0, 1, 2, 3, 4, 5, 6, 7));
}
DI void dma4(unsigned lds, const char* sb, unsigned v0, unsigned v1, unsigned v2, unsigned v3) {
  asm volatile("s_mov_b32 m0, %0\n\ts_nop 0\n\tglobal_load_lds_dwordx4 %1, %5\n\tglobal_load_lds_dwordx4 %2, %5 offset:1024\n\t"
               "global_load_lds_dwordx4 %3, %5 offset:2048\n\tglobal_load_lds_dwordx4 %4, %5 offset:3072"
               :: "s"(lds), "v"(v0), "v"(v1), "v"(v2), "v"(v3), "s"(sb) : "memory");
}
DI void dma2(unsigned lds, const char* sb, unsigned v0, unsigned v1) {
  asm volatile("s_mov_b32 m0, %0\n\ts_nop 0\n\tglobal_load_lds_dwordx4 %1, %3\n\tglobal_load_lds_dwordx4 %2, %3 offset:1024"
               :: "s"(lds), "v"(v0), "v"(v1), "s"(sb) : "memory");
}
template <int N> DI void wait_bar() { asm volatile("s_waitcnt vmcnt(%0) lgkmcnt(0)\n\ts_barrier" :: "i"(N) : "memory"); }
DI void drain() { asm volatile("s_waitcnt vmcnt(0)" ::: "memory"); }
DI unsigned tagv(float x, unsigned t) { return (__float_as_uint(x) & 0xffffff00u) | t; }
DI f2 cmul(f2 a, f2 b) { return (f2){a.x * b.x - a.y * b.y, a.x * b.y + a.y * b.x}; }
DI f2 cfma(f2 a, f2 b, f2 c) { return (f2){fmaf(a.x, b.x, fmaf(-a.y, b.y, c.x)), fmaf(a.x, b.y, fmaf(a.y, b.x, c.y))}; }
DI f2 fma2(f2 a, f2 b, f2 c) { return __builtin_elementwise_fma(a, b, c); }
DI float phi(float t) { return fmaxf(t, 0.f) + __builtin_amdgcn_fmed3f(__builtin_amdgcn_exp2f(t * 1.44269504088896341f), 0.f, 1.f) + 1e-6f; }

DI void st_out(__amdgpu_buffer_rsrc_t o, unsigned off, f4 v) { __builtin_amdgcn_raw_buffer_store_b128(__builtin_bit_cast(u4, v), o, off, 0, OUT_AUX); }
DI void wait_flags(__amdgpu_buffer_rsrc_t ws, unsigned off, bool active, unsigned tag) {
#pragma unroll 1
  for (unsigned sp = 0; sp < SPIN_CAP; ++sp) {
    u2 f = __builtin_amdgcn_raw_buffer_load_b64(ws, off, 0, SC1);
    bool ok = !active || (f.x == tag && f.y == ~tag);
    if (__all(ok)) break;
    __builtin_amdgcn_s_sleep(1);
  }
  asm volatile("" ::: "memory");
}

__global__ void __launch_bounds__(256, 2)
fused_kernel(const u16* __restrict__ xa, const u16* __restrict__ wqkv, const u16* __restrict__ wo, const u16* __restrict__ xb,
             const u16* __restrict__ lre, const u16* __restrict__ lim, const u16* __restrict__ bre, const u16* __restrict__ bim,
             const u16* __restrict__ cre, const u16* __restrict__ cim, const u16* __restrict__ ldt, const u16* __restrict__ dsk,
             float* __restrict__ out, char* wsp) {
  extern __shared__ __attribute__((aligned(16))) char smem_[];
  const char LAS* L = (const char LAS*)smem_;
  const unsigned L0 = (unsigned)(size_t)L;
  ST(0);
  const int lane = threadIdx.x & 63;
  const int w = __builtin_amdgcn_readfirstlane(threadIdx.x >> 6);
  const int bid = blockIdx.x, xcd = bid & 7, jj = bid >> 3;
  const int tile = (xcd & 3) * 4 + (jj >> 3), cg = jj & 7;
  const int rot = ROT ? ((cg + 3 * (jj >> 3) + xcd) & 7) : 0;
  unsigned epoch;
  asm volatile("" :: "s"(xa), "s"(wqkv), "s"(wo), "s"(xb), "s"(lre), "s"(lim), "s"(bre), "s"(bim), "s"(cre), "s"(cim), "s"(ldt), "s"(dsk), "s"(out), "s"(wsp));
  const __amdgpu_buffer_rsrc_t ws = __builtin_amdgcn_make_buffer_rsrc(wsp, 0, WS_END, 0x00020000);
  const __amdgpu_buffer_rsrc_t ro = __builtin_amdgcn_make_buffer_rsrc(out, 0, 2048 * 1024 * 4, 0x00020000);
  const int r8 = lane >> 3, c8 = lane & 7;
  unsigned tag;

  if (xcd < 4) {
    constexpr unsigned SLOT = 40960;
    unsigned vx[4], vw[2];
#pragma unroll
    for (int i = 0; i < 4; ++i) vx[i] = (32 * w + 8 * i + r8) * 1024 + ((c8 ^ ((4 * i + (r8 >> 1)) & 7)) << 4) - 1024 * i;
#pragma unroll
    for (int i = 0; i < 2; ++i) vw[i] = (16 * w + 8 * i + r8) * 3072 + ((c8 ^ ((((r8 >> 1) & 1) | (i << 1)) << 1)) << 4) - 1024 * i;
    const char* xbase = (const char*)xa + (size_t)tile * 131072;
    const char* wbase = (const char*)wqkv + cg * 128;
    auto issue = [&](int kk) {
      const int kc = (kk + rot) & 7;
      const unsigned s = L0 + (kk & 3) * SLOT;
      dma4(s + w * 4096, xbase + kc * 128, vx[0], vx[1], vx[2], vx[3]);
      const char* wb = wbase + kc * (64 * 3072);
      dma2(s + 16384 + w * 2048, wb, vw[0], vw[1]);
      dma2(s + 24576 + w * 2048, wb + 1024, vw[0], vw[1]);
      dma2(s + 32768 + w * 2048, wb + 2048, vw[0], vw[1]);
    };
    issue(0); issue(1); issue(2); issue(3);
    asm volatile("s_load_dword %0, %1, 0x0" : "=s"(epoch) : "s"(wsp) : "memory");
    unsigned vo[4];
#pragma unroll
    for (int i = 0; i < 4; ++i) vo[i] = (64 * w + 8 * i + r8) * 1024 + ((c8 ^ (((r8 >> 1) & 3) << 1)) << 4) - 1024 * i;
    const char* obase = (const char*)wo + cg * 128;
    auto issue_wo = [&](int piece) {
      dma4(L0 + piece * SLOT + w * 8192, obase + piece * 262144, vo[0], vo[1], vo[2], vo[3]);
      dma4(L0 + piece * SLOT + w * 8192 + 4096, obase + piece * 262144 + 32768, vo[0], vo[1], vo[2], vo[3]);
    };
    const int q = lane >> 4, l15 = lane & 15, qq = (lane >> 2) & 3, pp = lane & 3;
    const unsigned ax0 = l15 * 128 + ((q ^ (l15 >> 1)) << 4), ax1 = l15 * 128 + (((4 + q) ^ (l15 >> 1)) << 4);
    const unsigned aw = 16384 + (8 * q + qq) * 128 + (((2 * w + (pp >> 1)) ^ ((((qq >> 1) & 1) | ((q & 1) << 1)) << 1)) << 4) + 8 * (pp & 1);
    f4 ka[8], va[8], qa[8];
#pragma unroll
    for (int i = 0; i < 8; ++i) { ka[i] = (f4){0, 0, 0, 0}; va[i] = ka[i]; qa[i] = ka[i]; }
    auto compute = [&](int kk) {
      const char LAS* s = L + (kk & 3) * SLOT;
#pragma unroll
      for (int ks = 0; ks < 2; ++ks) {
        const char LAS* wp_ = s + aw + ks * 4096;
        const bf8 fq = ldstr(wp_, wp_ + 512), fk = ldstr(wp_ + 8192, wp_ + 8192 + 512), fv = ldstr(wp_ + 16384, wp_ + 16384 + 512);
#pragma unroll
        for (int mt = 0; mt < 8; ++mt) {
          const bf8 fx = ldsr(s + (ks ? ax1 : ax0) + mt * 2048);
          ka[mt] = mfma16(fx, fk, ka[mt]);
          va[mt] = mfma16(fx, fv, va[mt]);
          qa[mt] = mfma16(fq, fx, qa[mt]);
        }
      }
    };
    asm volatile("s_waitcnt vmcnt(30) lgkmcnt(0)\n\ts_barrier" : "+s"(epoch) :: "memory");
    ST(1);
    tag = epoch + 1;
    compute(0);
    wait_bar<20>(); issue(4); compute(1);
    wait_bar<20>(); issue(5); compute(2);
    wait_bar<20>(); issue(6); compute(3);
    wait_bar<20>(); issue(7); compute(4);
    wait_bar<20>(); issue_wo(0); compute(5);
    wait_bar<18>(); issue_wo(1); compute(6);
    wait_bar<16>(); ST(2); compute(7);
    ST(3);
    const unsigned t0 = tag & 0xffu, t1 = (tag >> 8) & 0xffu;
    const int head = cg * 4 + w;
    {
      f4 kv = {0, 0, 0, 0}, zz = {0, 0, 0, 0};
      const u4 ones = {0x3f803f80u, 0x3f803f80u, 0x3f803f80u, 0x3f803f80u};
#pragma unroll
      for (int mm = 0; mm < 4; ++mm) {
        const f4 a = ka[2 * mm], b = ka[2 * mm + 1], c = va[2 * mm], d = va[2 * mm + 1];
        const u4 pa = {pk2(phi(a[0]), phi(a[1])), pk2(phi(a[2]), phi(a[3])), pk2(phi(b[0]), phi(b[1])), pk2(phi(b[2]), phi(b[3]))};
        const u4 pb = {pk2(c[0], c[1]), pk2(c[2], c[3]), pk2(d[0], d[1]), pk2(d[2], d[3])};
        kv = mfma16(asbf8(pa), asbf8(pb), kv);
        zz = mfma16(asbf8(pa), asbf8(ones), zz);
      }
      const u4 skv = {tagv(kv[0], t0), tagv(kv[1], t1), tagv(kv[2], t0), tagv(kv[3], t1)};
      __builtin_amdgcn_raw_buffer_store_b128(skv, ws, WS_KV + (head * 16 + tile) * 1024 + lane * 16, 0, SC1);
      const u4 sz = {tagv(zz[0], t0), tagv(zz[1], t1), tagv(zz[2], t0), tagv(zz[3], t1)};
      if (l15 == 0) __builtin_amdgcn_raw_buffer_store_b128(sz, ws, WS_Z + (head * 16 + tile) * 64 + q * 16, 0, SC1);
    }
    ST(4);
    u2 pq[8];
#pragma unroll
    for (int mt = 0; mt < 8; ++mt) pq[mt] = (u2){pk2(phi(qa[mt][0]), phi(qa[mt][1])), pk2(phi(qa[mt][2]), phi(qa[mt][3]))};
    f4 kv, zz;
    for (unsigned sp = 0; sp < SPIN_CAP; ++sp) {
      u4 g[16];
      const u4 gz = __builtin_amdgcn_raw_buffer_load_b128(ws, WS_Z + head * 1024 + l15 * 64 + q * 16, 0, SC1);
#pragma unroll
      for (int i = 0; i < 16; ++i) g[i] = __builtin_amdgcn_raw_buffer_load_b128(ws, WS_KV + head * 16384 + lane * 16, i * 1024, SC1);
      const u4 tt = {t0, t1, t0, t1};
      const u4 cz = gz ^ tt;
      unsigned bad = cz.x | cz.y | cz.z | cz.w;
      kv = (f4){0, 0, 0, 0};
#pragma unroll
      for (int i = 0; i < 16; ++i) {
        const u4 c = g[i] ^ tt;
        bad |= c.x | c.y | c.z | c.w;
        kv += __builtin_bit_cast(f4, c);
      }
      zz = __builtin_bit_cast(f4, cz);
      if (__all((bad & 0xffu) == 0)) break;
      __builtin_amdgcn_s_sleep(1);
    }
    ST(5);
#pragma unroll
    for (int r = 0; r < 4; ++r) {
      float v = zz[r];
      v += __builtin_bit_cast(float, __builtin_amdgcn_update_dpp(0, __builtin_bit_cast(int, v), 0x128, 0xf, 0xf, false));
      v += __builtin_bit_cast(float, __builtin_amdgcn_update_dpp(0, __builtin_bit_cast(int, v), 0x124, 0xf, 0xf, false));
      v += __builtin_bit_cast(float, __builtin_amdgcn_update_dpp(0, __builtin_bit_cast(int, v), 0x122, 0xf, 0xf, false));
      v += __builtin_bit_cast(float, __builtin_amdgcn_update_dpp(0, __builtin_bit_cast(int, v), 0x121, 0xf, 0xf, false));
      zz[r] = v;
    }
    const u2 kvb = {pk2(kv[0], kv[1]), pk2(kv[2], kv[3])}, zb = {pk2(zz[0], zz[1]), pk2(zz[2], zz[3])};
    const f4 zero4 = {0, 0, 0, 0};
#pragma unroll
    for (int u = 0; u < 4; ++u) {
      u4 so;
#pragma unroll
      for (int hh = 0; hh < 2; ++hh) {
        const f4 num = mfma16k16(kvb, pq[2 * u + hh], zero4), den = mfma16k16(zb, pq[2 * u + hh], zero4);
        const float rc = __builtin_amdgcn_rcpf(den[0] + 1e-6f);
        so[2 * hh] = pk2(num[0] * rc, num[1] * rc);
        so[2 * hh + 1] = pk2(num[2] * rc, num[3] * rc);
      }
      __builtin_amdgcn_raw_buffer_store_b128(so, ws, WS_O + ((tile * 4 + u) * 32 + head) * 1024 + lane * 16, 0, SC1);
    }
    ST(6);
    drain();
    if (lane == 0) __builtin_amdgcn_raw_buffer_store_b64((u2){tag, ~tag}, ws, WS_FA + (tile * 32 + head) * 128, 0, SC1);
    asm volatile("s_barrier" ::: "memory");
    ST(7);
    wait_flags(ws, WS_FA + (tile * 32 + (lane & 31)) * 128, true, tag);
    ST(8);
    unsigned ao[4];
#pragma unroll
    for (int nt = 0; nt < 4; ++nt) ao[nt] = (4 * q + qq) * 128 + ((((2 * nt) ^ (((2 * q + (qq >> 1)) & 3) << 1)) + (pp >> 1)) << 4) + 8 * (pp & 1);
    f4 acc[2][4];
#pragma unroll
    for (int i = 0; i < 8; ++i) acc[i >> 2][i & 3] = zero4;
    const unsigned ob = WS_O + (tile * 4 + w) * 32768 + lane * 16;
    u4 of[32];
#pragma unroll
    for (int i = 0; i < 32; ++i) of[i] = __builtin_amdgcn_raw_buffer_load_b128(ws, ob, i * 1024, SC1);
#pragma unroll
    for (int k = 0; k < 16; ++k) {
      const u4 a = of[2 * k], b = of[2 * k + 1];
      const bf8 f0 = asbf8((u4){a.x, a.y, b.x, b.y}), f1 = asbf8((u4){a.z, a.w, b.z, b.w});
      const char LAS* base = L + (k >> 3) * SLOT + (k & 7) * 4096;
#pragma unroll
      for (int nt = 0; nt < 4; ++nt) {
        const bf8 fw = ldstr(base + ao[nt], base + ao[nt] + 2048);
        acc[0][nt] = mfma16(fw, f0, acc[0][nt]);
        acc[1][nt] = mfma16(fw, f1, acc[1][nt]);
      }
    }
    ST(9);
#pragma unroll
    for (int mt = 0; mt < 2; ++mt)
#pragma unroll
      for (int nt = 0; nt < 4; ++nt) {
        const f4 v = acc[mt][nt];
        const unsigned p0 = pk2(v[0], v[1]), p1 = pk2(v[2], v[3]);
        st_out(ro, ((tile * 128 + w * 32 + mt * 16 + l15) * 1024 + cg * 64 + nt * 16 + q * 4) * 4, (f4){bflo(p0), bfhi(p0), bflo(p1), bfhi(p1)});
      }
    ST(10);
    if (bid == 0 && w == 0) {
      wait_flags(ws, WS_FA + lane * 1024, true, tag);
      wait_flags(ws, WS_FA + 512 + lane * 1024, true, tag);
      wait_flags(ws, WS_FB + (lane >> 3) * 4096 + (lane & 7) * 256, true, tag);
      wait_flags(ws, WS_FB + 32768 + (lane >> 3) * 4096 + (lane & 7) * 256, true, tag);
      if (lane == 0) __builtin_amdgcn_raw_buffer_store_b32(tag, ws, WS_EPOCH, 0, SC1);
    }
  } else {
    constexpr unsigned SLOT = 32768;
    const int wp = w & 1, wt = w >> 1, l31 = lane & 31, hh = lane >> 5;
    const int pl = cg * 64 + wp * 32 + ((l31 & 0x13) | ((l31 & 4) << 1) | ((l31 & 8) >> 1));
    unsigned plr, pli, pdt;
    asm volatile("global_load_ushort %0, %3, %4\n\tglobal_load_ushort %1, %3, %5\n\tglobal_load_ushort %2, %3, %6"
                 : "=&v"(plr), "=&v"(pli), "=&v"(pdt) : "v"(pl * 2), "s"(lre), "s"(lim), "s"(ldt) : "memory");
    unsigned vx[4], vw[4];
#pragma unroll
    for (int i = 0; i < 4; ++i) {
      const unsigned sw = (c8 ^ ((4 * i + (r8 >> 1)) & 7)) << 4;
      vx[i] = (64 * (w >> 1) + 32 * (r8 >> 2) + 16 * (r8 & 1) + 8 * (w & 1) + ((r8 >> 1) & 1) + 2 * i) * 1024 + sw - 1024 * i;
      vw[i] = (16 * (i >> 1) + 8 * (r8 >> 2) + 4 * (i & 1) + (r8 & 3)) * 1024 + sw - 1024 * i;
    }
    const char* xbase = (const char*)xb + (size_t)tile * 131072;
    const char* wbase = (const char*)((w & 1) ? bim : bre) + (cg * 64 + (w >> 1) * 32) * 1024;
    auto issue = [&](int kk) {
      const int kc = (kk + rot) & 7;
      const unsigned s = L0 + (kk % 5) * SLOT + w * 4096;
      dma4(s, xbase + kc * 128, vx[0], vx[1], vx[2], vx[3]);
      dma4(s + 16384, wbase + kc * 128, vw[0], vw[1], vw[2], vw[3]);
    };
    issue(0); issue(1); issue(2); issue(3); issue(4);
    asm volatile("s_load_dword %0, %1, 0x0" : "=s"(epoch) : "s"(wsp) : "memory");
    unsigned vc[4];
#pragma unroll
    for (int i = 0; i < 4; ++i) vc[i] = (8 * i + r8) * 1024 + ((c8 ^ ((4 * i + (r8 >> 1)) & 7)) << 4) - 1024 * i;
    const char* cbase = (const char*)((w & 1) ? cim : cre) + cg * 65536;
    const int crot = cg + 2 * (jj >> 3);
    auto issue_c = [&](int piece) {
      const unsigned s = L0 + ((piece + 3) % 5) * SLOT + w * 8192;
      const char* cb = cbase + ((crot + 2 * piece + (w >> 1)) & 7) * 128;
      dma4(s, cb, vc[0], vc[1], vc[2], vc[3]);
      dma4(s + 4096, cb + 32768, vc[0], vc[1], vc[2], vc[3]);
    };
    asm volatile("s_waitcnt vmcnt(40)" : "+v"(plr), "+v"(pli), "+v"(pdt) :: "memory");
    f2 lam, kap, l16, l32, l64, l128;
    {
      const float lr = bflo(plr), li = bflo(pli);
      const float dt = bflo(pk2(expf(bflo(pdt)), 0.f));
      const float mag = expf(dt * lr);
      float sn, cs;
      sincosf(dt * li, &sn, &cs);
      lam = (f2){mag * cs, mag * sn};
      const float inv = 2.f / (lr * lr + li * li), nr = lam.x - 1.f, ni = lam.y;
      kap = (f2){(nr * lr + ni * li) * inv, (ni * lr - nr * li) * inv};
      f2 t = cmul(lam, lam); t = cmul(t, t); t = cmul(t, t);
      l16 = cmul(t, t); l32 = cmul(l16, l16); l64 = cmul(l32, l32); l128 = cmul(l64, l64);
    }
    unsigned ar[4];
#pragma unroll
    for (int s = 0; s < 4; ++s) ar[s] = l31 * 128 + (((2 * s + hh) ^ ((lane >> 1) & 7)) << 4);
    f16v ur[2], ui[2];
#pragma unroll
    for (int i = 0; i < 16; ++i) { ur[0][i] = 0; ur[1][i] = 0; ui[0][i] = 0; ui[1][i] = 0; }
    auto compute = [&](int kk) {
      const char LAS* sx = L + (kk % 5) * SLOT + wt * 8192;
      const char LAS* sw = L + (kk % 5) * SLOT + 16384 + wp * 8192;
#pragma unroll
      for (int s = 0; s < 4; ++s) {
        const bf8 x0 = ldsr(sx + ar[s]), x1 = ldsr(sx + ar[s] + 4096), b0 = ldsr(sw + ar[s]), b1 = ldsr(sw + ar[s] + 4096);
        ur[0] = mfma32(x0, b0, ur[0]); ui[0] = mfma32(x0, b1, ui[0]);
        ur[1] = mfma32(x1, b0, ur[1]); ui[1] = mfma32(x1, b1, ui[1]);
      }
    };
    asm volatile("s_waitcnt vmcnt(32) lgkmcnt(0)\n\ts_barrier" : "+s"(epoch) :: "memory");
    ST(1);
    tag = epoch + 1;
    compute(0);
    wait_bar<24>(); issue(5); compute(1);
    wait_bar<24>(); issue(6); compute(2);
    wait_bar<24>(); issue(7); compute(3);
    wait_bar<24>(); issue_c(0); compute(4);
    wait_bar<24>(); issue_c(1); compute(5);
    wait_bar<24>(); issue_c(2); compute(6);
    wait_bar<24>(); ST(2); issue_c(3); compute(7);
    ST(3);
    const unsigned t0 = tag & 0xffu, t1 = (tag >> 8) & 0xffu;
    const f2 LR = {lam.x, lam.x}, LI = {lam.y, lam.y};
    f2 er = {0, 0}, ei = {0, 0};
#pragma unroll
    for (int a = 0; a < 16; ++a) {
      const f2 xr = {ur[a >> 3][2 * (a & 7)], ur[a >> 3][2 * (a & 7) + 1]}, xi = {ui[a >> 3][2 * (a & 7)], ui[a >> 3][2 * (a & 7) + 1]};
      const f2 nr = fma2(LR, er, fma2(-LI, ei, xr)), ni = fma2(LR, ei, fma2(LI, er, xi));
      er = nr; ei = ni;
    }
    const f2 e0 = {er.x, ei.x};
    const f2 e32 = cfma(l16, e0, (f2){er.y, ei.y});
    const auto sx_ = __builtin_amdgcn_permlane32_swap(__float_as_uint(e32.x), __float_as_uint(e32.x), false, false);
    const auto sy_ = __builtin_amdgcn_permlane32_swap(__float_as_uint(e32.y), __float_as_uint(e32.y), false, false);
    const f2 elo = {__uint_as_float(sx_[0]), __uint_as_float(sy_[0])}, ehi = {__uint_as_float(sx_[1]), __uint_as_float(sy_[1])};
    const f2 e64 = cfma(l32, elo, ehi);
    const int J = tile * 2 + wt;
    const unsigned ecol = WS_EST + (cg * 64 + wp * 32 + l31) * 8;
    if (hh == 0) __builtin_amdgcn_raw_buffer_store_b64((u2){tagv(e64.x, t0), tagv(e64.y, t1)}, ws, ecol + J * 4096, 0, SC1);
    ST(4);
    {
      const f2 KR = {kap.x, kap.x}, KI = {kap.y, kap.y};
#pragma unroll
      for (int a = 0; a < 16; ++a) {
        const f2 xr = {ur[a >> 3][2 * (a & 7)], ur[a >> 3][2 * (a & 7) + 1]}, xi = {ui[a >> 3][2 * (a & 7)], ui[a >> 3][2 * (a & 7) + 1]};
        const f2 nr = fma2(KR, xr, -KI * xi), ni = fma2(-KR, xi, -KI * xr);
        ur[a >> 3][2 * (a & 7)] = nr.x; ur[a >> 3][2 * (a & 7) + 1] = nr.y;
        ui[a >> 3][2 * (a & 7)] = ni.x; ui[a >> 3][2 * (a & 7) + 1] = ni.y;
      }
    }
    f2 cin;
    for (unsigned sp = 0; sp < SPIN_CAP; ++sp) {
      u2 g[16];
#pragma unroll
      for (int m = 0; m < 16; ++m) {
        const int i = J - 1 - hh - 2 * m;
        g[m] = __builtin_amdgcn_raw_buffer_load_b64(ws, i >= 0 ? ecol + i * 4096 : 0xfffffff0u, 0, SC1);
      }
      unsigned bad = 0;
      cin = (f2){0, 0};
#pragma unroll
      for (int m = 15; m >= 0; --m) {
        const int i = J - 1 - hh - 2 * m;
        const unsigned cx = i >= 0 ? g[m].x ^ t0 : 0u, cy = i >= 0 ? g[m].y ^ t1 : 0u;
        bad |= cx | cy;
        cin = cfma(l128, cin, (f2){__uint_as_float(cx), __uint_as_float(cy)});
      }
      if (__all((bad & 0xffu) == 0)) break;
      __builtin_amdgcn_s_sleep(1);
    }
    ST(5);
    {
      const auto cx = __builtin_amdgcn_permlane32_swap(__float_as_uint(cin.x), __float_as_uint(cin.x), false, false);
      const auto cy = __builtin_amdgcn_permlane32_swap(__float_as_uint(cin.y), __float_as_uint(cin.y), false, false);
      cin = cfma(l64, (f2){__uint_as_float(cx[1]), __uint_as_float(cy[1])}, (f2){__uint_as_float(cx[0]), __uint_as_float(cy[0])});
    }
    const f2 up = cfma(l32, cin, elo);
    const f2 c0 = hh ? up : cin;
    const f2 c1 = cfma(l16, c0, e0);
    const f2 d0 = cmul(kap, c0), d1 = cmul(kap, c1);
    f2 hr = {d0.x, d1.x}, hi = {-d0.y, -d1.y};
    u4 idn[2];
#pragma unroll
    for (int s = 0; s < 2; ++s)
#pragma unroll
      for (int jq = 0; jq < 4; ++jq) {
        const int r0 = 16 * s + 8 * (jq >> 1) + 4 * hh + 2 * (jq & 1);
        idn[s][jq] = (l31 == r0 ? 0x3f80u : 0u) | (l31 == r0 + 1 ? 0x3f800000u : 0u);
      }
    f16v zero16;
#pragma unroll
    for (int i = 0; i < 16; ++i) zero16[i] = 0;
#pragma unroll
    for (int m = 0; m < 2; ++m) {
#pragma unroll
      for (int a = 0; a < 8; ++a) {
        const f2 xr = {ur[m][2 * a], ur[m][2 * a + 1]}, xi = {ui[m][2 * a], ui[m][2 * a + 1]};
        const f2 nr = fma2(LR, hr, fma2(LI, hi, xr)), ni = fma2(LR, hi, fma2(-LI, hr, xi));
        hr = nr; hi = ni;
        ur[m][2 * a] = nr.x; ur[m][2 * a + 1] = nr.y; ui[m][2 * a] = ni.x; ui[m][2 * a + 1] = ni.y;
      }
#pragma unroll
      for (int c = 0; c < 2; ++c) {
        const f16v X = c ? ui[m] : ur[m];
        const u4 a0 = {pk2(X[0], X[1]), pk2(X[2], X[3]), pk2(X[4], X[5]), pk2(X[6], X[7])};
        const u4 a1 = {pk2(X[8], X[9]), pk2(X[10], X[11]), pk2(X[12], X[13]), pk2(X[14], X[15])};
        f16v T = mfma32(asbf8(a0), asbf8(idn[0]), zero16);
        T = mfma32(asbf8(a1), asbf8(idn[1]), T);
        const u4 o0 = {pk2(T[0], T[1]), pk2(T[2], T[3]), pk2(T[4], T[5]), pk2(T[6], T[7])};
        const u4 o1 = {pk2(T[8], T[9]), pk2(T[10], T[11]), pk2(T[12], T[13]), pk2(T[14], T[15])};
        const unsigned ho = WS_H + (tile * 4 + wt * 2 + m) * 65536 + ((cg * 2 + wp) * 2 + c) * 2048 + lane * 16;
        __builtin_amdgcn_raw_buffer_store_b128(o0, ws, ho, 0, SC1);
        __builtin_amdgcn_raw_buffer_store_b128(o1, ws, ho + 1024, 0, SC1);
      }
    }
    ST(6);
    drain();
    if (lane == 0) __builtin_amdgcn_raw_buffer_store_b64((u2){tag, ~tag}, ws, WS_FB + ((tile * 2 + wt) * 16 + cg * 2 + wp) * 128, 0, SC1);
    asm volatile("s_barrier" ::: "memory");
    ST(7);
    const int tok = tile * 128 + 64 * (w >> 1) + 32 * ((l31 >> 2) & 1) + 16 * (l31 & 1) + 8 * (w & 1) + ((l31 >> 1) & 1) + 2 * (l31 >> 3);
    f16v acc[2];
#pragma unroll
    for (int nt = 0; nt < 2; ++nt)
#pragma unroll
      for (int g = 0; g < 4; ++g) {
        const int d = cg * 64 + nt * 32 + g * 8 + hh * 4;
        const u2 xv = *(const u2*)(xb + tok * 512 + d), dv = *(const u2*)(dsk + d);
        const unsigned p0 = pk2(bflo(xv.x) * bflo(dv.x), bfhi(xv.x) * bfhi(dv.x)), p1 = pk2(bflo(xv.y) * bflo(dv.y), bfhi(xv.y) * bfhi(dv.y));
        acc[nt][4 * g] = bflo(p0); acc[nt][4 * g + 1] = bfhi(p0); acc[nt][4 * g + 2] = bflo(p1); acc[nt][4 * g + 3] = bfhi(p1);
      }
    wait_flags(ws, WS_FB + ((tile * 2 + (w >> 1)) * 16 + (lane & 15)) * 128, true, tag);
    ST(8);
    const unsigned hb = WS_H + (tile * 4 + w) * 65536 + lane * 16;
    u4 hf[3][8];
    auto loadh = [&](int t) {
      const unsigned so = ((crot + t) & 7) * 8192;
#pragma unroll
      for (int i = 0; i < 8; ++i) hf[t % 3][i] = __builtin_amdgcn_raw_buffer_load_b128(ws, hb + i * 1024, so, SC1);
    };
    loadh(0); loadh(1);
#pragma unroll
    for (int t = 0; t < 8; ++t) {
      if (t + 2 < 8) loadh(t + 2);
      __builtin_amdgcn_sched_barrier(0);
      const char LAS* cb = L + (((t >> 1) + 3) % 5) * SLOT + (t & 1) * 16384;
#pragma unroll
      for (int i = 0; i < 8; ++i) {
        const char LAS* p = cb + ((i >> 1) & 1) * 8192 + ar[(i >> 2) * 2 + (i & 1)];
        acc[0] = mfma32(ldsr(p), asbf8(hf[t % 3][i]), acc[0]);
        acc[1] = mfma32(ldsr(p + 4096), asbf8(hf[t % 3][i]), acc[1]);
      }
      __builtin_amdgcn_sched_barrier(0);
    }
    ST(9);
#pragma unroll
    for (int nt = 0; nt < 2; ++nt)
#pragma unroll
      for (int g = 0; g < 4; ++g)
        st_out(ro, (tok * 1024 + 512 + cg * 64 + nt * 32 + g * 8 + hh * 4) * 4, (f4){acc[nt][4 * g], acc[nt][4 * g + 1], acc[nt][4 * g + 2], acc[nt][4 * g + 3]});
    ST(10);
  }
}

extern "C" void kernel_launch(void* const* d_in, const int* in_sizes, int n_in, void* d_out, int out_size, void* d_ws, size_t ws_size,
                              hipStream_t stream) {
  (void)hipFuncSetAttribute((const void*)fused_kernel, hipFuncAttributeMaxDynamicSharedMemorySize, LDS_BYTES);
  fused_kernel<<<256, 256, LDS_BYTES, stream>>>((const u16*)d_in[0], (const u16*)d_in[1], (const u16*)d_in[2], (const u16*)d_in[3],
                                               (const u16*)d_in[4], (const u16*)d_in[5], (const u16*)d_in[6], (const u16*)d_in[7],
                                               (const u16*)d_in[8], (const u16*)d_in[9], (const u16*)d_in[10], (const u16*)d_in[11],
                                               (float*)d_out, (char*)d_ws);
}
```

```cpp
#define PR_SEL 0
#define PR_K 0
#define PR_PTR d_in[0]
#include <hip/hip_runtime.h>
#include <hip/hip_bf16.h>

#define DI __device__ __forceinline__
#define LAS __attribute__((address_space(3)))
#define GAS __attribute__((address_space(1)))
typedef unsigned short u16;
typedef unsigned long long u64;
typedef __bf16 bf8 __attribute__((ext_vector_type(8)));
typedef __bf16 bf2 __attribute__((ext_vector_type(2)));
typedef short s4 __attribute__((ext_vector_type(4)));
typedef short s8 __attribute__((ext_vector_type(8)));
typedef float f16v __attribute__((ext_vector_type(16)));
typedef float f4 __attribute__((ext_vector_type(4)));
typedef float f2 __attribute__((ext_vector_type(2)));
typedef unsigned u4 __attribute__((ext_vector_type(4)));
typedef unsigned u2 __attribute__((ext_vector_type(2)));

constexpr unsigned WS_EPOCH = 0;
constexpr unsigned WS_FA = 4096;
constexpr unsigned WS_FB = WS_FA + 65536;
constexpr unsigned WS_EST = WS_FB + 65536;
constexpr unsigned WS_Z = WS_EST + 131072;
constexpr unsigned WS_KV = WS_Z + 32768;
constexpr unsigned WS_O = 1u << 20;
constexpr unsigned WS_H = 3u << 20;
constexpr unsigned WS_END = 7u << 20;
constexpr unsigned WS_STAMP = 8u << 20;
constexpr int LDS_BYTES = 160 * 1024;
constexpr unsigned SPIN_CAP = 200000;
constexpr int SC1 = 16;

#define ROT 1

#define ST(i) do {} while (0)

DI unsigned pk2(float a, float b) { f2 v = {a, b}; return __builtin_bit_cast(unsigned, __builtin_convertvector(v, bf2)); }
DI float bflo(unsigned v) { return __uint_as_float(v << 16); }
DI float bfhi(unsigned v) { return __uint_as_float(v & 0xffff0000u); }
DI bf8 asbf8(u4 v) { return __builtin_bit_cast(bf8, v); }
DI f16v mfma32(bf8 a, bf8 b, f16v c) { return __builtin_amdgcn_mfma_f32_32x32x16_bf16(a, b, c, 0, 0, 0); }
DI f4 mfma16(bf8 a, bf8 b, f4 c) { return __builtin_amdgcn_mfma_f32_16x16x32_bf16(a, b, c, 0, 0, 0); }
DI f4 mfma16k16(u2 a, u2 b, f4 c) { return __builtin_amdgcn_mfma_f32_16x16x16bf16_1k(__builtin_bit_cast(s4, a), __builtin_bit_cast(s4, b), c, 0, 0, 0); }
DI bf8 ldsr(const char LAS* p) { return *(const LAS bf8*)p; }
DI bf8 ldstr(const char LAS* p0, const char LAS* p1) {
  s4 a = __builtin_amdgcn_ds_read_tr16_b64_v4i16((LAS s4*)p0), b = __builtin_amdgcn_ds_read_tr16_b64_v4i16((LAS s4*)p1);
  return __builtin_bit_cast(bf8, (s8)__builtin_shufflevector(a, b, 0, 1, 2, 3, 4, 5, 6, 7));
}
DI void dma4(unsigned lds, const char* sb, unsigned v0, unsigned v1, unsigned v2, unsigned v3) {
  asm volatile("s_mov_b32 m0, %0\n\ts_nop 0\n\tglobal_load_lds_dwordx4 %1, %5\n\tglobal_load_lds_dwordx4 %2, %5 offset:1024\n\t"
               "global_load_lds_dwordx4 %3, %5 offset:2048\n\tglobal_load_lds_dwordx4 %4, %5 offset:3072"
               :: "s"(lds), "v"(v0), "v"(v1), "v"(v2), "v"(v3), "s"(sb) : "memory");
}
DI void dma2(unsigned lds, const char* sb, unsigned v0, unsigned v1) {
  asm volatile("s_mov_b32 m0, %0\n\ts_nop 0\n\tglobal_load_lds_dwordx4 %1, %3\n\tglobal_load_lds_dwordx4 %2, %3 offset:1024"
               :: "s"(lds), "v"(v0), "v"(v1), "s"(sb) : "memory");
}
template <int N> DI void wait_bar() { asm volatile("s_waitcnt vmcnt(%0) lgkmcnt(0)\n\ts_barrier" :: "i"(N) : "memory"); }
DI void drain() { asm volatile("s_waitcnt vmcnt(0)" ::: "memory"); }
DI unsigned tagv(float x, unsigned t) { return (__float_as_uint(x) & 0xffffff00u) | t; }
DI f2 cmul(f2 a, f2 b) { return (f2){a.x * b.x - a.y * b.y, a.x * b.y + a.y * b.x}; }
DI f2 cfma(f2 a, f2 b, f2 c) { return (f2){fmaf(a.x, b.x, fmaf(-a.y, b.y, c.x)), fmaf(a.x, b.y, fmaf(a.y, b.x, c.y))}; }
DI f2 fma2(f2 a, f2 b, f2 c) { return __builtin_elementwise_fma(a, b, c); }
DI float phi(float t) { return fmaxf(t, 0.f) + __builtin_amdgcn_fmed3f(__builtin_amdgcn_exp2f(t * 1.44269504088896341f), 0.f, 1.f) + 1e-6f; }

#define OUT_AUX 0
DI void st_out(__amdgpu_buffer_rsrc_t o, unsigned off, f4 v) { __builtin_amdgcn_raw_buffer_store_b128(__builtin_bit_cast(u4, v), o, off, 0, OUT_AUX); }
DI void wait_flags(__amdgpu_buffer_rsrc_t ws, unsigned off, bool active, unsigned tag) {
#pragma unroll 1
  for (unsigned sp = 0; sp < SPIN_CAP; ++sp) {
    u2 f = __builtin_amdgcn_raw_buffer_load_b64(ws, off, 0, SC1);
    bool ok = !active || (f.x == tag && f.y == ~tag);
    if (__all(ok)) break;
    __builtin_amdgcn_s_sleep(1);
  }
  asm volatile("" ::: "memory");
}

__global__ void __launch_bounds__(256, 2)
fused_kernel(const u16* __restrict__ xa, const u16* __restrict__ wqkv, const u16* __restrict__ wo, const u16* __restrict__ xb,
             const u16* __restrict__ lre, const u16* __restrict__ lim, const u16* __restrict__ bre, const u16* __restrict__ bim,
             const u16* __restrict__ cre, const u16* __restrict__ cim, const u16* __restrict__ ldt, const u16* __restrict__ dsk,
             float* __restrict__ out, char* wsp) {
  extern __shared__ __attribute__((aligned(16))) char smem_[];
  const char LAS* L = (const char LAS*)smem_;
  const unsigned L0 = (unsigned)(size_t)L;
  ST(0);
  const int lane = threadIdx.x & 63;
  const int w = __builtin_amdgcn_readfirstlane(threadIdx.x >> 6);
  const int bid = blockIdx.x, xcd = bid & 7, jj = bid >> 3;
  const int tile = (xcd & 3) * 4 + (jj >> 3), cg = jj & 7;
  const int rot = ROT ? ((cg + 3 * (jj >> 3) + xcd) & 7) : 0;
  unsigned epoch;
  asm volatile("" :: "s"(xa), "s"(wqkv), "s"(wo), "s"(xb), "s"(lre), "s"(lim), "s"(bre), "s"(bim), "s"(cre), "s"(cim), "s"(ldt), "s"(dsk), "s"(out), "s"(wsp));
  const __amdgpu_buffer_rsrc_t ws = __builtin_amdgcn_make_buffer_rsrc(wsp, 0, WS_END, 0x00020000);
  const __amdgpu_buffer_rsrc_t ro = __builtin_amdgcn_make_buffer_rsrc(out, 0, 2048 * 1024 * 4, 0x00020000);
  const int r8 = lane >> 3, c8 = lane & 7;
  unsigned tag;

  if (xcd < 4) {
    constexpr unsigned SLOT = 40960;
    unsigned vx[4], vw[2];
#pragma unroll
    for (int i = 0; i < 4; ++i) vx[i] = (32 * w + 8 * i + r8) * 1024 + ((c8 ^ ((4 * i + (r8 >> 1)) & 7)) << 4) - 1024 * i;
#pragma unroll
    for (int i = 0; i < 2; ++i) vw[i] = (16 * w + 8 * i + r8) * 3072 + ((c8 ^ ((((r8 >> 1) & 1) | (i << 1)) << 1)) << 4) - 1024 * i;
    const char* xbase = (const char*)xa + (size_t)tile * 131072;
    const char* wbase = (const char*)wqkv + cg * 128;
    auto issue = [&](int kk) {
      const int kc = (kk + rot) & 7;
      const unsigned s = L0 + (kk & 3) * SLOT;
      dma4(s + w * 4096, xbase + kc * 128, vx[0], vx[1], vx[2], vx[3]);
      const char* wb = wbase + kc * (64 * 3072);
      dma2(s + 16384 + w * 2048, wb, vw[0], vw[1]);
      dma2(s + 24576 + w * 2048, wb + 1024, vw[0], vw[1]);
      dma2(s + 32768 + w * 2048, wb + 2048, vw[0], vw[1]);
    };
    issue(0); issue(1); issue(2); issue(3);
    asm volatile("s_load_dword %0, %1, 0x0" : "=s"(epoch) : "s"(wsp) : "memory");
    unsigned vo[4];
#pragma unroll
    for (int i = 0; i < 4; ++i) vo[i] = (64 * w + 8 * i + r8) * 1024 + ((c8 ^ (((r8 >> 1) & 3) << 1)) << 4) - 1024 * i;
    const char* obase = (const char*)wo + cg * 128;
    auto issue_wo = [&](int piece) {
      dma4(L0 + piece * SLOT + w * 8192, obase + piece * 262144, vo[0], vo[1], vo[2], vo[3]);
      dma4(L0 + piece * SLOT + w * 8192 + 4096, obase + piece * 262144 + 32768, vo[0], vo[1], vo[2], vo[3]);
    };
    const int q = lane >> 4, l15 = lane & 15, qq = (lane >> 2) & 3, pp = lane & 3;
    const unsigned ax0 = l15 * 128 + ((q ^ (l15 >> 1)) << 4), ax1 = l15 * 128 + (((4 + q) ^ (l15 >> 1)) << 4);
    const unsigned aw = 16384 + (8 * q + qq) * 128 + (((2 * w + (pp >> 1)) ^ ((((qq >> 1) & 1) | ((q & 1) << 1)) << 1)) << 4) + 8 * (pp & 1);
    f4 ka[8], va[8], qa[8];
#pragma unroll
    for (int i = 0; i < 8; ++i) { ka[i] = (f4){0, 0, 0, 0}; va[i] = ka[i]; qa[i] = ka[i]; }
    auto compute = [&](int kk) {
      const char LAS* s = L + (kk & 3) * SLOT;
#pragma unroll
      for (int ks = 0; ks < 2; ++ks) {
        const char LAS* wp_ = s + aw + ks * 4096;
        const bf8 fq = ldstr(wp_, wp_ + 512), fk = ldstr(wp_ + 8192, wp_ + 8192 + 512), fv = ldstr(wp_ + 16384, wp_ + 16384 + 512);
#pragma unroll
        for (int mt = 0; mt < 8; ++mt) {
          const bf8 fx = ldsr(s + (ks ? ax1 : ax0) + mt * 2048);
          ka[mt] = mfma16(fx, fk, ka[mt]);
          va[mt] = mfma16(fx, fv, va[mt]);
          qa[mt] = mfma16(fq, fx, qa[mt]);
        }
      }
    };
    asm volatile("s_waitcnt vmcnt(30) lgkmcnt(0)\n\ts_barrier" : "+s"(epoch) :: "memory");
    ST(1);
    tag = epoch + 1;
    compute(0);
    wait_bar<20>(); issue(4); compute(1);
    wait_bar<20>(); issue(5); compute(2);
    wait_bar<20>(); issue(6); compute(3);
    wait_bar<20>(); issue(7); compute(4);
    wait_bar<20>(); issue_wo(0); compute(5);
    wait_bar<18>(); issue_wo(1); compute(6);
    wait_bar<16>(); ST(2); compute(7);
    ST(3);
    const unsigned t0 = tag & 0xffu, t1 = (tag >> 8) & 0xffu;
    const int head = cg * 4 + w;
    {
      f4 kv = {0, 0, 0, 0}, zz = {0, 0, 0, 0};
      const u4 ones = {0x3f803f80u, 0x3f803f80u, 0x3f803f80u, 0x3f803f80u};
#pragma unroll
      for (int mm = 0; mm < 4; ++mm) {
        const f4 a = ka[2 * mm], b = ka[2 * mm + 1], c = va[2 * mm], d = va[2 * mm + 1];
        const u4 pa = {pk2(phi(a[0]), phi(a[1])), pk2(phi(a[2]), phi(a[3])), pk2(phi(b[0]), phi(b[1])), pk2(phi(b[2]), phi(b[3]))};
        const u4 pb = {pk2(c[0], c[1]), pk2(c[2], c[3]), pk2(d[0], d[1]), pk2(d[2], d[3])};
        kv = mfma16(asbf8(pa), asbf8(pb), kv);
        zz = mfma16(asbf8(pa), asbf8(ones), zz);
      }
      const u4 skv = {tagv(kv[0], t0), tagv(kv[1], t1), tagv(kv[2], t0), tagv(kv[3], t1)};
      __builtin_amdgcn_raw_buffer_store_b128(skv, ws, WS_KV + (head * 16 + tile) * 1024 + lane * 16, 0, SC1);
      const u4 sz = {tagv(zz[0], t0), tagv(zz[1], t1), tagv(zz[2], t0), tagv(zz[3], t1)};
      if (l15 == 0) __builtin_amdgcn_raw_buffer_store_b128(sz, ws, WS_Z + (head * 16 + tile) * 64 + q * 16, 0, SC1);
    }
    ST(4);
    u2 pq[8];
#pragma unroll
    for (int mt = 0; mt < 8; ++mt) pq[mt] = (u2){pk2(phi(qa[mt][0]), phi(qa[mt][1])), pk2(phi(qa[mt][2]), phi(qa[mt][3]))};
    f4 kv, zz;
    for (unsigned sp = 0; sp < SPIN_CAP; ++sp) {
      u4 g[16];
      const u4 gz = __builtin_amdgcn_raw_buffer_load_b128(ws, WS_Z + head * 1024 + l15 * 64 + q * 16, 0, SC1);
#pragma unroll
      for (int i = 0; i < 16; ++i) g[i] = __builtin_amdgcn_raw_buffer_load_b128(ws, WS_KV + head * 16384 + lane * 16, i * 1024, SC1);
      const u4 tt = {t0, t1, t0, t1};
      const u4 cz = gz ^ tt;
      unsigned bad = cz.x | cz.y | cz.z | cz.w;
      kv = (f4){0, 0, 0, 0};
#pragma unroll
      for (int i = 0; i < 16; ++i) {
        const u4 c = g[i] ^ tt;
        bad |= c.x | c.y | c.z | c.w;
        kv += __builtin_bit_cast(f4, c);
      }
      zz = __builtin_bit_cast(f4, cz);
      if (__all((bad & 0xffu) == 0)) break;
      __builtin_amdgcn_s_sleep(1);
    }
    ST(5);
#pragma unroll
    for (int r = 0; r < 4; ++r) {
      float v = zz[r];
      v += __builtin_bit_cast(float, __builtin_amdgcn_update_dpp(0, __builtin_bit_cast(int, v), 0x128, 0xf, 0xf, false));
      v += __builtin_bit_cast(float, __builtin_amdgcn_update_dpp(0, __builtin_bit_cast(int, v), 0x124, 0xf, 0xf, false));
      v += __builtin_bit_cast(float, __builtin_amdgcn_update_dpp(0, __builtin_bit_cast(int, v), 0x122, 0xf, 0xf, false));
      v += __builtin_bit_cast(float, __builtin_amdgcn_update_dpp(0, __builtin_bit_cast(int, v), 0x121, 0xf, 0xf, false));
      zz[r] = v;
    }
    const u2 kvb = {pk2(kv[0], kv[1]), pk2(kv[2], kv[3])}, zb = {pk2(zz[0], zz[1]), pk2(zz[2], zz[3])};
    const f4 zero4 = {0, 0, 0, 0};
#pragma unroll
    for (int u = 0; u < 4; ++u) {
      u4 so;
#pragma unroll
      for (int hh = 0; hh < 2; ++hh) {
        const f4 num = mfma16k16(kvb, pq[2 * u + hh], zero4), den = mfma16k16(zb, pq[2 * u + hh], zero4);
        const float rc = __builtin_amdgcn_rcpf(den[0] + 1e-6f);
        so[2 * hh] = pk2(num[0] * rc, num[1] * rc);
        so[2 * hh + 1] = pk2(num[2] * rc, num[3] * rc);
      }
      __builtin_amdgcn_raw_buffer_store_b128(so, ws, WS_O + ((tile * 4 + u) * 32 + head) * 1024 + lane * 16, 0, SC1);
    }
    ST(6);
    drain();
    if (lane == 0) __builtin_amdgcn_raw_buffer_store_b64((u2){tag, ~tag}, ws, WS_FA + (tile * 32 + head) * 128, 0, SC1);
    asm volatile("s_barrier" ::: "memory");
    ST(7);
    wait_flags(ws, WS_FA + (tile * 32 + (lane & 31)) * 128, true, tag);
    ST(8);
    unsigned ao[4];
#pragma unroll
    for (int nt = 0; nt < 4; ++nt) ao[nt] = (4 * q + qq) * 128 + ((((2 * nt) ^ (((2 * q + (qq >> 1)) & 3) << 1)) + (pp >> 1)) << 4) + 8 * (pp & 1);
    f4 acc[2][4];
#pragma unroll
    for (int i = 0; i < 8; ++i) acc[i >> 2][i & 3] = zero4;
    const unsigned ob = WS_O + (tile * 4 + w) * 32768 + lane * 16;
    u4 of[32];
#pragma unroll
    for (int i = 0; i < 32; ++i) of[i] = __builtin_amdgcn_raw_buffer_load_b128(ws, ob, i * 1024, SC1);
#pragma unroll
    for (int k = 0; k < 16; ++k) {
      const u4 a = of[2 * k], b = of[2 * k + 1];
      const bf8 f0 = asbf8((u4){a.x, a.y, b.x, b.y}), f1 = asbf8((u4){a.z, a.w, b.z, b.w});
      const char LAS* base = L + (k >> 3) * SLOT + (k & 7) * 4096;
#pragma unroll
      for (int nt = 0; nt < 4; ++nt) {
        const bf8 fw = ldstr(base + ao[nt], base + ao[nt] + 2048);
        acc[0][nt] = mfma16(fw, f0, acc[0][nt]);
        acc[1][nt] = mfma16(fw, f1, acc[1][nt]);
      }
    }
    ST(9);
#pragma unroll
    for (int mt = 0; mt < 2; ++mt)
#pragma unroll
      for (int nt = 0; nt < 4; ++nt) {
        const f4 v = acc[mt][nt];
        const unsigned p0 = pk2(v[0], v[1]), p1 = pk2(v[2], v[3]);
        st_out(ro, ((tile * 128 + w * 32 + mt * 16 + l15) * 1024 + cg * 64 + nt * 16 + q * 4) * 4, (f4){bflo(p0), bfhi(p0), bflo(p1), bfhi(p1)});
      }
    ST(10);
    if (bid == 0 && w == 0) {
      wait_flags(ws, WS_FA + lane * 1024, true, tag);
      wait_flags(ws, WS_FA + 512 + lane * 1024, true, tag);
      wait_flags(ws, WS_FB + (lane >> 3) * 4096 + (lane & 7) * 256, true, tag);
      wait_flags(ws, WS_FB + 32768 + (lane >> 3) * 4096 + (lane & 7) * 256, true, tag);
      if (lane == 0) __builtin_amdgcn_raw_buffer_store_b32(tag, ws, WS_EPOCH, 0, SC1);
    }
  } else {
    constexpr unsigned SLOT = 32768;
    const int wp = w & 1, wt = w >> 1, l31 = lane & 31, hh = lane >> 5;
    const int pl = cg * 64 + wp * 32 + ((l31 & 0x13) | ((l31 & 4) << 1) | ((l31 & 8) >> 1));
    unsigned plr, pli, pdt;
    asm volatile("global_load_ushort %0, %3, %4\n\tglobal_load_ushort %1, %3, %5\n\tglobal_load_ushort %2, %3, %6"
                 : "=&v"(plr), "=&v"(pli), "=&v"(pdt) : "v"(pl * 2), "s"(lre), "s"(lim), "s"(ldt) : "memory");
    unsigned vx[4], vw[4];
#pragma unroll
    for (int i = 0; i < 4; ++i) {
      const unsigned sw = (c8 ^ ((4 * i + (r8 >> 1)) & 7)) << 4;
      vx[i] = (64 * (w >> 1) + 32 * (r8 >> 2) + 16 * (r8 & 1) + 8 * (w & 1) + ((r8 >> 1) & 1) + 2 * i) * 1024 + sw - 1024 * i;
      vw[i] = (16 * (i >> 1) + 8 * (r8 >> 2) + 4 * (i & 1) + (r8 & 3)) * 1024 + sw - 1024 * i;
    }
    const char* xbase = (const char*)xb + (size_t)tile * 131072;
    const char* wbase = (const char*)((w & 1) ? bim : bre) + (cg * 64 + (w >> 1) * 32) * 1024;
    auto issue = [&](int kk) {
      const int kc = (kk + rot) & 7;
      const unsigned s = L0 + (kk % 5) * SLOT + w * 4096;
      dma4(s, xbase + kc * 128, vx[0], vx[1], vx[2], vx[3]);
      dma4(s + 16384, wbase + kc * 128, vw[0], vw[1], vw[2], vw[3]);
    };
    issue(0); issue(1); issue(2); issue(3); issue(4);
    asm volatile("s_load_dword %0, %1, 0x0" : "=s"(epoch) : "s"(wsp) : "memory");
    unsigned vc[4];
#pragma unroll
    for (int i = 0; i < 4; ++i) vc[i] = (8 * i + r8) * 1024 + ((c8 ^ ((4 * i + (r8 >> 1)) & 7)) << 4) - 1024 * i;
    const char* cbase = (const char*)((w & 1) ? cim : cre) + cg * 65536;
    const int crot = cg + 2 * (jj >> 3);
    auto issue_c = [&](int piece) {
      const unsigned s = L0 + ((piece + 3) % 5) * SLOT + w * 8192;
      const char* cb = cbase + ((crot + 2 * piece + (w >> 1)) & 7) * 128;
      dma4(s, cb, vc[0], vc[1], vc[2], vc[3]);
      dma4(s + 4096, cb + 32768, vc[0], vc[1], vc[2], vc[3]);
    };
    asm volatile("s_waitcnt vmcnt(40)" : "+v"(plr), "+v"(pli), "+v"(pdt) :: "memory");
    f2 lam, kap, l16, l32, l64, l128;
    {
      const float lr = bflo(plr), li = bflo(pli);
      const float dt = bflo(pk2(expf(bflo(pdt)), 0.f));
      const float mag = expf(dt * lr);
      float sn, cs;
      sincosf(dt * li, &sn, &cs);
      lam = (f2){mag * cs, mag * sn};
      const float inv = 2.f / (lr * lr + li * li), nr = lam.x - 1.f, ni = lam.y;
      kap = (f2){(nr * lr + ni * li) * inv, (ni * lr - nr * li) * inv};
      f2 t = cmul(lam, lam); t = cmul(t, t); t = cmul(t, t);
      l16 = cmul(t, t); l32 = cmul(l16, l16); l64 = cmul(l32, l32); l128 = cmul(l64, l64);
    }
    unsigned ar[4];
#pragma unroll
    for (int s = 0; s < 4; ++s) ar[s] = l31 * 128 + (((2 * s + hh) ^ ((lane >> 1) & 7)) << 4);
    f16v ur[2], ui[2];
#pragma unroll
    for (int i = 0; i < 16; ++i) { ur[0][i] = 0; ur[1][i] = 0; ui[0][i] = 0; ui[1][i] = 0; }
    auto compute = [&](int kk) {
      const char LAS* sx = L + (kk % 5) * SLOT + wt * 8192;
      const char LAS* sw = L + (kk % 5) * SLOT + 16384 + wp * 8192;
#pragma unroll
      for (int s = 0; s < 4; ++s) {
        const bf8 x0 = ldsr(sx + ar[s]), x1 = ldsr(sx + ar[s] + 4096), b0 = ldsr(sw + ar[s]), b1 = ldsr(sw + ar[s] + 4096);
        ur[0] = mfma32(x0, b0, ur[0]); ui[0] = mfma32(x0, b1, ui[0]);
        ur[1] = mfma32(x1, b0, ur[1]); ui[1] = mfma32(x1, b1, ui[1]);
      }
    };
    asm volatile("s_waitcnt vmcnt(32) lgkmcnt(0)\n\ts_barrier" : "+s"(epoch) :: "memory");
    ST(1);
    tag = epoch + 1;
    compute(0);
    wait_bar<24>(); issue(5); compute(1);
    wait_bar<24>(); issue(6); compute(2);
    wait_bar<24>(); issue(7); compute(3);
    wait_bar<24>(); issue_c(0); compute(4);
    wait_bar<24>(); issue_c(1); compute(5);
    wait_bar<24>(); issue_c(2); compute(6);
    wait_bar<24>(); ST(2); issue_c(3); compute(7);
    ST(3);
    const unsigned t0 = tag & 0xffu, t1 = (tag >> 8) & 0xffu;
    const f2 LR = {lam.x, lam.x}, LI = {lam.y, lam.y};
    f2 er = {0, 0}, ei = {0, 0};
#pragma unroll
    for (int a = 0; a < 16; ++a) {
      const f2 xr = {ur[a >> 3][2 * (a & 7)], ur[a >> 3][2 * (a & 7) + 1]}, xi = {ui[a >> 3][2 * (a & 7)], ui[a >> 3][2 * (a & 7) + 1]};
      const f2 nr = fma2(LR, er, fma2(-LI, ei, xr)), ni = fma2(LR, ei, fma2(LI, er, xi));
      er = nr; ei = ni;
    }
    const f2 e0 = {er.x, ei.x};
    const f2 e32 = cfma(l16, e0, (f2){er.y, ei.y});
    const auto sx_ = __builtin_amdgcn_permlane32_swap(__float_as_uint(e32.x), __float_as_uint(e32.x), false, false);
    const auto sy_ = __builtin_amdgcn_permlane32_swap(__float_as_uint(e32.y), __float_as_uint(e32.y), false, false);
    const f2 elo = {__uint_as_float(sx_[0]), __uint_as_float(sy_[0])}, ehi = {__uint_as_float(sx_[1]), __uint_as_float(sy_[1])};
    const f2 e64 = cfma(l32, elo, ehi);
    const int J = tile * 2 + wt;
    const unsigned ecol = WS_EST + (cg * 64 + wp * 32 + l31) * 8;
    if (hh == 0) __builtin_amdgcn_raw_buffer_store_b64((u2){tagv(e64.x, t0), tagv(e64.y, t1)}, ws, ecol + J * 4096, 0, SC1);
    ST(4);
    {
      const f2 KR = {kap.x, kap.x}, KI = {kap.y, kap.y};
#pragma unroll
      for (int a = 0; a < 16; ++a) {
        const f2 xr = {ur[a >> 3][2 * (a & 7)], ur[a >> 3][2 * (a & 7) + 1]}, xi = {ui[a >> 3][2 * (a & 7)], ui[a >> 3][2 * (a & 7) + 1]};
        const f2 nr = fma2(KR, xr, -KI * xi), ni = fma2(-KR, xi, -KI * xr);
        ur[a >> 3][2 * (a & 7)] = nr.x; ur[a >> 3][2 * (a & 7) + 1] = nr.y;
        ui[a >> 3][2 * (a & 7)] = ni.x; ui[a >> 3][2 * (a & 7) + 1] = ni.y;
      }
    }
    f2 cin;
    for (unsigned sp = 0; sp < SPIN_CAP; ++sp) {
      u2 g[16];
#pragma unroll
      for (int m = 0; m < 16; ++m) {
        const int i = J - 1 - hh - 2 * m;
        g[m] = __builtin_amdgcn_raw_buffer_load_b64(ws, i >= 0 ? ecol + i * 4096 : 0xfffffff0u, 0, SC1);
      }
      unsigned bad = 0;
      cin = (f2){0, 0};
#pragma unroll
      for (int m = 15; m >= 0; --m) {
        const int i = J - 1 - hh - 2 * m;
        const unsigned cx = i >= 0 ? g[m].x ^ t0 : 0u, cy = i >= 0 ? g[m].y ^ t1 : 0u;
        bad |= cx | cy;
        cin = cfma(l128, cin, (f2){__uint_as_float(cx), __uint_as_float(cy)});
      }
      if (__all((bad & 0xffu) == 0)) break;
      __builtin_amdgcn_s_sleep(1);
    }
    ST(5);
    {
      const auto cx = __builtin_amdgcn_permlane32_swap(__float_as_uint(cin.x), __float_as_uint(cin.x), false, false);
      const auto cy = __builtin_amdgcn_permlane32_swap(__float_as_uint(cin.y), __float_as_uint(cin.y), false, false);
      cin = cfma(l64, (f2){__uint_as_float(cx[1]), __uint_as_float(cy[1])}, (f2){__uint_as_float(cx[0]), __uint_as_float(cy[0])});
    }
    const f2 up = cfma(l32, cin, elo);
    const f2 c0 = hh ? up : cin;
    const f2 c1 = cfma(l16, c0, e0);
    const f2 d0 = cmul(kap, c0), d1 = cmul(kap, c1);
    f2 hr = {d0.x, d1.x}, hi = {-d0.y, -d1.y};
    u4 idn[2];
#pragma unroll
    for (int s = 0; s < 2; ++s)
#pragma unroll
      for (int jq = 0; jq < 4; ++jq) {
        const int r0 = 16 * s + 8 * (jq >> 1) + 4 * hh + 2 * (jq & 1);
        idn[s][jq] = (l31 == r0 ? 0x3f80u : 0u) | (l31 == r0 + 1 ? 0x3f800000u : 0u);
      }
    f16v zero16;
#pragma unroll
    for (int i = 0; i < 16; ++i) zero16[i] = 0;
#pragma unroll
    for (int m = 0; m < 2; ++m) {
#pragma unroll
      for (int a = 0; a < 8; ++a) {
        const f2 xr = {ur[m][2 * a], ur[m][2 * a + 1]}, xi = {ui[m][2 * a], ui[m][2 * a + 1]};
        const f2 nr = fma2(LR, hr, fma2(LI, hi, xr)), ni = fma2(LR, hi, fma2(-LI, hr, xi));
        hr = nr; hi = ni;
        ur[m][2 * a] = nr.x; ur[m][2 * a + 1] = nr.y; ui[m][2 * a] = ni.x; ui[m][2 * a + 1] = ni.y;
      }
#pragma unroll
      for (int c = 0; c < 2; ++c) {
        const f16v X = c ? ui[m] : ur[m];
        const u4 a0 = {pk2(X[0], X[1]), pk2(X[2], X[3]), pk2(X[4], X[5]), pk2(X[6], X[7])};
        const u4 a1 = {pk2(X[8], X[9]), pk2(X[10], X[11]), pk2(X[12], X[13]), pk2(X[14], X[15])};
        f16v T = mfma32(asbf8(a0), asbf8(idn[0]), zero16);
        T = mfma32(asbf8(a1), asbf8(idn[1]), T);
        const u4 o0 = {pk2(T[0], T[1]), pk2(T[2], T[3]), pk2(T[4], T[5]), pk2(T[6], T[7])};
        const u4 o1 = {pk2(T[8], T[9]), pk2(T[10], T[11]), pk2(T[12], T[13]), pk2(T[14], T[15])};
        const unsigned ho = WS_H + (tile * 4 + wt * 2 + m) * 65536 + ((cg * 2 + wp) * 2 + c) * 2048 + lane * 16;
        __builtin_amdgcn_raw_buffer_store_b128(o0, ws, ho, 0, SC1);
        __builtin_amdgcn_raw_buffer_store_b128(o1, ws, ho + 1024, 0, SC1);
      }
    }
    ST(6);
    drain();
    if (lane == 0) __builtin_amdgcn_raw_buffer_store_b64((u2){tag, ~tag}, ws, WS_FB + ((tile * 2 + wt) * 16 + cg * 2 + wp) * 128, 0, SC1);
    asm volatile("s_barrier" ::: "memory");
    ST(7);
    const int tok = tile * 128 + 64 * (w >> 1) + 32 * ((l31 >> 2) & 1) + 16 * (l31 & 1) + 8 * (w & 1) + ((l31 >> 1) & 1) + 2 * (l31 >> 3);
    f16v acc[2];
#pragma unroll
    for (int nt = 0; nt < 2; ++nt)
#pragma unroll
      for (int g = 0; g < 4; ++g) {
        const int d = cg * 64 + nt * 32 + g * 8 + hh * 4;
        const u2 xv = *(const u2*)(xb + tok * 512 + d), dv = *(const u2*)(dsk + d);
        const unsigned p0 = pk2(bflo(xv.x) * bflo(dv.x), bfhi(xv.x) * bfhi(dv.x)), p1 = pk2(bflo(xv.y) * bflo(dv.y), bfhi(xv.y) * bfhi(dv.y));
        acc[nt][4 * g] = bflo(p0); acc[nt][4 * g + 1] = bfhi(p0); acc[nt][4 * g + 2] = bflo(p1); acc[nt][4 * g + 3] = bfhi(p1);
      }
    wait_flags(ws, WS_FB + ((tile * 2 + (w >> 1)) * 16 + (lane & 15)) * 128, true, tag);
    ST(8);
    const unsigned hb = WS_H + (tile * 4 + w) * 65536 + lane * 16;
    u4 hf[3][8];
    auto loadh = [&](int t) {
      const unsigned so = ((crot + t) & 7) * 8192;
#pragma unroll
      for (int i = 0; i < 8; ++i) hf[t % 3][i] = __builtin_amdgcn_raw_buffer_load_b128(ws, hb + i * 1024, so, SC1);
    };
    loadh(0); loadh(1);
#pragma unroll
    for (int t = 0; t < 8; ++t) {
      if (t + 2 < 8) loadh(t + 2);
      __builtin_amdgcn_sched_barrier(0);
      const char LAS* cb = L + (((t >> 1) + 3) % 5) * SLOT + (t & 1) * 16384;
#pragma unroll
      for (int i = 0; i < 8; ++i) {
        const char LAS* p = cb + ((i >> 1) & 1) * 8192 + ar[(i >> 2) * 2 + (i & 1)];
        acc[0] = mfma32(ldsr(p), asbf8(hf[t % 3][i]), acc[0]);
        acc[1] = mfma32(ldsr(p + 4096), asbf8(hf[t % 3][i]), acc[1]);
      }
      __builtin_amdgcn_sched_barrier(0);
    }
    ST(9);
#pragma unroll
    for (int nt = 0; nt < 2; ++nt)
#pragma unroll
      for (int g = 0; g < 4; ++g)
        st_out(ro, (tok * 1024 + 512 + cg * 64 + nt * 32 + g * 8 + hh * 4) * 4, (f4){acc[nt][4 * g], acc[nt][4 * g + 1], acc[nt][4 * g + 2], acc[nt][4 * g + 3]});
    ST(10);
  }
}

__global__ void probe_kernel(const char* p, char* sink) {
  unsigned v0, v1; u64 t0, t1, t2;
  asm volatile("s_memrealtime %0\n\ts_waitcnt lgkmcnt(0)" : "=s"(t0) :: "memory");
  asm volatile("global_load_dword %0, %1, %2\n\ts_waitcnt vmcnt(0)" : "=&v"(v0) : "v"(0), "s"(p) : "memory");
  asm volatile("s_memrealtime %0\n\ts_waitcnt lgkmcnt(0)" : "=s"(t1) :: "memory");
  unsigned acc = v0;
  for (int i = 1; i <= 8; ++i) {
    asm volatile("global_load_dword %0, %1, %2\n\ts_waitcnt vmcnt(0)" : "=&v"(v1) : "v"(i * 4096 + i * 256), "s"(p) : "memory");
    acc += v1;
  }
  asm volatile("s_memrealtime %0\n\ts_waitcnt lgkmcnt(0)" : "=s"(t2) :: "memory");
  const u64 d = PR_SEL ? (t2 - t1) * PR_K / 8 : (t1 - t0) * PR_K;
  while (__builtin_amdgcn_s_memrealtime() < t2 + d) __builtin_amdgcn_s_sleep(2);
  asm volatile("" :: "v"(acc));
}

extern "C" void kernel_launch(void* const* d_in, const int* in_sizes, int n_in, void* d_out, int out_size, void* d_ws, size_t ws_size,
                              hipStream_t stream) {
  (void)hipFuncSetAttribute((const void*)fused_kernel, hipFuncAttributeMaxDynamicSharedMemorySize, LDS_BYTES);
  probe_kernel<<<1, 64, 0, stream>>>((const char*)(PR_PTR), (char*)d_ws);
  fused_kernel<<<256, 256, LDS_BYTES, stream>>>((const u16*)d_in[0], (const u16*)d_in[1], (const u16*)d_in[2], (const u16*)d_in[3],
                                               (const u16*)d_in[4], (const u16*)d_in[5], (const u16*)d_in[6], (const u16*)d_in[7],
                                               (const u16*)d_in[8], (const u16*)d_in[9], (const u16*)d_in[10], (const u16*)d_in[11],
                                               (float*)d_out, (char*)d_ws);
}
```

```cpp
#define AMP_A 0
#define AMP_B 0
#define AMP_BID 0
#define AMP_K 0
#define PR_G 1
#define PR_AFTER 1
#define PR_K 0
#include <hip/hip_runtime.h>
#include <hip/hip_bf16.h>

#define DI __device__ __forceinline__
#define LAS __attribute__((address_space(3)))
#define GAS __attribute__((address_space(1)))
typedef unsigned short u16;
typedef unsigned long long u64;
typedef __bf16 bf8 __attribute__((ext_vector_type(8)));
typedef __bf16 bf2 __attribute__((ext_vector_type(2)));
typedef short s4 __attribute__((ext_vector_type(4)));
typedef short s8 __attribute__((ext_vector_type(8)));
typedef float f16v __attribute__((ext_vector_type(16)));
typedef float f4 __attribute__((ext_vector_type(4)));
typedef float f2 __attribute__((ext_vector_type(2)));
typedef unsigned u4 __attribute__((ext_vector_type(4)));
typedef unsigned u2 __attribute__((ext_vector_type(2)));

constexpr unsigned WS_EPOCH = 0;
constexpr unsigned WS_FA = 4096;
constexpr unsigned WS_FB = WS_FA + 65536;
constexpr unsigned WS_EST = WS_FB + 65536;
constexpr unsigned WS_Z = WS_EST + 131072;
constexpr unsigned WS_KV = WS_Z + 32768;
constexpr unsigned WS_O = 1u << 20;
constexpr unsigned WS_H = 3u << 20;
constexpr unsigned WS_END = 7u << 20;
constexpr unsigned WS_STAMP = 8u << 20;
constexpr int LDS_BYTES = 160 * 1024;
constexpr unsigned SPIN_CAP = 200000;
constexpr int SC1 = 16;

#define ROT 1

#define ST(i) do { __builtin_amdgcn_sched_barrier(0); asm volatile("s_memrealtime %0\n\ts_waitcnt lgkmcnt(0)" : "=s"(st[i]) :: "memory"); __builtin_amdgcn_sched_barrier(0); } while (0)

DI unsigned pk2(float a, float b) { f2 v = {a, b}; return __builtin_bit_cast(unsigned, __builtin_convertvector(v, bf2)); }
DI float bflo(unsigned v) { return __uint_as_float(v << 16); }
DI float bfhi(unsigned v) { return __uint_as_float(v & 0xffff0000u); }
DI bf8 asbf8(u4 v) { return __builtin_bit_cast(bf8, v); }
DI f16v mfma32(bf8 a, bf8 b, f16v c) { return __builtin_amdgcn_mfma_f32_32x32x16_bf16(a, b, c, 0, 0, 0); }
DI f4 mfma16(bf8 a, bf8 b, f4 c) { return __builtin_amdgcn_mfma_f32_16x16x32_bf16(a, b, c, 0, 0, 0); }
DI f4 mfma16k16(u2 a, u2 b, f4 c) { return __builtin_amdgcn_mfma_f32_16x16x16bf16_1k(__builtin_bit_cast(s4, a), __builtin_bit_cast(s4, b), c, 0, 0, 0); }
DI bf8 ldsr(const char LAS* p) { return *(const LAS bf8*)p; }
DI bf8 ldstr(const char LAS* p0, const char LAS* p1) {
  s4 a = __builtin_amdgcn_ds_read_tr16_b64_v4i16((LAS s4*)p0), b = __builtin_amdgcn_ds_read_tr16_b64_v4i16((LAS s4*)p1);
  return __builtin_bit_cast(bf8, (s8)__builtin_shufflevector(a, b, 0, 1, 2, 3, 4, 5, 6, 7));
}
DI void dma4(unsigned lds, const char* sb, unsigned v0, unsigned v1, unsigned v2, unsigned v3) {
  asm volatile("s_mov_b32 m0, %0\n\ts_nop 0\n\tglobal_load_lds_dwordx4 %1, %5\n\tglobal_load_lds_dwordx4 %2, %5 offset:1024\n\t"
               "global_load_lds_dwordx4 %3, %5 offset:2048\n\tglobal_load_lds_dwordx4 %4, %5 offset:3072"
               :: "s"(lds), "v"(v0), "v"(v1), "v"(v2), "v"(v3), "s"(sb) : "memory");
}
DI void dma2(unsigned lds, const char* sb, unsigned v0, unsigned v1) {
  asm volatile("s_mov_b32 m0, %0\n\ts_nop 0\n\tglobal_load_lds_dwordx4 %1, %3\n\tglobal_load_lds_dwordx4 %2, %3 offset:1024"
               :: "s"(lds), "v"(v0), "v"(v1), "s"(sb) : "memory");
}
template <int N> DI void wait_bar() { asm volatile("s_waitcnt vmcnt(%0) lgkmcnt(0)\n\ts_barrier" :: "i"(N) : "memory"); }
DI void drain() { asm volatile("s_waitcnt vmcnt(0)" ::: "memory"); }
DI unsigned tagv(float x, unsigned t) { return (__float_as_uint(x) & 0xffffff00u) | t; }
DI f2 cmul(f2 a, f2 b) { return (f2){a.x * b.x - a.y * b.y, a.x * b.y + a.y * b.x}; }
DI f2 cfma(f2 a, f2 b, f2 c) { return (f2){fmaf(a.x, b.x, fmaf(-a.y, b.y, c.x)), fmaf(a.x, b.y, fmaf(a.y, b.x, c.y))}; }
DI f2 fma2(f2 a, f2 b, f2 c) { return __builtin_elementwise_fma(a, b, c); }
DI float phi(float t) { return fmaxf(t, 0.f) + __builtin_amdgcn_fmed3f(__builtin_amdgcn_exp2f(t * 1.44269504088896341f), 0.f, 1.f) + 1e-6f; }

#define OUT_AUX 0
DI void st_out(__amdgpu_buffer_rsrc_t o, unsigned off, f4 v) { __builtin_amdgcn_raw_buffer_store_b128(__builtin_bit_cast(u4, v), o, off, 0, OUT_AUX); }
DI void wait_flags(__amdgpu_buffer_rsrc_t ws, unsigned off, bool active, unsigned tag) {
#pragma unroll 1
  for (unsigned sp = 0; sp < SPIN_CAP; ++sp) {
    u2 f = __builtin_amdgcn_raw_buffer_load_b64(ws, off, 0, SC1);
    bool ok = !active || (f.x == tag && f.y == ~tag);
    if (__all(ok)) break;
    __builtin_amdgcn_s_sleep(1);
  }
  asm volatile("" ::: "memory");
}

__global__ void __launch_bounds__(256, 2)
fused_kernel(const u16* __restrict__ xa, const u16* __restrict__ wqkv, const u16* __restrict__ wo, const u16* __restrict__ xb,
             const u16* __restrict__ lre, const u16* __restrict__ lim, const u16* __restrict__ bre, const u16* __restrict__ bim,
             const u16* __restrict__ cre, const u16* __restrict__ cim, const u16* __restrict__ ldt, const u16* __restrict__ dsk,
             float* __restrict__ out, char* wsp) {
  extern __shared__ __attribute__((aligned(16))) char smem_[];
  const char LAS* L = (const char LAS*)smem_;
  const unsigned L0 = (unsigned)(size_t)L;
  u64 st[12];
  for (int i = 0; i < 12; ++i) st[i] = 0;
  ST(0);
  const int lane = threadIdx.x & 63;
  const int w = __builtin_amdgcn_readfirstlane(threadIdx.x >> 6);
  const int bid = blockIdx.x, xcd = bid & 7, jj = bid >> 3;
  const int tile = (xcd & 3) * 4 + (jj >> 3), cg = jj & 7;
  const int rot = ROT ? ((cg + 3 * (jj >> 3) + xcd) & 7) : 0;
  unsigned epoch;
  asm volatile("" :: "s"(xa), "s"(wqkv), "s"(wo), "s"(xb), "s"(lre), "s"(lim), "s"(bre), "s"(bim), "s"(cre), "s"(cim), "s"(ldt), "s"(dsk), "s"(out), "s"(wsp));
  const __amdgpu_buffer_rsrc_t ws = __builtin_amdgcn_make_buffer_rsrc(wsp, 0, WS_END, 0x00020000);
  const __amdgpu_buffer_rsrc_t ro = __builtin_amdgcn_make_buffer_rsrc(out, 0, 2048 * 1024 * 4, 0x00020000);
  const int r8 = lane >> 3, c8 = lane & 7;
  unsigned tag;

  if (xcd < 4) {
    constexpr unsigned SLOT = 40960;
    unsigned vx[4], vw[2];
#pragma unroll
    for (int i = 0; i < 4; ++i) vx[i] = (32 * w + 8 * i + r8) * 1024 + ((c8 ^ ((4 * i + (r8 >> 1)) & 7)) << 4) - 1024 * i;
#pragma unroll
    for (int i = 0; i < 2; ++i) vw[i] = (16 * w + 8 * i + r8) * 3072 + ((c8 ^ ((((r8 >> 1) & 1) | (i << 1)) << 1)) << 4) - 1024 * i;
    const char* xbase = (const char*)xa + (size_t)tile * 131072;
    const char* wbase = (const char*)wqkv + cg * 128;
    auto issue = [&](int kk) {
      const int kc = (kk + rot) & 7;
      const unsigned s = L0 + (kk & 3) * SLOT;
      dma4(s + w * 4096, xbase + kc * 128, vx[0], vx[1], vx[2], vx[3]);
      const char* wb = wbase + kc * (64 * 3072);
      dma2(s + 16384 + w * 2048, wb, vw[0], vw[1]);
      dma2(s + 24576 + w * 2048, wb + 1024, vw[0], vw[1]);
      dma2(s + 32768 + w * 2048, wb + 2048, vw[0], vw[1]);
    };
    issue(0); issue(1); issue(2); issue(3);
    asm volatile("s_load_dword %0, %1, 0x0" : "=s"(epoch) : "s"(wsp) : "memory");
    unsigned vo[4];
#pragma unroll
    for (int i = 0; i < 4; ++i) vo[i] = (64 * w + 8 * i + r8) * 1024 + ((c8 ^ (((r8 >> 1) & 3) << 1)) << 4) - 1024 * i;
    const char* obase = (const char*)wo + cg * 128;
    auto issue_wo = [&](int piece) {
      dma4(L0 + piece * SLOT + w * 8192, obase + piece * 262144, vo[0], vo[1], vo[2], vo[3]);
      dma4(L0 + piece * SLOT + w * 8192 + 4096, obase + piece * 262144 + 32768, vo[0], vo[1], vo[2], vo[3]);
    };
    const int q = lane >> 4, l15 = lane & 15, qq = (lane >> 2) & 3, pp = lane & 3;
    const unsigned ax0 = l15 * 128 + ((q ^ (l15 >> 1)) << 4), ax1 = l15 * 128 + (((4 + q) ^ (l15 >> 1)) << 4);
    const unsigned aw = 16384 + (8 * q + qq) * 128 + (((2 * w + (pp >> 1)) ^ ((((qq >> 1) & 1) | ((q & 1) << 1)) << 1)) << 4) + 8 * (pp & 1);
    f4 ka[8], va[8], qa[8];
#pragma unroll
    for (int i = 0; i < 8; ++i) { ka[i] = (f4){0, 0, 0, 0}; va[i] = ka[i]; qa[i] = ka[i]; }
    auto compute = [&](int kk) {
      const char LAS* s = L + (kk & 3) * SLOT;
#pragma unroll
      for (int ks = 0; ks < 2; ++ks) {
        const char LAS* wp_ = s + aw + ks * 4096;
        const bf8 fq = ldstr(wp_, wp_ + 512), fk = ldstr(wp_ + 8192, wp_ + 8192 + 512), fv = ldstr(wp_ + 16384, wp_ + 16384 + 512);
#pragma unroll
        for (int mt = 0; mt < 8; ++mt) {
          const bf8 fx = ldsr(s + (ks ? ax1 : ax0) + mt * 2048);
          ka[mt] = mfma16(fx, fk, ka[mt]);
          va[mt] = mfma16(fx, fv, va[mt]);
          qa[mt] = mfma16(fq, fx, qa[mt]);
        }
      }
    };
    asm volatile("s_waitcnt vmcnt(30) lgkmcnt(0)\n\ts_barrier" : "+s"(epoch) :: "memory");
    ST(1);
    tag = epoch + 1;
    compute(0);
    wait_bar<20>(); issue(4); compute(1);
    wait_bar<20>(); issue(5); compute(2);
    wait_bar<20>(); issue(6); compute(3);
    wait_bar<20>(); issue(7); compute(4);
    wait_bar<20>(); issue_wo(0); compute(5);
    wait_bar<18>(); issue_wo(1); compute(6);
    wait_bar<16>(); ST(2); compute(7);
    ST(3);
    const unsigned t0 = tag & 0xffu, t1 = (tag >> 8) & 0xffu;
    const int head = cg * 4 + w;
    {
      f4 kv = {0, 0, 0, 0}, zz = {0, 0, 0, 0};
      const u4 ones = {0x3f803f80u, 0x3f803f80u, 0x3f803f80u, 0x3f803f80u};
#pragma unroll
      for (int mm = 0; mm < 4; ++mm) {
        const f4 a = ka[2 * mm], b = ka[2 * mm + 1], c = va[2 * mm], d = va[2 * mm + 1];
        const u4 pa = {pk2(phi(a[0]), phi(a[1])), pk2(phi(a[2]), phi(a[3])), pk2(phi(b[0]), phi(b[1])), pk2(phi(b[2]), phi(b[3]))};
        const u4 pb = {pk2(c[0], c[1]), pk2(c[2], c[3]), pk2(d[0], d[1]), pk2(d[2], d[3])};
        kv = mfma16(asbf8(pa), asbf8(pb), kv);
        zz = mfma16(asbf8(pa), asbf8(ones), zz);
      }
      const u4 skv = {tagv(kv[0], t0), tagv(kv[1], t1), tagv(kv[2], t0), tagv(kv[3], t1)};
      __builtin_amdgcn_raw_buffer_store_b128(skv, ws, WS_KV + (head * 16 + tile) * 1024 + lane * 16, 0, SC1);
      const u4 sz = {tagv(zz[0], t0), tagv(zz[1], t1), tagv(zz[2], t0), tagv(zz[3], t1)};
      if (l15 == 0) __builtin_amdgcn_raw_buffer_store_b128(sz, ws, WS_Z + (head * 16 + tile) * 64 + q * 16, 0, SC1);
    }
    ST(4);
    u2 pq[8];
#pragma unroll
    for (int mt = 0; mt < 8; ++mt) pq[mt] = (u2){pk2(phi(qa[mt][0]), phi(qa[mt][1])), pk2(phi(qa[mt][2]), phi(qa[mt][3]))};
    f4 kv, zz;
    for (unsigned sp = 0; sp < SPIN_CAP; ++sp) {
      u4 g[16];
      const u4 gz = __builtin_amdgcn_raw_buffer_load_b128(ws, WS_Z + head * 1024 + l15 * 64 + q * 16, 0, SC1);
#pragma unroll
      for (int i = 0; i < 16; ++i) g[i] = __builtin_amdgcn_raw_buffer_load_b128(ws, WS_KV + head * 16384 + lane * 16, i * 1024, SC1);
      const u4 tt = {t0, t1, t0, t1};
      const u4 cz = gz ^ tt;
      unsigned bad = cz.x | cz.y | cz.z | cz.w;
      kv = (f4){0, 0, 0, 0};
#pragma unroll
      for (int i = 0; i < 16; ++i) {
        const u4 c = g[i] ^ tt;
        bad |= c.x | c.y | c.z | c.w;
        kv += __builtin_bit_cast(f4, c);
      }
      zz = __builtin_bit_cast(f4, cz);
      if (__all((bad & 0xffu) == 0)) break;
      __builtin_amdgcn_s_sleep(1);
    }
    ST(5);
#pragma unroll
    for (int r = 0; r < 4; ++r) {
      float v = zz[r];
      v += __builtin_bit_cast(float, __builtin_amdgcn_update_dpp(0, __builtin_bit_cast(int, v), 0x128, 0xf, 0xf, false));
      v += __builtin_bit_cast(float, __builtin_amdgcn_update_dpp(0, __builtin_bit_cast(int, v), 0x124, 0xf, 0xf, false));
      v += __builtin_bit_cast(float, __builtin_amdgcn_update_dpp(0, __builtin_bit_cast(int, v), 0x122, 0xf, 0xf, false));
      v += __builtin_bit_cast(float, __builtin_amdgcn_update_dpp(0, __builtin_bit_cast(int, v), 0x121, 0xf, 0xf, false));
      zz[r] = v;
    }
    const u2 kvb = {pk2(kv[0], kv[1]), pk2(kv[2], kv[3])}, zb = {pk2(zz[0], zz[1]), pk2(zz[2], zz[3])};
    const f4 zero4 = {0, 0, 0, 0};
#pragma unroll
    for (int u = 0; u < 4; ++u) {
      u4 so;
#pragma unroll
      for (int hh = 0; hh < 2; ++hh) {
        const f4 num = mfma16k16(kvb, pq[2 * u + hh], zero4), den = mfma16k16(zb, pq[2 * u + hh], zero4);
        const float rc = __builtin_amdgcn_rcpf(den[0] + 1e-6f);
        so[2 * hh] = pk2(num[0] * rc, num[1] * rc);
        so[2 * hh + 1] = pk2(num[2] * rc, num[3] * rc);
      }
      __builtin_amdgcn_raw_buffer_store_b128(so, ws, WS_O + ((tile * 4 + u) * 32 + head) * 1024 + lane * 16, 0, SC1);
    }
    ST(6);
    drain();
    if (lane == 0) __builtin_amdgcn_raw_buffer_store_b64((u2){tag, ~tag}, ws, WS_FA + (tile * 32 + head) * 128, 0, SC1);
    asm volatile("s_barrier" ::: "memory");
    ST(7);
    wait_flags(ws, WS_FA + (tile * 32 + (lane & 31)) * 128, true, tag);
    ST(8);
    unsigned ao[4];
#pragma unroll
    for (int nt = 0; nt < 4; ++nt) ao[nt] = (4 * q + qq) * 128 + ((((2 * nt) ^ (((2 * q + (qq >> 1)) & 3) << 1)) + (pp >> 1)) << 4) + 8 * (pp & 1);
    f4 acc[2][4];
#pragma unroll
    for (int i = 0; i < 8; ++i) acc[i >> 2][i & 3] = zero4;
    const unsigned ob = WS_O + (tile * 4 + w) * 32768 + lane * 16;
    u4 of[32];
#pragma unroll
    for (int i = 0; i < 32; ++i) of[i] = __builtin_amdgcn_raw_buffer_load_b128(ws, ob, i * 1024, SC1);
#pragma unroll
    for (int k = 0; k < 16; ++k) {
      const u4 a = of[2 * k], b = of[2 * k + 1];
      const bf8 f0 = asbf8((u4){a.x, a.y, b.x, b.y}), f1 = asbf8((u4){a.z, a.w, b.z, b.w});
      const char LAS* base = L + (k >> 3) * SLOT + (k & 7) * 4096;
#pragma unroll
      for (int nt = 0; nt < 4; ++nt) {
        const bf8 fw = ldstr(base + ao[nt], base + ao[nt] + 2048);
        acc[0][nt] = mfma16(fw, f0, acc[0][nt]);
        acc[1][nt] = mfma16(fw, f1, acc[1][nt]);
      }
    }
    ST(9);
#pragma unroll
    for (int mt = 0; mt < 2; ++mt)
#pragma unroll
      for (int nt = 0; nt < 4; ++nt) {
        const f4 v = acc[mt][nt];
        const unsigned p0 = pk2(v[0], v[1]), p1 = pk2(v[2], v[3]);
        st_out(ro, ((tile * 128 + w * 32 + mt * 16 + l15) * 1024 + cg * 64 + nt * 16 + q * 4) * 4, (f4){bflo(p0), bfhi(p0), bflo(p1), bfhi(p1)});
      }
    ST(10);
    if (bid == 0 && w == 0) {
      wait_flags(ws, WS_FA + lane * 1024, true, tag);
      wait_flags(ws, WS_FA + 512 + lane * 1024, true, tag);
      wait_flags(ws, WS_FB + (lane >> 3) * 4096 + (lane & 7) * 256, true, tag);
      wait_flags(ws, WS_FB + 32768 + (lane >> 3) * 4096 + (lane & 7) * 256, true, tag);
      if (lane == 0) __builtin_amdgcn_raw_buffer_store_b32(tag, ws, WS_EPOCH, 0, SC1);
    }
  } else {
    constexpr unsigned SLOT = 32768;
    const int wp = w & 1, wt = w >> 1, l31 = lane & 31, hh = lane >> 5;
    const int pl = cg * 64 + wp * 32 + ((l31 & 0x13) | ((l31 & 4) << 1) | ((l31 & 8) >> 1));
    unsigned plr, pli, pdt;
    asm volatile("global_load_ushort %0, %3, %4\n\tglobal_load_ushort %1, %3, %5\n\tglobal_load_ushort %2, %3, %6"
                 : "=&v"(plr), "=&v"(pli), "=&v"(pdt) : "v"(pl * 2), "s"(lre), "s"(lim), "s"(ldt) : "memory");
    unsigned vx[4], vw[4];
#pragma unroll
    for (int i = 0; i < 4; ++i) {
      const unsigned sw = (c8 ^ ((4 * i + (r8 >> 1)) & 7)) << 4;
      vx[i] = (64 * (w >> 1) + 32 * (r8 >> 2) + 16 * (r8 & 1) + 8 * (w & 1) + ((r8 >> 1) & 1) + 2 * i) * 1024 + sw - 1024 * i;
      vw[i] = (16 * (i >> 1) + 8 * (r8 >> 2) + 4 * (i & 1) + (r8 & 3)) * 1024 + sw - 1024 * i;
    }
    const char* xbase = (const char*)xb + (size_t)tile * 131072;
    const char* wbase = (const char*)((w & 1) ? bim : bre) + (cg * 64 + (w >> 1) * 32) * 1024;
    auto issue = [&](int kk) {
      const int kc = (kk + rot) & 7;
      const unsigned s = L0 + (kk % 5) * SLOT + w * 4096;
      dma4(s, xbase + kc * 128, vx[0], vx[1], vx[2], vx[3]);
      dma4(s + 16384, wbase + kc * 128, vw[0], vw[1], vw[2], vw[3]);
    };
    issue(0); issue(1); issue(2); issue(3); issue(4);
    asm volatile("s_load_dword %0, %1, 0x0" : "=s"(epoch) : "s"(wsp) : "memory");
    unsigned vc[4];
#pragma unroll
    for (int i = 0; i < 4; ++i) vc[i] = (8 * i + r8) * 1024 + ((c8 ^ ((4 * i + (r8 >> 1)) & 7)) << 4) - 1024 * i;
    const char* cbase = (const char*)((w & 1) ? cim : cre) + cg * 65536;
    const int crot = cg + 2 * (jj >> 3);
    auto issue_c = [&](int piece) {
      const unsigned s = L0 + ((piece + 3) % 5) * SLOT + w * 8192;
      const char* cb = cbase + ((crot + 2 * piece + (w >> 1)) & 7) * 128;
      dma4(s, cb, vc[0], vc[1], vc[2], vc[3]);
      dma4(s + 4096, cb + 32768, vc[0], vc[1], vc[2], vc[3]);
    };
    asm volatile("s_waitcnt vmcnt(40)" : "+v"(plr), "+v"(pli), "+v"(pdt) :: "memory");
    f2 lam, kap, l16, l32, l64, l128;
    {
      const float lr = bflo(plr), li = bflo(pli);
      const float dt = bflo(pk2(expf(bflo(pdt)), 0.f));
      const float mag = expf(dt * lr);
      float sn, cs;
      sincosf(dt * li, &sn, &cs);
      lam = (f2){mag * cs, mag * sn};
      const float inv = 2.f / (lr * lr + li * li), nr = lam.x - 1.f, ni = lam.y;
      kap = (f2){(nr * lr + ni * li) * inv, (ni * lr - nr * li) * inv};
      f2 t = cmul(lam, lam); t = cmul(t, t); t = cmul(t, t);
      l16 = cmul(t, t); l32 = cmul(l16, l16); l64 = cmul(l32, l32); l128 = cmul(l64, l64);
    }
    unsigned ar[4];
#pragma unroll
    for (int s = 0; s < 4; ++s) ar[s] = l31 * 128 + (((2 * s + hh) ^ ((lane >> 1) & 7)) << 4);
    f16v ur[2], ui[2];
#pragma unroll
    for (int i = 0; i < 16; ++i) { ur[0][i] = 0; ur[1][i] = 0; ui[0][i] = 0; ui[1][i] = 0; }
    auto compute = [&](int kk) {
      const char LAS* sx = L + (kk % 5) * SLOT + wt * 8192;
      const char LAS* sw = L + (kk % 5) * SLOT + 16384 + wp * 8192;
#pragma unroll
      for (int s = 0; s < 4; ++s) {
        const bf8 x0 = ldsr(sx + ar[s]), x1 = ldsr(sx + ar[s] + 4096), b0 = ldsr(sw + ar[s]), b1 = ldsr(sw + ar[s] + 4096);
        ur[0] = mfma32(x0, b0, ur[0]); ui[0] = mfma32(x0, b1, ui[0]);
        ur[1] = mfma32(x1, b0, ur[1]); ui[1] = mfma32(x1, b1, ui[1]);
      }
    };
    asm volatile("s_waitcnt vmcnt(32) lgkmcnt(0)\n\ts_barrier" : "+s"(epoch) :: "memory");
    ST(1);
    tag = epoch + 1;
    compute(0);
    wait_bar<24>(); issue(5); compute(1);
    wait_bar<24>(); issue(6); compute(2);
    wait_bar<24>(); issue(7); compute(3);
    wait_bar<24>(); issue_c(0); compute(4);
    wait_bar<24>(); issue_c(1); compute(5);
    wait_bar<24>(); issue_c(2); compute(6);
    wait_bar<24>(); ST(2); issue_c(3); compute(7);
    ST(3);
    const unsigned t0 = tag & 0xffu, t1 = (tag >> 8) & 0xffu;
    const f2 LR = {lam.x, lam.x}, LI = {lam.y, lam.y};
    f2 er = {0, 0}, ei = {0, 0};
#pragma unroll
    for (int a = 0; a < 16; ++a) {
      const f2 xr = {ur[a >> 3][2 * (a & 7)], ur[a >> 3][2 * (a & 7) + 1]}, xi = {ui[a >> 3][2 * (a & 7)], ui[a >> 3][2 * (a & 7) + 1]};
      const f2 nr = fma2(LR, er, fma2(-LI, ei, xr)), ni = fma2(LR, ei, fma2(LI, er, xi));
      er = nr; ei = ni;
    }
    const f2 e0 = {er.x, ei.x};
    const f2 e32 = cfma(l16, e0, (f2){er.y, ei.y});
    const auto sx_ = __builtin_amdgcn_permlane32_swap(__float_as_uint(e32.x), __float_as_uint(e32.x), false, false);
    const auto sy_ = __builtin_amdgcn_permlane32_swap(__float_as_uint(e32.y), __float_as_uint(e32.y), false, false);
    const f2 elo = {__uint_as_float(sx_[0]), __uint_as_float(sy_[0])}, ehi = {__uint_as_float(sx_[1]), __uint_as_float(sy_[1])};
    const f2 e64 = cfma(l32, elo, ehi);
    const int J = tile * 2 + wt;
    const unsigned ecol = WS_EST + (cg * 64 + wp * 32 + l31) * 8;
    if (hh == 0) __builtin_amdgcn_raw_buffer_store_b64((u2){tagv(e64.x, t0), tagv(e64.y, t1)}, ws, ecol + J * 4096, 0, SC1);
    ST(4);
    {
      const f2 KR = {kap.x, kap.x}, KI = {kap.y, kap.y};
#pragma unroll
      for (int a = 0; a < 16; ++a) {
        const f2 xr = {ur[a >> 3][2 * (a & 7)], ur[a >> 3][2 * (a & 7) + 1]}, xi = {ui[a >> 3][2 * (a & 7)], ui[a >> 3][2 * (a & 7) + 1]};
        const f2 nr = fma2(KR, xr, -KI * xi), ni = fma2(-KR, xi, -KI * xr);
        ur[a >> 3][2 * (a & 7)] = nr.x; ur[a >> 3][2 * (a & 7) + 1] = nr.y;
        ui[a >> 3][2 * (a & 7)] = ni.x; ui[a >> 3][2 * (a & 7) + 1] = ni.y;
      }
    }
    f2 cin;
    for (unsigned sp = 0; sp < SPIN_CAP; ++sp) {
      u2 g[16];
#pragma unroll
      for (int m = 0; m < 16; ++m) {
        const int i = J - 1 - hh - 2 * m;
        g[m] = __builtin_amdgcn_raw_buffer_load_b64(ws, i >= 0 ? ecol + i * 4096 : 0xfffffff0u, 0, SC1);
      }
      unsigned bad = 0;
      cin = (f2){0, 0};
#pragma unroll
      for (int m = 15; m >= 0; --m) {
        const int i = J - 1 - hh - 2 * m;
        const unsigned cx = i >= 0 ? g[m].x ^ t0 : 0u, cy = i >= 0 ? g[m].y ^ t1 : 0u;
        bad |= cx | cy;
        cin = cfma(l128, cin, (f2){__uint_as_float(cx), __uint_as_float(cy)});
      }
      if (__all((bad & 0xffu) == 0)) break;
      __builtin_amdgcn_s_sleep(1);
    }
    ST(5);
    {
      const auto cx = __builtin_amdgcn_permlane32_swap(__float_as_uint(cin.x), __float_as_uint(cin.x), false, false);
      const auto cy = __builtin_amdgcn_permlane32_swap(__float_as_uint(cin.y), __float_as_uint(cin.y), false, false);
      cin = cfma(l64, (f2){__uint_as_float(cx[1]), __uint_as_float(cy[1])}, (f2){__uint_as_float(cx[0]), __uint_as_float(cy[0])});
    }
    const f2 up = cfma(l32, cin, elo);
    const f2 c0 = hh ? up : cin;
    const f2 c1 = cfma(l16, c0, e0);
    const f2 d0 = cmul(kap, c0), d1 = cmul(kap, c1);
    f2 hr = {d0.x, d1.x}, hi = {-d0.y, -d1.y};
    u4 idn[2];
#pragma unroll
    for (int s = 0; s < 2; ++s)
#pragma unroll
      for (int jq = 0; jq < 4; ++jq) {
        const int r0 = 16 * s + 8 * (jq >> 1) + 4 * hh + 2 * (jq & 1);
        idn[s][jq] = (l31 == r0 ? 0x3f80u : 0u) | (l31 == r0 + 1 ? 0x3f800000u : 0u);
      }
    f16v zero16;
#pragma unroll
    for (int i = 0; i < 16; ++i) zero16[i] = 0;
#pragma unroll
    for (int m = 0; m < 2; ++m) {
#pragma unroll
      for (int a = 0; a < 8; ++a) {
        const f2 xr = {ur[m][2 * a], ur[m][2 * a + 1]}, xi = {ui[m][2 * a], ui[m][2 * a + 1]};
        const f2 nr = fma2(LR, hr, fma2(LI, hi, xr)), ni = fma2(LR, hi, fma2(-LI, hr, xi));
        hr = nr; hi = ni;
        ur[m][2 * a] = nr.x; ur[m][2 * a + 1] = nr.y; ui[m][2 * a] = ni.x; ui[m][2 * a + 1] = ni.y;
      }
#pragma unroll
      for (int c = 0; c < 2; ++c) {
        const f16v X = c ? ui[m] : ur[m];
        const u4 a0 = {pk2(X[0], X[1]), pk2(X[2], X[3]), pk2(X[4], X[5]), pk2(X[6], X[7])};
        const u4 a1 = {pk2(X[8], X[9]), pk2(X[10], X[11]), pk2(X[12], X[13]), pk2(X[14], X[15])};
        f16v T = mfma32(asbf8(a0), asbf8(idn[0]), zero16);
        T = mfma32(asbf8(a1), asbf8(idn[1]), T);
        const u4 o0 = {pk2(T[0], T[1]), pk2(T[2], T[3]), pk2(T[4], T[5]), pk2(T[6], T[7])};
        const u4 o1 = {pk2(T[8], T[9]), pk2(T[10], T[11]), pk2(T[12], T[13]), pk2(T[14], T[15])};
        const unsigned ho = WS_H + (tile * 4 + wt * 2 + m) * 65536 + ((cg * 2 + wp) * 2 + c) * 2048 + lane * 16;
        __builtin_amdgcn_raw_buffer_store_b128(o0, ws, ho, 0, SC1);
        __builtin_amdgcn_raw_buffer_store_b128(o1, ws, ho + 1024, 0, SC1);
      }
    }
    ST(6);
    drain();
    if (lane == 0) __builtin_amdgcn_raw_buffer_store_b64((u2){tag, ~tag}, ws, WS_FB + ((tile * 2 + wt) * 16 + cg * 2 + wp) * 128, 0, SC1);
    asm volatile("s_barrier" ::: "memory");
    ST(7);
    const int tok = tile * 128 + 64 * (w >> 1) + 32 * ((l31 >> 2) & 1) + 16 * (l31 & 1) + 8 * (w & 1) + ((l31 >> 1) & 1) + 2 * (l31 >> 3);
    f16v acc[2];
#pragma unroll
    for (int nt = 0; nt < 2; ++nt)
#pragma unroll
      for (int g = 0; g < 4; ++g) {
        const int d = cg * 64 + nt * 32 + g * 8 + hh * 4;
        const u2 xv = *(const u2*)(xb + tok * 512 + d), dv = *(const u2*)(dsk + d);
        const unsigned p0 = pk2(bflo(xv.x) * bflo(dv.x), bfhi(xv.x) * bfhi(dv.x)), p1 = pk2(bflo(xv.y) * bflo(dv.y), bfhi(xv.y) * bfhi(dv.y));
        acc[nt][4 * g] = bflo(p0); acc[nt][4 * g + 1] = bfhi(p0); acc[nt][4 * g + 2] = bflo(p1); acc[nt][4 * g + 3] = bfhi(p1);
      }
    wait_flags(ws, WS_FB + ((tile * 2 + (w >> 1)) * 16 + (lane & 15)) * 128, true, tag);
    ST(8);
    const unsigned hb = WS_H + (tile * 4 + w) * 65536 + lane * 16;
    u4 hf[3][8];
    auto loadh = [&](int t) {
      const unsigned so = ((crot + t) & 7) * 8192;
#pragma unroll
      for (int i = 0; i < 8; ++i) hf[t % 3][i] = __builtin_amdgcn_raw_buffer_load_b128(ws, hb + i * 1024, so, SC1);
    };
    loadh(0); loadh(1);
#pragma unroll
    for (int t = 0; t < 8; ++t) {
      if (t + 2 < 8) loadh(t + 2);
      __builtin_amdgcn_sched_barrier(0);
      const char LAS* cb = L + (((t >> 1) + 3) % 5) * SLOT + (t & 1) * 16384;
#pragma unroll
      for (int i = 0; i < 8; ++i) {
        const char LAS* p = cb + ((i >> 1) & 1) * 8192 + ar[(i >> 2) * 2 + (i & 1)];
        acc[0] = mfma32(ldsr(p), asbf8(hf[t % 3][i]), acc[0]);
        acc[1] = mfma32(ldsr(p + 4096), asbf8(hf[t % 3][i]), acc[1]);
      }
      __builtin_amdgcn_sched_barrier(0);
    }
    ST(9);
#pragma unroll
    for (int nt = 0; nt < 2; ++nt)
#pragma unroll
      for (int g = 0; g < 4; ++g)
        st_out(ro, (tok * 1024 + 512 + cg * 64 + nt * 32 + g * 8 + hh * 4) * 4, (f4){acc[nt][4 * g], acc[nt][4 * g + 1], acc[nt][4 * g + 2], acc[nt][4 * g + 3]});
    ST(10);
  }
  drain();
  ST(11);
  if (bid == AMP_BID && w == 0) {
    if (PR_AFTER) { if (lane == 0) __hip_atomic_store((u64*)(wsp + WS_STAMP), st[11], __ATOMIC_RELAXED, __HIP_MEMORY_SCOPE_AGENT); }
    else {
      const u64 e = __hip_atomic_load((u64*)(wsp + WS_STAMP), __ATOMIC_RELAXED, __HIP_MEMORY_SCOPE_AGENT);
      const u64 d = (st[0] - e) * PR_K;
      if (d < 100000) while (__builtin_amdgcn_s_memrealtime() < st[11] + d) __builtin_amdgcn_s_sleep(2);
    }
  }
}

__global__ void probe_kernel(char* wsp) {
  u64 t0;
  asm volatile("s_memrealtime %0\n\ts_waitcnt lgkmcnt(0)" : "=s"(t0) :: "memory");
  if (PR_AFTER) {
    const u64 e = __hip_atomic_load((u64*)(wsp + WS_STAMP), __ATOMIC_RELAXED, __HIP_MEMORY_SCOPE_AGENT);
    const u64 d = (t0 - e) * PR_K;
    if (d < 100000) while (__builtin_amdgcn_s_memrealtime() < t0 + d) __builtin_amdgcn_s_sleep(2);
  } else {
    if (threadIdx.x == 0) __hip_atomic_store((u64*)(wsp + WS_STAMP), t0, __ATOMIC_RELAXED, __HIP_MEMORY_SCOPE_AGENT);
  }
}

extern "C" void kernel_launch(void* const* d_in, const int* in_sizes, int n_in, void* d_out, int out_size, void* d_ws, size_t ws_size,
                              hipStream_t stream) {
  (void)hipFuncSetAttribute((const void*)fused_kernel, hipFuncAttributeMaxDynamicSharedMemorySize, LDS_BYTES);
  if (!PR_AFTER) probe_kernel<<<PR_G, 64, 0, stream>>>((char*)d_ws);
  fused_kernel<<<256, 256, LDS_BYTES, stream>>>((const u16*)d_in[0], (const u16*)d_in[1], (const u16*)d_in[2], (const u16*)d_in[3],
                                               (const u16*)d_in[4], (const u16*)d_in[5], (const u16*)d_in[6], (const u16*)d_in[7],
                                               (const u16*)d_in[8], (const u16*)d_in[9], (const u16*)d_in[10], (const u16*)d_in[11],
                                               (float*)d_out, (char*)d_ws);
  if (PR_AFTER) probe_kernel<<<PR_G, 64, 0, stream>>>((char*)d_ws);
}
```

```cpp
#define AMP_A 0
#define AMP_B 0
#define AMP_BID 0
#define AMP_K 0
#include <hip/hip_runtime.h>
#include <hip/hip_bf16.h>

#define DI __device__ __forceinline__
#define LAS __attribute__((address_space(3)))
#define GAS __attribute__((address_space(1)))
typedef unsigned short u16;
typedef unsigned long long u64;
typedef __bf16 bf8 __attribute__((ext_vector_type(8)));
typedef __bf16 bf2 __attribute__((ext_vector_type(2)));
typedef short s4 __attribute__((ext_vector_type(4)));
typedef short s8 __attribute__((ext_vector_type(8)));
typedef float f16v __attribute__((ext_vector_type(16)));
typedef float f4 __attribute__((ext_vector_type(4)));
typedef float f2 __attribute__((ext_vector_type(2)));
typedef unsigned u4 __attribute__((ext_vector_type(4)));
typedef unsigned u2 __attribute__((ext_vector_type(2)));

constexpr unsigned WS_EPOCH = 0;
constexpr unsigned WS_FA = 4096;
constexpr unsigned WS_FB = WS_FA + 65536;
constexpr unsigned WS_EST = WS_FB + 65536;
constexpr unsigned WS_Z = WS_EST + 131072;
constexpr unsigned WS_KV = WS_Z + 32768;
constexpr unsigned WS_O = 1u << 20;
constexpr unsigned WS_H = 3u << 20;
constexpr unsigned WS_END = 7u << 20;
constexpr unsigned WS_STAMP = 8u << 20;
constexpr int LDS_BYTES = 160 * 1024;
constexpr unsigned SPIN_CAP = 200000;
constexpr int SC1 = 16;

#define ROT 1

#define ST(i) do { __builtin_amdgcn_sched_barrier(0); asm volatile("s_memrealtime %0\n\ts_waitcnt lgkmcnt(0)" : "=s"(st[i]) :: "memory"); __builtin_amdgcn_sched_barrier(0); } while (0)

DI unsigned pk2(float a, float b) { f2 v = {a, b}; return __builtin_bit_cast(unsigned, __builtin_convertvector(v, bf2)); }
DI float bflo(unsigned v) { return __uint_as_float(v << 16); }
DI float bfhi(unsigned v) { return __uint_as_float(v & 0xffff0000u); }
DI bf8 asbf8(u4 v) { return __builtin_bit_cast(bf8, v); }
DI f16v mfma32(bf8 a, bf8 b, f16v c) { return __builtin_amdgcn_mfma_f32_32x32x16_bf16(a, b, c, 0, 0, 0); }
DI f4 mfma16(bf8 a, bf8 b, f4 c) { return __builtin_amdgcn_mfma_f32_16x16x32_bf16(a, b, c, 0, 0, 0); }
DI f4 mfma16k16(u2 a, u2 b, f4 c) { return __builtin_amdgcn_mfma_f32_16x16x16bf16_1k(__builtin_bit_cast(s4, a), __builtin_bit_cast(s4, b), c, 0, 0, 0); }
DI bf8 ldsr(const char LAS* p) { return *(const LAS bf8*)p; }
DI bf8 ldstr(const char LAS* p0, const char LAS* p1) {
  s4 a = __builtin_amdgcn_ds_read_tr16_b64_v4i16((LAS s4*)p0), b = __builtin_amdgcn_ds_read_tr16_b64_v4i16((LAS s4*)p1);
  return __builtin_bit_cast(bf8, (s8)__builtin_shufflevector(a, b, 0, 1, 2, 3, 4, 5, 6, 7));
}
DI void dma4(unsigned lds, const GAS char* sb, unsigned v0, unsigned v1, unsigned v2, unsigned v3) {
  asm volatile("s_mov_b32 m0, %0\n\ts_nop 0\n\tglobal_load_lds_dwordx4 %1, %5\n\tglobal_load_lds_dwordx4 %2, %5 offset:1024\n\t"
               "global_load_lds_dwordx4 %3, %5 offset:2048\n\tglobal_load_lds_dwordx4 %4, %5 offset:3072"
               :: "s"(lds), "v"(v0), "v"(v1), "v"(v2), "v"(v3), "s"(sb) : "memory");
}
DI void dma2(unsigned lds, const GAS char* sb, unsigned v0, unsigned v1) {
  asm volatile("s_mov_b32 m0, %0\n\ts_nop 0\n\tglobal_load_lds_dwordx4 %1, %3\n\tglobal_load_lds_dwordx4 %2, %3 offset:1024"
               :: "s"(lds), "v"(v0), "v"(v1), "s"(sb) : "memory");
}
template <int N> DI void wait_bar() { asm volatile("s_waitcnt vmcnt(%0) lgkmcnt(0)\n\ts_barrier" :: "i"(N) : "memory"); }
DI void drain() { asm volatile("s_waitcnt vmcnt(0)" ::: "memory"); }
DI unsigned tagv(float x, unsigned t) { return (__float_as_uint(x) & 0xffffff00u) | t; }
DI f2 cmul(f2 a, f2 b) { return (f2){a.x * b.x - a.y * b.y, a.x * b.y + a.y * b.x}; }
DI f2 cfma(f2 a, f2 b, f2 c) { return (f2){fmaf(a.x, b.x, fmaf(-a.y, b.y, c.x)), fmaf(a.x, b.y, fmaf(a.y, b.x, c.y))}; }
DI f2 fma2(f2 a, f2 b, f2 c) { return __builtin_elementwise_fma(a, b, c); }
DI float phi(float t) { return fmaxf(t, 0.f) + __builtin_amdgcn_fmed3f(__builtin_amdgcn_exp2f(t * 1.44269504088896341f), 0.f, 1.f) + 1e-6f; }

#define OUT_AUX 0
DI void st_out(__amdgpu_buffer_rsrc_t o, unsigned off, f4 v) { __builtin_amdgcn_raw_buffer_store_b128(__builtin_bit_cast(u4, v), o, off, 0, OUT_AUX); }
DI void wait_flags(__amdgpu_buffer_rsrc_t ws, unsigned off, bool active, unsigned tag) {
#pragma unroll 1
  for (unsigned sp = 0; sp < SPIN_CAP; ++sp) {
    u2 f = __builtin_amdgcn_raw_buffer_load_b64(ws, off, 0, SC1);
    bool ok = !active || (f.x == tag && f.y == ~tag);
    if (__all(ok)) break;
    __builtin_amdgcn_s_sleep(1);
  }
  asm volatile("" ::: "memory");
}

__global__ void __launch_bounds__(256, 2)
fused_kernel(const u16* a0, const u16* a1, const u16* a2, const u16* a3, const u16* a4, const u16* a5, const u16* a6, const u16* a7,
             const u16* a8, const u16* a9, const u16* a10, const u16* a11, float* a12, char* a13) {
  extern __shared__ __attribute__((aligned(16))) char smem_[];
  const char LAS* L = (const char LAS*)smem_;
  const unsigned L0 = (unsigned)(size_t)L;
  u64 st[13];
  for (int i = 0; i < 13; ++i) st[i] = 0;
  ST(0);
  typedef u64 u64x8 __attribute__((ext_vector_type(8)));
  typedef u64 u64x4 __attribute__((ext_vector_type(4)));
  typedef u64 u64x2 __attribute__((ext_vector_type(2)));
  u64x8 ka; u64x4 kb; u64x2 kc_;
  asm volatile("s_load_dwordx16 %0, %3, 0x0\n\ts_load_dwordx8 %1, %3, 0x40\n\ts_load_dwordx4 %2, %3, 0x60\n\ts_waitcnt lgkmcnt(0)"
               : "=&s"(ka), "=&s"(kb), "=&s"(kc_) : "s"(__builtin_amdgcn_kernarg_segment_ptr()) : "memory");
  const GAS u16 *xa = (const GAS u16*)ka[0], *wqkv = (const GAS u16*)ka[1], *wo = (const GAS u16*)ka[2], *xb = (const GAS u16*)ka[3];
  const GAS u16 *lre = (const GAS u16*)ka[4], *lim = (const GAS u16*)ka[5], *bre = (const GAS u16*)ka[6], *bim = (const GAS u16*)ka[7];
  const GAS u16 *cre = (const GAS u16*)kb[0], *cim = (const GAS u16*)kb[1], *ldt = (const GAS u16*)kb[2], *dsk = (const GAS u16*)kb[3];
  float* out = (float*)kc_[0]; char* wsp = (char*)kc_[1];
  ST(12);
  const int lane = threadIdx.x & 63;
  const int w = __builtin_amdgcn_readfirstlane(threadIdx.x >> 6);
  const int bid = blockIdx.x, xcd = bid & 7, jj = bid >> 3;
  const int tile = (xcd & 3) * 4 + (jj >> 3), cg = jj & 7;
  const int rot = ROT ? ((cg + 3 * (jj >> 3) + xcd) & 7) : 0;
  unsigned epoch;
  const __amdgpu_buffer_rsrc_t ws = __builtin_amdgcn_make_buffer_rsrc(wsp, 0, WS_END, 0x00020000);
  const __amdgpu_buffer_rsrc_t ro = __builtin_amdgcn_make_buffer_rsrc(out, 0, 2048 * 1024 * 4, 0x00020000);
  const int r8 = lane >> 3, c8 = lane & 7;
  unsigned tag;

  if (xcd < 4) {
    constexpr unsigned SLOT = 40960;
    unsigned vx[4], vw[2];
#pragma unroll
    for (int i = 0; i < 4; ++i) vx[i] = (32 * w + 8 * i + r8) * 1024 + ((c8 ^ ((4 * i + (r8 >> 1)) & 7)) << 4) - 1024 * i;
#pragma unroll
    for (int i = 0; i < 2; ++i) vw[i] = (16 * w + 8 * i + r8) * 3072 + ((c8 ^ ((((r8 >> 1) & 1) | (i << 1)) << 1)) << 4) - 1024 * i;
    const GAS char* xbase = (const GAS char*)xa + (size_t)tile * 131072;
    const GAS char* wbase = (const GAS char*)wqkv + cg * 128;
    auto issue = [&](int kk) {
      const int kc = (kk + rot) & 7;
      const unsigned s = L0 + (kk & 3) * SLOT;
      dma4(s + w * 4096, xbase + kc * 128, vx[0], vx[1], vx[2], vx[3]);
      const GAS char* wb = wbase + kc * (64 * 3072);
      dma2(s + 16384 + w * 2048, wb, vw[0], vw[1]);
      dma2(s + 24576 + w * 2048, wb + 1024, vw[0], vw[1]);
      dma2(s + 32768 + w * 2048, wb + 2048, vw[0], vw[1]);
    };
    issue(0); issue(1); issue(2); issue(3);
    asm volatile("s_load_dword %0, %1, 0x0" : "=s"(epoch) : "s"(wsp) : "memory");
    unsigned vo[4];
#pragma unroll
    for (int i = 0; i < 4; ++i) vo[i] = (64 * w + 8 * i + r8) * 1024 + ((c8 ^ (((r8 >> 1) & 3) << 1)) << 4) - 1024 * i;
    const GAS char* obase = (const GAS char*)wo + cg * 128;
    auto issue_wo = [&](int piece) {
      dma4(L0 + piece * SLOT + w * 8192, obase + piece * 262144, vo[0], vo[1], vo[2], vo[3]);
      dma4(L0 + piece * SLOT + w * 8192 + 4096, obase + piece * 262144 + 32768, vo[0], vo[1], vo[2], vo[3]);
    };
    const int q = lane >> 4, l15 = lane & 15, qq = (lane >> 2) & 3, pp = lane & 3;
    const unsigned ax0 = l15 * 128 + ((q ^ (l15 >> 1)) << 4), ax1 = l15 * 128 + (((4 + q) ^ (l15 >> 1)) << 4);
    const unsigned aw = 16384 + (8 * q + qq) * 128 + (((2 * w + (pp >> 1)) ^ ((((qq >> 1) & 1) | ((q & 1) << 1)) << 1)) << 4) + 8 * (pp & 1);
    f4 ka[8], va[8], qa[8];
#pragma unroll
    for (int i = 0; i < 8; ++i) { ka[i] = (f4){0, 0, 0, 0}; va[i] = ka[i]; qa[i] = ka[i]; }
    auto compute = [&](int kk) {
      const char LAS* s = L + (kk & 3) * SLOT;
#pragma unroll
      for (int ks = 0; ks < 2; ++ks) {
        const char LAS* wp_ = s + aw + ks * 4096;
        const bf8 fq = ldstr(wp_, wp_ + 512), fk = ldstr(wp_ + 8192, wp_ + 8192 + 512), fv = ldstr(wp_ + 16384, wp_ + 16384 + 512);
#pragma unroll
        for (int mt = 0; mt < 8; ++mt) {
          const bf8 fx = ldsr(s + (ks ? ax1 : ax0) + mt * 2048);
          ka[mt] = mfma16(fx, fk, ka[mt]);
          va[mt] = mfma16(fx, fv, va[mt]);
          qa[mt] = mfma16(fq, fx, qa[mt]);
        }
      }
    };
    asm volatile("s_waitcnt vmcnt(30) lgkmcnt(0)\n\ts_barrier" : "+s"(epoch) :: "memory");
    ST(1);
    tag = epoch + 1;
    compute(0);
    wait_bar<20>(); issue(4); compute(1);
    wait_bar<20>(); issue(5); compute(2);
    wait_bar<20>(); issue(6); compute(3);
    wait_bar<20>(); issue(7); compute(4);
    wait_bar<20>(); issue_wo(0); compute(5);
    wait_bar<18>(); issue_wo(1); compute(6);
    wait_bar<16>(); ST(2); compute(7);
    ST(3);
    const unsigned t0 = tag & 0xffu, t1 = (tag >> 8) & 0xffu;
    const int head = cg * 4 + w;
    {
      f4 kv = {0, 0, 0, 0}, zz = {0, 0, 0, 0};
      const u4 ones = {0x3f803f80u, 0x3f803f80u, 0x3f803f80u, 0x3f803f80u};
#pragma unroll
      for (int mm = 0; mm < 4; ++mm) {
        const f4 a = ka[2 * mm], b = ka[2 * mm + 1], c = va[2 * mm], d = va[2 * mm + 1];
        const u4 pa = {pk2(phi(a[0]), phi(a[1])), pk2(phi(a[2]), phi(a[3])), pk2(phi(b[0]), phi(b[1])), pk2(phi(b[2]), phi(b[3]))};
        const u4 pb = {pk2(c[0], c[1]), pk2(c[2], c[3]), pk2(d[0], d[1]), pk2(d[2], d[3])};
        kv = mfma16(asbf8(pa), asbf8(pb), kv);
        zz = mfma16(asbf8(pa), asbf8(ones), zz);
      }
      const u4 skv = {tagv(kv[0], t0), tagv(kv[1], t1), tagv(kv[2], t0), tagv(kv[3], t1)};
      __builtin_amdgcn_raw_buffer_store_b128(skv, ws, WS_KV + (head * 16 + tile) * 1024 + lane * 16, 0, SC1);
      const u4 sz = {tagv(zz[0], t0), tagv(zz[1], t1), tagv(zz[2], t0), tagv(zz[3], t1)};
      if (l15 == 0) __builtin_amdgcn_raw_buffer_store_b128(sz, ws, WS_Z + (head * 16 + tile) * 64 + q * 16, 0, SC1);
    }
    ST(4);
    u2 pq[8];
#pragma unroll
    for (int mt = 0; mt < 8; ++mt) pq[mt] = (u2){pk2(phi(qa[mt][0]), phi(qa[mt][1])), pk2(phi(qa[mt][2]), phi(qa[mt][3]))};
    f4 kv, zz;
    for (unsigned sp = 0; sp < SPIN_CAP; ++sp) {
      u4 g[16];
      const u4 gz = __builtin_amdgcn_raw_buffer_load_b128(ws, WS_Z + head * 1024 + l15 * 64 + q * 16, 0, SC1);
#pragma unroll
      for (int i = 0; i < 16; ++i) g[i] = __builtin_amdgcn_raw_buffer_load_b128(ws, WS_KV + head * 16384 + lane * 16, i * 1024, SC1);
      const u4 tt = {t0, t1, t0, t1};
      const u4 cz = gz ^ tt;
      unsigned bad = cz.x | cz.y | cz.z | cz.w;
      kv = (f4){0, 0, 0, 0};
#pragma unroll
      for (int i = 0; i < 16; ++i) {
        const u4 c = g[i] ^ tt;
        bad |= c.x | c.y | c.z | c.w;
        kv += __builtin_bit_cast(f4, c);
      }
      zz = __builtin_bit_cast(f4, cz);
      if (__all((bad & 0xffu) == 0)) break;
      __builtin_amdgcn_s_sleep(1);
    }
    ST(5);
#pragma unroll
    for (int r = 0; r < 4; ++r) {
      float v = zz[r];
      v += __builtin_bit_cast(float, __builtin_amdgcn_update_dpp(0, __builtin_bit_cast(int, v), 0x128, 0xf, 0xf, false));
      v += __builtin_bit_cast(float, __builtin_amdgcn_update_dpp(0, __builtin_bit_cast(int, v), 0x124, 0xf, 0xf, false));
      v += __builtin_bit_cast(float, __builtin_amdgcn_update_dpp(0, __builtin_bit_cast(int, v), 0x122, 0xf, 0xf, false));
      v += __builtin_bit_cast(float, __builtin_amdgcn_update_dpp(0, __builtin_bit_cast(int, v), 0x121, 0xf, 0xf, false));
      zz[r] = v;
    }
    const u2 kvb = {pk2(kv[0], kv[1]), pk2(kv[2], kv[3])}, zb = {pk2(zz[0], zz[1]), pk2(zz[2], zz[3])};
    const f4 zero4 = {0, 0, 0, 0};
#pragma unroll
    for (int u = 0; u < 4; ++u) {
      u4 so;
#pragma unroll
      for (int hh = 0; hh < 2; ++hh) {
        const f4 num = mfma16k16(kvb, pq[2 * u + hh], zero4), den = mfma16k16(zb, pq[2 * u + hh], zero4);
        const float rc = __builtin_amdgcn_rcpf(den[0] + 1e-6f);
        so[2 * hh] = pk2(num[0] * rc, num[1] * rc);
        so[2 * hh + 1] = pk2(num[2] * rc, num[3] * rc);
      }
      __builtin_amdgcn_raw_buffer_store_b128(so, ws, WS_O + ((tile * 4 + u) * 32 + head) * 1024 + lane * 16, 0, SC1);
    }
    ST(6);
    drain();
    if (lane == 0) __builtin_amdgcn_raw_buffer_store_b64((u2){tag, ~tag}, ws, WS_FA + (tile * 32 + head) * 128, 0, SC1);
    asm volatile("s_barrier" ::: "memory");
    ST(7);
    wait_flags(ws, WS_FA + (tile * 32 + (lane & 31)) * 128, true, tag);
    ST(8);
    unsigned ao[4];
#pragma unroll
    for (int nt = 0; nt < 4; ++nt) ao[nt] = (4 * q + qq) * 128 + ((((2 * nt) ^ (((2 * q + (qq >> 1)) & 3) << 1)) + (pp >> 1)) << 4) + 8 * (pp & 1);
    f4 acc[2][4];
#pragma unroll
    for (int i = 0; i < 8; ++i) acc[i >> 2][i & 3] = zero4;
    const unsigned ob = WS_O + (tile * 4 + w) * 32768 + lane * 16;
    u4 of[32];
#pragma unroll
    for (int i = 0; i < 32; ++i) of[i] = __builtin_amdgcn_raw_buffer_load_b128(ws, ob, i * 1024, SC1);
#pragma unroll
    for (int k = 0; k < 16; ++k) {
      const u4 a = of[2 * k], b = of[2 * k + 1];
      const bf8 f0 = asbf8((u4){a.x, a.y, b.x, b.y}), f1 = asbf8((u4){a.z, a.w, b.z, b.w});
      const char LAS* base = L + (k >> 3) * SLOT + (k & 7) * 4096;
#pragma unroll
      for (int nt = 0; nt < 4; ++nt) {
        const bf8 fw = ldstr(base + ao[nt], base + ao[nt] + 2048);
        acc[0][nt] = mfma16(fw, f0, acc[0][nt]);
        acc[1][nt] = mfma16(fw, f1, acc[1][nt]);
      }
    }
    ST(9);
#pragma unroll
    for (int mt = 0; mt < 2; ++mt)
#pragma unroll
      for (int nt = 0; nt < 4; ++nt) {
        const f4 v = acc[mt][nt];
        const unsigned p0 = pk2(v[0], v[1]), p1 = pk2(v[2], v[3]);
        st_out(ro, ((tile * 128 + w * 32 + mt * 16 + l15) * 1024 + cg * 64 + nt * 16 + q * 4) * 4, (f4){bflo(p0), bfhi(p0), bflo(p1), bfhi(p1)});
      }
    ST(10);
    if (bid == 0 && w == 0) {
      wait_flags(ws, WS_FA + lane * 1024, true, tag);
      wait_flags(ws, WS_FA + 512 + lane * 1024, true, tag);
      wait_flags(ws, WS_FB + (lane >> 3) * 4096 + (lane & 7) * 256, true, tag);
      wait_flags(ws, WS_FB + 32768 + (lane >> 3) * 4096 + (lane & 7) * 256, true, tag);
      if (lane == 0) __builtin_amdgcn_raw_buffer_store_b32(tag, ws, WS_EPOCH, 0, SC1);
    }
  } else {
    constexpr unsigned SLOT = 32768;
    const int wp = w & 1, wt = w >> 1, l31 = lane & 31, hh = lane >> 5;
    const int pl = cg * 64 + wp * 32 + ((l31 & 0x13) | ((l31 & 4) << 1) | ((l31 & 8) >> 1));
    unsigned plr, pli, pdt;
    asm volatile("global_load_ushort %0, %3, %4\n\tglobal_load_ushort %1, %3, %5\n\tglobal_load_ushort %2, %3, %6"
                 : "=&v"(plr), "=&v"(pli), "=&v"(pdt) : "v"(pl * 2), "s"(lre), "s"(lim), "s"(ldt) : "memory");
    unsigned vx[4], vw[4];
#pragma unroll
    for (int i = 0; i < 4; ++i) {
      const unsigned sw = (c8 ^ ((4 * i + (r8 >> 1)) & 7)) << 4;
      vx[i] = (64 * (w >> 1) + 32 * (r8 >> 2) + 16 * (r8 & 1) + 8 * (w & 1) + ((r8 >> 1) & 1) + 2 * i) * 1024 + sw - 1024 * i;
      vw[i] = (16 * (i >> 1) + 8 * (r8 >> 2) + 4 * (i & 1) + (r8 & 3)) * 1024 + sw - 1024 * i;
    }
    const GAS char* xbase = (const GAS char*)xb + (size_t)tile * 131072;
    const GAS char* wbase = (const GAS char*)((w & 1) ? bim : bre) + (cg * 64 + (w >> 1) * 32) * 1024;
    auto issue = [&](int kk) {
      const int kc = (kk + rot) & 7;
      const unsigned s = L0 + (kk % 5) * SLOT + w * 4096;
      dma4(s, xbase + kc * 128, vx[0], vx[1], vx[2], vx[3]);
      dma4(s + 16384, wbase + kc * 128, vw[0], vw[1], vw[2], vw[3]);
    };
    issue(0); issue(1); issue(2); issue(3); issue(4);
    asm volatile("s_load_dword %0, %1, 0x0" : "=s"(epoch) : "s"(wsp) : "memory");
    unsigned vc[4];
#pragma unroll
    for (int i = 0; i < 4; ++i) vc[i] = (8 * i + r8) * 1024 + ((c8 ^ ((4 * i + (r8 >> 1)) & 7)) << 4) - 1024 * i;
    const GAS char* cbase = (const GAS char*)((w & 1) ? cim : cre) + cg * 65536;
    const int crot = cg + 2 * (jj >> 3);
    auto issue_c = [&](int piece) {
      const unsigned s = L0 + ((piece + 3) % 5) * SLOT + w * 8192;
      const GAS char* cb = cbase + ((crot + 2 * piece + (w >> 1)) & 7) * 128;
      dma4(s, cb, vc[0], vc[1], vc[2], vc[3]);
      dma4(s + 4096, cb + 32768, vc[0], vc[1], vc[2], vc[3]);
    };
    asm volatile("s_waitcnt vmcnt(40)" : "+v"(plr), "+v"(pli), "+v"(pdt) :: "memory");
    f2 lam, kap, l16, l32, l64, l128;
    {
      const float lr = bflo(plr), li = bflo(pli);
      const float dt = bflo(pk2(expf(bflo(pdt)), 0.f));
      const float mag = expf(dt * lr);
      float sn, cs;
      sincosf(dt * li, &sn, &cs);
      lam = (f2){mag * cs, mag * sn};
      const float inv = 2.f / (lr * lr + li * li), nr = lam.x - 1.f, ni = lam.y;
      kap = (f2){(nr * lr + ni * li) * inv, (ni * lr - nr * li) * inv};
      f2 t = cmul(lam, lam); t = cmul(t, t); t = cmul(t, t);
      l16 = cmul(t, t); l32 = cmul(l16, l16); l64 = cmul(l32, l32); l128 = cmul(l64, l64);
    }
    unsigned ar[4];
#pragma unroll
    for (int s = 0; s < 4; ++s) ar[s] = l31 * 128 + (((2 * s + hh) ^ ((lane >> 1) & 7)) << 4);
    f16v ur[2], ui[2];
#pragma unroll
    for (int i = 0; i < 16; ++i) { ur[0][i] = 0; ur[1][i] = 0; ui[0][i] = 0; ui[1][i] = 0; }
    auto compute = [&](int kk) {
      const char LAS* sx = L + (kk % 5) * SLOT + wt * 8192;
      const char LAS* sw = L + (kk % 5) * SLOT + 16384 + wp * 8192;
#pragma unroll
      for (int s = 0; s < 4; ++s) {
        const bf8 x0 = ldsr(sx + ar[s]), x1 = ldsr(sx + ar[s] + 4096), b0 = ldsr(sw + ar[s]), b1 = ldsr(sw + ar[s] + 4096);
        ur[0] = mfma32(x0, b0, ur[0]); ui[0] = mfma32(x0, b1, ui[0]);
        ur[1] = mfma32(x1, b0, ur[1]); ui[1] = mfma32(x1, b1, ui[1]);
      }
    };
    asm volatile("s_waitcnt vmcnt(32) lgkmcnt(0)\n\ts_barrier" : "+s"(epoch) :: "memory");
    ST(1);
    tag = epoch + 1;
    compute(0);
    wait_bar<24>(); issue(5); compute(1);
    wait_bar<24>(); issue(6); compute(2);
    wait_bar<24>(); issue(7); compute(3);
    wait_bar<24>(); issue_c(0); compute(4);
    wait_bar<24>(); issue_c(1); compute(5);
    wait_bar<24>(); issue_c(2); compute(6);
    wait_bar<24>(); ST(2); issue_c(3); compute(7);
    ST(3);
    const unsigned t0 = tag & 0xffu, t1 = (tag >> 8) & 0xffu;
    const f2 LR = {lam.x, lam.x}, LI = {lam.y, lam.y};
    f2 er = {0, 0}, ei = {0, 0};
#pragma unroll
    for (int a = 0; a < 16; ++a) {
      const f2 xr = {ur[a >> 3][2 * (a & 7)], ur[a >> 3][2 * (a & 7) + 1]}, xi = {ui[a >> 3][2 * (a & 7)], ui[a >> 3][2 * (a & 7) + 1]};
      const f2 nr = fma2(LR, er, fma2(-LI, ei, xr)), ni = fma2(LR, ei, fma2(LI, er, xi));
      er = nr; ei = ni;
    }
    const f2 e0 = {er.x, ei.x};
    const f2 e32 = cfma(l16, e0, (f2){er.y, ei.y});
    const auto sx_ = __builtin_amdgcn_permlane32_swap(__float_as_uint(e32.x), __float_as_uint(e32.x), false, false);
    const auto sy_ = __builtin_amdgcn_permlane32_swap(__float_as_uint(e32.y), __float_as_uint(e32.y), false, false);
    const f2 elo = {__uint_as_float(sx_[0]), __uint_as_float(sy_[0])}, ehi = {__uint_as_float(sx_[1]), __uint_as_float(sy_[1])};
    const f2 e64 = cfma(l32, elo, ehi);
    const int J = tile * 2 + wt;
    const unsigned ecol = WS_EST + (cg * 64 + wp * 32 + l31) * 8;
    if (hh == 0) __builtin_amdgcn_raw_buffer_store_b64((u2){tagv(e64.x, t0), tagv(e64.y, t1)}, ws, ecol + J * 4096, 0, SC1);
    ST(4);
    {
      const f2 KR = {kap.x, kap.x}, KI = {kap.y, kap.y};
#pragma unroll
      for (int a = 0; a < 16; ++a) {
        const f2 xr = {ur[a >> 3][2 * (a & 7)], ur[a >> 3][2 * (a & 7) + 1]}, xi = {ui[a >> 3][2 * (a & 7)], ui[a >> 3][2 * (a & 7) + 1]};
        const f2 nr = fma2(KR, xr, -KI * xi), ni = fma2(-KR, xi, -KI * xr);
        ur[a >> 3][2 * (a & 7)] = nr.x; ur[a >> 3][2 * (a & 7) + 1] = nr.y;
        ui[a >> 3][2 * (a & 7)] = ni.x; ui[a >> 3][2 * (a & 7) + 1] = ni.y;
      }
    }
    f2 cin;
    for (unsigned sp = 0; sp < SPIN_CAP; ++sp) {
      u2 g[16];
#pragma unroll
      for (int m = 0; m < 16; ++m) {
        const int i = J - 1 - hh - 2 * m;
        g[m] = __builtin_amdgcn_raw_buffer_load_b64(ws, i >= 0 ? ecol + i * 4096 : 0xfffffff0u, 0, SC1);
      }
      unsigned bad = 0;
      cin = (f2){0, 0};
#pragma unroll
      for (int m = 15; m >= 0; --m) {
        const int i = J - 1 - hh - 2 * m;
        const unsigned cx = i >= 0 ? g[m].x ^ t0 : 0u, cy = i >= 0 ? g[m].y ^ t1 : 0u;
        bad |= cx | cy;
        cin = cfma(l128, cin, (f2){__uint_as_float(cx), __uint_as_float(cy)});
      }
      if (__all((bad & 0xffu) == 0)) break;
      __builtin_amdgcn_s_sleep(1);
    }
    ST(5);
    {
      const auto cx = __builtin_amdgcn_permlane32_swap(__float_as_uint(cin.x), __float_as_uint(cin.x), false, false);
      const auto cy = __builtin_amdgcn_permlane32_swap(__float_as_uint(cin.y), __float_as_uint(cin.y), false, false);
      cin = cfma(l64, (f2){__uint_as_float(cx[1]), __uint_as_float(cy[1])}, (f2){__uint_as_float(cx[0]), __uint_as_float(cy[0])});
    }
    const f2 up = cfma(l32, cin, elo);
    const f2 c0 = hh ? up : cin;
    const f2 c1 = cfma(l16, c0, e0);
    const f2 d0 = cmul(kap, c0), d1 = cmul(kap, c1);
    f2 hr = {d0.x, d1.x}, hi = {-d0.y, -d1.y};
    u4 idn[2];
#pragma unroll
    for (int s = 0; s < 2; ++s)
#pragma unroll
      for (int jq = 0; jq < 4; ++jq) {
        const int r0 = 16 * s + 8 * (jq >> 1) + 4 * hh + 2 * (jq & 1);
        idn[s][jq] = (l31 == r0 ? 0x3f80u : 0u) | (l31 == r0 + 1 ? 0x3f800000u : 0u);
      }
    f16v zero16;
#pragma unroll
    for (int i = 0; i < 16; ++i) zero16[i] = 0;
#pragma unroll
    for (int m = 0; m < 2; ++m) {
#pragma unroll
      for (int a = 0; a < 8; ++a) {
        const f2 xr = {ur[m][2 * a], ur[m][2 * a + 1]}, xi = {ui[m][2 * a], ui[m][2 * a + 1]};
        const f2 nr = fma2(LR, hr, fma2(LI, hi, xr)), ni = fma2(LR, hi, fma2(-LI, hr, xi));
        hr = nr; hi = ni;
        ur[m][2 * a] = nr.x; ur[m][2 * a + 1] = nr.y; ui[m][2 * a] = ni.x; ui[m][2 * a + 1] = ni.y;
      }
#pragma unroll
      for (int c = 0; c < 2; ++c) {
        const f16v X = c ? ui[m] : ur[m];
        const u4 a0 = {pk2(X[0], X[1]), pk2(X[2], X[3]), pk2(X[4], X[5]), pk2(X[6], X[7])};
        const u4 a1 = {pk2(X[8], X[9]), pk2(X[10], X[11]), pk2(X[12], X[13]), pk2(X[14], X[15])};
        f16v T = mfma32(asbf8(a0), asbf8(idn[0]), zero16);
        T = mfma32(asbf8(a1), asbf8(idn[1]), T);
        const u4 o0 = {pk2(T[0], T[1]), pk2(T[2], T[3]), pk2(T[4], T[5]), pk2(T[6], T[7])};
        const u4 o1 = {pk2(T[8], T[9]), pk2(T[10], T[11]), pk2(T[12], T[13]), pk2(T[14], T[15])};
        const unsigned ho = WS_H + (tile * 4 + wt * 2 + m) * 65536 + ((cg * 2 + wp) * 2 + c) * 2048 + lane * 16;
        __builtin_amdgcn_raw_buffer_store_b128(o0, ws, ho, 0, SC1);
        __builtin_amdgcn_raw_buffer_store_b128(o1, ws, ho + 1024, 0, SC1);
      }
    }
    ST(6);
    drain();
    if (lane == 0) __builtin_amdgcn_raw_buffer_store_b64((u2){tag, ~tag}, ws, WS_FB + ((tile * 2 + wt) * 16 + cg * 2 + wp) * 128, 0, SC1);
    asm volatile("s_barrier" ::: "memory");
    ST(7);
    const int tok = tile * 128 + 64 * (w >> 1) + 32 * ((l31 >> 2) & 1) + 16 * (l31 & 1) + 8 * (w & 1) + ((l31 >> 1) & 1) + 2 * (l31 >> 3);
    f16v acc[2];
#pragma unroll
    for (int nt = 0; nt < 2; ++nt)
#pragma unroll
      for (int g = 0; g < 4; ++g) {
        const int d = cg * 64 + nt * 32 + g * 8 + hh * 4;
        const u2 xv = *(const GAS u2*)(xb + tok * 512 + d), dv = *(const GAS u2*)(dsk + d);
        const unsigned p0 = pk2(bflo(xv.x) * bflo(dv.x), bfhi(xv.x) * bfhi(dv.x)), p1 = pk2(bflo(xv.y) * bflo(dv.y), bfhi(xv.y) * bfhi(dv.y));
        acc[nt][4 * g] = bflo(p0); acc[nt][4 * g + 1] = bfhi(p0); acc[nt][4 * g + 2] = bflo(p1); acc[nt][4 * g + 3] = bfhi(p1);
      }
    wait_flags(ws, WS_FB + ((tile * 2 + (w >> 1)) * 16 + (lane & 15)) * 128, true, tag);
    ST(8);
    const unsigned hb = WS_H + (tile * 4 + w) * 65536 + lane * 16;
    u4 hf[3][8];
    auto loadh = [&](int t) {
      const unsigned so = ((crot + t) & 7) * 8192;
#pragma unroll
      for (int i = 0; i < 8; ++i) hf[t % 3][i] = __builtin_amdgcn_raw_buffer_load_b128(ws, hb + i * 1024, so, SC1);
    };
    loadh(0); loadh(1);
#pragma unroll
    for (int t = 0; t < 8; ++t) {
      if (t + 2 < 8) loadh(t + 2);
      __builtin_amdgcn_sched_barrier(0);
      const char LAS* cb = L + (((t >> 1) + 3) % 5) * SLOT + (t & 1) * 16384;
#pragma unroll
      for (int i = 0; i < 8; ++i) {
        const char LAS* p = cb + ((i >> 1) & 1) * 8192 + ar[(i >> 2) * 2 + (i & 1)];
        acc[0] = mfma32(ldsr(p), asbf8(hf[t % 3][i]), acc[0]);
        acc[1] = mfma32(ldsr(p + 4096), asbf8(hf[t % 3][i]), acc[1]);
      }
      __builtin_amdgcn_sched_barrier(0);
    }
    ST(9);
#pragma unroll
    for (int nt = 0; nt < 2; ++nt)
#pragma unroll
      for (int g = 0; g < 4; ++g)
        st_out(ro, (tok * 1024 + 512 + cg * 64 + nt * 32 + g * 8 + hh * 4) * 4, (f4){acc[nt][4 * g], acc[nt][4 * g + 1], acc[nt][4 * g + 2], acc[nt][4 * g + 3]});
    ST(10);
  }
  drain();
  ST(11);
  if (bid == AMP_BID && w == 0) {
    const u64 until = st[11] + AMP_K * (st[AMP_B] - st[AMP_A]);
    while (__builtin_amdgcn_s_memrealtime() < until) __builtin_amdgcn_s_sleep(2);
  }
}

extern "C" void kernel_launch(void* const* d_in, const int* in_sizes, int n_in, void* d_out, int out_size, void* d_ws, size_t ws_size,
                              hipStream_t stream) {
  (void)hipFuncSetAttribute((const void*)fused_kernel, hipFuncAttributeMaxDynamicSharedMemorySize, LDS_BYTES);
  fused_kernel<<<256, 256, LDS_BYTES, stream>>>((const u16*)d_in[0], (const u16*)d_in[1], (const u16*)d_in[2], (const u16*)d_in[3],
                                               (const u16*)d_in[4], (const u16*)d_in[5], (const u16*)d_in[6], (const u16*)d_in[7],
                                               (const u16*)d_in[8], (const u16*)d_in[9], (const u16*)d_in[10], (const u16*)d_in[11],
                                               (float*)d_out, (char*)d_ws);
}
```

```cpp
#define DUMMY 1
#include <hip/hip_runtime.h>
#include <hip/hip_bf16.h>

#define DI __device__ __forceinline__
#define LAS __attribute__((address_space(3)))
#define GAS __attribute__((address_space(1)))
typedef unsigned short u16;
typedef unsigned long long u64;
typedef __bf16 bf8 __attribute__((ext_vector_type(8)));
typedef __bf16 bf2 __attribute__((ext_vector_type(2)));
typedef short s4 __attribute__((ext_vector_type(4)));
typedef short s8 __attribute__((ext_vector_type(8)));
typedef float f16v __attribute__((ext_vector_type(16)));
typedef float f4 __attribute__((ext_vector_type(4)));
typedef float f2 __attribute__((ext_vector_type(2)));
typedef unsigned u4 __attribute__((ext_vector_type(4)));
typedef unsigned u2 __attribute__((ext_vector_type(2)));

constexpr unsigned WS_EPOCH = 0;
constexpr unsigned WS_FA = 4096;
constexpr unsigned WS_FB = WS_FA + 65536;
constexpr unsigned WS_EST = WS_FB + 65536;
constexpr unsigned WS_Z = WS_EST + 131072;
constexpr unsigned WS_KV = WS_Z + 32768;
constexpr unsigned WS_O = 1u << 20;
constexpr unsigned WS_H = 3u << 20;
constexpr unsigned WS_END = 7u << 20;
constexpr unsigned WS_STAMP = 8u << 20;
constexpr int LDS_BYTES = 160 * 1024;
constexpr unsigned SPIN_CAP = 200000;
constexpr int SC1 = 16;

#define ROT 1
#define NC_EARLY 3
#define OUT_AUX 16
#define PAY_AUX 16
#define OD 6
#define HD 20

#define CT(i) do {} while (0)
#define ST(i) do {} while (0)
#define HFLUSH() do {} while (0)
#define SB() __builtin_amdgcn_sched_barrier(0)
#define SG(m, n) __builtin_amdgcn_sched_group_barrier(m, n, 0)

DI unsigned pk2(float a, float b) { f2 v = {a, b}; return __builtin_bit_cast(unsigned, __builtin_convertvector(v, bf2)); }
DI unsigned pk2(f2 v) { return __builtin_bit_cast(unsigned, __builtin_convertvector(v, bf2)); }
DI float bflo(unsigned v) { return __uint_as_float(v << 16); }
DI float bfhi(unsigned v) { return __uint_as_float(v & 0xffff0000u); }
DI bf8 asbf8(u4 v) { return __builtin_bit_cast(bf8, v); }
DI f16v mfma32(bf8 a, bf8 b, f16v c) { return __builtin_amdgcn_mfma_f32_32x32x16_bf16(a, b, c, 0, 0, 0); }
DI f4 mfma16(bf8 a, bf8 b, f4 c) { return __builtin_amdgcn_mfma_f32_16x16x32_bf16(a, b, c, 0, 0, 0); }
DI f4 mfma16k16(u2 a, u2 b, f4 c) { return __builtin_amdgcn_mfma_f32_16x16x16bf16_1k(__builtin_bit_cast(s4, a), __builtin_bit_cast(s4, b), c, 0, 0, 0); }
DI bf8 ldsr(const char LAS* p) { return *(const LAS bf8*)p; }
DI bf8 ldstr(const char LAS* p0, const char LAS* p1) {
  s4 a = __builtin_amdgcn_ds_read_tr16_b64_v4i16((LAS s4*)p0), b = __builtin_amdgcn_ds_read_tr16_b64_v4i16((LAS s4*)p1);
  return __builtin_bit_cast(bf8, (s8)__builtin_shufflevector(a, b, 0, 1, 2, 3, 4, 5, 6, 7));
}
DI void dma4(unsigned lds, const GAS char* sb, unsigned v0, unsigned v1, unsigned v2, unsigned v3) {
  asm volatile("s_mov_b32 m0, %0\n\ts_nop 0\n\tglobal_load_lds_dwordx4 %1, %5\n\tglobal_load_lds_dwordx4 %2, %5 offset:1024\n\t"
               "global_load_lds_dwordx4 %3, %5 offset:2048\n\tglobal_load_lds_dwordx4 %4, %5 offset:3072"
               :: "s"(lds), "v"(v0), "v"(v1), "v"(v2), "v"(v3), "s"(sb) : "memory");
}
DI void dma2(unsigned lds, const GAS char* sb, unsigned v0, unsigned v1) {
  asm volatile("s_mov_b32 m0, %0\n\ts_nop 0\n\tglobal_load_lds_dwordx4 %1, %3\n\tglobal_load_lds_dwordx4 %2, %3 offset:1024"
               :: "s"(lds), "v"(v0), "v"(v1), "s"(sb) : "memory");
}
DI void lbar() { asm volatile("s_waitcnt lgkmcnt(0)\n\ts_barrier" ::: "memory"); }
template <int N> DI void wait_bar() { asm volatile("s_waitcnt vmcnt(%0) lgkmcnt(0)\n\ts_barrier" :: "i"(N) : "memory"); }
DI void drain() { asm volatile("s_waitcnt vmcnt(0)" ::: "memory"); }
DI unsigned tagv(float x, unsigned t) { return (__float_as_uint(x) & 0xffffff00u) | t; }
DI f2 cmul(f2 a, f2 b) { return (f2){a.x * b.x - a.y * b.y, a.x * b.y + a.y * b.x}; }
DI f2 cfma(f2 a, f2 b, f2 c) { return (f2){fmaf(a.x, b.x, fmaf(-a.y, b.y, c.x)), fmaf(a.x, b.y, fmaf(a.y, b.x, c.y))}; }
DI f2 fma2(f2 a, f2 b, f2 c) { return __builtin_elementwise_fma(a, b, c); }
constexpr float EPS = 1e-6f, LOG2E = 1.44269504088896341f;
DI unsigned phi2(float s0, float s1) {
  const f2 a = fma2((f2){s0, s1}, (f2){LOG2E, LOG2E}, (f2){-EPS * LOG2E, -EPS * LOG2E});
  const f2 e = {__builtin_amdgcn_fmed3f(__builtin_amdgcn_exp2f(a.x), 0.f, 1.f), __builtin_amdgcn_fmed3f(__builtin_amdgcn_exp2f(a.y), 0.f, 1.f)};
  return pk2(e + (f2){__builtin_amdgcn_fmed3f(s0, EPS, __builtin_inff()), __builtin_amdgcn_fmed3f(s1, EPS, __builtin_inff())});
}
DI f2 swap_lo(f2 v, f2& hi) {
  const auto x = __builtin_amdgcn_permlane32_swap(__float_as_uint(v.x), __float_as_uint(v.x), false, false);
  const auto y = __builtin_amdgcn_permlane32_swap(__float_as_uint(v.y), __float_as_uint(v.y), false, false);
  hi = (f2){__uint_as_float(x[1]), __uint_as_float(y[1])};
  return (f2){__uint_as_float(x[0]), __uint_as_float(y[0])};
}
DI void wait_flags(__amdgpu_buffer_rsrc_t ws, unsigned off, unsigned tag) {
#pragma unroll 1
  for (unsigned sp = 0; sp < SPIN_CAP; ++sp) {
      asm volatile("" ::: "memory");
    const u2 f = __builtin_amdgcn_raw_buffer_load_b64(ws, off, 0, SC1);
    if (__all(f.x == tag && f.y == ~tag)) break;
    __builtin_amdgcn_s_sleep(1);
  }
  asm volatile("" ::: "memory");
}

__global__ void __launch_bounds__(512, 2)
fused_kernel(const u16* a0, const u16* a1, const u16* a2, const u16* a3, const u16* a4, const u16* a5, const u16* a6, const u16* a7,
             const u16* a8, const u16* a9, const u16* a10, const u16* a11, float* a12, char* a13) {
  extern __shared__ __attribute__((aligned(16))) char smem_[];
  char LAS* L = (char LAS*)smem_;
  const unsigned L0 = (unsigned)(size_t)L;
  ST(0);
  typedef u64 u64x8 __attribute__((ext_vector_type(8)));
  typedef u64 u64x4 __attribute__((ext_vector_type(4)));
  typedef u64 u64x2 __attribute__((ext_vector_type(2)));
  u64x8 ka; u64x4 kb; u64x2 kc_;
  asm volatile("s_load_dwordx16 %0, %3, 0x0\n\ts_load_dwordx8 %1, %3, 0x40\n\ts_load_dwordx4 %2, %3, 0x60\n\ts_waitcnt lgkmcnt(0)"
               : "=&s"(ka), "=&s"(kb), "=&s"(kc_) : "s"(__builtin_amdgcn_kernarg_segment_ptr()) : "memory");
  const GAS u16 *xa = (const GAS u16*)ka[0], *wqkv = (const GAS u16*)ka[1], *wo = (const GAS u16*)ka[2], *xb = (const GAS u16*)ka[3];
  const GAS u16 *lre = (const GAS u16*)ka[4], *lim = (const GAS u16*)ka[5], *bre = (const GAS u16*)ka[6], *bim = (const GAS u16*)ka[7];
  const GAS u16 *cre = (const GAS u16*)kb[0], *cim = (const GAS u16*)kb[1], *ldt = (const GAS u16*)kb[2], *dsk = (const GAS u16*)kb[3];
  float* out = (float*)kc_[0]; char* wsp = (char*)kc_[1];
  const int lane = threadIdx.x & 63;
  const int w8 = __builtin_amdgcn_readfirstlane(threadIdx.x >> 6), w = w8 & 3;
  const bool helper = w8 >= 4;
  const int bid = blockIdx.x, xcd = bid & 7, jj = bid >> 3;
  const int tile = (xcd >> 1) * 4 + (jj >> 3), cg = jj & 7;
  const int rot = ROT == 1 ? ((cg + 3 * (jj >> 3) + xcd) & 7) : ROT == 2 ? cg : ROT == 3 ? 2 * (jj >> 3) : 0;
  const __amdgpu_buffer_rsrc_t ws = __builtin_amdgcn_make_buffer_rsrc(wsp, 0, WS_END, 0x00020000);
  const __amdgpu_buffer_rsrc_t ro = __builtin_amdgcn_make_buffer_rsrc(out, 0, 2048 * 1024 * 4, 0x00020000);
  const int r8 = lane >> 3, c8 = lane & 7;
  const int q = lane >> 4, l15 = lane & 15, l31 = lane & 31, hh = lane >> 5;
  unsigned epoch;
#define LOAD_EPOCH() asm volatile("s_load_dword %0, %1, 0x0" : "=s"(epoch) : "s"(wsp) : "memory")

  if ((xcd & 1) == 0) {
    constexpr unsigned SLOT = 40960, SCR = 32768;
    const int head = cg * 4 + w;
    if (helper) {
      unsigned vx[4], vw[2], vo[4];
#pragma unroll
      for (int i = 0; i < 4; ++i) vx[i] = (32 * w + 8 * i + r8) * 1024 + ((c8 ^ ((4 * i + (r8 >> 1)) & 7)) << 4) - 1024 * i;
#pragma unroll
      for (int i = 0; i < 2; ++i) vw[i] = (16 * w + 8 * i + r8) * 3072 + ((c8 ^ ((((r8 >> 1) & 1) | (i << 1)) << 1)) << 4) - 1024 * i;
      const GAS char* xbase = (const GAS char*)xa + (size_t)tile * 131072;
      const GAS char* wbase = (const GAS char*)wqkv + cg * 128;
      auto issue = [&](int kk) {
        const int kc = (kk + rot) & 7;
        const unsigned s = L0 + (kk & 3) * SLOT;
        dma4(s + w * 4096, xbase + kc * 128, vx[0], vx[1], vx[2], vx[3]);
        const GAS char* wb = wbase + kc * (64 * 3072);
        dma2(s + 16384 + w * 2048, wb, vw[0], vw[1]);
        dma2(s + 24576 + w * 2048, wb + 1024, vw[0], vw[1]);
        dma2(s + 32768 + w * 2048, wb + 2048, vw[0], vw[1]);
      };
      issue(0); issue(1);
      LOAD_EPOCH();
#pragma unroll
      for (int i = 0; i < 4; ++i) vo[i] = (8 * i + r8) * 1024 + ((c8 ^ (((r8 >> 1) & 3) << 1)) << 4) - 1024 * i;
      const GAS char* obase = (const GAS char*)wo + cg * 128;
      auto issue_wo = [&](int piece) {
#pragma unroll
        for (int h = 0; h < 2; ++h)
          dma4(L0 + piece * SLOT + w * 8192 + h * 4096, obase + ((8 * piece + 2 * w + h + 2 * cg) & 15) * 32768, vo[0], vo[1], vo[2], vo[3]);
      };
      asm volatile("s_waitcnt vmcnt(10) lgkmcnt(0)\n\ts_barrier" : "+s"(epoch) :: "memory");
      issue(2);
      wait_bar<10>(); issue(3);
      wait_bar<10>(); issue(4);
      wait_bar<10>(); issue(5);
      wait_bar<10>(); issue(6);
      wait_bar<10>(); issue(7);
      wait_bar<10>(); issue_wo(0);
      wait_bar<8>(); ST(1); issue_wo(1); ST(2);
      const unsigned tag = epoch + 1, t = tag & 0xffu;
      const GAS char* kvb_ = (const GAS char*)wsp + WS_KV + head * 16384;
      const unsigned vk0 = lane * 16, vk1 = vk0 + 4096, vk2 = vk0 + 8192, vk3 = vk0 + 12288;
      u4 g[16], gz;
#pragma unroll
      for (int i = 0; i < 16; ++i) g[i] = (u4){0, 0, 0, 0};
      f4 kv;
#pragma unroll 1
      for (unsigned rep = 0; rep < 64; ++rep) {
        unsigned have = 0;
#pragma unroll 1
        for (unsigned sp = 0; sp < SPIN_CAP; ++sp) {
      asm volatile("" ::: "memory");
          gz = __builtin_amdgcn_raw_buffer_load_b128(ws, WS_Z + head * 1024 + l15 * 64 + q * 16, 0, SC1);
          gz.x ^= t; gz.z ^= t;
          const u64 m = __ballot(((gz.x | gz.z) & 0xffu) == 0);
          const unsigned m2 = (unsigned)m & (unsigned)(m >> 32);
          const unsigned nw = m2 & (m2 >> 16) & 0xffffu & ~have;
#define KVLD(i, v) if (nw & (1u << i)) asm volatile("global_load_dwordx4 %0, %1, %2 offset:%3 sc1" : "+v"(g[i]) : "v"(v), "s"(kvb_), "i"((i & 3) * 1024) : "memory")
          KVLD(0, vk0); KVLD(1, vk0); KVLD(2, vk0); KVLD(3, vk0); KVLD(4, vk1); KVLD(5, vk1); KVLD(6, vk1); KVLD(7, vk1);
          KVLD(8, vk2); KVLD(9, vk2); KVLD(10, vk2); KVLD(11, vk2); KVLD(12, vk3); KVLD(13, vk3); KVLD(14, vk3); KVLD(15, vk3);
          have |= nw;
          if (have == 0xffffu) break;
          if (!nw) __builtin_amdgcn_s_sleep(1);
        }
        ST(4);
        asm volatile("s_waitcnt vmcnt(0)" : "+v"(g[0]), "+v"(g[1]), "+v"(g[2]), "+v"(g[3]), "+v"(g[4]), "+v"(g[5]), "+v"(g[6]), "+v"(g[7]),
                     "+v"(g[8]), "+v"(g[9]), "+v"(g[10]), "+v"(g[11]), "+v"(g[12]), "+v"(g[13]), "+v"(g[14]), "+v"(g[15]) :: "memory");
        ST(5);
        unsigned bad = 0;
        kv = (f4){0, 0, 0, 0};
#pragma unroll
        for (int i = 0; i < 16; ++i) {
          g[i].x ^= t; g[i].z ^= t;
          bad |= g[i].x | g[i].z;
          kv += __builtin_bit_cast(f4, g[i]);
        }
        if (__all((bad & 0xffu) == 0)) break;
      }
      f4 zz = __builtin_bit_cast(f4, gz);
#pragma unroll
      for (int r = 0; r < 4; ++r) {
        float v = zz[r];
        v += __builtin_bit_cast(float, __builtin_amdgcn_mov_dpp(__builtin_bit_cast(int, v), 0x128, 0xf, 0xf, true));
        v += __builtin_bit_cast(float, __builtin_amdgcn_mov_dpp(__builtin_bit_cast(int, v), 0x124, 0xf, 0xf, true));
        v += __builtin_bit_cast(float, __builtin_amdgcn_mov_dpp(__builtin_bit_cast(int, v), 0x122, 0xf, 0xf, true));
        v += __builtin_bit_cast(float, __builtin_amdgcn_mov_dpp(__builtin_bit_cast(int, v), 0x121, 0xf, 0xf, true));
        zz[r] = v;
      }
      *(LAS u4*)(L + SCR + w * 1024 + lane * 16) = (u4){pk2(kv[0], kv[1]), pk2(kv[2], kv[3]), pk2(zz[0], zz[1]), pk2(zz[2], zz[3])};
      ST(6);
      wait_bar<0>();
      ST(7); HFLUSH();
      if (bid == 0 && w == 0) {
#pragma unroll 1
        for (unsigned sp = 0; sp < SPIN_CAP; ++sp) {
      asm volatile("" ::: "memory");
          const u2 f0 = __builtin_amdgcn_raw_buffer_load_b64(ws, WS_FA + lane * 1024, 0, SC1);
          const u2 f1 = __builtin_amdgcn_raw_buffer_load_b64(ws, WS_FA + 512 + lane * 1024, 0, SC1);
          const u2 f2_ = __builtin_amdgcn_raw_buffer_load_b64(ws, WS_FB + (lane >> 3) * 4096 + (lane & 7) * 256, 0, SC1);
          const u2 f3 = __builtin_amdgcn_raw_buffer_load_b64(ws, WS_FB + 32768 + (lane >> 3) * 4096 + (lane & 7) * 256, 0, SC1);
          if (__all(f0.x == tag && f0.y == ~tag && f1.x == tag && f1.y == ~tag && f2_.x == tag && f2_.y == ~tag && f3.x == tag && f3.y == ~tag)) break;
          __builtin_amdgcn_s_sleep(8);
        }
        if (lane == 0) __builtin_amdgcn_raw_buffer_store_b32(tag, ws, WS_EPOCH, 0, SC1);
      }
      return;
    }
    LOAD_EPOCH();
    const int qq = (lane >> 2) & 3, pp = lane & 3;
    const unsigned ax0 = l15 * 128 + ((q ^ (l15 >> 1)) << 4), ax1 = l15 * 128 + (((4 + q) ^ (l15 >> 1)) << 4);
    const unsigned aw = 16384 + (8 * q + qq) * 128 + (((2 * w + (pp >> 1)) ^ ((((qq >> 1) & 1) | ((q & 1) << 1)) << 1)) << 4) + 8 * (pp & 1);
    f4 ka[8], va[8], qa[8];
#pragma unroll
    for (int i = 0; i < 8; ++i) { ka[i] = (f4){EPS, EPS, EPS, EPS}; qa[i] = ka[i]; va[i] = (f4){0, 0, 0, 0}; }
    bf8 X0[4], X1[4], W0[3], W1[3];
    auto rdx = [&](bf8 (&X)[4], int kk, int ks, int h) {
#pragma unroll
      for (int i = 0; i < 4; ++i) X[i] = ldsr(L + (kk & 3) * SLOT + (ks ? ax1 : ax0) + (4 * h + i) * 2048);
    };
    auto rdw = [&](bf8 (&W)[3], int kk, int ks) {
#pragma unroll
      for (int i = 0; i < 3; ++i) { const char LAS* p = L + (kk & 3) * SLOT + aw + ks * 4096 + i * 8192; W[i] = ldstr(p, p + 512); }
    };
    auto mm = [&](const bf8 (&X)[4], const bf8 (&W)[3], int h) {
#pragma unroll
      for (int i = 0; i < 4; ++i) {
        ka[4 * h + i] = mfma16(X[i], W[1], ka[4 * h + i]);
        va[4 * h + i] = mfma16(X[i], W[2], va[4 * h + i]);
        qa[4 * h + i] = mfma16(W[0], X[i], qa[4 * h + i]);
      }
    };
#define ILV(nr, nm) do { _Pragma("unroll") for (int i_ = 0; i_ < nr; ++i_) { SG(0x008, 1); SG(0x100, 1); } SG(0x008, nm - nr); } while (0)
    asm volatile("s_waitcnt lgkmcnt(0)\n\ts_barrier" : "+s"(epoch) :: "memory");
    ST(1); CT(13);
    const unsigned tag = epoch + 1, t = tag & 0xffu;
    rdw(W0, 0, 0); rdx(X0, 0, 0, 0);
    SB();
#pragma unroll
    for (int kk = 0; kk < 8; ++kk) {
      rdx(X1, kk, 0, 1); mm(X0, W0, 0); ILV(4, 12); SB();
      rdw(W1, kk, 1); rdx(X0, kk, 1, 0); mm(X1, W0, 1); ILV(10, 12); SB();
      rdx(X1, kk, 1, 1); mm(X0, W1, 0); ILV(4, 12); SB();
      if (kk < 7) { lbar(); if (kk == 6) ST(2); rdw(W0, kk + 1, 0); rdx(X0, kk + 1, 0, 0); mm(X1, W1, 1); ILV(10, 12); }
      else mm(X1, W1, 1);
      SB();
    }
    ST(3); CT(14);
    {
      f4 kv = {0, 0, 0, 0}, zz = {0, 0, 0, 0};
      const u4 ones = {0x3f803f80u, 0x3f803f80u, 0x3f803f80u, 0x3f803f80u};
#pragma unroll
      for (int m = 0; m < 4; ++m) {
        const f4 a = ka[2 * m], b = ka[2 * m + 1], c = va[2 * m], d = va[2 * m + 1];
        const u4 pa = {phi2(a[0], a[1]), phi2(a[2], a[3]), phi2(b[0], b[1]), phi2(b[2], b[3])};
        const u4 pb = {pk2(c[0], c[1]), pk2(c[2], c[3]), pk2(d[0], d[1]), pk2(d[2], d[3])};
        kv = mfma16(asbf8(pa), asbf8(pb), kv);
        zz = mfma16(asbf8(pa), asbf8(ones), zz);
      }
      const u4 skv = {tagv(kv[0], t), __float_as_uint(kv[1]), tagv(kv[2], t), __float_as_uint(kv[3])};
      __builtin_amdgcn_raw_buffer_store_b128(skv, ws, WS_KV + (head * 16 + tile) * 1024 + lane * 16, 0, SC1);
      const u4 sz = {tagv(zz[0], t), __float_as_uint(zz[1]), tagv(zz[2], t), __float_as_uint(zz[3])};
      __builtin_amdgcn_raw_buffer_store_b128(sz, ws, l15 == 0 ? WS_Z + (head * 16 + tile) * 64 + q * 16 : 0x80000000u, 0, SC1);
    }
    SB();
    ST(4);
    u2 pq[8];
#pragma unroll
    for (int mt = 0; mt < 8; ++mt) pq[mt] = (u2){phi2(qa[mt][0], qa[mt][1]), phi2(qa[mt][2], qa[mt][3])};
    unsigned ao[4];
#pragma unroll
    for (int nt = 0; nt < 4; ++nt) ao[nt] = (4 * q + qq) * 128 + ((((2 * nt) ^ (((2 * q + (qq >> 1)) & 3) << 1)) + (pp >> 1)) << 4) + 8 * (pp & 1);
    asm volatile("s_barrier" : "+v"(pq[0]), "+v"(pq[1]), "+v"(pq[2]), "+v"(pq[3]), "+v"(pq[4]), "+v"(pq[5]), "+v"(pq[6]), "+v"(pq[7]) :: "memory");
    ST(5);
    const u4 kz = *(const LAS u4*)(L + SCR + w * 1024 + lane * 16);
    const u2 kvb = {kz.x, kz.y}, zb = {kz.z, kz.w};
    const f4 zero4 = {0, 0, 0, 0}, eps4 = {EPS, EPS, EPS, EPS};
    f4 num[8], den[8];
#pragma unroll
    for (int mt = 0; mt < 8; ++mt) { den[mt] = mfma16k16(zb, pq[mt], eps4); num[mt] = mfma16k16(kvb, pq[mt], zero4); }
    SB();
#pragma unroll
    for (int u = 0; u < 4; ++u) {
      u4 so;
#pragma unroll
      for (int h = 0; h < 2; ++h) {
        const float rc = __builtin_amdgcn_rcpf(den[2 * u + h][0]);
        const f4 n = num[2 * u + h];
        so[2 * h] = pk2((f2){n[0], n[1]} * rc);
        so[2 * h + 1] = pk2((f2){n[2], n[3]} * rc);
      }
      __builtin_amdgcn_raw_buffer_store_b128(so, ws, WS_O + ((tile * 4 + u) * 32 + head) * 1024 + lane * 16, 0, SC1);
    }
    ST(6);
    drain();
    __builtin_amdgcn_raw_buffer_store_b64((u2){tag, ~tag}, ws, lane == 0 ? WS_FA + (tile * 32 + head) * 128 : 0x80000000u, 0, SC1);
    ST(7);
    f4 acc[2][4];
#pragma unroll
    for (int i = 0; i < 8; ++i) acc[i >> 2][i & 3] = zero4;
    const unsigned ob = WS_O + (tile * 4 + w) * 32768 + lane * 16;
    u4 of[32];
    bf8 wf[2][4];
    auto rdo = [&](int k) {
      const char LAS* base = L + (k >> 3) * SLOT + (k & 7) * 4096;
#pragma unroll
      for (int nt = 0; nt < 4; ++nt) wf[k & 1][nt] = ldstr(base + ao[nt], base + ao[nt] + 2048);
    };
    auto ldo = [&](int k) {
      const unsigned so = ((k + 2 * cg) & 15) * 2048;
      of[2 * k] = __builtin_amdgcn_raw_buffer_load_b128(ws, ob, so, PAY_AUX);
      of[2 * k + 1] = __builtin_amdgcn_raw_buffer_load_b128(ws, ob + 1024, so, PAY_AUX);
    };
    rdo(0);
    wait_flags(ws, WS_FA + (tile * 32 + l31) * 128, tag);
    ST(8);
#pragma unroll
    for (int k = 0; k < OD; ++k) ldo(k);
    SB();
#pragma unroll
    for (int k = 0; k < 16; ++k) {
      if (k + OD < 16) ldo(k + OD);
      if (k + 1 < 16) rdo(k + 1);
      const u4 a = of[2 * k], b = of[2 * k + 1];
      const bf8 f0 = asbf8((u4){a.x, a.y, b.x, b.y}), f1 = asbf8((u4){a.z, a.w, b.z, b.w});
#pragma unroll
      for (int nt = 0; nt < 4; ++nt) {
        acc[0][nt] = mfma16(wf[k & 1][nt], f0, acc[0][nt]);
        acc[1][nt] = mfma16(wf[k & 1][nt], f1, acc[1][nt]);
      }
      SG(0x002, 4);
#pragma unroll
      for (int i_ = 0; i_ < 8; ++i_) { SG(0x008, 1); SG(0x100, 1); if (i_ == 1 || i_ == 5) SG(0x020, 1); }
      SB();
    }
    ST(9); CT(15);
    char LAS* tr = L + 2 * SLOT + w * 8192;
#pragma unroll
    for (int mt = 0; mt < 2; ++mt)
#pragma unroll
      for (int nt = 0; nt < 4; ++nt) {
        const f4 v = acc[mt][nt];
        const unsigned p0 = pk2(v[0], v[1]), p1 = pk2(v[2], v[3]);
        *(LAS f4*)(tr + (mt * 16 + l15) * 256 + (((4 * nt + q) ^ l15) << 4)) = (f4){bflo(p0), bfhi(p0), bflo(p1), bfhi(p1)};
      }
#pragma unroll
    for (int i = 0; i < 8; ++i) {
      const int row = 4 * i + q;
      const u4 v = *(const LAS u4*)(tr + row * 256 + ((l15 ^ (row & 15)) << 4));
      __builtin_amdgcn_raw_buffer_store_b128(v, ro, ((tile * 128 + w * 32 + row) * 1024 + cg * 64 + l15 * 4) * 4, 0, OUT_AUX);
    }
    ST(10);
  } else {
    constexpr unsigned SLOT = 32768, SCR = SLOT, PAR = 4 * SLOT;
    const int crot = cg + 2 * (jj >> 3);
    const int wp = w & 1, wt = w >> 1;
    const unsigned ecol = WS_EST + (cg * 64 + wp * 32 + l31) * 8;
    if (helper) {
      unsigned vx[4], vw[4], vc[4];
#pragma unroll
      for (int i = 0; i < 4; ++i) {
        const unsigned sw = (c8 ^ ((4 * i + (r8 >> 1)) & 7)) << 4;
        vx[i] = (64 * (w >> 1) + 32 * (r8 >> 2) + 16 * (r8 & 1) + 8 * (w & 1) + ((r8 >> 1) & 1) + 2 * i) * 1024 + sw - 1024 * i;
        vw[i] = (16 * (i >> 1) + 8 * (r8 >> 2) + 4 * (i & 1) + (r8 & 3)) * 1024 + sw - 1024 * i;
        vc[i] = (8 * i + r8) * 1024 + sw - 1024 * i;
      }
      const GAS char* xbase = (const GAS char*)xb + (size_t)tile * 131072;
      const GAS char* wbase = (const GAS char*)((w & 1) ? bim : bre) + (cg * 64 + (w >> 1) * 32) * 1024;
      auto issue = [&](int kk) {
        const int kc = (kk + rot) & 7;
        const unsigned s = L0 + (kk % 5) * SLOT + w * 4096;
        dma4(s, xbase + kc * 128, vx[0], vx[1], vx[2], vx[3]);
        dma4(s + 16384, wbase + kc * 128, vw[0], vw[1], vw[2], vw[3]);
      };
      issue(0); issue(1);
      LOAD_EPOCH();
      const GAS char* cbase = (const GAS char*)((w & 1) ? cim : cre) + cg * 65536;
      auto issue_c = [&](int piece) {
        const unsigned s = L0 + ((piece + 3) % 5) * SLOT + w * 8192;
        const GAS char* cb = cbase + ((crot + 2 * piece + (w >> 1)) & 7) * 128;
        dma4(s, cb, vc[0], vc[1], vc[2], vc[3]);
        dma4(s + 4096, cb + 32768, vc[0], vc[1], vc[2], vc[3]);
      };
      asm volatile("s_waitcnt vmcnt(8) lgkmcnt(0)\n\ts_barrier" : "+s"(epoch) :: "memory");
      issue(2);
      const f4 par = *(const LAS f4*)(L + PAR + w * 1024 + lane * 16);
      const f2 l64 = {par[0], par[1]}, l128 = {par[2], par[3]};
      f2 l256 = cmul(l128, l128);
      f2 wgt = hh ? l64 : (f2){1.f, 0.f};
      if (wt) wgt = cmul(wgt, l128);
      const unsigned tag = epoch + 1, t = tag & 0xffu;
      unsigned eo[8], et[8];
#pragma unroll
      for (int m = 0; m < 8; ++m) {
        const int i = 2 * tile - 1 - (2 * wt + hh) - 4 * m;
        eo[m] = i >= 0 ? ecol + i * 4096 : 0x80000000u;
        et[m] = i >= 0 ? t : 0u;
        asm volatile("" : "+v"(eo[m]), "+v"(et[m]));
      }
      asm volatile("" : "+v"(l256), "+v"(wgt));
      wait_bar<8>(); issue(3);
      wait_bar<8>(); issue(4);
      wait_bar<8>(); issue(5);
      wait_bar<8>(); issue(6);
      wait_bar<8>(); issue(7);
      wait_bar<8>(); issue_c(0);
      wait_bar<8>(); ST(1);
      if (NC_EARLY > 1) issue_c(1);
      if (NC_EARLY > 2) issue_c(2);
      ST(2);
      f2 cin;
#pragma unroll 1
      for (unsigned sp = 0; sp < SPIN_CAP; ++sp) {
      asm volatile("" ::: "memory");
        u2 g[8];
#pragma unroll
        for (int m = 0; m < 8; ++m) g[m] = __builtin_amdgcn_raw_buffer_load_b64(ws, eo[m], 0, SC1);
        unsigned bad = 0;
        cin = (f2){0, 0};
#pragma unroll
        for (int m = 7; m >= 0; --m) {
          const unsigned cx = g[m].x ^ et[m];
          bad |= cx;
          cin = cfma(l256, cin, (f2){__uint_as_float(cx), __uint_as_float(g[m].y)});
        }
        if (__all((bad & 0xffu) == 0)) break;
      }
      ST(5);
      cin = cmul(cin, wgt);
      f2 chi;
      const f2 clo = swap_lo(cin, chi);
      if (hh == 0) *(LAS f2*)(L + SCR + (wp * 2 + wt) * 256 + l31 * 8) = clo + chi;
      ST(6);
      lbar();
      lbar();
      if (NC_EARLY <= 1) issue_c(1);
      if (NC_EARLY <= 2) issue_c(2);
      issue_c(3);
      ST(7);
      wait_bar<0>();
      ST(8); HFLUSH();
      return;
    }
    LOAD_EPOCH();
    const int pl = cg * 64 + wp * 32 + ((l31 & 0x13) | ((l31 & 4) << 1) | ((l31 & 8) >> 1));
    const unsigned plr = lre[pl], pli = lim[pl], pdt = ldt[pl];
    auto tokr = [&](int r) { return tile * 128 + 64 * (w >> 1) + 32 * ((r >> 2) & 1) + 16 * (r & 1) + 8 * (w & 1) + ((r >> 1) & 1) + 2 * (r >> 3); };
    u2 xd[8];
    {
      const u2 dv = *(const GAS u2*)(dsk + cg * 64 + l15 * 4);
#pragma unroll
      for (int i = 0; i < 8; ++i) {
        const u2 xv = *(const GAS u2*)(xb + tokr(4 * i + q) * 512 + cg * 64 + l15 * 4);
        xd[i] = (u2){pk2(bflo(xv.x) * bflo(dv.x), bfhi(xv.x) * bfhi(dv.x)), pk2(bflo(xv.y) * bflo(dv.y), bfhi(xv.y) * bfhi(dv.y))};
      }
    }
    f2 lam, kap, l16, l32, l64, l128;
    {
      const float lr = bflo(plr), li = bflo(pli);
      const float dt = bflo(pk2(expf(bflo(pdt)), 0.f));
      const float mag = expf(dt * lr);
      float sn, cs;
      sincosf(dt * li, &sn, &cs);
      lam = (f2){mag * cs, mag * sn};
      const float inv = 2.f / (lr * lr + li * li), nr = lam.x - 1.f, ni = lam.y;
      kap = (f2){(nr * lr + ni * li) * inv, (ni * lr - nr * li) * inv};
      f2 t = cmul(lam, lam); t = cmul(t, t); t = cmul(t, t);
      l16 = cmul(t, t); l32 = cmul(l16, l16); l64 = cmul(l32, l32); l128 = cmul(l64, l64);
      *(LAS f4*)(L + PAR + w * 1024 + lane * 16) = (f4){l64.x, l64.y, l128.x, l128.y};
    }
    unsigned ar[4];
#pragma unroll
    for (int s = 0; s < 4; ++s) ar[s] = l31 * 128 + (((2 * s + hh) ^ ((lane >> 1) & 7)) << 4);
    u4 idn[2];
#pragma unroll
    for (int s = 0; s < 2; ++s)
#pragma unroll
      for (int jq = 0; jq < 4; ++jq) {
        const int r0 = 16 * s + 8 * (jq >> 1) + 4 * hh + 2 * (jq & 1);
        idn[s][jq] = (l31 == r0 ? 0x3f80u : 0u) | (l31 == r0 + 1 ? 0x3f800000u : 0u);
      }
    f16v ur[2], ui[2];
#pragma unroll
    for (int i = 0; i < 16; ++i) { ur[0][i] = 0; ur[1][i] = 0; ui[0][i] = 0; ui[1][i] = 0; }
    bf8 F0[4], F1[4];
    auto rd = [&](bf8 (&F)[4], int kk, int s) {
      const char LAS* sx = L + (kk % 5) * SLOT + wt * 8192 + ar[s];
      const char LAS* sw = L + (kk % 5) * SLOT + 16384 + wp * 8192 + ar[s];
      F[0] = ldsr(sx); F[1] = ldsr(sw); F[2] = ldsr(sw + 4096); F[3] = ldsr(sx + 4096);
    };
    auto mm = [&](const bf8 (&F)[4]) {
      ur[0] = mfma32(F[0], F[1], ur[0]); ui[0] = mfma32(F[0], F[2], ui[0]);
      ur[1] = mfma32(F[3], F[1], ur[1]); ui[1] = mfma32(F[3], F[2], ui[1]);
    };
#define ILB() do { SG(0x008, 1); SG(0x100, 2); SG(0x008, 1); SG(0x100, 2); SG(0x008, 2); } while (0)
    asm volatile("s_waitcnt lgkmcnt(0)\n\ts_barrier" : "+s"(epoch), "+v"(lam), "+v"(kap), "+v"(l16), "+v"(l32), "+v"(l64), "+v"(idn[0]), "+v"(idn[1]),
                 "+v"(xd[0]), "+v"(xd[1]), "+v"(xd[2]), "+v"(xd[3]), "+v"(xd[4]), "+v"(xd[5]), "+v"(xd[6]), "+v"(xd[7]) :: "memory");
    ST(1); CT(13);
    const unsigned tag = epoch + 1, t = tag & 0xffu;
    rd(F0, 0, 0);
    SB();
#pragma unroll
    for (int kk = 0; kk < 8; ++kk) {
      rd(F1, kk, 1); mm(F0); ILB(); SB();
      rd(F0, kk, 2); mm(F1); ILB(); SB();
      rd(F1, kk, 3); mm(F0); ILB(); SB();
      if (kk < 7) { lbar(); if (kk == 6) ST(2); rd(F0, kk + 1, 0); mm(F1); ILB(); }
      else mm(F1);
      SB();
    }
    ST(3); CT(14);
    const f2 LR = {lam.x, lam.x}, LI = {lam.y, lam.y};
    f2 er = {ur[0][0], ur[0][1]}, ei = {ui[0][0], ui[0][1]};
#pragma unroll
    for (int a = 1; a < 16; ++a) {
      const f2 xr = {ur[a >> 3][2 * (a & 7)], ur[a >> 3][2 * (a & 7) + 1]}, xi = {ui[a >> 3][2 * (a & 7)], ui[a >> 3][2 * (a & 7) + 1]};
      const f2 nr = fma2(LR, er, fma2(-LI, ei, xr)), ni = fma2(LR, ei, fma2(LI, er, xi));
      er = nr; ei = ni;
    }
    const f2 e0 = {er.x, ei.x};
    const f2 e32 = cfma(l16, e0, (f2){er.y, ei.y});
    f2 ehi;
    const f2 elo = swap_lo(e32, ehi);
    const f2 e64 = cfma(l32, elo, ehi);
    __builtin_amdgcn_raw_buffer_store_b64((u2){tagv(e64.x, t), __float_as_uint(e64.y)}, ws, hh == 0 ? ecol + (tile * 2 + wt) * 4096 : 0x80000000u, 0, SC1);
    if (wt == 0 && hh == 0) *(LAS f2*)(L + SCR + 1024 + wp * 256 + l31 * 8) = e64;
    SB();
    ST(4);
    {
      const f2 KR = {kap.x, kap.x}, KI = {kap.y, kap.y};
#pragma unroll
      for (int a = 0; a < 16; ++a) {
        const f2 xr = {ur[a >> 3][2 * (a & 7)], ur[a >> 3][2 * (a & 7) + 1]}, xi = {ui[a >> 3][2 * (a & 7)], ui[a >> 3][2 * (a & 7) + 1]};
        const f2 nr = fma2(KR, xr, -KI * xi), ni = fma2(-KR, xi, -KI * xr);
        ur[a >> 3][2 * (a & 7)] = nr.x; ur[a >> 3][2 * (a & 7) + 1] = nr.y;
        ui[a >> 3][2 * (a & 7)] = ni.x; ui[a >> 3][2 * (a & 7) + 1] = ni.y;
      }
    }
    f2 m0 = hh ? cmul(kap, l32) : kap, b0 = hh ? cmul(kap, elo) : (f2){0.f, 0.f};
    f2 m1 = cmul(l16, m0), b1 = cfma(l16, b0, cmul(kap, e0));
    asm volatile("s_waitcnt lgkmcnt(0)\n\ts_barrier" : "+v"(ur[0]), "+v"(ur[1]), "+v"(ui[0]), "+v"(ui[1]), "+v"(m0), "+v"(b0), "+v"(m1), "+v"(b1) :: "memory");
    ST(5);
    f2 cin = *(const LAS f2*)(L + SCR + wp * 512 + l31 * 8) + *(const LAS f2*)(L + SCR + wp * 512 + 256 + l31 * 8);
    const f2 esib = *(const LAS f2*)(L + SCR + 1024 + wp * 256 + l31 * 8);
    lbar();
    if (wt) cin = cfma(l64, cin, esib);
    const f2 d0 = cfma(m0, cin, b0), d1 = cfma(m1, cin, b1);
    f2 hr = {d0.x, d1.x}, hi = {-d0.y, -d1.y};
    f16v zero16;
#pragma unroll
    for (int i = 0; i < 16; ++i) zero16[i] = 0;
#pragma unroll
    for (int m = 0; m < 2; ++m) {
#pragma unroll
      for (int a = 0; a < 8; ++a) {
        const f2 xr = {ur[m][2 * a], ur[m][2 * a + 1]}, xi = {ui[m][2 * a], ui[m][2 * a + 1]};
        const f2 nr = fma2(LR, hr, fma2(LI, hi, xr)), ni = fma2(LR, hi, fma2(-LI, hr, xi));
        hr = nr; hi = ni;
        ur[m][2 * a] = nr.x; ur[m][2 * a + 1] = nr.y; ui[m][2 * a] = ni.x; ui[m][2 * a + 1] = ni.y;
      }
#pragma unroll
      for (int c = 0; c < 2; ++c) {
        const f16v X = c ? ui[m] : ur[m];
        const u4 a0 = {pk2(X[0], X[1]), pk2(X[2], X[3]), pk2(X[4], X[5]), pk2(X[6], X[7])};
        const u4 a1 = {pk2(X[8], X[9]), pk2(X[10], X[11]), pk2(X[12], X[13]), pk2(X[14], X[15])};
        f16v T = mfma32(asbf8(a0), asbf8(idn[0]), zero16);
        T = mfma32(asbf8(a1), asbf8(idn[1]), T);
        const u4 o0 = {pk2(T[0], T[1]), pk2(T[2], T[3]), pk2(T[4], T[5]), pk2(T[6], T[7])};
        const u4 o1 = {pk2(T[8], T[9]), pk2(T[10], T[11]), pk2(T[12], T[13]), pk2(T[14], T[15])};
        const unsigned ho = WS_H + (tile * 4 + wt * 2 + m) * 65536 + ((cg * 2 + wp) * 2 + c) * 2048 + lane * 16;
        __builtin_amdgcn_raw_buffer_store_b128(o0, ws, ho, 0, SC1);
        __builtin_amdgcn_raw_buffer_store_b128(o1, ws, ho + 1024, 0, SC1);
      }
    }
    ST(6);
    drain();
    __builtin_amdgcn_raw_buffer_store_b64((u2){tag, ~tag}, ws, lane == 0 ? WS_FB + ((tile * 2 + wt) * 16 + cg * 2 + wp) * 128 : 0x80000000u, 0, SC1);
    asm volatile("s_barrier" ::: "memory");
    ST(7);
    f16v acc[2] = {zero16, zero16};
    const unsigned hb = WS_H + (tile * 4 + w) * 65536 + lane * 16;
    u4 hf[64];
    bf8 cf[2][4];
    auto ldh = [&](int i) { hf[i] = __builtin_amdgcn_raw_buffer_load_b128(ws, hb + (i & 7) * 1024, ((crot + (i >> 3)) & 7) * 8192, PAY_AUX); };
    auto rdc = [&](int g) {
      const int tt = g >> 2, i = (g & 3) * 2;
      const char LAS* cb = L + (((tt >> 1) + 3) % 5) * SLOT + (tt & 1) * 16384 + ((i >> 1) & 1) * 8192;
      cf[g & 1][0] = ldsr(cb + ar[(i >> 2) * 2]); cf[g & 1][1] = ldsr(cb + ar[(i >> 2) * 2] + 4096);
      cf[g & 1][2] = ldsr(cb + ar[(i >> 2) * 2 + 1]); cf[g & 1][3] = ldsr(cb + ar[(i >> 2) * 2 + 1] + 4096);
    };
    rdc(0);
    wait_flags(ws, WS_FB + ((tile * 2 + (w >> 1)) * 16 + l15) * 128, tag);
    ST(8);
#pragma unroll
    for (int i = 0; i < HD; ++i) ldh(i);
    SB();
#pragma unroll
    for (int g = 0; g < 32; ++g) {
      if (2 * g + HD < 64) { ldh(2 * g + HD); ldh(2 * g + HD + 1); }
      if (g + 1 < 32) rdc(g + 1);
      acc[0] = mfma32(cf[g & 1][0], asbf8(hf[2 * g]), acc[0]);
      acc[1] = mfma32(cf[g & 1][1], asbf8(hf[2 * g]), acc[1]);
      acc[0] = mfma32(cf[g & 1][2], asbf8(hf[2 * g + 1]), acc[0]);
      acc[1] = mfma32(cf[g & 1][3], asbf8(hf[2 * g + 1]), acc[1]);
      SG(0x008, 1); SG(0x100, 2); SG(0x008, 1); SG(0x100, 2); SG(0x008, 1); SG(0x020, 1); SG(0x008, 1); SG(0x020, 1);
      SB();
    }
    ST(9); CT(15);
    char LAS* tr = L + 2 * SLOT + w * 8192;
#pragma unroll
    for (int nt = 0; nt < 2; ++nt)
#pragma unroll
      for (int g = 0; g < 4; ++g)
        *(LAS f4*)(tr + l31 * 256 + (((8 * nt + 2 * g + hh) ^ l15) << 4)) = (f4){acc[nt][4 * g], acc[nt][4 * g + 1], acc[nt][4 * g + 2], acc[nt][4 * g + 3]};
#pragma unroll
    for (int i = 0; i < 8; ++i) {
      const int row = 4 * i + q;
      const f4 v = *(const LAS f4*)(tr + row * 256 + ((l15 ^ (row & 15)) << 4)) + (f4){bflo(xd[i].x), bfhi(xd[i].x), bflo(xd[i].y), bfhi(xd[i].y)};
      __builtin_amdgcn_raw_buffer_store_b128(__builtin_bit_cast(u4, v), ro, (tokr(row) * 1024 + 512 + cg * 64 + l15 * 4) * 4, 0, OUT_AUX);
    }
    ST(10);
  }
}

extern "C" void kernel_launch(void* const* d_in, const int* in_sizes, int n_in, void* d_out, int out_size, void* d_ws, size_t ws_size,
                              hipStream_t stream) {
  (void)hipFuncSetAttribute((const void*)fused_kernel, hipFuncAttributeMaxDynamicSharedMemorySize, LDS_BYTES);
  fused_kernel<<<256, 512, LDS_BYTES, stream>>>((const u16*)d_in[0], (const u16*)d_in[1], (const u16*)d_in[2], (const u16*)d_in[3],
                                               (const u16*)d_in[4], (const u16*)d_in[5], (const u16*)d_in[6], (const u16*)d_in[7],
                                               (const u16*)d_in[8], (const u16*)d_in[9], (const u16*)d_in[10], (const u16*)d_in[11],
                                               (float*)d_out, (char*)d_ws);
}
```

```cpp
#define PULL 0
#define TOUCH 1
#define PAY_AUX 0
#include <hip/hip_runtime.h>
#include <hip/hip_bf16.h>

#define DI __device__ __forceinline__
#define LAS __attribute__((address_space(3)))
#define GAS __attribute__((address_space(1)))
typedef unsigned short u16;
typedef unsigned long long u64;
typedef __bf16 bf8 __attribute__((ext_vector_type(8)));
typedef __bf16 bf2 __attribute__((ext_vector_type(2)));
typedef short s4 __attribute__((ext_vector_type(4)));
typedef short s8 __attribute__((ext_vector_type(8)));
typedef float f16v __attribute__((ext_vector_type(16)));
typedef float f4 __attribute__((ext_vector_type(4)));
typedef float f2 __attribute__((ext_vector_type(2)));
typedef unsigned u4 __attribute__((ext_vector_type(4)));
typedef unsigned u2 __attribute__((ext_vector_type(2)));

constexpr unsigned WS_EPOCH = 0;
constexpr unsigned WS_FA = 4096;
constexpr unsigned WS_FB = WS_FA + 65536;
constexpr unsigned WS_EST = WS_FB + 65536;
constexpr unsigned WS_Z = WS_EST + 131072;
constexpr unsigned WS_KV = WS_Z + 32768;
constexpr unsigned WS_O = 1u << 20;
constexpr unsigned WS_H = 3u << 20;
constexpr unsigned WS_END = 7u << 20;
constexpr unsigned WS_STAMP = 8u << 20;
constexpr int LDS_BYTES = 160 * 1024;
constexpr unsigned SPIN_CAP = 200000;
constexpr int SC1 = 16;

#define ROT 1
#define NC_EARLY 3
#define OUT_AUX 16
#define OD 6
#define HD 20

#define CT(i) do {} while (0)
#define ST(i) do {} while (0)
#define HFLUSH() do {} while (0)
#define SB() __builtin_amdgcn_sched_barrier(0)
#define SG(m, n) __builtin_amdgcn_sched_group_barrier(m, n, 0)

DI unsigned pk2(float a, float b) { f2 v = {a, b}; return __builtin_bit_cast(unsigned, __builtin_convertvector(v, bf2)); }
DI unsigned pk2(f2 v) { return __builtin_bit_cast(unsigned, __builtin_convertvector(v, bf2)); }
DI float bflo(unsigned v) { return __uint_as_float(v << 16); }
DI float bfhi(unsigned v) { return __uint_as_float(v & 0xffff0000u); }
DI bf8 asbf8(u4 v) { return __builtin_bit_cast(bf8, v); }
DI f16v mfma32(bf8 a, bf8 b, f16v c) { return __builtin_amdgcn_mfma_f32_32x32x16_bf16(a, b, c, 0, 0, 0); }
DI f4 mfma16(bf8 a, bf8 b, f4 c) { return __builtin_amdgcn_mfma_f32_16x16x32_bf16(a, b, c, 0, 0, 0); }
DI f4 mfma16k16(u2 a, u2 b, f4 c) { return __builtin_amdgcn_mfma_f32_16x16x16bf16_1k(__builtin_bit_cast(s4, a), __builtin_bit_cast(s4, b), c, 0, 0, 0); }
DI bf8 ldsr(const char LAS* p) { return *(const LAS bf8*)p; }
DI bf8 ldstr(const char LAS* p0, const char LAS* p1) {
  s4 a = __builtin_amdgcn_ds_read_tr16_b64_v4i16((LAS s4*)p0), b = __builtin_amdgcn_ds_read_tr16_b64_v4i16((LAS s4*)p1);
  return __builtin_bit_cast(bf8, (s8)__builtin_shufflevector(a, b, 0, 1, 2, 3, 4, 5, 6, 7));
}
DI void dma4(unsigned lds, const GAS char* sb, unsigned v0, unsigned v1, unsigned v2, unsigned v3) {
  asm volatile("s_mov_b32 m0, %0\n\ts_nop 0\n\tglobal_load_lds_dwordx4 %1, %5\n\tglobal_load_lds_dwordx4 %2, %5 offset:1024\n\t"
               "global_load_lds_dwordx4 %3, %5 offset:2048\n\tglobal_load_lds_dwordx4 %4, %5 offset:3072"
               :: "s"(lds), "v"(v0), "v"(v1), "v"(v2), "v"(v3), "s"(sb) : "memory");
}
DI void dma2(unsigned lds, const GAS char* sb, unsigned v0, unsigned v1) {
  asm volatile("s_mov_b32 m0, %0\n\ts_nop 0\n\tglobal_load_lds_dwordx4 %1, %3\n\tglobal_load_lds_dwordx4 %2, %3 offset:1024"
               :: "s"(lds), "v"(v0), "v"(v1), "s"(sb) : "memory");
}
DI void lbar() { asm volatile("s_waitcnt lgkmcnt(0)\n\ts_barrier" ::: "memory"); }
template <int N> DI void wait_bar() { asm volatile("s_waitcnt vmcnt(%0) lgkmcnt(0)\n\ts_barrier" :: "i"(N) : "memory"); }
DI void drain() { asm volatile("s_waitcnt vmcnt(0)" ::: "memory"); }
DI unsigned tagv(float x, unsigned t) { return (__float_as_uint(x) & 0xffffff00u) | t; }
DI f2 cmul(f2 a, f2 b) { return (f2){a.x * b.x - a.y * b.y, a.x * b.y + a.y * b.x}; }
DI f2 cfma(f2 a, f2 b, f2 c) { return (f2){fmaf(a.x, b.x, fmaf(-a.y, b.y, c.x)), fmaf(a.x, b.y, fmaf(a.y, b.x, c.y))}; }
DI f2 fma2(f2 a, f2 b, f2 c) { return __builtin_elementwise_fma(a, b, c); }
constexpr float EPS = 1e-6f, LOG2E = 1.44269504088896341f;
DI unsigned phi2(float s0, float s1) {
  const f2 a = fma2((f2){s0, s1}, (f2){LOG2E, LOG2E}, (f2){-EPS * LOG2E, -EPS * LOG2E});
  const f2 e = {__builtin_amdgcn_fmed3f(__builtin_amdgcn_exp2f(a.x), 0.f, 1.f), __builtin_amdgcn_fmed3f(__builtin_amdgcn_exp2f(a.y), 0.f, 1.f)};
  return pk2(e + (f2){__builtin_amdgcn_fmed3f(s0, EPS, __builtin_inff()), __builtin_amdgcn_fmed3f(s1, EPS, __builtin_inff())});
}
DI f2 swap_lo(f2 v, f2& hi) {
  const auto x = __builtin_amdgcn_permlane32_swap(__float_as_uint(v.x), __float_as_uint(v.x), false, false);
  const auto y = __builtin_amdgcn_permlane32_swap(__float_as_uint(v.y), __float_as_uint(v.y), false, false);
  hi = (f2){__uint_as_float(x[1]), __uint_as_float(y[1])};
  return (f2){__uint_as_float(x[0]), __uint_as_float(y[0])};
}
DI void wait_flags(__amdgpu_buffer_rsrc_t ws, unsigned off, unsigned tag) {
#pragma unroll 1
  for (unsigned sp = 0; sp < SPIN_CAP; ++sp) {
      asm volatile("" ::: "memory");
    const u2 f = __builtin_amdgcn_raw_buffer_load_b64(ws, off, 0, SC1);
    if (__all(f.x == tag && f.y == ~tag)) break;
    __builtin_amdgcn_s_sleep(1);
  }
  asm volatile("" ::: "memory");
}

__global__ void __launch_bounds__(512, 2)
fused_kernel(const u16* a0, const u16* a1, const u16* a2, const u16* a3, const u16* a4, const u16* a5, const u16* a6, const u16* a7,
             const u16* a8, const u16* a9, const u16* a10, const u16* a11, float* a12, char* a13) {
  extern __shared__ __attribute__((aligned(16))) char smem_[];
  char LAS* L = (char LAS*)smem_;
  const unsigned L0 = (unsigned)(size_t)L;
  ST(0);
  typedef u64 u64x8 __attribute__((ext_vector_type(8)));
  typedef u64 u64x4 __attribute__((ext_vector_type(4)));
  typedef u64 u64x2 __attribute__((ext_vector_type(2)));
  u64x8 ka; u64x4 kb; u64x2 kc_;
  asm volatile("s_load_dwordx16 %0, %3, 0x0\n\ts_load_dwordx8 %1, %3, 0x40\n\ts_load_dwordx4 %2, %3, 0x60\n\ts_waitcnt lgkmcnt(0)"
               : "=&s"(ka), "=&s"(kb), "=&s"(kc_) : "s"(__builtin_amdgcn_kernarg_segment_ptr()) : "memory");
  const GAS u16 *xa = (const GAS u16*)ka[0], *wqkv = (const GAS u16*)ka[1], *wo = (const GAS u16*)ka[2], *xb = (const GAS u16*)ka[3];
  const GAS u16 *lre = (const GAS u16*)ka[4], *lim = (const GAS u16*)ka[5], *bre = (const GAS u16*)ka[6], *bim = (const GAS u16*)ka[7];
  const GAS u16 *cre = (const GAS u16*)kb[0], *cim = (const GAS u16*)kb[1], *ldt = (const GAS u16*)kb[2], *dsk = (const GAS u16*)kb[3];
  float* out = (float*)kc_[0]; char* wsp = (char*)kc_[1];
  const int lane = threadIdx.x & 63;
  const int w8 = __builtin_amdgcn_readfirstlane(threadIdx.x >> 6), w = w8 & 3;
  const bool helper = w8 >= 4;
  const int bid = blockIdx.x, xcd = bid & 7, jj = bid >> 3;
  const int tile = (xcd >> 1) * 4 + (jj >> 3), cg = jj & 7;
  const int rot = ROT == 1 ? ((cg + 3 * (jj >> 3) + xcd) & 7) : ROT == 2 ? cg : ROT == 3 ? 2 * (jj >> 3) : 0;
  const __amdgpu_buffer_rsrc_t ws = __builtin_amdgcn_make_buffer_rsrc(wsp, 0, WS_END, 0x00020000);
  const __amdgpu_buffer_rsrc_t ro = __builtin_amdgcn_make_buffer_rsrc(out, 0, 2048 * 1024 * 4, 0x00020000);
  const int r8 = lane >> 3, c8 = lane & 7;
  const int q = lane >> 4, l15 = lane & 15, l31 = lane & 31, hh = lane >> 5;
  unsigned epoch;
#define LOAD_EPOCH() asm volatile("s_load_dword %0, %1, 0x0" : "=s"(epoch) : "s"(wsp) : "memory")

  if ((xcd & 1) == 0) {
    constexpr unsigned SLOT = 40960, SCR = 32768;
    const int head = cg * 4 + w;
    if (helper) {
      unsigned vx[4], vw[2], vo[4];
#pragma unroll
      for (int i = 0; i < 4; ++i) vx[i] = (32 * w + 8 * i + r8) * 1024 + ((c8 ^ ((4 * i + (r8 >> 1)) & 7)) << 4) - 1024 * i;
#pragma unroll
      for (int i = 0; i < 2; ++i) vw[i] = (16 * w + 8 * i + r8) * 3072 + ((c8 ^ ((((r8 >> 1) & 1) | (i << 1)) << 1)) << 4) - 1024 * i;
      const GAS char* xbase = (const GAS char*)xa + (size_t)tile * 131072;
      const GAS char* wbase = (const GAS char*)wqkv + cg * 128;
      auto issue = [&](int kk) {
        const int kc = (kk + rot) & 7;
        const unsigned s = L0 + (kk & 3) * SLOT;
        dma4(s + w * 4096, xbase + kc * 128, vx[0], vx[1], vx[2], vx[3]);
        const GAS char* wb = wbase + kc * (64 * 3072);
        dma2(s + 16384 + w * 2048, wb, vw[0], vw[1]);
        dma2(s + 24576 + w * 2048, wb + 1024, vw[0], vw[1]);
        dma2(s + 32768 + w * 2048, wb + 2048, vw[0], vw[1]);
      };
      issue(0); issue(1);
      LOAD_EPOCH();
#pragma unroll
      for (int i = 0; i < 4; ++i) vo[i] = (8 * i + r8) * 1024 + ((c8 ^ (((r8 >> 1) & 3) << 1)) << 4) - 1024 * i;
      const GAS char* obase = (const GAS char*)wo + cg * 128;
      auto issue_wo = [&](int piece) {
#pragma unroll
        for (int h = 0; h < 2; ++h)
          dma4(L0 + piece * SLOT + w * 8192 + h * 4096, obase + ((8 * piece + 2 * w + h + 2 * cg) & 15) * 32768, vo[0], vo[1], vo[2], vo[3]);
      };
      asm volatile("s_waitcnt vmcnt(10) lgkmcnt(0)\n\ts_barrier" : "+s"(epoch) :: "memory");
      issue(2);
      wait_bar<10>(); issue(3);
      wait_bar<10>(); issue(4);
      wait_bar<10>(); issue(5);
      wait_bar<10>(); issue(6);
      wait_bar<10>(); issue(7);
      wait_bar<10>(); issue_wo(0);
      wait_bar<8>(); ST(1); issue_wo(1); ST(2);
      const unsigned tag = epoch + 1, t = tag & 0xffu;
      const GAS char* kvb_ = (const GAS char*)wsp + WS_KV + head * 16384;
      const unsigned vk0 = lane * 16, vk1 = vk0 + 4096, vk2 = vk0 + 8192, vk3 = vk0 + 12288;
      u4 g[16], gz;
#pragma unroll
      for (int i = 0; i < 16; ++i) g[i] = (u4){0, 0, 0, 0};
      f4 kv;
#pragma unroll 1
      for (unsigned rep = 0; rep < 64; ++rep) {
        unsigned have = 0;
#pragma unroll 1
        for (unsigned sp = 0; sp < SPIN_CAP; ++sp) {
      asm volatile("" ::: "memory");
          gz = __builtin_amdgcn_raw_buffer_load_b128(ws, WS_Z + head * 1024 + l15 * 64 + q * 16, 0, SC1);
          gz.x ^= t; gz.z ^= t;
          const u64 m = __ballot(((gz.x | gz.z) & 0xffu) == 0);
          const unsigned m2 = (unsigned)m & (unsigned)(m >> 32);
          const unsigned nw = m2 & (m2 >> 16) & 0xffffu & ~have;
#define KVLD(i, v) if (nw & (1u << i)) asm volatile("global_load_dwordx4 %0, %1, %2 offset:%3 sc1" : "+v"(g[i]) : "v"(v), "s"(kvb_), "i"((i & 3) * 1024) : "memory")
          KVLD(0, vk0); KVLD(1, vk0); KVLD(2, vk0); KVLD(3, vk0); KVLD(4, vk1); KVLD(5, vk1); KVLD(6, vk1); KVLD(7, vk1);
          KVLD(8, vk2); KVLD(9, vk2); KVLD(10, vk2); KVLD(11, vk2); KVLD(12, vk3); KVLD(13, vk3); KVLD(14, vk3); KVLD(15, vk3);
          have |= nw;
          if (have == 0xffffu) break;
          if (!nw) __builtin_amdgcn_s_sleep(1);
        }
        ST(4);
        asm volatile("s_waitcnt vmcnt(0)" : "+v"(g[0]), "+v"(g[1]), "+v"(g[2]), "+v"(g[3]), "+v"(g[4]), "+v"(g[5]), "+v"(g[6]), "+v"(g[7]),
                     "+v"(g[8]), "+v"(g[9]), "+v"(g[10]), "+v"(g[11]), "+v"(g[12]), "+v"(g[13]), "+v"(g[14]), "+v"(g[15]) :: "memory");
        ST(5);
        unsigned bad = 0;
        kv = (f4){0, 0, 0, 0};
#pragma unroll
        for (int i = 0; i < 16; ++i) {
          g[i].x ^= t; g[i].z ^= t;
          bad |= g[i].x | g[i].z;
          kv += __builtin_bit_cast(f4, g[i]);
        }
        if (__all((bad & 0xffu) == 0)) break;
      }
      f4 zz = __builtin_bit_cast(f4, gz);
#pragma unroll
      for (int r = 0; r < 4; ++r) {
        float v = zz[r];
        v += __builtin_bit_cast(float, __builtin_amdgcn_mov_dpp(__builtin_bit_cast(int, v), 0x128, 0xf, 0xf, true));
        v += __builtin_bit_cast(float, __builtin_amdgcn_mov_dpp(__builtin_bit_cast(int, v), 0x124, 0xf, 0xf, true));
        v += __builtin_bit_cast(float, __builtin_amdgcn_mov_dpp(__builtin_bit_cast(int, v), 0x122, 0xf, 0xf, true));
        v += __builtin_bit_cast(float, __builtin_amdgcn_mov_dpp(__builtin_bit_cast(int, v), 0x121, 0xf, 0xf, true));
        zz[r] = v;
      }
      *(LAS u4*)(L + SCR + w * 1024 + lane * 16) = (u4){pk2(kv[0], kv[1]), pk2(kv[2], kv[3]), pk2(zz[0], zz[1]), pk2(zz[2], zz[3])};
      ST(6);
      wait_bar<0>();
      ST(7); HFLUSH();
      if (bid == 0 && w == 0) {
#pragma unroll 1
        for (unsigned sp = 0; sp < SPIN_CAP; ++sp) {
      asm volatile("" ::: "memory");
          const u2 f0 = __builtin_amdgcn_raw_buffer_load_b64(ws, WS_FA + lane * 1024, 0, SC1);
          const u2 f1 = __builtin_amdgcn_raw_buffer_load_b64(ws, WS_FA + 512 + lane * 1024, 0, SC1);
          const u2 f2_ = __builtin_amdgcn_raw_buffer_load_b64(ws, WS_FB + (lane >> 3) * 4096 + (lane & 7) * 256, 0, SC1);
          const u2 f3 = __builtin_amdgcn_raw_buffer_load_b64(ws, WS_FB + 32768 + (lane >> 3) * 4096 + (lane & 7) * 256, 0, SC1);
          if (__all(f0.x == tag && f0.y == ~tag && f1.x == tag && f1.y == ~tag && f2_.x == tag && f2_.y == ~tag && f3.x == tag && f3.y == ~tag)) break;
          __builtin_amdgcn_s_sleep(8);
        }
        if (lane == 0) __builtin_amdgcn_raw_buffer_store_b32(tag, ws, WS_EPOCH, 0, SC1);
      }
      return;
    }
    LOAD_EPOCH();
    unsigned warm0 = 0, warm1 = 0;
    if (TOUCH) {
      warm0 = __builtin_amdgcn_raw_buffer_load_b32(ws, lane < 32 ? WS_FA + (tile * 32 + lane) * 128 : lane < 40 ? WS_Z + head * 1024 + (lane - 32) * 128
                                                   : lane == 40 ? WS_KV + head * 16384 : lane == 41 ? WS_O + tile * 131072 : 0x80000000u, 0, SC1);
      warm1 = __builtin_amdgcn_raw_buffer_load_b32(ro, lane < 2 ? (tile * 128 + w * 32 + lane * 16) * 4096 : 0x80000000u, 0, SC1);
    }
    const int qq = (lane >> 2) & 3, pp = lane & 3;
    const unsigned ax0 = l15 * 128 + ((q ^ (l15 >> 1)) << 4), ax1 = l15 * 128 + (((4 + q) ^ (l15 >> 1)) << 4);
    const unsigned aw = 16384 + (8 * q + qq) * 128 + (((2 * w + (pp >> 1)) ^ ((((qq >> 1) & 1) | ((q & 1) << 1)) << 1)) << 4) + 8 * (pp & 1);
    f4 ka[8], va[8], qa[8];
#pragma unroll
    for (int i = 0; i < 8; ++i) { ka[i] = (f4){EPS, EPS, EPS, EPS}; qa[i] = ka[i]; va[i] = (f4){0, 0, 0, 0}; }
    bf8 X0[4], X1[4], W0[3], W1[3];
    auto rdx = [&](bf8 (&X)[4], int kk, int ks, int h) {
#pragma unroll
      for (int i = 0; i < 4; ++i) X[i] = ldsr(L + (kk & 3) * SLOT + (ks ? ax1 : ax0) + (4 * h + i) * 2048);
    };
    auto rdw = [&](bf8 (&W)[3], int kk, int ks) {
#pragma unroll
      for (int i = 0; i < 3; ++i) { const char LAS* p = L + (kk & 3) * SLOT + aw + ks * 4096 + i * 8192; W[i] = ldstr(p, p + 512); }
    };
    auto mm = [&](const bf8 (&X)[4], const bf8 (&W)[3], int h) {
#pragma unroll
      for (int i = 0; i < 4; ++i) {
        ka[4 * h + i] = mfma16(X[i], W[1], ka[4 * h + i]);
        va[4 * h + i] = mfma16(X[i], W[2], va[4 * h + i]);
        qa[4 * h + i] = mfma16(W[0], X[i], qa[4 * h + i]);
      }
    };
#define ILV(nr, nm) do { _Pragma("unroll") for (int i_ = 0; i_ < nr; ++i_) { SG(0x008, 1); SG(0x100, 1); } SG(0x008, nm - nr); } while (0)
    asm volatile("s_waitcnt lgkmcnt(0)\n\ts_barrier" : "+s"(epoch) : "v"(warm0), "v"(warm1) : "memory");
    ST(1); CT(13);
    const unsigned tag = epoch + 1, t = tag & 0xffu;
    rdw(W0, 0, 0); rdx(X0, 0, 0, 0);
    SB();
#pragma unroll
    for (int kk = 0; kk < 8; ++kk) {
      rdx(X1, kk, 0, 1); mm(X0, W0, 0); ILV(4, 12); SB();
      rdw(W1, kk, 1); rdx(X0, kk, 1, 0); mm(X1, W0, 1); ILV(10, 12); SB();
      rdx(X1, kk, 1, 1); mm(X0, W1, 0); ILV(4, 12); SB();
      if (kk < 7) { lbar(); if (kk == 6) ST(2); rdw(W0, kk + 1, 0); rdx(X0, kk + 1, 0, 0); mm(X1, W1, 1); ILV(10, 12); }
      else mm(X1, W1, 1);
      SB();
    }
    ST(3); CT(14);
    {
      f4 kv = {0, 0, 0, 0}, zz = {0, 0, 0, 0};
      const u4 ones = {0x3f803f80u, 0x3f803f80u, 0x3f803f80u, 0x3f803f80u};
#pragma unroll
      for (int m = 0; m < 4; ++m) {
        const f4 a = ka[2 * m], b = ka[2 * m + 1], c = va[2 * m], d = va[2 * m + 1];
        const u4 pa = {phi2(a[0], a[1]), phi2(a[2], a[3]), phi2(b[0], b[1]), phi2(b[2], b[3])};
        const u4 pb = {pk2(c[0], c[1]), pk2(c[2], c[3]), pk2(d[0], d[1]), pk2(d[2], d[3])};
        kv = mfma16(asbf8(pa), asbf8(pb), kv);
        zz = mfma16(asbf8(pa), asbf8(ones), zz);
      }
      const u4 skv = {tagv(kv[0], t), __float_as_uint(kv[1]), tagv(kv[2], t), __float_as_uint(kv[3])};
      __builtin_amdgcn_raw_buffer_store_b128(skv, ws, WS_KV + (head * 16 + tile) * 1024 + lane * 16, 0, SC1);
      const u4 sz = {tagv(zz[0], t), __float_as_uint(zz[1]), tagv(zz[2], t), __float_as_uint(zz[3])};
      __builtin_amdgcn_raw_buffer_store_b128(sz, ws, l15 == 0 ? WS_Z + (head * 16 + tile) * 64 + q * 16 : 0x80000000u, 0, SC1);
    }
    SB();
    ST(4);
    u2 pq[8];
#pragma unroll
    for (int mt = 0; mt < 8; ++mt) pq[mt] = (u2){phi2(qa[mt][0], qa[mt][1]), phi2(qa[mt][2], qa[mt][3])};
    unsigned ao[4];
#pragma unroll
    for (int nt = 0; nt < 4; ++nt) ao[nt] = (4 * q + qq) * 128 + ((((2 * nt) ^ (((2 * q + (qq >> 1)) & 3) << 1)) + (pp >> 1)) << 4) + 8 * (pp & 1);
    asm volatile("s_barrier" : "+v"(pq[0]), "+v"(pq[1]), "+v"(pq[2]), "+v"(pq[3]), "+v"(pq[4]), "+v"(pq[5]), "+v"(pq[6]), "+v"(pq[7]) :: "memory");
    ST(5);
    const u4 kz = *(const LAS u4*)(L + SCR + w * 1024 + lane * 16);
    const u2 kvb = {kz.x, kz.y}, zb = {kz.z, kz.w};
    const f4 zero4 = {0, 0, 0, 0}, eps4 = {EPS, EPS, EPS, EPS};
    f4 num[8], den[8];
#pragma unroll
    for (int mt = 0; mt < 8; ++mt) { den[mt] = mfma16k16(zb, pq[mt], eps4); num[mt] = mfma16k16(kvb, pq[mt], zero4); }
    SB();
#pragma unroll
    for (int u = 0; u < 4; ++u) {
      u4 so;
#pragma unroll
      for (int h = 0; h < 2; ++h) {
        const float rc = __builtin_amdgcn_rcpf(den[2 * u + h][0]);
        const f4 n = num[2 * u + h];
        so[2 * h] = pk2((f2){n[0], n[1]} * rc);
        so[2 * h + 1] = pk2((f2){n[2], n[3]} * rc);
      }
      __builtin_amdgcn_raw_buffer_store_b128(so, ws, WS_O + ((tile * 4 + u) * 32 + head) * 1024 + lane * 16, 0, SC1);
    }
    ST(6);
    drain();
    __builtin_amdgcn_raw_buffer_store_b64((u2){tag, ~tag}, ws, lane == 0 ? WS_FA + (tile * 32 + head) * 128 : 0x80000000u, 0, SC1);
    unsigned pull = 0;
    if (PULL) pull = __builtin_amdgcn_raw_buffer_load_b32(ws, lane < 32 ? WS_O + ((tile * 4 + (lane >> 3)) * 32 + head) * 1024 + (lane & 7) * 128 : 0x80000000u, 0, SC1);
    unsigned tw[4], trd[4], so_ = ((tile * 128 + w * 32 + q) * 1024 + cg * 64 + l15 * 4) * 4;
#pragma unroll
    for (int i = 0; i < 4; ++i) {
      tw[i] = 2 * SLOT + w * 8192 + l15 * 256 + (((4 * i + q) ^ l15) << 4);
      trd[i] = 2 * SLOT + w * 8192 + (4 * i + q) * 256 + ((l15 ^ (4 * i + q)) << 4);
      asm volatile("" : "+v"(tw[i]), "+v"(trd[i]));
    }
    asm volatile("" : "+v"(so_));
    ST(7);
    f4 acc[2][4];
#pragma unroll
    for (int i = 0; i < 8; ++i) acc[i >> 2][i & 3] = zero4;
    const unsigned ob = WS_O + (tile * 4 + w) * 32768 + lane * 16;
    u4 of[32];
    bf8 wf[2][4];
    auto rdo = [&](int k) {
      const char LAS* base = L + (k >> 3) * SLOT + (k & 7) * 4096;
#pragma unroll
      for (int nt = 0; nt < 4; ++nt) wf[k & 1][nt] = ldstr(base + ao[nt], base + ao[nt] + 2048);
    };
    auto ldo = [&](int k) {
      const unsigned so = ((k + 2 * cg) & 15) * 2048;
      of[2 * k] = __builtin_amdgcn_raw_buffer_load_b128(ws, ob, so, PAY_AUX);
      of[2 * k + 1] = __builtin_amdgcn_raw_buffer_load_b128(ws, ob + 1024, so, PAY_AUX);
    };
    rdo(0);
    wait_flags(ws, WS_FA + (tile * 32 + l31) * 128, tag);
    asm volatile("" :: "v"(pull));
    ST(8);
#pragma unroll
    for (int k = 0; k < OD; ++k) ldo(k);
    SB();
#pragma unroll
    for (int k = 0; k < 16; ++k) {
      if (k + OD < 16) ldo(k + OD);
      if (k + 1 < 16) rdo(k + 1);
      const u4 a = of[2 * k], b = of[2 * k + 1];
      const bf8 f0 = asbf8((u4){a.x, a.y, b.x, b.y}), f1 = asbf8((u4){a.z, a.w, b.z, b.w});
#pragma unroll
      for (int nt = 0; nt < 4; ++nt) {
        acc[0][nt] = mfma16(wf[k & 1][nt], f0, acc[0][nt]);
        acc[1][nt] = mfma16(wf[k & 1][nt], f1, acc[1][nt]);
      }
      SG(0x002, 4);
#pragma unroll
      for (int i_ = 0; i_ < 8; ++i_) { SG(0x008, 1); SG(0x100, 1); if (i_ == 1 || i_ == 5) SG(0x020, 1); }
      SB();
    }
    ST(9); CT(15);
#pragma unroll
    for (int mt = 0; mt < 2; ++mt)
#pragma unroll
      for (int nt = 0; nt < 4; ++nt) {
        const f4 v = acc[mt][nt];
        const unsigned p0 = pk2(v[0], v[1]), p1 = pk2(v[2], v[3]);
        *(LAS f4*)(L + tw[nt] + mt * 4096) = (f4){bflo(p0), bfhi(p0), bflo(p1), bfhi(p1)};
      }
    u4 ov[8];
#pragma unroll
    for (int i = 0; i < 8; ++i) ov[i] = *(const LAS u4*)(L + trd[i & 3] + (i >> 2) * 4096);
    SB();
#pragma unroll
    for (int i = 0; i < 8; ++i) __builtin_amdgcn_raw_buffer_store_b128(ov[i], ro, so_, i * 16384, OUT_AUX);
    ST(10);
  } else {
    constexpr unsigned SLOT = 32768, SCR = SLOT, PAR = 4 * SLOT;
    const int crot = cg + 2 * (jj >> 3);
    const int wp = w & 1, wt = w >> 1;
    const unsigned ecol = WS_EST + (cg * 64 + wp * 32 + l31) * 8;
    if (helper) {
      unsigned vx[4], vw[4], vc[4];
#pragma unroll
      for (int i = 0; i < 4; ++i) {
        const unsigned sw = (c8 ^ ((4 * i + (r8 >> 1)) & 7)) << 4;
        vx[i] = (64 * (w >> 1) + 32 * (r8 >> 2) + 16 * (r8 & 1) + 8 * (w & 1) + ((r8 >> 1) & 1) + 2 * i) * 1024 + sw - 1024 * i;
        vw[i] = (16 * (i >> 1) + 8 * (r8 >> 2) + 4 * (i & 1) + (r8 & 3)) * 1024 + sw - 1024 * i;
        vc[i] = (8 * i + r8) * 1024 + sw - 1024 * i;
      }
      const GAS char* xbase = (const GAS char*)xb + (size_t)tile * 131072;
      const GAS char* wbase = (const GAS char*)((w & 1) ? bim : bre) + (cg * 64 + (w >> 1) * 32) * 1024;
      auto issue = [&](int kk) {
        const int kc = (kk + rot) & 7;
        const unsigned s = L0 + (kk % 5) * SLOT + w * 4096;
        dma4(s, xbase + kc * 128, vx[0], vx[1], vx[2], vx[3]);
        dma4(s + 16384, wbase + kc * 128, vw[0], vw[1], vw[2], vw[3]);
      };
      issue(0); issue(1);
      LOAD_EPOCH();
      const GAS char* cbase = (const GAS char*)((w & 1) ? cim : cre) + cg * 65536;
      auto issue_c = [&](int piece) {
        const unsigned s = L0 + ((piece + 3) % 5) * SLOT + w * 8192;
        const GAS char* cb = cbase + ((crot + 2 * piece + (w >> 1)) & 7) * 128;
        dma4(s, cb, vc[0], vc[1], vc[2], vc[3]);
        dma4(s + 4096, cb + 32768, vc[0], vc[1], vc[2], vc[3]);
      };
      asm volatile("s_waitcnt vmcnt(8) lgkmcnt(0)\n\ts_barrier" : "+s"(epoch) :: "memory");
      issue(2);
      const f4 par = *(const LAS f4*)(L + PAR + w * 1024 + lane * 16);
      const f2 l64 = {par[0], par[1]}, l128 = {par[2], par[3]};
      f2 l256 = cmul(l128, l128);
      f2 wgt = hh ? l64 : (f2){1.f, 0.f};
      if (wt) wgt = cmul(wgt, l128);
      const unsigned tag = epoch + 1, t = tag & 0xffu;
      unsigned eo[8], et[8];
#pragma unroll
      for (int m = 0; m < 8; ++m) {
        const int i = 2 * tile - 1 - (2 * wt + hh) - 4 * m;
        eo[m] = i >= 0 ? ecol + i * 4096 : 0x80000000u;
        et[m] = i >= 0 ? t : 0u;
        asm volatile("" : "+v"(eo[m]), "+v"(et[m]));
      }
      asm volatile("" : "+v"(l256), "+v"(wgt));
      wait_bar<8>(); issue(3);
      wait_bar<8>(); issue(4);
      wait_bar<8>(); issue(5);
      wait_bar<8>(); issue(6);
      wait_bar<8>(); issue(7);
      wait_bar<8>(); issue_c(0);
      wait_bar<8>(); ST(1);
      if (NC_EARLY > 1) issue_c(1);
      if (NC_EARLY > 2) issue_c(2);
      ST(2);
      f2 cin;
#pragma unroll 1
      for (unsigned sp = 0; sp < SPIN_CAP; ++sp) {
      asm volatile("" ::: "memory");
        u2 g[8];
#pragma unroll
        for (int m = 0; m < 8; ++m) g[m] = __builtin_amdgcn_raw_buffer_load_b64(ws, eo[m], 0, SC1);
        unsigned bad = 0;
        cin = (f2){0, 0};
#pragma unroll
        for (int m = 7; m >= 0; --m) {
          const unsigned cx = g[m].x ^ et[m];
          bad |= cx;
          cin = cfma(l256, cin, (f2){__uint_as_float(cx), __uint_as_float(g[m].y)});
        }
        if (__all((bad & 0xffu) == 0)) break;
      }
      ST(5);
      cin = cmul(cin, wgt);
      f2 chi;
      const f2 clo = swap_lo(cin, chi);
      if (hh == 0) *(LAS f2*)(L + SCR + (wp * 2 + wt) * 256 + l31 * 8) = clo + chi;
      ST(6);
      lbar();
      lbar();
      if (NC_EARLY <= 1) issue_c(1);
      if (NC_EARLY <= 2) issue_c(2);
      issue_c(3);
      ST(7);
      wait_bar<0>();
      ST(8); HFLUSH();
      return;
    }
    LOAD_EPOCH();
    const int pl = cg * 64 + wp * 32 + ((l31 & 0x13) | ((l31 & 4) << 1) | ((l31 & 8) >> 1));
    const unsigned plr = lre[pl], pli = lim[pl], pdt = ldt[pl];
    unsigned warm0 = 0, warm1 = 0;
    if (TOUCH) {
      warm0 = __builtin_amdgcn_raw_buffer_load_b32(ws, lane < 16 ? WS_FB + ((tile * 2 + (w >> 1)) * 16 + lane) * 128 : lane == 16 ? WS_EST + tile * 8192
                                                   : lane == 17 ? WS_H + (tile * 4 + w) * 65536 : 0x80000000u, 0, SC1);
      warm1 = __builtin_amdgcn_raw_buffer_load_b32(ro, lane < 2 ? (tile * 128 + w * 32 + lane * 16) * 4096 + 2048 : 0x80000000u, 0, SC1);
    }
    auto tokr = [&](int r) { return tile * 128 + 64 * (w >> 1) + 32 * ((r >> 2) & 1) + 16 * (r & 1) + 8 * (w & 1) + ((r >> 1) & 1) + 2 * (r >> 3); };
    u2 xd[8];
    {
      const u2 dv = *(const GAS u2*)(dsk + cg * 64 + l15 * 4);
#pragma unroll
      for (int i = 0; i < 8; ++i) {
        const u2 xv = *(const GAS u2*)(xb + tokr(4 * i + q) * 512 + cg * 64 + l15 * 4);
        xd[i] = (u2){pk2(bflo(xv.x) * bflo(dv.x), bfhi(xv.x) * bfhi(dv.x)), pk2(bflo(xv.y) * bflo(dv.y), bfhi(xv.y) * bfhi(dv.y))};
      }
    }
    f2 lam, kap, l16, l32, l64, l128, pw[16];
    {
      const float lr = bflo(plr), li = bflo(pli);
      const float dt = bflo(pk2(expf(bflo(pdt)), 0.f));
      const float mag = expf(dt * lr);
      float sn, cs;
      sincosf(dt * li, &sn, &cs);
      lam = (f2){mag * cs, mag * sn};
      const float inv = 2.f / (lr * lr + li * li), nr = lam.x - 1.f, ni = lam.y;
      kap = (f2){(nr * lr + ni * li) * inv, (ni * lr - nr * li) * inv};
      pw[0] = (f2){lam.x, -lam.y};
#pragma unroll
      for (int k = 1; k < 16; ++k) { pw[k] = cmul(pw[k >> 1], pw[(k - 1) >> 1]); asm volatile("" : "+v"(pw[k])); }
      l16 = (f2){pw[15].x, -pw[15].y}; l32 = cmul(l16, l16); l64 = cmul(l32, l32); l128 = cmul(l64, l64);
      *(LAS f4*)(L + PAR + w * 1024 + lane * 16) = (f4){l64.x, l64.y, l128.x, l128.y};
    }
    unsigned ar[4];
#pragma unroll
    for (int s = 0; s < 4; ++s) ar[s] = l31 * 128 + (((2 * s + hh) ^ ((lane >> 1) & 7)) << 4);
    u4 idn[2];
#pragma unroll
    for (int s = 0; s < 2; ++s)
#pragma unroll
      for (int jq = 0; jq < 4; ++jq) {
        const int r0 = 16 * s + 8 * (jq >> 1) + 4 * hh + 2 * (jq & 1);
        idn[s][jq] = (l31 == r0 ? 0x3f80u : 0u) | (l31 == r0 + 1 ? 0x3f800000u : 0u);
      }
    f16v ur[2], ui[2];
#pragma unroll
    for (int i = 0; i < 16; ++i) { ur[0][i] = 0; ur[1][i] = 0; ui[0][i] = 0; ui[1][i] = 0; }
    bf8 F0[4], F1[4];
    auto rd = [&](bf8 (&F)[4], int kk, int s) {
      const char LAS* sx = L + (kk % 5) * SLOT + wt * 8192 + ar[s];
      const char LAS* sw = L + (kk % 5) * SLOT + 16384 + wp * 8192 + ar[s];
      F[0] = ldsr(sx); F[1] = ldsr(sw); F[2] = ldsr(sw + 4096); F[3] = ldsr(sx + 4096);
    };
    auto mm = [&](const bf8 (&F)[4]) {
      ur[0] = mfma32(F[0], F[1], ur[0]); ui[0] = mfma32(F[0], F[2], ui[0]);
      ur[1] = mfma32(F[3], F[1], ur[1]); ui[1] = mfma32(F[3], F[2], ui[1]);
    };
#define ILB() do { SG(0x008, 1); SG(0x100, 2); SG(0x008, 1); SG(0x100, 2); SG(0x008, 2); } while (0)
    asm volatile("s_waitcnt lgkmcnt(0)\n\ts_barrier" : "+s"(epoch), "+v"(lam), "+v"(kap), "+v"(l16), "+v"(l32), "+v"(l64), "+v"(idn[0]), "+v"(idn[1]),
                 "+v"(xd[0]), "+v"(xd[1]), "+v"(xd[2]), "+v"(xd[3]), "+v"(xd[4]), "+v"(xd[5]), "+v"(xd[6]), "+v"(xd[7]) : "v"(warm0), "v"(warm1) : "memory");
    ST(1); CT(13);
    const unsigned tag = epoch + 1, t = tag & 0xffu;
    rd(F0, 0, 0);
    SB();
#pragma unroll
    for (int kk = 0; kk < 8; ++kk) {
      rd(F1, kk, 1); mm(F0); ILB(); SB();
      rd(F0, kk, 2); mm(F1); ILB(); SB();
      rd(F1, kk, 3); mm(F0); ILB(); SB();
      if (kk < 7) { lbar(); if (kk == 6) ST(2); rd(F0, kk + 1, 0); mm(F1); ILB(); }
      else mm(F1);
      SB();
    }
    ST(3); CT(14);
    f2 gr[16], gi[16];
#pragma unroll
    for (int a = 0; a < 16; ++a) {
      gr[a] = (f2){ur[a >> 3][2 * (a & 7)], ur[a >> 3][2 * (a & 7) + 1]};
      gi[a] = (f2){ui[a >> 3][2 * (a & 7)], ui[a >> 3][2 * (a & 7) + 1]};
    }
    {
      f2 LR = {lam.x, lam.x}, LI = {lam.y, lam.y}, LN = {-lam.y, -lam.y};
      asm volatile("s_nop 11" : "+v"(gr[0]), "+v"(gi[0]), "+v"(gr[8]), "+v"(gi[8]), "+v"(LR), "+v"(LI), "+v"(LN));
#pragma unroll
      for (int a = 1; a < 16; ++a)
        asm volatile("v_pk_fma_f32 %0, %6, %3, %0\n\tv_pk_fma_f32 %1, %5, %2, %1\n\tv_pk_fma_f32 %0, %4, %2, %0\n\tv_pk_fma_f32 %1, %4, %3, %1"
                     : "+v"(gr[a]), "+v"(gi[a]) : "v"(gr[a - 1]), "v"(gi[a - 1]), "v"(LR), "v"(LI), "v"(LN));
    }
    const f2 er = gr[15], ei = gi[15];
    const f2 e0 = {er.x, ei.x};
    const f2 e32 = cfma(l16, e0, (f2){er.y, ei.y});
    f2 ehi;
    const f2 elo = swap_lo(e32, ehi);
    const f2 e64 = cfma(l32, elo, ehi);
    __builtin_amdgcn_raw_buffer_store_b64((u2){tagv(e64.x, t), __float_as_uint(e64.y)}, ws, hh == 0 ? ecol + (tile * 2 + wt) * 4096 : 0x80000000u, 0, SC1);
    if (wt == 0 && hh == 0) *(LAS f2*)(L + SCR + 1024 + wp * 256 + l31 * 8) = e64;
    SB();
    ST(4);
    {
      const f2 KR = {kap.x, kap.x}, KI = {kap.y, kap.y};
#pragma unroll
      for (int a = 0; a < 16; ++a) {
        const f2 xr = gr[a], xi = gi[a];
        gr[a] = fma2(KR, xr, -KI * xi); gi[a] = fma2(-KR, xi, -KI * xr);
        asm volatile("" : "+v"(gr[a]), "+v"(gi[a]));
      }
    }
    f2 m0 = hh ? cmul(kap, l32) : kap, b0 = hh ? cmul(kap, elo) : (f2){0.f, 0.f};
    f2 m1 = cmul(l16, m0), b1 = cfma(l16, b0, cmul(kap, e0));
    unsigned ho = WS_H + (tile * 4 + wt * 2) * 65536 + (cg * 2 + wp) * 4096 + lane * 16;
    asm volatile("s_waitcnt lgkmcnt(0)\n\ts_barrier" : "+v"(ho), "+v"(m0), "+v"(b0), "+v"(m1), "+v"(b1) :: "memory");
    ST(5);
    f2 cin = *(const LAS f2*)(L + SCR + wp * 512 + l31 * 8) + *(const LAS f2*)(L + SCR + wp * 512 + 256 + l31 * 8);
    const f2 esib = *(const LAS f2*)(L + SCR + 1024 + wp * 256 + l31 * 8);
    lbar();
    if (wt) cin = cfma(l64, cin, esib);
    const f2 d0 = cfma(m0, cin, b0), d1 = cfma(m1, cin, b1);
    const f2 DR = {d0.x, d1.x}, DJ = {-d0.y, -d1.y};
    f16v zero16;
#pragma unroll
    for (int i = 0; i < 16; ++i) zero16[i] = 0;
    auto fix = [&](int m, int c) {
      u4 a[2];
#pragma unroll
      for (int i = 0; i < 8; ++i) {
        const f2 P = pw[8 * m + i], PR = {P.x, P.x}, PI = {P.y, P.y};
        const f2 h = c ? fma2(PI, DR, fma2(PR, DJ, gi[8 * m + i])) : fma2(-PI, DJ, fma2(PR, DR, gr[8 * m + i]));
        a[i >> 2][i & 3] = pk2(h);
      }
      return mfma32(asbf8(a[1]), asbf8(idn[1]), mfma32(asbf8(a[0]), asbf8(idn[0]), zero16));
    };
    auto put = [&](const f16v& T, int m, int c) {
      const u4 o0 = {pk2(T[0], T[1]), pk2(T[2], T[3]), pk2(T[4], T[5]), pk2(T[6], T[7])};
      const u4 o1 = {pk2(T[8], T[9]), pk2(T[10], T[11]), pk2(T[12], T[13]), pk2(T[14], T[15])};
      __builtin_amdgcn_raw_buffer_store_b128(o0, ws, ho + c * 2048, m * 65536, SC1);
      __builtin_amdgcn_raw_buffer_store_b128(o1, ws, ho + c * 2048 + 1024, m * 65536, SC1);
    };
    {
      const f16v T0 = fix(0, 0); SB();
      const f16v T1 = fix(0, 1); SB();
      put(T0, 0, 0);
      const f16v T2 = fix(1, 0); SB();
      put(T1, 0, 1);
      const f16v T3 = fix(1, 1); SB();
      put(T2, 1, 0); SB();
      put(T3, 1, 1);
    }
    ST(6);
    drain();
    __builtin_amdgcn_raw_buffer_store_b64((u2){tag, ~tag}, ws, lane == 0 ? WS_FB + ((tile * 2 + wt) * 16 + cg * 2 + wp) * 128 : 0x80000000u, 0, SC1);
    unsigned pull = 0;
    if (PULL) pull = __builtin_amdgcn_raw_buffer_load_b32(ws, WS_H + (tile * 4 + wt * 2 + hh) * 65536 + (cg * 2 + wp) * 4096 + l31 * 128, 0, SC1);
    unsigned tw[8], trd[4], so_ = ((tile * 128 + 64 * (w >> 1) + 8 * (w & 1) + 16 * (q & 1) + (q >> 1)) * 1024 + 512 + cg * 64 + l15 * 4) * 4;
#pragma unroll
    for (int i = 0; i < 8; ++i) { tw[i] = 2 * SLOT + w * 8192 + l31 * 256 + (((2 * i + hh) ^ l15) << 4); asm volatile("" : "+v"(tw[i])); }
#pragma unroll
    for (int i = 0; i < 4; ++i) { trd[i] = 2 * SLOT + w * 8192 + (4 * i + q) * 256 + ((l15 ^ (4 * i + q)) << 4); asm volatile("" : "+v"(trd[i])); }
    asm volatile("" : "+v"(so_));
#pragma unroll
    for (int i = 0; i < 8; ++i) *(LAS f4*)(L + trd[i & 3] + (i >> 2) * 4096) = (f4){bflo(xd[i].x), bfhi(xd[i].x), bflo(xd[i].y), bfhi(xd[i].y)};
    f16v acc[2];
#pragma unroll
    for (int i = 0; i < 8; ++i) {
      const f4 v = *(const LAS f4*)(L + tw[i]);
#pragma unroll
      for (int j = 0; j < 4; ++j) acc[i >> 2][4 * (i & 3) + j] = v[j];
    }
    asm volatile("s_barrier" : "+v"(acc[0]), "+v"(acc[1]) :: "memory");
    ST(7);
    const unsigned hb = WS_H + (tile * 4 + w) * 65536 + lane * 16;
    u4 hf[64];
    bf8 cf[2][4];
    auto ldh = [&](int i) { hf[i] = __builtin_amdgcn_raw_buffer_load_b128(ws, hb + (i & 7) * 1024, ((crot + (i >> 3)) & 7) * 8192, PAY_AUX); };
    auto rdc = [&](int g) {
      const int tt = g >> 2, i = (g & 3) * 2;
      const char LAS* cb = L + (((tt >> 1) + 3) % 5) * SLOT + (tt & 1) * 16384 + ((i >> 1) & 1) * 8192;
      cf[g & 1][0] = ldsr(cb + ar[(i >> 2) * 2]); cf[g & 1][1] = ldsr(cb + ar[(i >> 2) * 2] + 4096);
      cf[g & 1][2] = ldsr(cb + ar[(i >> 2) * 2 + 1]); cf[g & 1][3] = ldsr(cb + ar[(i >> 2) * 2 + 1] + 4096);
    };
    rdc(0);
    wait_flags(ws, WS_FB + ((tile * 2 + (w >> 1)) * 16 + l15) * 128, tag);
    asm volatile("" :: "v"(pull));
    ST(8);
#pragma unroll
    for (int i = 0; i < HD; ++i) ldh(i);
    SB();
#pragma unroll
    for (int g = 0; g < 32; ++g) {
      if (2 * g + HD < 64) { ldh(2 * g + HD); ldh(2 * g + HD + 1); }
      if (g + 1 < 32) rdc(g + 1);
      acc[0] = mfma32(cf[g & 1][0], asbf8(hf[2 * g]), acc[0]);
      acc[1] = mfma32(cf[g & 1][1], asbf8(hf[2 * g]), acc[1]);
      acc[0] = mfma32(cf[g & 1][2], asbf8(hf[2 * g + 1]), acc[0]);
      acc[1] = mfma32(cf[g & 1][3], asbf8(hf[2 * g + 1]), acc[1]);
      SG(0x008, 1); SG(0x100, 2); SG(0x008, 1); SG(0x100, 2); SG(0x008, 1); SG(0x020, 1); SG(0x008, 1); SG(0x020, 1);
      SB();
    }
    ST(9); CT(15);
#pragma unroll
    for (int i = 0; i < 8; ++i)
      *(LAS f4*)(L + tw[i]) = (f4){acc[i >> 2][4 * (i & 3)], acc[i >> 2][4 * (i & 3) + 1], acc[i >> 2][4 * (i & 3) + 2], acc[i >> 2][4 * (i & 3) + 3]};
    u4 ov[8];
#pragma unroll
    for (int i = 0; i < 8; ++i) ov[i] = *(const LAS u4*)(L + trd[i & 3] + (i >> 2) * 4096);
    SB();
#pragma unroll
    for (int i = 0; i < 8; ++i) __builtin_amdgcn_raw_buffer_store_b128(ov[i], ro, so_, (32 * (i & 1) + 2 * (i >> 1)) * 4096, OUT_AUX);
    ST(10);
  }
}

extern "C" void kernel_launch(void* const* d_in, const int* in_sizes, int n_in, void* d_out, int out_size, void* d_ws, size_t ws_size,
                              hipStream_t stream) {
  (void)hipFuncSetAttribute((const void*)fused_kernel, hipFuncAttributeMaxDynamicSharedMemorySize, LDS_BYTES);
  fused_kernel<<<256, 512, LDS_BYTES, stream>>>((const u16*)d_in[0], (const u16*)d_in[1], (const u16*)d_in[2], (const u16*)d_in[3],
                                               (const u16*)d_in[4], (const u16*)d_in[5], (const u16*)d_in[6], (const u16*)d_in[7],
                                               (const u16*)d_in[8], (const u16*)d_in[9], (const u16*)d_in[10], (const u16*)d_in[11],
                                               (float*)d_out, (char*)d_ws);
}
```

```cpp
#define PULL 0
#define AMP_A 0
#define AMP_B 0
#define AMP_BID 255
#define AMP_K 0
#include <hip/hip_runtime.h>
#include <hip/hip_bf16.h>

#define DI __device__ __forceinline__
#define LAS __attribute__((address_space(3)))
#define GAS __attribute__((address_space(1)))
typedef unsigned short u16;
typedef unsigned long long u64;
typedef __bf16 bf8 __attribute__((ext_vector_type(8)));
typedef __bf16 bf2 __attribute__((ext_vector_type(2)));
typedef short s4 __attribute__((ext_vector_type(4)));
typedef short s8 __attribute__((ext_vector_type(8)));
typedef float f16v __attribute__((ext_vector_type(16)));
typedef float f4 __attribute__((ext_vector_type(4)));
typedef float f2 __attribute__((ext_vector_type(2)));
typedef unsigned u4 __attribute__((ext_vector_type(4)));
typedef unsigned u2 __attribute__((ext_vector_type(2)));

constexpr unsigned WS_EPOCH = 0;
constexpr unsigned WS_FA = 4096;
constexpr unsigned WS_FB = WS_FA + 65536;
constexpr unsigned WS_EST = WS_FB + 65536;
constexpr unsigned WS_Z = WS_EST + 131072;
constexpr unsigned WS_KV = WS_Z + 32768;
constexpr unsigned WS_O = 1u << 20;
constexpr unsigned WS_H = 3u << 20;
constexpr unsigned WS_END = 7u << 20;
constexpr unsigned WS_STAMP = 8u << 20;
constexpr int LDS_BYTES = 160 * 1024;
constexpr unsigned SPIN_CAP = 200000;
constexpr int SC1 = 16;

#define ROT 1
#define NC_EARLY 3
#define OUT_AUX 16
#define PAY_AUX 16
#define TOUCH 1
#define OD 6
#define HD 20

#define ST(i) do { __builtin_amdgcn_sched_barrier(0); asm volatile("s_memrealtime %0\n\ts_waitcnt lgkmcnt(0)" : "=s"(st[i]) :: "memory"); __builtin_amdgcn_sched_barrier(0); } while (0)
#define CT(i) do { __builtin_amdgcn_sched_barrier(0); asm volatile("s_memtime %0\n\ts_waitcnt lgkmcnt(0)" : "=s"(st[i]) :: "memory"); __builtin_amdgcn_sched_barrier(0); } while (0)
#define HFLUSH() do {} while (0)
#define SB() __builtin_amdgcn_sched_barrier(0)
#define SG(m, n) __builtin_amdgcn_sched_group_barrier(m, n, 0)

DI unsigned pk2(float a, float b) { f2 v = {a, b}; return __builtin_bit_cast(unsigned, __builtin_convertvector(v, bf2)); }
DI unsigned pk2(f2 v) { return __builtin_bit_cast(unsigned, __builtin_convertvector(v, bf2)); }
DI float bflo(unsigned v) { return __uint_as_float(v << 16); }
DI float bfhi(unsigned v) { return __uint_as_float(v & 0xffff0000u); }
DI bf8 asbf8(u4 v) { return __builtin_bit_cast(bf8, v); }
DI f16v mfma32(bf8 a, bf8 b, f16v c) { return __builtin_amdgcn_mfma_f32_32x32x16_bf16(a, b, c, 0, 0, 0); }
DI f4 mfma16(bf8 a, bf8 b, f4 c) { return __builtin_amdgcn_mfma_f32_16x16x32_bf16(a, b, c, 0, 0, 0); }
DI f4 mfma16k16(u2 a, u2 b, f4 c) { return __builtin_amdgcn_mfma_f32_16x16x16bf16_1k(__builtin_bit_cast(s4, a), __builtin_bit_cast(s4, b), c, 0, 0, 0); }
DI bf8 ldsr(const char LAS* p) { return *(const LAS bf8*)p; }
DI bf8 ldstr(const char LAS* p0, const char LAS* p1) {
  s4 a = __builtin_amdgcn_ds_read_tr16_b64_v4i16((LAS s4*)p0), b = __builtin_amdgcn_ds_read_tr16_b64_v4i16((LAS s4*)p1);
  return __builtin_bit_cast(bf8, (s8)__builtin_shufflevector(a, b, 0, 1, 2, 3, 4, 5, 6, 7));
}
DI void dma4(unsigned lds, const GAS char* sb, unsigned v0, unsigned v1, unsigned v2, unsigned v3) {
  asm volatile("s_mov_b32 m0, %0\n\ts_nop 0\n\tglobal_load_lds_dwordx4 %1, %5\n\tglobal_load_lds_dwordx4 %2, %5 offset:1024\n\t"
               "global_load_lds_dwordx4 %3, %5 offset:2048\n\tglobal_load_lds_dwordx4 %4, %5 offset:3072"
               :: "s"(lds), "v"(v0), "v"(v1), "v"(v2), "v"(v3), "s"(sb) : "memory");
}
DI void dma2(unsigned lds, const GAS char* sb, unsigned v0, unsigned v1) {
  asm volatile("s_mov_b32 m0, %0\n\ts_nop 0\n\tglobal_load_lds_dwordx4 %1, %3\n\tglobal_load_lds_dwordx4 %2, %3 offset:1024"
               :: "s"(lds), "v"(v0), "v"(v1), "s"(sb) : "memory");
}
DI void lbar() { asm volatile("s_waitcnt lgkmcnt(0)\n\ts_barrier" ::: "memory"); }
template <int N> DI void wait_bar() { asm volatile("s_waitcnt vmcnt(%0) lgkmcnt(0)\n\ts_barrier" :: "i"(N) : "memory"); }
DI void drain() { asm volatile("s_waitcnt vmcnt(0)" ::: "memory"); }
DI unsigned tagv(float x, unsigned t) { return (__float_as_uint(x) & 0xffffff00u) | t; }
DI f2 cmul(f2 a, f2 b) { return (f2){a.x * b.x - a.y * b.y, a.x * b.y + a.y * b.x}; }
DI f2 cfma(f2 a, f2 b, f2 c) { return (f2){fmaf(a.x, b.x, fmaf(-a.y, b.y, c.x)), fmaf(a.x, b.y, fmaf(a.y, b.x, c.y))}; }
DI f2 fma2(f2 a, f2 b, f2 c) { return __builtin_elementwise_fma(a, b, c); }
constexpr float EPS = 1e-6f, LOG2E = 1.44269504088896341f;
DI unsigned phi2(float s0, float s1) {
  const f2 a = fma2((f2){s0, s1}, (f2){LOG2E, LOG2E}, (f2){-EPS * LOG2E, -EPS * LOG2E});
  const f2 e = {__builtin_amdgcn_fmed3f(__builtin_amdgcn_exp2f(a.x), 0.f, 1.f), __builtin_amdgcn_fmed3f(__builtin_amdgcn_exp2f(a.y), 0.f, 1.f)};
  return pk2(e + (f2){__builtin_amdgcn_fmed3f(s0, EPS, __builtin_inff()), __builtin_amdgcn_fmed3f(s1, EPS, __builtin_inff())});
}
DI f2 swap_lo(f2 v, f2& hi) {
  const auto x = __builtin_amdgcn_permlane32_swap(__float_as_uint(v.x), __float_as_uint(v.x), false, false);
  const auto y = __builtin_amdgcn_permlane32_swap(__float_as_uint(v.y), __float_as_uint(v.y), false, false);
  hi = (f2){__uint_as_float(x[1]), __uint_as_float(y[1])};
  return (f2){__uint_as_float(x[0]), __uint_as_float(y[0])};
}
DI void wait_flags(__amdgpu_buffer_rsrc_t ws, unsigned off, unsigned tag) {
#pragma unroll 1
  for (unsigned sp = 0; sp < SPIN_CAP; ++sp) {
      asm volatile("" ::: "memory");
    const u2 f = __builtin_amdgcn_raw_buffer_load_b64(ws, off, 0, SC1);
    if (__all(f.x == tag && f.y == ~tag)) break;
    __builtin_amdgcn_s_sleep(1);
  }
  asm volatile("" ::: "memory");
}

__global__ void __launch_bounds__(512, 2)
fused_kernel(const u16* a0, const u16* a1, const u16* a2, const u16* a3, const u16* a4, const u16* a5, const u16* a6, const u16* a7,
             const u16* a8, const u16* a9, const u16* a10, const u16* a11, float* a12, char* a13) {
  extern __shared__ __attribute__((aligned(16))) char smem_[];
  char LAS* L = (char LAS*)smem_;
  const unsigned L0 = (unsigned)(size_t)L;
  u64 st[16];
  for (int i = 0; i < 16; ++i) st[i] = 0;
  ST(0);
  typedef u64 u64x8 __attribute__((ext_vector_type(8)));
  typedef u64 u64x4 __attribute__((ext_vector_type(4)));
  typedef u64 u64x2 __attribute__((ext_vector_type(2)));
  u64x8 ka; u64x4 kb; u64x2 kc_;
  asm volatile("s_load_dwordx16 %0, %3, 0x0\n\ts_load_dwordx8 %1, %3, 0x40\n\ts_load_dwordx4 %2, %3, 0x60\n\ts_waitcnt lgkmcnt(0)"
               : "=&s"(ka), "=&s"(kb), "=&s"(kc_) : "s"(__builtin_amdgcn_kernarg_segment_ptr()) : "memory");
  const GAS u16 *xa = (const GAS u16*)ka[0], *wqkv = (const GAS u16*)ka[1], *wo = (const GAS u16*)ka[2], *xb = (const GAS u16*)ka[3];
  const GAS u16 *lre = (const GAS u16*)ka[4], *lim = (const GAS u16*)ka[5], *bre = (const GAS u16*)ka[6], *bim = (const GAS u16*)ka[7];
  const GAS u16 *cre = (const GAS u16*)kb[0], *cim = (const GAS u16*)kb[1], *ldt = (const GAS u16*)kb[2], *dsk = (const GAS u16*)kb[3];
  float* out = (float*)kc_[0]; char* wsp = (char*)kc_[1];
  const int lane = threadIdx.x & 63;
  const int w8 = __builtin_amdgcn_readfirstlane(threadIdx.x >> 6), w = w8 & 3;
  const bool helper = w8 >= 4;
  const int bid = blockIdx.x, xcd = bid & 7, jj = bid >> 3;
  const int tile = (xcd >> 1) * 4 + (jj >> 3), cg = jj & 7;
  const int rot = ROT == 1 ? ((cg + 3 * (jj >> 3) + xcd) & 7) : ROT == 2 ? cg : ROT == 3 ? 2 * (jj >> 3) : 0;
  const __amdgpu_buffer_rsrc_t ws = __builtin_amdgcn_make_buffer_rsrc(wsp, 0, WS_END, 0x00020000);
  const __amdgpu_buffer_rsrc_t ro = __builtin_amdgcn_make_buffer_rsrc(out, 0, 2048 * 1024 * 4, 0x00020000);
  const int r8 = lane >> 3, c8 = lane & 7;
  const int q = lane >> 4, l15 = lane & 15, l31 = lane & 31, hh = lane >> 5;
  unsigned epoch;
#define LOAD_EPOCH() asm volatile("s_load_dword %0, %1, 0x0" : "=s"(epoch) : "s"(wsp) : "memory")

  if ((xcd & 1) == 0) {
    constexpr unsigned SLOT = 40960, SCR = 32768;
    const int head = cg * 4 + w;
    if (helper) {
      unsigned vx[4], vw[2], vo[4];
#pragma unroll
      for (int i = 0; i < 4; ++i) vx[i] = (32 * w + 8 * i + r8) * 1024 + ((c8 ^ ((4 * i + (r8 >> 1)) & 7)) << 4) - 1024 * i;
#pragma unroll
      for (int i = 0; i < 2; ++i) vw[i] = (16 * w + 8 * i + r8) * 3072 + ((c8 ^ ((((r8 >> 1) & 1) | (i << 1)) << 1)) << 4) - 1024 * i;
      const GAS char* xbase = (const GAS char*)xa + (size_t)tile * 131072;
      const GAS char* wbase = (const GAS char*)wqkv + cg * 128;
      auto issue = [&](int kk) {
        const int kc = (kk + rot) & 7;
        const unsigned s = L0 + (kk & 3) * SLOT;
        dma4(s + w * 4096, xbase + kc * 128, vx[0], vx[1], vx[2], vx[3]);
        const GAS char* wb = wbase + kc * (64 * 3072);
        dma2(s + 16384 + w * 2048, wb, vw[0], vw[1]);
        dma2(s + 24576 + w * 2048, wb + 1024, vw[0], vw[1]);
        dma2(s + 32768 + w * 2048, wb + 2048, vw[0], vw[1]);
      };
      issue(0); issue(1);
      LOAD_EPOCH();
#pragma unroll
      for (int i = 0; i < 4; ++i) vo[i] = (8 * i + r8) * 1024 + ((c8 ^ (((r8 >> 1) & 3) << 1)) << 4) - 1024 * i;
      const GAS char* obase = (const GAS char*)wo + cg * 128;
      auto issue_wo = [&](int piece) {
#pragma unroll
        for (int h = 0; h < 2; ++h)
          dma4(L0 + piece * SLOT + w * 8192 + h * 4096, obase + ((8 * piece + 2 * w + h + 2 * cg) & 15) * 32768, vo[0], vo[1], vo[2], vo[3]);
      };
      asm volatile("s_waitcnt vmcnt(10) lgkmcnt(0)\n\ts_barrier" : "+s"(epoch) :: "memory");
      issue(2);
      wait_bar<10>(); issue(3);
      wait_bar<10>(); issue(4);
      wait_bar<10>(); issue(5);
      wait_bar<10>(); issue(6);
      wait_bar<10>(); issue(7);
      wait_bar<10>(); issue_wo(0);
      wait_bar<8>(); ST(1); issue_wo(1); ST(2);
      const unsigned tag = epoch + 1, t = tag & 0xffu;
      const GAS char* kvb_ = (const GAS char*)wsp + WS_KV + head * 16384;
      const unsigned vk0 = lane * 16, vk1 = vk0 + 4096, vk2 = vk0 + 8192, vk3 = vk0 + 12288;
      u4 g[16], gz;
#pragma unroll
      for (int i = 0; i < 16; ++i) g[i] = (u4){0, 0, 0, 0};
      f4 kv;
#pragma unroll 1
      for (unsigned rep = 0; rep < 64; ++rep) {
        unsigned have = 0;
#pragma unroll 1
        for (unsigned sp = 0; sp < SPIN_CAP; ++sp) {
      asm volatile("" ::: "memory");
          gz = __builtin_amdgcn_raw_buffer_load_b128(ws, WS_Z + head * 1024 + l15 * 64 + q * 16, 0, SC1);
          gz.x ^= t; gz.z ^= t;
          const u64 m = __ballot(((gz.x | gz.z) & 0xffu) == 0);
          const unsigned m2 = (unsigned)m & (unsigned)(m >> 32);
          const unsigned nw = m2 & (m2 >> 16) & 0xffffu & ~have;
#define KVLD(i, v) if (nw & (1u << i)) asm volatile("global_load_dwordx4 %0, %1, %2 offset:%3 sc1" : "+v"(g[i]) : "v"(v), "s"(kvb_), "i"((i & 3) * 1024) : "memory")
          KVLD(0, vk0); KVLD(1, vk0); KVLD(2, vk0); KVLD(3, vk0); KVLD(4, vk1); KVLD(5, vk1); KVLD(6, vk1); KVLD(7, vk1);
          KVLD(8, vk2); KVLD(9, vk2); KVLD(10, vk2); KVLD(11, vk2); KVLD(12, vk3); KVLD(13, vk3); KVLD(14, vk3); KVLD(15, vk3);
          have |= nw;
          if (have == 0xffffu) break;
          if (!nw) __builtin_amdgcn_s_sleep(1);
        }
        ST(4);
        asm volatile("s_waitcnt vmcnt(0)" : "+v"(g[0]), "+v"(g[1]), "+v"(g[2]), "+v"(g[3]), "+v"(g[4]), "+v"(g[5]), "+v"(g[6]), "+v"(g[7]),
                     "+v"(g[8]), "+v"(g[9]), "+v"(g[10]), "+v"(g[11]), "+v"(g[12]), "+v"(g[13]), "+v"(g[14]), "+v"(g[15]) :: "memory");
        ST(5);
        unsigned bad = 0;
        kv = (f4){0, 0, 0, 0};
#pragma unroll
        for (int i = 0; i < 16; ++i) {
          g[i].x ^= t; g[i].z ^= t;
          bad |= g[i].x | g[i].z;
          kv += __builtin_bit_cast(f4, g[i]);
        }
        if (__all((bad & 0xffu) == 0)) break;
      }
      f4 zz = __builtin_bit_cast(f4, gz);
#pragma unroll
      for (int r = 0; r < 4; ++r) {
        float v = zz[r];
        v += __builtin_bit_cast(float, __builtin_amdgcn_mov_dpp(__builtin_bit_cast(int, v), 0x128, 0xf, 0xf, true));
        v += __builtin_bit_cast(float, __builtin_amdgcn_mov_dpp(__builtin_bit_cast(int, v), 0x124, 0xf, 0xf, true));
        v += __builtin_bit_cast(float, __builtin_amdgcn_mov_dpp(__builtin_bit_cast(int, v), 0x122, 0xf, 0xf, true));
        v += __builtin_bit_cast(float, __builtin_amdgcn_mov_dpp(__builtin_bit_cast(int, v), 0x121, 0xf, 0xf, true));
        zz[r] = v;
      }
      *(LAS u4*)(L + SCR + w * 1024 + lane * 16) = (u4){pk2(kv[0], kv[1]), pk2(kv[2], kv[3]), pk2(zz[0], zz[1]), pk2(zz[2], zz[3])};
      ST(6);
      wait_bar<0>();
      ST(7); HFLUSH();
      if (bid == 0 && w == 0) {
#pragma unroll 1
        for (unsigned sp = 0; sp < SPIN_CAP; ++sp) {
      asm volatile("" ::: "memory");
          const u2 f0 = __builtin_amdgcn_raw_buffer_load_b64(ws, WS_FA + lane * 1024, 0, SC1);
          const u2 f1 = __builtin_amdgcn_raw_buffer_load_b64(ws, WS_FA + 512 + lane * 1024, 0, SC1);
          const u2 f2_ = __builtin_amdgcn_raw_buffer_load_b64(ws, WS_FB + (lane >> 3) * 4096 + (lane & 7) * 256, 0, SC1);
          const u2 f3 = __builtin_amdgcn_raw_buffer_load_b64(ws, WS_FB + 32768 + (lane >> 3) * 4096 + (lane & 7) * 256, 0, SC1);
          if (__all(f0.x == tag && f0.y == ~tag && f1.x == tag && f1.y == ~tag && f2_.x == tag && f2_.y == ~tag && f3.x == tag && f3.y == ~tag)) break;
          __builtin_amdgcn_s_sleep(8);
        }
        if (lane == 0) __builtin_amdgcn_raw_buffer_store_b32(tag, ws, WS_EPOCH, 0, SC1);
      }
      return;
    }
    LOAD_EPOCH();
    unsigned warm0 = 0, warm1 = 0;
    if (TOUCH) {
      warm0 = __builtin_amdgcn_raw_buffer_load_b32(ws, lane < 32 ? WS_FA + (tile * 32 + lane) * 128 : lane < 40 ? WS_Z + head * 1024 + (lane - 32) * 128
                                                   : lane == 40 ? WS_KV + head * 16384 : lane == 41 ? WS_O + tile * 131072 : 0x80000000u, 0, SC1);
      warm1 = __builtin_amdgcn_raw_buffer_load_b32(ro, lane < 2 ? (tile * 128 + w * 32 + lane * 16) * 4096 : 0x80000000u, 0, SC1);
    }
    const int qq = (lane >> 2) & 3, pp = lane & 3;
    const unsigned ax0 = l15 * 128 + ((q ^ (l15 >> 1)) << 4), ax1 = l15 * 128 + (((4 + q) ^ (l15 >> 1)) << 4);
    const unsigned aw = 16384 + (8 * q + qq) * 128 + (((2 * w + (pp >> 1)) ^ ((((qq >> 1) & 1) | ((q & 1) << 1)) << 1)) << 4) + 8 * (pp & 1);
    f4 ka[8], va[8], qa[8];
#pragma unroll
    for (int i = 0; i < 8; ++i) { ka[i] = (f4){EPS, EPS, EPS, EPS}; qa[i] = ka[i]; va[i] = (f4){0, 0, 0, 0}; }
    bf8 X0[4], X1[4], W0[3], W1[3];
    auto rdx = [&](bf8 (&X)[4], int kk, int ks, int h) {
#pragma unroll
      for (int i = 0; i < 4; ++i) X[i] = ldsr(L + (kk & 3) * SLOT + (ks ? ax1 : ax0) + (4 * h + i) * 2048);
    };
    auto rdw = [&](bf8 (&W)[3], int kk, int ks) {
#pragma unroll
      for (int i = 0; i < 3; ++i) { const char LAS* p = L + (kk & 3) * SLOT + aw + ks * 4096 + i * 8192; W[i] = ldstr(p, p + 512); }
    };
    auto mm = [&](const bf8 (&X)[4], const bf8 (&W)[3], int h) {
#pragma unroll
      for (int i = 0; i < 4; ++i) {
        ka[4 * h + i] = mfma16(X[i], W[1], ka[4 * h + i]);
        va[4 * h + i] = mfma16(X[i], W[2], va[4 * h + i]);
        qa[4 * h + i] = mfma16(W[0], X[i], qa[4 * h + i]);
      }
    };
#define ILV(nr, nm) do { _Pragma("unroll") for (int i_ = 0; i_ < nr; ++i_) { SG(0x008, 1); SG(0x100, 1); } SG(0x008, nm - nr); } while (0)
    asm volatile("s_waitcnt lgkmcnt(0)\n\ts_barrier" : "+s"(epoch) : "v"(warm0), "v"(warm1) : "memory");
    ST(1); CT(13);
    const unsigned tag = epoch + 1, t = tag & 0xffu;
    rdw(W0, 0, 0); rdx(X0, 0, 0, 0);
    SB();
#pragma unroll
    for (int kk = 0; kk < 8; ++kk) {
      rdx(X1, kk, 0, 1); mm(X0, W0, 0); ILV(4, 12); SB();
      rdw(W1, kk, 1); rdx(X0, kk, 1, 0); mm(X1, W0, 1); ILV(10, 12); SB();
      rdx(X1, kk, 1, 1); mm(X0, W1, 0); ILV(4, 12); SB();
      if (kk < 7) { lbar(); if (kk == 6) ST(2); rdw(W0, kk + 1, 0); rdx(X0, kk + 1, 0, 0); mm(X1, W1, 1); ILV(10, 12); }
      else mm(X1, W1, 1);
      SB();
    }
    ST(3); CT(14);
    {
      f4 kv = {0, 0, 0, 0}, zz = {0, 0, 0, 0};
      const u4 ones = {0x3f803f80u, 0x3f803f80u, 0x3f803f80u, 0x3f803f80u};
#pragma unroll
      for (int m = 0; m < 4; ++m) {
        const f4 a = ka[2 * m], b = ka[2 * m + 1], c = va[2 * m], d = va[2 * m + 1];
        const u4 pa = {phi2(a[0], a[1]), phi2(a[2], a[3]), phi2(b[0], b[1]), phi2(b[2], b[3])};
        const u4 pb = {pk2(c[0], c[1]), pk2(c[2], c[3]), pk2(d[0], d[1]), pk2(d[2], d[3])};
        kv = mfma16(asbf8(pa), asbf8(pb), kv);
        zz = mfma16(asbf8(pa), asbf8(ones), zz);
      }
      const u4 skv = {tagv(kv[0], t), __float_as_uint(kv[1]), tagv(kv[2], t), __float_as_uint(kv[3])};
      __builtin_amdgcn_raw_buffer_store_b128(skv, ws, WS_KV + (head * 16 + tile) * 1024 + lane * 16, 0, SC1);
      const u4 sz = {tagv(zz[0], t), __float_as_uint(zz[1]), tagv(zz[2], t), __float_as_uint(zz[3])};
      __builtin_amdgcn_raw_buffer_store_b128(sz, ws, l15 == 0 ? WS_Z + (head * 16 + tile) * 64 + q * 16 : 0x80000000u, 0, SC1);
    }
    SB();
    ST(4);
    u2 pq[8];
#pragma unroll
    for (int mt = 0; mt < 8; ++mt) pq[mt] = (u2){phi2(qa[mt][0], qa[mt][1]), phi2(qa[mt][2], qa[mt][3])};
    unsigned ao[4];
#pragma unroll
    for (int nt = 0; nt < 4; ++nt) ao[nt] = (4 * q + qq) * 128 + ((((2 * nt) ^ (((2 * q + (qq >> 1)) & 3) << 1)) + (pp >> 1)) << 4) + 8 * (pp & 1);
    asm volatile("s_barrier" : "+v"(pq[0]), "+v"(pq[1]), "+v"(pq[2]), "+v"(pq[3]), "+v"(pq[4]), "+v"(pq[5]), "+v"(pq[6]), "+v"(pq[7]) :: "memory");
    ST(5);
    const u4 kz = *(const LAS u4*)(L + SCR + w * 1024 + lane * 16);
    const u2 kvb = {kz.x, kz.y}, zb = {kz.z, kz.w};
    const f4 zero4 = {0, 0, 0, 0}, eps4 = {EPS, EPS, EPS, EPS};
    f4 num[8], den[8];
#pragma unroll
    for (int mt = 0; mt < 8; ++mt) { den[mt] = mfma16k16(zb, pq[mt], eps4); num[mt] = mfma16k16(kvb, pq[mt], zero4); }
    SB();
#pragma unroll
    for (int u = 0; u < 4; ++u) {
      u4 so;
#pragma unroll
      for (int h = 0; h < 2; ++h) {
        const float rc = __builtin_amdgcn_rcpf(den[2 * u + h][0]);
        const f4 n = num[2 * u + h];
        so[2 * h] = pk2((f2){n[0], n[1]} * rc);
        so[2 * h + 1] = pk2((f2){n[2], n[3]} * rc);
      }
      __builtin_amdgcn_raw_buffer_store_b128(so, ws, WS_O + ((tile * 4 + u) * 32 + head) * 1024 + lane * 16, 0, SC1);
    }
    ST(6);
    drain();
    __builtin_amdgcn_raw_buffer_store_b64((u2){tag, ~tag}, ws, lane == 0 ? WS_FA + (tile * 32 + head) * 128 : 0x80000000u, 0, SC1);
    unsigned pull = 0;
    if (PULL) pull = __builtin_amdgcn_raw_buffer_load_b32(ws, lane < 32 ? WS_O + ((tile * 4 + (lane >> 3)) * 32 + head) * 1024 + (lane & 7) * 128 : 0x80000000u, 0, SC1);
    unsigned tw[4], trd[4], so_ = ((tile * 128 + w * 32 + q) * 1024 + cg * 64 + l15 * 4) * 4;
#pragma unroll
    for (int i = 0; i < 4; ++i) {
      tw[i] = 2 * SLOT + w * 8192 + l15 * 256 + (((4 * i + q) ^ l15) << 4);
      trd[i] = 2 * SLOT + w * 8192 + (4 * i + q) * 256 + ((l15 ^ (4 * i + q)) << 4);
      asm volatile("" : "+v"(tw[i]), "+v"(trd[i]));
    }
    asm volatile("" : "+v"(so_));
    ST(7);
    f4 acc[2][4];
#pragma unroll
    for (int i = 0; i < 8; ++i) acc[i >> 2][i & 3] = zero4;
    const unsigned ob = WS_O + (tile * 4 + w) * 32768 + lane * 16;
    u4 of[32];
    bf8 wf[2][4];
    auto rdo = [&](int k) {
      const char LAS* base = L + (k >> 3) * SLOT + (k & 7) * 4096;
#pragma unroll
      for (int nt = 0; nt < 4; ++nt) wf[k & 1][nt] = ldstr(base + ao[nt], base + ao[nt] + 2048);
    };
    auto ldo = [&](int k) {
      const unsigned so = ((k + 2 * cg) & 15) * 2048;
      of[2 * k] = __builtin_amdgcn_raw_buffer_load_b128(ws, ob, so, PAY_AUX);
      of[2 * k + 1] = __builtin_amdgcn_raw_buffer_load_b128(ws, ob + 1024, so, PAY_AUX);
    };
    rdo(0);
    wait_flags(ws, WS_FA + (tile * 32 + l31) * 128, tag);
    asm volatile("" :: "v"(pull));
    ST(8);
#pragma unroll
    for (int k = 0; k < OD; ++k) ldo(k);
    SB();
#pragma unroll
    for (int k = 0; k < 16; ++k) {
      if (k + OD < 16) ldo(k + OD);
      if (k + 1 < 16) rdo(k + 1);
      const u4 a = of[2 * k], b = of[2 * k + 1];
      const bf8 f0 = asbf8((u4){a.x, a.y, b.x, b.y}), f1 = asbf8((u4){a.z, a.w, b.z, b.w});
#pragma unroll
      for (int nt = 0; nt < 4; ++nt) {
        acc[0][nt] = mfma16(wf[k & 1][nt], f0, acc[0][nt]);
        acc[1][nt] = mfma16(wf[k & 1][nt], f1, acc[1][nt]);
      }
      SG(0x002, 4);
#pragma unroll
      for (int i_ = 0; i_ < 8; ++i_) { SG(0x008, 1); SG(0x100, 1); if (i_ == 1 || i_ == 5) SG(0x020, 1); }
      SB();
    }
    ST(9); CT(15);
#pragma unroll
    for (int mt = 0; mt < 2; ++mt)
#pragma unroll
      for (int nt = 0; nt < 4; ++nt) {
        const f4 v = acc[mt][nt];
        const unsigned p0 = pk2(v[0], v[1]), p1 = pk2(v[2], v[3]);
        *(LAS f4*)(L + tw[nt] + mt * 4096) = (f4){bflo(p0), bfhi(p0), bflo(p1), bfhi(p1)};
      }
    u4 ov[8];
#pragma unroll
    for (int i = 0; i < 8; ++i) ov[i] = *(const LAS u4*)(L + trd[i & 3] + (i >> 2) * 4096);
    SB();
#pragma unroll
    for (int i = 0; i < 8; ++i) __builtin_amdgcn_raw_buffer_store_b128(ov[i], ro, so_, i * 16384, OUT_AUX);
    ST(10);
  } else {
    constexpr unsigned SLOT = 32768, SCR = SLOT, PAR = 4 * SLOT;
    const int crot = cg + 2 * (jj >> 3);
    const int wp = w & 1, wt = w >> 1;
    const unsigned ecol = WS_EST + (cg * 64 + wp * 32 + l31) * 8;
    if (helper) {
      unsigned vx[4], vw[4], vc[4];
#pragma unroll
      for (int i = 0; i < 4; ++i) {
        const unsigned sw = (c8 ^ ((4 * i + (r8 >> 1)) & 7)) << 4;
        vx[i] = (64 * (w >> 1) + 32 * (r8 >> 2) + 16 * (r8 & 1) + 8 * (w & 1) + ((r8 >> 1) & 1) + 2 * i) * 1024 + sw - 1024 * i;
        vw[i] = (16 * (i >> 1) + 8 * (r8 >> 2) + 4 * (i & 1) + (r8 & 3)) * 1024 + sw - 1024 * i;
        vc[i] = (8 * i + r8) * 1024 + sw - 1024 * i;
      }
      const GAS char* xbase = (const GAS char*)xb + (size_t)tile * 131072;
      const GAS char* wbase = (const GAS char*)((w & 1) ? bim : bre) + (cg * 64 + (w >> 1) * 32) * 1024;
      auto issue = [&](int kk) {
        const int kc = (kk + rot) & 7;
        const unsigned s = L0 + (kk % 5) * SLOT + w * 4096;
        dma4(s, xbase + kc * 128, vx[0], vx[1], vx[2], vx[3]);
        dma4(s + 16384, wbase + kc * 128, vw[0], vw[1], vw[2], vw[3]);
      };
      issue(0); issue(1);
      LOAD_EPOCH();
      const GAS char* cbase = (const GAS char*)((w & 1) ? cim : cre) + cg * 65536;
      auto issue_c = [&](int piece) {
        const unsigned s = L0 + ((piece + 3) % 5) * SLOT + w * 8192;
        const GAS char* cb = cbase + ((crot + 2 * piece + (w >> 1)) & 7) * 128;
        dma4(s, cb, vc[0], vc[1], vc[2], vc[3]);
        dma4(s + 4096, cb + 32768, vc[0], vc[1], vc[2], vc[3]);
      };
      asm volatile("s_waitcnt vmcnt(8) lgkmcnt(0)\n\ts_barrier" : "+s"(epoch) :: "memory");
      issue(2);
      const f4 par = *(const LAS f4*)(L + PAR + w * 1024 + lane * 16);
      const f2 l64 = {par[0], par[1]}, l128 = {par[2], par[3]};
      f2 l256 = cmul(l128, l128);
      f2 wgt = hh ? l64 : (f2){1.f, 0.f};
      if (wt) wgt = cmul(wgt, l128);
      const unsigned tag = epoch + 1, t = tag & 0xffu;
      unsigned eo[8], et[8];
#pragma unroll
      for (int m = 0; m < 8; ++m) {
        const int i = 2 * tile - 1 - (2 * wt + hh) - 4 * m;
        eo[m] = i >= 0 ? ecol + i * 4096 : 0x80000000u;
        et[m] = i >= 0 ? t : 0u;
        asm volatile("" : "+v"(eo[m]), "+v"(et[m]));
      }
      asm volatile("" : "+v"(l256), "+v"(wgt));
      wait_bar<8>(); issue(3);
      wait_bar<8>(); issue(4);
      wait_bar<8>(); issue(5);
      wait_bar<8>(); issue(6);
      wait_bar<8>(); issue(7);
      wait_bar<8>(); issue_c(0);
      wait_bar<8>(); ST(1);
      if (NC_EARLY > 1) issue_c(1);
      if (NC_EARLY > 2) issue_c(2);
      ST(2);
      f2 cin;
#pragma unroll 1
      for (unsigned sp = 0; sp < SPIN_CAP; ++sp) {
      asm volatile("" ::: "memory");
        u2 g[8];
#pragma unroll
        for (int m = 0; m < 8; ++m) g[m] = __builtin_amdgcn_raw_buffer_load_b64(ws, eo[m], 0, SC1);
        unsigned bad = 0;
        cin = (f2){0, 0};
#pragma unroll
        for (int m = 7; m >= 0; --m) {
          const unsigned cx = g[m].x ^ et[m];
          bad |= cx;
          cin = cfma(l256, cin, (f2){__uint_as_float(cx), __uint_as_float(g[m].y)});
        }
        if (__all((bad & 0xffu) == 0)) break;
      }
      ST(5);
      cin = cmul(cin, wgt);
      f2 chi;
      const f2 clo = swap_lo(cin, chi);
      if (hh == 0) *(LAS f2*)(L + SCR + (wp * 2 + wt) * 256 + l31 * 8) = clo + chi;
      ST(6);
      lbar();
      lbar();
      if (NC_EARLY <= 1) issue_c(1);
      if (NC_EARLY <= 2) issue_c(2);
      issue_c(3);
      ST(7);
      wait_bar<0>();
      ST(8); HFLUSH();
      return;
    }
    LOAD_EPOCH();
    const int pl = cg * 64 + wp * 32 + ((l31 & 0x13) | ((l31 & 4) << 1) | ((l31 & 8) >> 1));
    const unsigned plr = lre[pl], pli = lim[pl], pdt = ldt[pl];
    unsigned warm0 = 0, warm1 = 0;
    if (TOUCH) {
      warm0 = __builtin_amdgcn_raw_buffer_load_b32(ws, lane < 16 ? WS_FB + ((tile * 2 + (w >> 1)) * 16 + lane) * 128 : lane == 16 ? WS_EST + tile * 8192
                                                   : lane == 17 ? WS_H + (tile * 4 + w) * 65536 : 0x80000000u, 0, SC1);
      warm1 = __builtin_amdgcn_raw_buffer_load_b32(ro, lane < 2 ? (tile * 128 + w * 32 + lane * 16) * 4096 + 2048 : 0x80000000u, 0, SC1);
    }
    auto tokr = [&](int r) { return tile * 128 + 64 * (w >> 1) + 32 * ((r >> 2) & 1) + 16 * (r & 1) + 8 * (w & 1) + ((r >> 1) & 1) + 2 * (r >> 3); };
    u2 xd[8];
    {
      const u2 dv = *(const GAS u2*)(dsk + cg * 64 + l15 * 4);
#pragma unroll
      for (int i = 0; i < 8; ++i) {
        const u2 xv = *(const GAS u2*)(xb + tokr(4 * i + q) * 512 + cg * 64 + l15 * 4);
        xd[i] = (u2){pk2(bflo(xv.x) * bflo(dv.x), bfhi(xv.x) * bfhi(dv.x)), pk2(bflo(xv.y) * bflo(dv.y), bfhi(xv.y) * bfhi(dv.y))};
      }
    }
    f2 lam, kap, l16, l32, l64, l128, pw[16];
    {
      const float lr = bflo(plr), li = bflo(pli);
      const float dt = bflo(pk2(expf(bflo(pdt)), 0.f));
      const float mag = expf(dt * lr);
      float sn, cs;
      sincosf(dt * li, &sn, &cs);
      lam = (f2){mag * cs, mag * sn};
      const float inv = 2.f / (lr * lr + li * li), nr = lam.x - 1.f, ni = lam.y;
      kap = (f2){(nr * lr + ni * li) * inv, (ni * lr - nr * li) * inv};
      pw[0] = (f2){lam.x, -lam.y};
#pragma unroll
      for (int k = 1; k < 16; ++k) { pw[k] = cmul(pw[k >> 1], pw[(k - 1) >> 1]); asm volatile("" : "+v"(pw[k])); }
      l16 = (f2){pw[15].x, -pw[15].y}; l32 = cmul(l16, l16); l64 = cmul(l32, l32); l128 = cmul(l64, l64);
      *(LAS f4*)(L + PAR + w * 1024 + lane * 16) = (f4){l64.x, l64.y, l128.x, l128.y};
    }
    unsigned ar[4];
#pragma unroll
    for (int s = 0; s < 4; ++s) ar[s] = l31 * 128 + (((2 * s + hh) ^ ((lane >> 1) & 7)) << 4);
    u4 idn[2];
#pragma unroll
    for (int s = 0; s < 2; ++s)
#pragma unroll
      for (int jq = 0; jq < 4; ++jq) {
        const int r0 = 16 * s + 8 * (jq >> 1) + 4 * hh + 2 * (jq & 1);
        idn[s][jq] = (l31 == r0 ? 0x3f80u : 0u) | (l31 == r0 + 1 ? 0x3f800000u : 0u);
      }
    f16v ur[2], ui[2];
#pragma unroll
    for (int i = 0; i < 16; ++i) { ur[0][i] = 0; ur[1][i] = 0; ui[0][i] = 0; ui[1][i] = 0; }
    bf8 F0[4], F1[4];
    auto rd = [&](bf8 (&F)[4], int kk, int s) {
      const char LAS* sx = L + (kk % 5) * SLOT + wt * 8192 + ar[s];
      const char LAS* sw = L + (kk % 5) * SLOT + 16384 + wp * 8192 + ar[s];
      F[0] = ldsr(sx); F[1] = ldsr(sw); F[2] = ldsr(sw + 4096); F[3] = ldsr(sx + 4096);
    };
    auto mm = [&](const bf8 (&F)[4]) {
      ur[0] = mfma32(F[0], F[1], ur[0]); ui[0] = mfma32(F[0], F[2], ui[0]);
      ur[1] = mfma32(F[3], F[1], ur[1]); ui[1] = mfma32(F[3], F[2], ui[1]);
    };
#define ILB() do { SG(0x008, 1); SG(0x100, 2); SG(0x008, 1); SG(0x100, 2); SG(0x008, 2); } while (0)
    asm volatile("s_waitcnt lgkmcnt(0)\n\ts_barrier" : "+s"(epoch), "+v"(lam), "+v"(kap), "+v"(l16), "+v"(l32), "+v"(l64), "+v"(idn[0]), "+v"(idn[1]),
                 "+v"(xd[0]), "+v"(xd[1]), "+v"(xd[2]), "+v"(xd[3]), "+v"(xd[4]), "+v"(xd[5]), "+v"(xd[6]), "+v"(xd[7]) : "v"(warm0), "v"(warm1) : "memory");
    ST(1); CT(13);
    const unsigned tag = epoch + 1, t = tag & 0xffu;
    rd(F0, 0, 0);
    SB();
#pragma unroll
    for (int kk = 0; kk < 8; ++kk) {
      rd(F1, kk, 1); mm(F0); ILB(); SB();
      rd(F0, kk, 2); mm(F1); ILB(); SB();
      rd(F1, kk, 3); mm(F0); ILB(); SB();
      if (kk < 7) { lbar(); if (kk == 6) ST(2); rd(F0, kk + 1, 0); mm(F1); ILB(); }
      else mm(F1);
      SB();
    }
    ST(3); CT(14);
    f2 gr[16], gi[16];
#pragma unroll
    for (int a = 0; a < 16; ++a) {
      gr[a] = (f2){ur[a >> 3][2 * (a & 7)], ur[a >> 3][2 * (a & 7) + 1]};
      gi[a] = (f2){ui[a >> 3][2 * (a & 7)], ui[a >> 3][2 * (a & 7) + 1]};
    }
    {
      f2 LR = {lam.x, lam.x}, LI = {lam.y, lam.y}, LN = {-lam.y, -lam.y};
      asm volatile("s_nop 11" : "+v"(gr[0]), "+v"(gi[0]), "+v"(gr[8]), "+v"(gi[8]), "+v"(LR), "+v"(LI), "+v"(LN));
#pragma unroll
      for (int a = 1; a < 16; ++a)
        asm volatile("v_pk_fma_f32 %0, %6, %3, %0\n\tv_pk_fma_f32 %1, %5, %2, %1\n\tv_pk_fma_f32 %0, %4, %2, %0\n\tv_pk_fma_f32 %1, %4, %3, %1"
                     : "+v"(gr[a]), "+v"(gi[a]) : "v"(gr[a - 1]), "v"(gi[a - 1]), "v"(LR), "v"(LI), "v"(LN));
    }
    const f2 er = gr[15], ei = gi[15];
    const f2 e0 = {er.x, ei.x};
    const f2 e32 = cfma(l16, e0, (f2){er.y, ei.y});
    f2 ehi;
    const f2 elo = swap_lo(e32, ehi);
    const f2 e64 = cfma(l32, elo, ehi);
    __builtin_amdgcn_raw_buffer_store_b64((u2){tagv(e64.x, t), __float_as_uint(e64.y)}, ws, hh == 0 ? ecol + (tile * 2 + wt) * 4096 : 0x80000000u, 0, SC1);
    if (wt == 0 && hh == 0) *(LAS f2*)(L + SCR + 1024 + wp * 256 + l31 * 8) = e64;
    SB();
    ST(4);
    {
      const f2 KR = {kap.x, kap.x}, KI = {kap.y, kap.y};
#pragma unroll
      for (int a = 0; a < 16; ++a) {
        const f2 xr = gr[a], xi = gi[a];
        gr[a] = fma2(KR, xr, -KI * xi); gi[a] = fma2(-KR, xi, -KI * xr);
        asm volatile("" : "+v"(gr[a]), "+v"(gi[a]));
      }
    }
    f2 m0 = hh ? cmul(kap, l32) : kap, b0 = hh ? cmul(kap, elo) : (f2){0.f, 0.f};
    f2 m1 = cmul(l16, m0), b1 = cfma(l16, b0, cmul(kap, e0));
    unsigned ho = WS_H + (tile * 4 + wt * 2) * 65536 + (cg * 2 + wp) * 4096 + lane * 16;
    asm volatile("s_waitcnt lgkmcnt(0)\n\ts_barrier" : "+v"(ho), "+v"(m0), "+v"(b0), "+v"(m1), "+v"(b1) :: "memory");
    ST(5);
    f2 cin = *(const LAS f2*)(L + SCR + wp * 512 + l31 * 8) + *(const LAS f2*)(L + SCR + wp * 512 + 256 + l31 * 8);
    const f2 esib = *(const LAS f2*)(L + SCR + 1024 + wp * 256 + l31 * 8);
    lbar();
    if (wt) cin = cfma(l64, cin, esib);
    const f2 d0 = cfma(m0, cin, b0), d1 = cfma(m1, cin, b1);
    const f2 DR = {d0.x, d1.x}, DJ = {-d0.y, -d1.y};
    f16v zero16;
#pragma unroll
    for (int i = 0; i < 16; ++i) zero16[i] = 0;
    auto fix = [&](int m, int c) {
      u4 a[2];
#pragma unroll
      for (int i = 0; i < 8; ++i) {
        const f2 P = pw[8 * m + i], PR = {P.x, P.x}, PI = {P.y, P.y};
        const f2 h = c ? fma2(PI, DR, fma2(PR, DJ, gi[8 * m + i])) : fma2(-PI, DJ, fma2(PR, DR, gr[8 * m + i]));
        a[i >> 2][i & 3] = pk2(h);
      }
      return mfma32(asbf8(a[1]), asbf8(idn[1]), mfma32(asbf8(a[0]), asbf8(idn[0]), zero16));
    };
    auto put = [&](const f16v& T, int m, int c) {
      const u4 o0 = {pk2(T[0], T[1]), pk2(T[2], T[3]), pk2(T[4], T[5]), pk2(T[6], T[7])};
      const u4 o1 = {pk2(T[8], T[9]), pk2(T[10], T[11]), pk2(T[12], T[13]), pk2(T[14], T[15])};
      __builtin_amdgcn_raw_buffer_store_b128(o0, ws, ho + c * 2048, m * 65536, SC1);
      __builtin_amdgcn_raw_buffer_store_b128(o1, ws, ho + c * 2048 + 1024, m * 65536, SC1);
    };
    {
      const f16v T0 = fix(0, 0); SB();
      const f16v T1 = fix(0, 1); SB();
      put(T0, 0, 0);
      const f16v T2 = fix(1, 0); SB();
      put(T1, 0, 1);
      const f16v T3 = fix(1, 1); SB();
      put(T2, 1, 0); SB();
      put(T3, 1, 1);
    }
    ST(6);
    drain();
    __builtin_amdgcn_raw_buffer_store_b64((u2){tag, ~tag}, ws, lane == 0 ? WS_FB + ((tile * 2 + wt) * 16 + cg * 2 + wp) * 128 : 0x80000000u, 0, SC1);
    unsigned pull = 0;
    if (PULL) pull = __builtin_amdgcn_raw_buffer_load_b32(ws, WS_H + (tile * 4 + wt * 2 + hh) * 65536 + (cg * 2 + wp) * 4096 + l31 * 128, 0, SC1);
    unsigned tw[8], trd[4], so_ = ((tile * 128 + 64 * (w >> 1) + 8 * (w & 1) + 16 * (q & 1) + (q >> 1)) * 1024 + 512 + cg * 64 + l15 * 4) * 4;
#pragma unroll
    for (int i = 0; i < 8; ++i) { tw[i] = 2 * SLOT + w * 8192 + l31 * 256 + (((2 * i + hh) ^ l15) << 4); asm volatile("" : "+v"(tw[i])); }
#pragma unroll
    for (int i = 0; i < 4; ++i) { trd[i] = 2 * SLOT + w * 8192 + (4 * i + q) * 256 + ((l15 ^ (4 * i + q)) << 4); asm volatile("" : "+v"(trd[i])); }
    asm volatile("" : "+v"(so_));
#pragma unroll
    for (int i = 0; i < 8; ++i) *(LAS f4*)(L + trd[i & 3] + (i >> 2) * 4096) = (f4){bflo(xd[i].x), bfhi(xd[i].x), bflo(xd[i].y), bfhi(xd[i].y)};
    f16v acc[2];
#pragma unroll
    for (int i = 0; i < 8; ++i) {
      const f4 v = *(const LAS f4*)(L + tw[i]);
#pragma unroll
      for (int j = 0; j < 4; ++j) acc[i >> 2][4 * (i & 3) + j] = v[j];
    }
    asm volatile("s_barrier" : "+v"(acc[0]), "+v"(acc[1]) :: "memory");
    ST(7);
    const unsigned hb = WS_H + (tile * 4 + w) * 65536 + lane * 16;
    u4 hf[64];
    bf8 cf[2][4];
    auto ldh = [&](int i) { hf[i] = __builtin_amdgcn_raw_buffer_load_b128(ws, hb + (i & 7) * 1024, ((crot + (i >> 3)) & 7) * 8192, PAY_AUX); };
    auto rdc = [&](int g) {
      const int tt = g >> 2, i = (g & 3) * 2;
      const char LAS* cb = L + (((tt >> 1) + 3) % 5) * SLOT + (tt & 1) * 16384 + ((i >> 1) & 1) * 8192;
      cf[g & 1][0] = ldsr(cb + ar[(i >> 2) * 2]); cf[g & 1][1] = ldsr(cb + ar[(i >> 2) * 2] + 4096);
      cf[g & 1][2] = ldsr(cb + ar[(i >> 2) * 2 + 1]); cf[g & 1][3] = ldsr(cb + ar[(i >> 2) * 2 + 1] + 4096);
    };
    rdc(0);
    wait_flags(ws, WS_FB + ((tile * 2 + (w >> 1)) * 16 + l15) * 128, tag);
    asm volatile("" :: "v"(pull));
    ST(8);
#pragma unroll
    for (int i = 0; i < HD; ++i) ldh(i);
    SB();
#pragma unroll
    for (int g = 0; g < 32; ++g) {
      if (2 * g + HD < 64) { ldh(2 * g + HD); ldh(2 * g + HD + 1); }
      if (g + 1 < 32) rdc(g + 1);
      acc[0] = mfma32(cf[g & 1][0], asbf8(hf[2 * g]), acc[0]);
      acc[1] = mfma32(cf[g & 1][1], asbf8(hf[2 * g]), acc[1]);
      acc[0] = mfma32(cf[g & 1][2], asbf8(hf[2 * g + 1]), acc[0]);
      acc[1] = mfma32(cf[g & 1][3], asbf8(hf[2 * g + 1]), acc[1]);
      SG(0x008, 1); SG(0x100, 2); SG(0x008, 1); SG(0x100, 2); SG(0x008, 1); SG(0x020, 1); SG(0x008, 1); SG(0x020, 1);
      SB();
    }
    ST(9); CT(15);
#pragma unroll
    for (int i = 0; i < 8; ++i)
      *(LAS f4*)(L + tw[i]) = (f4){acc[i >> 2][4 * (i & 3)], acc[i >> 2][4 * (i & 3) + 1], acc[i >> 2][4 * (i & 3) + 2], acc[i >> 2][4 * (i & 3) + 3]};
    u4 ov[8];
#pragma unroll
    for (int i = 0; i < 8; ++i) ov[i] = *(const LAS u4*)(L + trd[i & 3] + (i >> 2) * 4096);
    SB();
#pragma unroll
    for (int i = 0; i < 8; ++i) __builtin_amdgcn_raw_buffer_store_b128(ov[i], ro, so_, (32 * (i & 1) + 2 * (i >> 1)) * 4096, OUT_AUX);
    ST(10);
  }
  drain();
  ST(11);
  if (bid == AMP_BID && w == 0) {
    const u64 until = st[11] + AMP_K * (st[AMP_B] - st[AMP_A]);
    while (__builtin_amdgcn_s_memrealtime() < until) __builtin_amdgcn_s_sleep(2);
  }
}

extern "C" void kernel_launch(void* const* d_in, const int* in_sizes, int n_in, void* d_out, int out_size, void* d_ws, size_t ws_size,
                              hipStream_t stream) {
  (void)hipFuncSetAttribute((const void*)fused_kernel, hipFuncAttributeMaxDynamicSharedMemorySize, LDS_BYTES);
  fused_kernel<<<256, 512, LDS_BYTES, stream>>>((const u16*)d_in[0], (const u16*)d_in[1], (const u16*)d_in[2], (const u16*)d_in[3],
                                               (const u16*)d_in[4], (const u16*)d_in[5], (const u16*)d_in[6], (const u16*)d_in[7],
                                               (const u16*)d_in[8], (const u16*)d_in[9], (const u16*)d_in[10], (const u16*)d_in[11],
                                               (float*)d_out, (char*)d_ws);
}
```

```cpp
#define ROT 8
#define XNT 0
#include <hip/hip_runtime.h>
#include <hip/hip_bf16.h>

#define DI __device__ __forceinline__
#define LAS __attribute__((address_space(3)))
#define GAS __attribute__((address_space(1)))
typedef unsigned short u16;
typedef unsigned long long u64;
typedef __bf16 bf8 __attribute__((ext_vector_type(8)));
typedef __bf16 bf2 __attribute__((ext_vector_type(2)));
typedef short s4 __attribute__((ext_vector_type(4)));
typedef short s8 __attribute__((ext_vector_type(8)));
typedef float f16v __attribute__((ext_vector_type(16)));
typedef float f4 __attribute__((ext_vector_type(4)));
typedef float f2 __attribute__((ext_vector_type(2)));
typedef unsigned u4 __attribute__((ext_vector_type(4)));
typedef unsigned u2 __attribute__((ext_vector_type(2)));

constexpr unsigned WS_EPOCH = 0;
constexpr unsigned WS_FA = 4096;
constexpr unsigned WS_FB = WS_FA + 65536;
constexpr unsigned WS_EST = WS_FB + 65536;
constexpr unsigned WS_Z = WS_EST + 131072;
constexpr unsigned WS_KV = WS_Z + 32768;
constexpr unsigned WS_O = 1u << 20;
constexpr unsigned WS_H = 3u << 20;
constexpr unsigned WS_END = 7u << 20;
constexpr unsigned WS_STAMP = 8u << 20;
constexpr int LDS_BYTES = 160 * 1024;
constexpr unsigned SPIN_CAP = 200000;
constexpr int SC1 = 16;

#define NC_EARLY 2
#define OUT_AUX 16
#define PAY_AUX 16
#define PULL 0
#define TOUCH 1
#define CPF 9
#define HROT 1
#define OD 6
#define HD 20

#define CT(i) do {} while (0)
#define ST(i) do {} while (0)
#define HFLUSH() do {} while (0)
#define SB() __builtin_amdgcn_sched_barrier(0)
#define SG(m, n) __builtin_amdgcn_sched_group_barrier(m, n, 0)

DI unsigned pk2(float a, float b) { f2 v = {a, b}; return __builtin_bit_cast(unsigned, __builtin_convertvector(v, bf2)); }
DI unsigned pk2(f2 v) { return __builtin_bit_cast(unsigned, __builtin_convertvector(v, bf2)); }
DI float bflo(unsigned v) { return __uint_as_float(v << 16); }
DI float bfhi(unsigned v) { return __uint_as_float(v & 0xffff0000u); }
DI bf8 asbf8(u4 v) { return __builtin_bit_cast(bf8, v); }
DI f16v mfma32(bf8 a, bf8 b, f16v c) { return __builtin_amdgcn_mfma_f32_32x32x16_bf16(a, b, c, 0, 0, 0); }
DI f4 mfma16(bf8 a, bf8 b, f4 c) { return __builtin_amdgcn_mfma_f32_16x16x32_bf16(a, b, c, 0, 0, 0); }
DI f4 mfma16k16(u2 a, u2 b, f4 c) { return __builtin_amdgcn_mfma_f32_16x16x16bf16_1k(__builtin_bit_cast(s4, a), __builtin_bit_cast(s4, b), c, 0, 0, 0); }
DI bf8 ldsr(const char LAS* p) { return *(const LAS bf8*)p; }
DI bf8 ldstr(const char LAS* p0, const char LAS* p1) {
  s4 a = __builtin_amdgcn_ds_read_tr16_b64_v4i16((LAS s4*)p0), b = __builtin_amdgcn_ds_read_tr16_b64_v4i16((LAS s4*)p1);
  return __builtin_bit_cast(bf8, (s8)__builtin_shufflevector(a, b, 0, 1, 2, 3, 4, 5, 6, 7));
}
DI void dma4(unsigned lds, const GAS char* sb, unsigned v0, unsigned v1, unsigned v2, unsigned v3) {
  asm volatile("s_mov_b32 m0, %0\n\ts_nop 0\n\tglobal_load_lds_dwordx4 %1, %5\n\tglobal_load_lds_dwordx4 %2, %5 offset:1024\n\t"
               "global_load_lds_dwordx4 %3, %5 offset:2048\n\tglobal_load_lds_dwordx4 %4, %5 offset:3072"
               :: "s"(lds), "v"(v0), "v"(v1), "v"(v2), "v"(v3), "s"(sb) : "memory");
}
DI void dma4nt(unsigned lds, const GAS char* sb, unsigned v0, unsigned v1, unsigned v2, unsigned v3) {
  asm volatile("s_mov_b32 m0, %0\n\ts_nop 0\n\tglobal_load_lds_dwordx4 %1, %5 nt\n\tglobal_load_lds_dwordx4 %2, %5 offset:1024 nt\n\t"
               "global_load_lds_dwordx4 %3, %5 offset:2048 nt\n\tglobal_load_lds_dwordx4 %4, %5 offset:3072 nt"
               :: "s"(lds), "v"(v0), "v"(v1), "v"(v2), "v"(v3), "s"(sb) : "memory");
}
DI void dmax(unsigned lds, const GAS char* sb, unsigned v0, unsigned v1, unsigned v2, unsigned v3) { if (XNT) dma4nt(lds, sb, v0, v1, v2, v3); else dma4(lds, sb, v0, v1, v2, v3); }
DI void dma2(unsigned lds, const GAS char* sb, unsigned v0, unsigned v1) {
  asm volatile("s_mov_b32 m0, %0\n\ts_nop 0\n\tglobal_load_lds_dwordx4 %1, %3\n\tglobal_load_lds_dwordx4 %2, %3 offset:1024"
               :: "s"(lds), "v"(v0), "v"(v1), "s"(sb) : "memory");
}
DI void lbar() { asm volatile("s_waitcnt lgkmcnt(0)\n\ts_barrier" ::: "memory"); }
template <int N> DI void wait_bar() { asm volatile("s_waitcnt vmcnt(%0) lgkmcnt(0)\n\ts_barrier" :: "i"(N) : "memory"); }
DI void drain() { asm volatile("s_waitcnt vmcnt(0)" ::: "memory"); }
DI unsigned tagv(float x, unsigned t) { return (__float_as_uint(x) & 0xffffff00u) | t; }
DI f2 cmul(f2 a, f2 b) { return (f2){a.x * b.x - a.y * b.y, a.x * b.y + a.y * b.x}; }
DI f2 cfma(f2 a, f2 b, f2 c) { return (f2){fmaf(a.x, b.x, fmaf(-a.y, b.y, c.x)), fmaf(a.x, b.y, fmaf(a.y, b.x, c.y))}; }
DI f2 fma2(f2 a, f2 b, f2 c) { return __builtin_elementwise_fma(a, b, c); }
constexpr float EPS = 1e-6f, LOG2E = 1.44269504088896341f;
DI unsigned phi2(float s0, float s1) {
  const f2 a = fma2((f2){s0, s1}, (f2){LOG2E, LOG2E}, (f2){-EPS * LOG2E, -EPS * LOG2E});
  const f2 e = {__builtin_amdgcn_fmed3f(__builtin_amdgcn_exp2f(a.x), 0.f, 1.f), __builtin_amdgcn_fmed3f(__builtin_amdgcn_exp2f(a.y), 0.f, 1.f)};
  return pk2(e + (f2){__builtin_amdgcn_fmed3f(s0, EPS, __builtin_inff()), __builtin_amdgcn_fmed3f(s1, EPS, __builtin_inff())});
}
DI f2 swap_lo(f2 v, f2& hi) {
  const auto x = __builtin_amdgcn_permlane32_swap(__float_as_uint(v.x), __float_as_uint(v.x), false, false);
  const auto y = __builtin_amdgcn_permlane32_swap(__float_as_uint(v.y), __float_as_uint(v.y), false, false);
  hi = (f2){__uint_as_float(x[1]), __uint_as_float(y[1])};
  return (f2){__uint_as_float(x[0]), __uint_as_float(y[0])};
}
DI void wait_flags(__amdgpu_buffer_rsrc_t ws, unsigned off, unsigned tag) {
#pragma unroll 1
  for (unsigned sp = 0; sp < SPIN_CAP; ++sp) {
      asm volatile("" ::: "memory");
    const u2 f = __builtin_amdgcn_raw_buffer_load_b64(ws, off, 0, SC1);
    if (__all(f.x == tag && f.y == ~tag)) break;
    __builtin_amdgcn_s_sleep(1);
  }
  asm volatile("" ::: "memory");
}

__global__ void __launch_bounds__(512, 2)
fused_kernel(const u16* a0, const u16* a1, const u16* a2, const u16* a3, const u16* a4, const u16* a5, const u16* a6, const u16* a7,
             const u16* a8, const u16* a9, const u16* a10, const u16* a11, float* a12, char* a13) {
  extern __shared__ __attribute__((aligned(16))) char smem_[];
  char LAS* L = (char LAS*)smem_;
  const unsigned L0 = (unsigned)(size_t)L;
  ST(0);
  const int lane = threadIdx.x & 63;
  const int w8 = __builtin_amdgcn_readfirstlane(threadIdx.x >> 6), w = w8 & 3;
  unsigned cpf = 0;
  if (CPF && w8 == 0) {
    u64 pc;
    asm volatile("s_getpc_b64 %0" : "=s"(pc));
    cpf = __builtin_amdgcn_raw_buffer_load_b32(__builtin_amdgcn_make_buffer_rsrc((char*)(pc & ~127ull), 0, CPF * 4096, 0x00020000),
                                               lane < CPF ? ((blockIdx.x >> 3) * CPF + lane) * 128 : 0x80000000u, 0, 0);
  }
  typedef u64 u64x8 __attribute__((ext_vector_type(8)));
  typedef u64 u64x4 __attribute__((ext_vector_type(4)));
  typedef u64 u64x2 __attribute__((ext_vector_type(2)));
  u64x8 ka; u64x4 kb; u64x2 kc_;
  asm volatile("s_load_dwordx16 %0, %3, 0x0\n\ts_load_dwordx8 %1, %3, 0x40\n\ts_load_dwordx4 %2, %3, 0x60\n\ts_waitcnt lgkmcnt(0)"
               : "=&s"(ka), "=&s"(kb), "=&s"(kc_) : "s"(__builtin_amdgcn_kernarg_segment_ptr()) : "memory");
  const GAS u16 *xa = (const GAS u16*)ka[0], *wqkv = (const GAS u16*)ka[1], *wo = (const GAS u16*)ka[2], *xb = (const GAS u16*)ka[3];
  const GAS u16 *lre = (const GAS u16*)ka[4], *lim = (const GAS u16*)ka[5], *bre = (const GAS u16*)ka[6], *bim = (const GAS u16*)ka[7];
  const GAS u16 *cre = (const GAS u16*)kb[0], *cim = (const GAS u16*)kb[1], *ldt = (const GAS u16*)kb[2], *dsk = (const GAS u16*)kb[3];
  float* out = (float*)kc_[0]; char* wsp = (char*)kc_[1];
  const bool helper = w8 >= 4;
  const int bid = blockIdx.x, xcd = bid & 7, jj = bid >> 3;
  const int tile = (xcd >> 1) * 4 + (jj >> 3), cg = jj & 7;
  const int rot = ROT == 1 ? ((cg + 3 * (jj >> 3) + xcd) & 7) : ROT == 7 ? (((cg & 3) * 2 + (xcd >> 1)) & 7) : ((((cg + (jj >> 3)) % 3) * 3 + (xcd >> 1)) & 7);
  const int hr = HROT == 8 ? cg : HROT == 4 ? 2 * (cg & 3) : HROT == 2 ? 4 * (cg & 1) : 0;
  const int orot = 2 * hr + 4 * (jj >> 3);
  const __amdgpu_buffer_rsrc_t ws = __builtin_amdgcn_make_buffer_rsrc(wsp, 0, WS_END, 0x00020000);
  const __amdgpu_buffer_rsrc_t ro = __builtin_amdgcn_make_buffer_rsrc(out, 0, 2048 * 1024 * 4, 0x00020000);
  const int r8 = lane >> 3, c8 = lane & 7;
  const int q = lane >> 4, l15 = lane & 15, l31 = lane & 31, hh = lane >> 5;
  unsigned epoch;
#define LOAD_EPOCH() asm volatile("s_load_dword %0, %1, 0x0" : "=s"(epoch) : "s"(wsp) : "memory")

  if ((xcd & 1) == 0) {
    constexpr unsigned WA = 131072, WB = 147456, SCR = WA, TRB = 81920;
    constexpr unsigned QBUF[4] = {WB, 0, WA, WB}, OBUF[4] = {32768, 49152, 65536, 0};
    constexpr unsigned WBUF[8] = {WA, WB, 7 * 16384, 0, WA, WB, 0, WA};
    if (helper) {
      const int head = cg * 4 + w;
      unsigned vx[4], vw[2], vo[4];
#pragma unroll
      for (int i = 0; i < 4; ++i) vx[i] = (32 * w + 8 * i + r8) * 1024 + ((c8 ^ ((4 * i + (r8 >> 1)) & 7)) << 4) - 1024 * i;
#pragma unroll
      for (int i = 0; i < 2; ++i) vw[i] = (16 * w + 8 * i + r8) * 3072 + ((c8 ^ ((((r8 >> 1) & 1) | (i << 1)) << 1)) << 4) - 1024 * i;
      const GAS char* xbase = (const GAS char*)xa + (size_t)tile * 131072;
      const GAS char* wbase = (const GAS char*)wqkv + cg * 128;
      auto isx = [&](int kk, int slot) { dmax(L0 + slot * 16384 + w * 4096, xbase + ((kk + rot) & 7) * 128, vx[0], vx[1], vx[2], vx[3]); };
      auto isw = [&](unsigned dst, int kk, int seg) { dma2(L0 + dst + w * 2048, wbase + ((kk + rot) & 7) * (64 * 3072) + seg * 1024, vw[0], vw[1]); };
      auto issue = [&](int kk) { isx(kk, kk); isw(WBUF[kk], kk, 1); isw(WBUF[kk] + 8192, kk, 2); };
      auto isq = [&](int p) { isw(QBUF[p], 2 * p + 1, 0); isw(QBUF[p] + 8192, 2 * p + 2, 0); };
      ST(9); issue(0); ST(10); issue(1); ST(11);
      LOAD_EPOCH();
#pragma unroll
      for (int i = 0; i < 4; ++i) vo[i] = (8 * i + r8) * 1024 + ((c8 ^ (((r8 >> 1) & 3) << 1)) << 4) - 1024 * i;
      const GAS char* obase = (const GAS char*)wo + cg * 128;
      asm volatile("s_waitcnt vmcnt(8) lgkmcnt(0)\n\ts_barrier" : "+s"(epoch) :: "memory");
      issue(2);
      wait_bar<8>(); issue(3);
      wait_bar<8>(); issue(4);
      wait_bar<8>(); issue(5);
      wait_bar<8>(); issue(6);
      wait_bar<8>(); issue(7);
      wait_bar<8>(); isq(0);
      wait_bar<4>(); isq(1);
      wait_bar<4>(); isq(2);
      wait_bar<4>(); isq(3); isx(0, 1);
      wait_bar<8>();
      ST(1);
      const unsigned tag = epoch + 1, t = tag & 0xffu;
      const GAS char* kvb_ = (const GAS char*)wsp + WS_KV + head * 16384;
      const unsigned vk0 = lane * 16, vk1 = vk0 + 4096, vk2 = vk0 + 8192, vk3 = vk0 + 12288;
      u4 g[16], gz;
#pragma unroll
      for (int i = 0; i < 16; ++i) g[i] = (u4){0, 0, 0, 0};
      unsigned nw = 0xffffu;
      const GAS char* zb_ = (const GAS char*)wsp + WS_Z + head * 1024;
      const unsigned vz = l15 * 64 + q * 16;
      gz = (u4){0, 0, 0, 0};
#pragma unroll 1
      for (unsigned rep = 0; rep < SPIN_CAP; ++rep) {
        asm volatile("global_load_dwordx4 %0, %1, %2 sc1" : "+v"(gz) : "v"(vz), "s"(zb_) : "memory");
#define KVLD(i, v) if (nw & (1u << i)) asm volatile("global_load_dwordx4 %0, %1, %2 offset:%3 sc1" : "+v"(g[i]) : "v"(v), "s"(kvb_), "i"((i & 3) * 1024) : "memory")
        KVLD(0, vk0); KVLD(1, vk0); KVLD(2, vk0); KVLD(3, vk0); KVLD(4, vk1); KVLD(5, vk1); KVLD(6, vk1); KVLD(7, vk1);
        KVLD(8, vk2); KVLD(9, vk2); KVLD(10, vk2); KVLD(11, vk2); KVLD(12, vk3); KVLD(13, vk3); KVLD(14, vk3); KVLD(15, vk3);
        if (rep == 0) {
          asm volatile("s_waitcnt vmcnt(17)\n\ts_barrier" ::: "memory");
        }
        drain();
        asm volatile("" : "+v"(g[0]), "+v"(g[1]), "+v"(g[2]), "+v"(g[3]), "+v"(g[4]), "+v"(g[5]), "+v"(g[6]), "+v"(g[7]),
                     "+v"(g[8]), "+v"(g[9]), "+v"(g[10]), "+v"(g[11]), "+v"(g[12]), "+v"(g[13]), "+v"(g[14]), "+v"(g[15]), "+v"(gz) :: "memory");
        unsigned bad = (gz.x ^ t) | (gz.z ^ t);
#pragma unroll
        for (int i = 0; i < 16; ++i) bad |= (g[i].x ^ t) | (g[i].z ^ t);
        if (__all((bad & 0xffu) == 0)) break;
        nw = 0;
#pragma unroll
        for (int i = 0; i < 16; ++i) nw |= __all((((g[i].x ^ t) | (g[i].z ^ t)) & 0xffu) == 0) ? 0u : 1u << i;
      }
      ST(5);
      f4 kv = {0, 0, 0, 0};
#pragma unroll
      for (int i = 0; i < 16; ++i) { g[i].x ^= t; g[i].z ^= t; kv += __builtin_bit_cast(f4, g[i]); }
      gz.x ^= t; gz.z ^= t;
      f4 zz = __builtin_bit_cast(f4, gz);
#pragma unroll
      for (int r = 0; r < 4; ++r) {
        float v = zz[r];
        v += __builtin_bit_cast(float, __builtin_amdgcn_mov_dpp(__builtin_bit_cast(int, v), 0x128, 0xf, 0xf, true));
        v += __builtin_bit_cast(float, __builtin_amdgcn_mov_dpp(__builtin_bit_cast(int, v), 0x124, 0xf, 0xf, true));
        v += __builtin_bit_cast(float, __builtin_amdgcn_mov_dpp(__builtin_bit_cast(int, v), 0x122, 0xf, 0xf, true));
        v += __builtin_bit_cast(float, __builtin_amdgcn_mov_dpp(__builtin_bit_cast(int, v), 0x121, 0xf, 0xf, true));
        zz[r] = v;
      }
      *(LAS u4*)(L + SCR + w * 1024 + lane * 16) = (u4){pk2(kv[0], kv[1]), pk2(kv[2], kv[3]), pk2(zz[0], zz[1]), pk2(zz[2], zz[3])};
      ST(6);
      lbar();
      ST(7);
      lbar();
#pragma unroll
      for (int i = 0; i < 4; ++i)
        dma4(L0 + OBUF[i] + w * 4096, obase + ((4 * i + w + orot) & 15) * 32768, vo[0], vo[1], vo[2], vo[3]);
      wait_bar<0>();
      ST(8);
      HFLUSH();
      if (bid == 0 && w == 0) {
#pragma unroll 1
        for (unsigned sp = 0; sp < SPIN_CAP; ++sp) {
          asm volatile("" ::: "memory");
          const unsigned fo = (lane >> 3) * 4096 + (lane & 7) * 256;
          const u2 f0 = __builtin_amdgcn_raw_buffer_load_b64(ws, WS_FA + fo, 0, SC1), f1 = __builtin_amdgcn_raw_buffer_load_b64(ws, WS_FA + 32768 + fo, 0, SC1);
          const u2 f2_ = __builtin_amdgcn_raw_buffer_load_b64(ws, WS_FB + fo, 0, SC1), f3 = __builtin_amdgcn_raw_buffer_load_b64(ws, WS_FB + 32768 + fo, 0, SC1);
          if (__all(f0.x == tag && f0.y == ~tag && f1.x == tag && f1.y == ~tag && f2_.x == tag && f2_.y == ~tag && f3.x == tag && f3.y == ~tag)) break;
          __builtin_amdgcn_s_sleep(8);
        }
        if (lane == 0) __builtin_amdgcn_raw_buffer_store_b32(tag, ws, WS_EPOCH, 0, SC1);
      }
      return;
    }
    LOAD_EPOCH();
    const int hp = w & 1, th = w >> 1;
    unsigned warm0 = 0, warm1 = 0;
    if (TOUCH) {
      warm0 = __builtin_amdgcn_raw_buffer_load_b32(ws, lane < 16 ? WS_FA + ((tile * 2 + th) * 16 + lane) * 128 : lane < 24 ? WS_Z + (cg * 4 + w) * 1024 + (lane - 16) * 128
                                                   : lane == 24 ? WS_KV + (cg * 4 + w) * 16384 : lane == 25 ? WS_O + tile * 131072 : 0x80000000u, 0, SC1);
      warm1 = __builtin_amdgcn_raw_buffer_load_b32(ro, lane < 2 ? (tile * 128 + w * 32 + lane * 16) * 4096 : 0x80000000u, 0, SC1);
    }
    const int qq = (lane >> 2) & 3, pp = lane & 3;
    const unsigned ax0 = l15 * 128 + ((q ^ (l15 >> 1)) << 4), ax1 = l15 * 128 + (((4 + q) ^ (l15 >> 1)) << 4);
    auto waddr = [&](int h) { return (8 * q + qq) * 128 + (((2 * h + (pp >> 1)) ^ ((((qq >> 1) & 1) | ((q & 1) << 1)) << 1)) << 4) + 8 * (pp & 1); };
    const unsigned aw = waddr(w), aq0 = waddr(2 * hp), aq1 = waddr(2 * hp + 1);
    f4 ka[8], va[8];
#pragma unroll
    for (int i = 0; i < 8; ++i) { ka[i] = (f4){EPS, EPS, EPS, EPS}; va[i] = (f4){0, 0, 0, 0}; }
    bf8 X0[4], X1[4], W0[2], W1[2];
    auto rdx = [&](bf8 (&X)[4], int kk, int ks, unsigned off) {
#pragma unroll
      for (int i = 0; i < 4; ++i) X[i] = ldsr(L + kk * 16384 + (ks ? ax1 : ax0) + off + i * 2048);
    };
    auto rdw = [&](bf8 (&W)[2], int kk, int ks) {
#pragma unroll
      for (int i = 0; i < 2; ++i) { const char LAS* p = L + WBUF[kk] + aw + ks * 4096 + i * 8192; W[i] = ldstr(p, p + 512); }
    };
    auto mm = [&](const bf8 (&X)[4], const bf8 (&W)[2], int h) {
#pragma unroll
      for (int i = 0; i < 4; ++i) {
        ka[4 * h + i] = mfma16(X[i], W[0], ka[4 * h + i]);
        va[4 * h + i] = mfma16(X[i], W[1], va[4 * h + i]);
      }
    };
#define ILV(nr, nm) do { _Pragma("unroll") for (int i_ = 0; i_ < nr; ++i_) { SG(0x008, 1); SG(0x100, 1); } if (nm > nr) SG(0x008, nm - nr); } while (0)
    asm volatile("s_waitcnt lgkmcnt(0)\n\ts_barrier" : "+s"(epoch) : "v"(warm0), "v"(warm1), "v"(cpf) : "memory");
    ST(1);
    const unsigned tag = epoch + 1, t = tag & 0xffu;
    rdw(W0, 0, 0); rdx(X0, 0, 0, 0);
    SB();
#pragma unroll
    for (int kk = 0; kk < 8; ++kk) {
      rdx(X1, kk, 0, 8192); mm(X0, W0, 0); ILV(4, 8); SB();
      rdw(W1, kk, 1); rdx(X0, kk, 1, 0); mm(X1, W0, 1); ILV(8, 8); SB();
      rdx(X1, kk, 1, 8192); mm(X0, W1, 0); ILV(4, 8); SB();
      if (kk < 7) { lbar(); rdw(W0, kk + 1, 0); rdx(X0, kk + 1, 0, 0); mm(X1, W1, 1); ILV(8, 8); }
      else mm(X1, W1, 1);
      SB();
    }
    ST(2);
    const unsigned xq = th * 8192;
    auto rdq = [&](bf8 (&X)[4], bf8 (&W)[2], int s) {
      const char LAS* p = L + QBUF[s >> 2] + ((s >> 1) & 1) * 8192 + (s & 1) * 4096;
      W[0] = ldstr(p + aq0, p + aq0 + 512); W[1] = ldstr(p + aq1, p + aq1 + 512);
      rdx(X, s < 14 ? (s >> 1) + 1 : 1, s & 1, xq);
    };
    {
      const int head = cg * 4 + w;
      f4 kv = {0, 0, 0, 0}, zz = {0, 0, 0, 0};
      const u4 ones = {0x3f803f80u, 0x3f803f80u, 0x3f803f80u, 0x3f803f80u};
#pragma unroll
      for (int m = 0; m < 4; ++m) {
        const f4 a = ka[2 * m], b = ka[2 * m + 1], c = va[2 * m], d = va[2 * m + 1];
        const u4 pa = {phi2(a[0], a[1]), phi2(a[2], a[3]), phi2(b[0], b[1]), phi2(b[2], b[3])};
        const u4 pb = {pk2(c[0], c[1]), pk2(c[2], c[3]), pk2(d[0], d[1]), pk2(d[2], d[3])};
        kv = mfma16(asbf8(pa), asbf8(pb), kv);
        zz = mfma16(asbf8(pa), asbf8(ones), zz);
      }
      const u4 skv = {tagv(kv[0], t), __float_as_uint(kv[1]), tagv(kv[2], t), __float_as_uint(kv[3])};
      __builtin_amdgcn_raw_buffer_store_b128(skv, ws, WS_KV + (head * 16 + tile) * 1024 + lane * 16, 0, SC1);
      const u4 sz = {tagv(zz[0], t), __float_as_uint(zz[1]), tagv(zz[2], t), __float_as_uint(zz[3])};
      __builtin_amdgcn_raw_buffer_store_b128(sz, ws, l15 == 0 ? WS_Z + (head * 16 + tile) * 64 + q * 16 : 0x80000000u, 0, SC1);
    }
    SB();
    ST(3);
    lbar();
    rdq(X0, W0, 0);
    SB();
    f4 qa[2][4];
#pragma unroll
    for (int i = 0; i < 8; ++i) qa[i >> 2][i & 3] = (f4){EPS, EPS, EPS, EPS};
    auto mq = [&](const bf8 (&X)[4], const bf8 (&W)[2]) {
#pragma unroll
      for (int i = 0; i < 4; ++i) { qa[0][i] = mfma16(W[0], X[i], qa[0][i]); qa[1][i] = mfma16(W[1], X[i], qa[1][i]); }
    };
#pragma unroll
    for (int s = 0; s < 16; s += 2) {
      rdq(X1, W1, s + 1); mq(X0, W0); ILV(8, 8); SB();
      if (s < 14) { if ((s & 3) == 2) lbar(); rdq(X0, W0, s + 2); mq(X1, W1); ILV(8, 8); }
      else mq(X1, W1);
      SB();
    }
    ST(4);
    u2 pq[2][4];
#pragma unroll
    for (int i = 0; i < 8; ++i) pq[i >> 2][i & 3] = (u2){phi2(qa[i >> 2][i & 3][0], qa[i >> 2][i & 3][1]), phi2(qa[i >> 2][i & 3][2], qa[i >> 2][i & 3][3])};
    unsigned ao[4], og[4];
#pragma unroll
    for (int i = 0; i < 4; ++i) og[i] = WS_O + ((tile * 8 + th * 4 + i) * 16 + cg * 2 + hp) * 1024 + lane * 16;
#pragma unroll
    for (int nt = 0; nt < 4; ++nt) ao[nt] = (4 * q + qq) * 128 + ((((2 * nt) ^ (((2 * q + (qq >> 1)) & 3) << 1)) + (pp >> 1)) << 4) + 8 * (pp & 1);
    asm volatile("s_barrier" : "+v"(pq[0][0]), "+v"(pq[0][1]), "+v"(pq[0][2]), "+v"(pq[0][3]), "+v"(pq[1][0]), "+v"(pq[1][1]), "+v"(pq[1][2]), "+v"(pq[1][3]),
                 "+v"(og[0]), "+v"(og[1]), "+v"(og[2]), "+v"(og[3]) :: "memory");
    ST(5);
    const f4 zero4 = {0, 0, 0, 0}, eps4 = {EPS, EPS, EPS, EPS};
    f4 num[2][4], den[2][4];
#pragma unroll
    for (int h = 0; h < 2; ++h) {
      const u4 kz = *(const LAS u4*)(L + SCR + (2 * hp + h) * 1024 + lane * 16);
#pragma unroll
      for (int i = 0; i < 4; ++i) { den[h][i] = mfma16k16((u2){kz.z, kz.w}, pq[h][i], eps4); num[h][i] = mfma16k16((u2){kz.x, kz.y}, pq[h][i], zero4); }
    }
    SB();
#pragma unroll
    for (int i = 0; i < 4; ++i) {
      u4 so;
#pragma unroll
      for (int h = 0; h < 2; ++h) {
        const float rc = __builtin_amdgcn_rcpf(den[h][i][0]);
        const f4 n = num[h][i];
        so[2 * h] = pk2((f2){n[0], n[1]} * rc);
        so[2 * h + 1] = pk2((f2){n[2], n[3]} * rc);
      }
      __builtin_amdgcn_raw_buffer_store_b128(so, ws, og[i], 0, SC1);
    }
    asm volatile("s_barrier" ::: "memory");
    ST(6);
    drain();
    __builtin_amdgcn_raw_buffer_store_b64((u2){tag, ~tag}, ws, lane == 0 ? WS_FA + ((tile * 2 + th) * 16 + cg * 2 + hp) * 128 : 0x80000000u, 0, SC1);
    unsigned tw[4], trd[4], so_ = ((tile * 128 + w * 32 + q) * 1024 + cg * 64 + l15 * 4) * 4;
#pragma unroll
    for (int i = 0; i < 4; ++i) {
      tw[i] = TRB + w * 8192 + l15 * 256 + (((4 * i + q) ^ l15) << 4);
      trd[i] = TRB + w * 8192 + (4 * i + q) * 256 + ((l15 ^ (4 * i + q)) << 4);
      asm volatile("" : "+v"(tw[i]), "+v"(trd[i]));
    }
    asm volatile("s_barrier" : "+v"(so_) :: "memory");
    ST(7);
    f4 acc[2][4];
#pragma unroll
    for (int i = 0; i < 8; ++i) acc[i >> 2][i & 3] = zero4;
    const unsigned ob = WS_O + (tile * 8 + w * 2) * 16384 + lane * 16;
    u4 of[32];
    bf8 wf[2][4];
    auto rdo = [&](int k) {
#pragma unroll
      for (int nt = 0; nt < 4; ++nt) { const char LAS* p = L + OBUF[k >> 2] + (k & 3) * 4096 + ao[nt]; wf[k & 1][nt] = ldstr(p, p + 2048); }
    };
    auto ldo = [&](int k) {
      const unsigned so = ((k + orot) & 15) * 1024;
      of[2 * k] = __builtin_amdgcn_raw_buffer_load_b128(ws, ob, so, PAY_AUX);
      of[2 * k + 1] = __builtin_amdgcn_raw_buffer_load_b128(ws, ob + 16384, so, PAY_AUX);
    };
    rdo(0);
    wait_flags(ws, WS_FA + ((tile * 2 + th) * 16 + l15) * 128, tag);
    ST(8); CT(14);
#pragma unroll
    for (int k = 0; k < OD; ++k) ldo(k);
    SB();
#pragma unroll
    for (int k = 0; k < 16; ++k) {
      if (k + OD < 16) ldo(k + OD);
      if (k + 1 < 16) rdo(k + 1);
#pragma unroll
      for (int nt = 0; nt < 4; ++nt) {
        acc[0][nt] = mfma16(wf[k & 1][nt], asbf8(of[2 * k]), acc[0][nt]);
        acc[1][nt] = mfma16(wf[k & 1][nt], asbf8(of[2 * k + 1]), acc[1][nt]);
      }
#pragma unroll
      for (int i_ = 0; i_ < 8; ++i_) { SG(0x008, 1); SG(0x100, 1); if (i_ == 1 || i_ == 5) SG(0x020, 1); }
      SB();
    }
    ST(9); CT(15);
#pragma unroll
    for (int mt = 0; mt < 2; ++mt)
#pragma unroll
      for (int nt = 0; nt < 4; ++nt) {
        const f4 v = acc[mt][nt];
        const unsigned p0 = pk2(v[0], v[1]), p1 = pk2(v[2], v[3]);
        *(LAS f4*)(L + tw[nt] + mt * 4096) = (f4){bflo(p0), bfhi(p0), bflo(p1), bfhi(p1)};
      }
    u4 ov[8];
#pragma unroll
    for (int i = 0; i < 8; ++i) ov[i] = *(const LAS u4*)(L + trd[i & 3] + (i >> 2) * 4096);
    SB();
#pragma unroll
    for (int i = 0; i < 8; ++i) __builtin_amdgcn_raw_buffer_store_b128(ov[i], ro, so_, i * 16384, OUT_AUX);
    ST(10);
  } else {
    constexpr unsigned SLOT = 32768, SCR = SLOT, PAR = 4 * SLOT;
    const int crot = hr + 2 * (jj >> 3);
    const int wp = w & 1, wt = w >> 1;
    const unsigned ecol = WS_EST + (cg * 64 + wp * 32 + l31) * 8;
    if (helper) {
      unsigned vx[4], vw[4], vc[4];
#pragma unroll
      for (int i = 0; i < 4; ++i) {
        const unsigned sw = (c8 ^ ((4 * i + (r8 >> 1)) & 7)) << 4;
        vx[i] = (64 * (w >> 1) + 32 * (r8 >> 2) + 16 * (r8 & 1) + 8 * (w & 1) + ((r8 >> 1) & 1) + 2 * i) * 1024 + sw - 1024 * i;
        vw[i] = (16 * (i >> 1) + 8 * (r8 >> 2) + 4 * (i & 1) + (r8 & 3)) * 1024 + sw - 1024 * i;
        vc[i] = (8 * i + r8) * 1024 + sw - 1024 * i;
      }
      const GAS char* xbase = (const GAS char*)xb + (size_t)tile * 131072;
      const GAS char* wbase = (const GAS char*)((w & 1) ? bim : bre) + (cg * 64 + (w >> 1) * 32) * 1024;
      auto issue = [&](int kk) {
        const int kc = (kk + rot) & 7;
        const unsigned s = L0 + (kk % 5) * SLOT + w * 4096;
        dmax(s, xbase + kc * 128, vx[0], vx[1], vx[2], vx[3]);
        dma4(s + 16384, wbase + kc * 128, vw[0], vw[1], vw[2], vw[3]);
      };
      ST(9); issue(0); ST(10); issue(1); ST(11);
      LOAD_EPOCH();
      const GAS char* cbase = (const GAS char*)((w & 1) ? cim : cre) + cg * 65536;
      auto issue_c = [&](int piece) {
        const unsigned s = L0 + ((piece + 3) % 5) * SLOT + w * 8192;
        const GAS char* cb = cbase + ((crot + 2 * piece + (w >> 1)) & 7) * 128;
        dma4(s, cb, vc[0], vc[1], vc[2], vc[3]);
        dma4(s + 4096, cb + 32768, vc[0], vc[1], vc[2], vc[3]);
      };
      asm volatile("s_waitcnt vmcnt(8) lgkmcnt(0)\n\ts_barrier" : "+s"(epoch) :: "memory");
      issue(2);
      const f4 par = *(const LAS f4*)(L + PAR + w * 1024 + lane * 16);
      const f2 l64 = {par[0], par[1]}, l128 = {par[2], par[3]};
      f2 l256 = cmul(l128, l128);
      f2 wgt = hh ? l64 : (f2){1.f, 0.f};
      if (wt) wgt = cmul(wgt, l128);
      const unsigned tag = epoch + 1, t = tag & 0xffu;
      unsigned eo[8], et[8];
#pragma unroll
      for (int m = 0; m < 8; ++m) {
        const int i = 2 * tile - 1 - (2 * wt + hh) - 4 * m;
        eo[m] = i >= 0 ? ecol + i * 4096 : 0x80000000u;
        et[m] = i >= 0 ? t : 0u;
        asm volatile("" : "+v"(eo[m]), "+v"(et[m]));
      }
      asm volatile("" : "+v"(l256), "+v"(wgt));
      wait_bar<8>(); issue(3);
      wait_bar<8>(); issue(4);
      wait_bar<8>(); issue(5);
      wait_bar<8>(); issue(6);
      wait_bar<8>(); issue(7);
      wait_bar<8>(); issue_c(0);
      wait_bar<8>(); ST(1);
      if (NC_EARLY > 1) issue_c(1);
      if (NC_EARLY > 2) issue_c(2);
      ST(2);
      f2 cin;
#pragma unroll 1
      for (unsigned sp = 0; sp < SPIN_CAP; ++sp) {
      asm volatile("" ::: "memory");
        u2 g[8];
#pragma unroll
        for (int m = 0; m < 8; ++m) g[m] = __builtin_amdgcn_raw_buffer_load_b64(ws, eo[m], 0, SC1);
        unsigned bad = 0;
        cin = (f2){0, 0};
#pragma unroll
        for (int m = 7; m >= 0; --m) {
          const unsigned cx = g[m].x ^ et[m];
          bad |= cx;
          cin = cfma(l256, cin, (f2){__uint_as_float(cx), __uint_as_float(g[m].y)});
        }
        if (__all((bad & 0xffu) == 0)) break;
      }
      ST(5);
      cin = cmul(cin, wgt);
      f2 chi;
      const f2 clo = swap_lo(cin, chi);
      if (hh == 0) *(LAS f2*)(L + SCR + (wp * 2 + wt) * 256 + l31 * 8) = clo + chi;
      ST(6);
      lbar();
      lbar();
      ST(7);
      if (NC_EARLY <= 1) issue_c(1);
      if (NC_EARLY <= 2) issue_c(2);
      issue_c(3);
      wait_bar<0>();
      ST(8); HFLUSH();
      return;
    }
    LOAD_EPOCH();
    const int pl = cg * 64 + wp * 32 + ((l31 & 0x13) | ((l31 & 4) << 1) | ((l31 & 8) >> 1));
    const unsigned plr = lre[pl], pli = lim[pl], pdt = ldt[pl];
    unsigned warm0 = 0, warm1 = 0;
    if (TOUCH) {
      warm0 = __builtin_amdgcn_raw_buffer_load_b32(ws, lane < 16 ? WS_FB + ((tile * 2 + (w >> 1)) * 16 + lane) * 128 : lane == 16 ? WS_EST + tile * 8192
                                                   : lane == 17 ? WS_H + (tile * 4 + w) * 65536 : 0x80000000u, 0, SC1);
      warm1 = __builtin_amdgcn_raw_buffer_load_b32(ro, lane < 2 ? (tile * 128 + w * 32 + lane * 16) * 4096 + 2048 : 0x80000000u, 0, SC1);
    }
    f2 lam, kap, l16, l32, l64, l128;
    {
      const float lr = bflo(plr), li = bflo(pli);
      const float dt = bflo(pk2(expf(bflo(pdt)), 0.f));
      const float mag = expf(dt * lr);
      float sn, cs;
      sincosf(dt * li, &sn, &cs);
      lam = (f2){mag * cs, mag * sn};
      const float inv = 2.f / (lr * lr + li * li), nr = lam.x - 1.f, ni = lam.y;
      kap = (f2){(nr * lr + ni * li) * inv, (ni * lr - nr * li) * inv};
      f2 t = cmul(lam, lam); t = cmul(t, t); t = cmul(t, t);
      l16 = cmul(t, t); l32 = cmul(l16, l16); l64 = cmul(l32, l32); l128 = cmul(l64, l64);
      *(LAS f4*)(L + PAR + w * 1024 + lane * 16) = (f4){l64.x, l64.y, l128.x, l128.y};
    }
    unsigned ar[4];
#pragma unroll
    for (int s = 0; s < 4; ++s) ar[s] = l31 * 128 + (((2 * s + hh) ^ ((lane >> 1) & 7)) << 4);
    f16v ur[2], ui[2];
#pragma unroll
    for (int i = 0; i < 16; ++i) { ur[0][i] = 0; ur[1][i] = 0; ui[0][i] = 0; ui[1][i] = 0; }
    bf8 F0[4], F1[4];
    auto rd = [&](bf8 (&F)[4], int kk, int s) {
      const char LAS* sx = L + (kk % 5) * SLOT + wt * 8192 + ar[s];
      const char LAS* sw = L + (kk % 5) * SLOT + 16384 + wp * 8192 + ar[s];
      F[0] = ldsr(sx); F[1] = ldsr(sw); F[2] = ldsr(sw + 4096); F[3] = ldsr(sx + 4096);
    };
    auto mm = [&](const bf8 (&F)[4]) {
      ur[0] = mfma32(F[0], F[1], ur[0]); ui[0] = mfma32(F[0], F[2], ui[0]);
      ur[1] = mfma32(F[3], F[1], ur[1]); ui[1] = mfma32(F[3], F[2], ui[1]);
    };
#define ILB() do { SG(0x008, 1); SG(0x100, 2); SG(0x008, 1); SG(0x100, 2); SG(0x008, 2); } while (0)
    asm volatile("s_waitcnt lgkmcnt(0)\n\ts_barrier" : "+s"(epoch), "+v"(lam), "+v"(kap), "+v"(l16), "+v"(l32), "+v"(l64) : "v"(warm0), "v"(warm1), "v"(cpf) : "memory");
    ST(1);
    const unsigned tag = epoch + 1, t = tag & 0xffu;
    rd(F0, 0, 0);
    SB();
#pragma unroll
    for (int kk = 0; kk < 8; ++kk) {
      rd(F1, kk, 1); mm(F0); ILB(); SB();
      rd(F0, kk, 2); mm(F1); ILB(); SB();
      rd(F1, kk, 3); mm(F0); ILB(); SB();
      if (kk < 7) { lbar(); if (kk == 6) ST(2); rd(F0, kk + 1, 0); mm(F1); ILB(); }
      else mm(F1);
      SB();
    }
    ST(3);
    f2 gr[16], gi[16];
#pragma unroll
    for (int a = 0; a < 16; ++a) {
      gr[a] = (f2){ur[a >> 3][2 * (a & 7)], ur[a >> 3][2 * (a & 7) + 1]};
      gi[a] = (f2){ui[a >> 3][2 * (a & 7)], ui[a >> 3][2 * (a & 7) + 1]};
    }
    {
      f2 LR = {lam.x, lam.x}, LI = {lam.y, lam.y}, LN = {-lam.y, -lam.y};
      asm volatile("s_nop 11" : "+v"(gr[0]), "+v"(gi[0]), "+v"(gr[8]), "+v"(gi[8]), "+v"(LR), "+v"(LI), "+v"(LN));
#pragma unroll
      for (int a = 1; a < 16; ++a)
        asm volatile("v_pk_fma_f32 %0, %6, %3, %0\n\tv_pk_fma_f32 %1, %5, %2, %1\n\tv_pk_fma_f32 %0, %4, %2, %0\n\tv_pk_fma_f32 %1, %4, %3, %1"
                     : "+v"(gr[a]), "+v"(gi[a]) : "v"(gr[a - 1]), "v"(gi[a - 1]), "v"(LR), "v"(LI), "v"(LN));
    }
    const f2 er = gr[15], ei = gi[15];
    const f2 e0 = {er.x, ei.x};
    const f2 e32 = cfma(l16, e0, (f2){er.y, ei.y});
    f2 ehi;
    const f2 elo = swap_lo(e32, ehi);
    const f2 e64 = cfma(l32, elo, ehi);
    __builtin_amdgcn_raw_buffer_store_b64((u2){tagv(e64.x, t), __float_as_uint(e64.y)}, ws, hh == 0 ? ecol + (tile * 2 + wt) * 4096 : 0x80000000u, 0, SC1);
    if (wt == 0 && hh == 0) *(LAS f2*)(L + SCR + 1024 + wp * 256 + l31 * 8) = e64;
    SB();
    ST(4);
    auto tokr = [&](int r) { return tile * 128 + 64 * (w >> 1) + 32 * ((r >> 2) & 1) + 16 * (r & 1) + 8 * (w & 1) + ((r >> 1) & 1) + 2 * (r >> 3); };
    u2 xd[8];
    const u2 dv = *(const GAS u2*)(dsk + cg * 64 + l15 * 4);
#pragma unroll
    for (int i = 0; i < 8; ++i) xd[i] = *(const GAS u2*)(xb + tokr(4 * i + q) * 512 + cg * 64 + l15 * 4);
    SB();
    {
      const f2 KR = {kap.x, kap.x}, KI = {kap.y, kap.y};
#pragma unroll
      for (int a = 0; a < 16; ++a) {
        const f2 xr = gr[a], xi = gi[a];
        gr[a] = fma2(KR, xr, -KI * xi); gi[a] = fma2(-KR, xi, -KI * xr);
        asm volatile("" : "+v"(gr[a]), "+v"(gi[a]));
      }
    }
    f2 pw[16];
    pw[0] = (f2){lam.x, -lam.y};
#pragma unroll
    for (int k = 1; k < 16; ++k) { pw[k] = cmul(pw[k >> 1], pw[(k - 1) >> 1]); asm volatile("" : "+v"(pw[k])); }
    u4 idn[2];
#pragma unroll
    for (int s = 0; s < 2; ++s)
#pragma unroll
      for (int jq = 0; jq < 4; ++jq) {
        const int r0 = 16 * s + 8 * (jq >> 1) + 4 * hh + 2 * (jq & 1);
        idn[s][jq] = (l31 == r0 ? 0x3f80u : 0u) | (l31 == r0 + 1 ? 0x3f800000u : 0u);
      }
    asm volatile("" : "+v"(idn[0]), "+v"(idn[1]));
    f2 m0 = hh ? cmul(kap, l32) : kap, b0 = hh ? cmul(kap, elo) : (f2){0.f, 0.f};
    f2 m1 = cmul(l16, m0), b1 = cfma(l16, b0, cmul(kap, e0));
    unsigned ho[2];
    ho[0] = WS_H + (tile * 4 + wt * 2) * 65536 + (cg * 2 + wp) * 4096 + lane * 16; ho[1] = ho[0] + 65536;
    asm volatile("s_waitcnt lgkmcnt(0)\n\ts_barrier" : "+v"(ho[0]), "+v"(ho[1]), "+v"(m0), "+v"(b0), "+v"(m1), "+v"(b1) :: "memory");
    ST(5);
    f2 cin = *(const LAS f2*)(L + SCR + wp * 512 + l31 * 8) + *(const LAS f2*)(L + SCR + wp * 512 + 256 + l31 * 8);
    const f2 esib = *(const LAS f2*)(L + SCR + 1024 + wp * 256 + l31 * 8);
    if (wt) cin = cfma(l64, cin, esib);
    const f2 d0 = cfma(m0, cin, b0), d1 = cfma(m1, cin, b1);
    const f2 DR = {d0.x, d1.x}, DJ = {-d0.y, -d1.y};
    f16v zero16;
#pragma unroll
    for (int i = 0; i < 16; ++i) zero16[i] = 0;
    auto fix = [&](int m, int c) {
      u4 a[2];
#pragma unroll
      for (int i = 0; i < 8; ++i) {
        const f2 P = pw[8 * m + i], PR = {P.x, P.x}, PI = {P.y, P.y};
        const f2 h = c ? fma2(PI, DR, fma2(PR, DJ, gi[8 * m + i])) : fma2(-PI, DJ, fma2(PR, DR, gr[8 * m + i]));
        a[i >> 2][i & 3] = pk2(h);
      }
      return mfma32(asbf8(a[1]), asbf8(idn[1]), mfma32(asbf8(a[0]), asbf8(idn[0]), zero16));
    };
    auto put = [&](const f16v& T, int m, int c) {
      const u4 o0 = {pk2(T[0], T[1]), pk2(T[2], T[3]), pk2(T[4], T[5]), pk2(T[6], T[7])};
      const u4 o1 = {pk2(T[8], T[9]), pk2(T[10], T[11]), pk2(T[12], T[13]), pk2(T[14], T[15])};
      __builtin_amdgcn_raw_buffer_store_b128(o0, ws, ho[m] + c * 2048, 0, SC1);
      __builtin_amdgcn_raw_buffer_store_b128(o1, ws, ho[m] + c * 2048 + 1024, 0, SC1);
    };
    {
      const f16v T0 = fix(0, 0); SB();
      const f16v T1 = fix(0, 1); SB();
      put(T0, 0, 0);
      const f16v T2 = fix(1, 0); SB();
      put(T1, 0, 1);
      const f16v T3 = fix(1, 1); SB();
      put(T2, 1, 0); SB();
      put(T3, 1, 1);
    }
    asm volatile("s_barrier" ::: "memory");
    ST(6);
    drain();
    __builtin_amdgcn_raw_buffer_store_b64((u2){tag, ~tag}, ws, lane == 0 ? WS_FB + ((tile * 2 + wt) * 16 + cg * 2 + wp) * 128 : 0x80000000u, 0, SC1);
    ST(7);
    unsigned pull = 0;
    if (PULL) pull = __builtin_amdgcn_raw_buffer_load_b32(ws, WS_H + (tile * 4 + wt * 2 + hh) * 65536 + (cg * 2 + wp) * 4096 + l31 * 128, 0, SC1);
    unsigned tw[8], trd[4], so_ = ((tile * 128 + 64 * (w >> 1) + 8 * (w & 1) + 16 * (q & 1) + (q >> 1)) * 1024 + 512 + cg * 64 + l15 * 4) * 4;
#pragma unroll
    for (int i = 0; i < 8; ++i) { tw[i] = 2 * SLOT + w * 8192 + l31 * 256 + (((2 * i + hh) ^ l15) << 4); asm volatile("" : "+v"(tw[i])); }
#pragma unroll
    for (int i = 0; i < 4; ++i) { trd[i] = 2 * SLOT + w * 8192 + (4 * i + q) * 256 + ((l15 ^ (4 * i + q)) << 4); asm volatile("" : "+v"(trd[i])); }
    asm volatile("" : "+v"(so_));
#pragma unroll
    for (int i = 0; i < 8; ++i) {
      const unsigned p0 = pk2(bflo(xd[i].x) * bflo(dv.x), bfhi(xd[i].x) * bfhi(dv.x)), p1 = pk2(bflo(xd[i].y) * bflo(dv.y), bfhi(xd[i].y) * bfhi(dv.y));
      *(LAS f4*)(L + trd[i & 3] + (i >> 2) * 4096) = (f4){bflo(p0), bfhi(p0), bflo(p1), bfhi(p1)};
    }
    f16v acc[2];
#pragma unroll
    for (int i = 0; i < 8; ++i) {
      const f4 v = *(const LAS f4*)(L + tw[i]);
#pragma unroll
      for (int j = 0; j < 4; ++j) acc[i >> 2][4 * (i & 3) + j] = v[j];
    }
    asm volatile("s_barrier" : "+v"(acc[0]), "+v"(acc[1]) :: "memory");
    const unsigned hb = WS_H + (tile * 4 + w) * 65536 + lane * 16;
    u4 hf[64];
    bf8 cf[2][4];
    auto ldh = [&](int i) { hf[i] = __builtin_amdgcn_raw_buffer_load_b128(ws, hb + (i & 7) * 1024, ((crot + (i >> 3)) & 7) * 8192, PAY_AUX); };
    auto rdc = [&](int g) {
      const int tt = g >> 2, i = (g & 3) * 2;
      const char LAS* cb = L + (((tt >> 1) + 3) % 5) * SLOT + (tt & 1) * 16384 + ((i >> 1) & 1) * 8192;
      cf[g & 1][0] = ldsr(cb + ar[(i >> 2) * 2]); cf[g & 1][1] = ldsr(cb + ar[(i >> 2) * 2] + 4096);
      cf[g & 1][2] = ldsr(cb + ar[(i >> 2) * 2 + 1]); cf[g & 1][3] = ldsr(cb + ar[(i >> 2) * 2 + 1] + 4096);
    };
    rdc(0);
    wait_flags(ws, WS_FB + ((tile * 2 + (w >> 1)) * 16 + l15) * 128, tag);
    asm volatile("" :: "v"(pull));
    ST(8); CT(14);
#pragma unroll
    for (int i = 0; i < HD; ++i) ldh(i);
    SB();
#pragma unroll
    for (int g = 0; g < 32; ++g) {
      if (2 * g + HD < 64) { ldh(2 * g + HD); ldh(2 * g + HD + 1); }
      if (g + 1 < 32) rdc(g + 1);
      acc[0] = mfma32(cf[g & 1][0], asbf8(hf[2 * g]), acc[0]);
      acc[1] = mfma32(cf[g & 1][1], asbf8(hf[2 * g]), acc[1]);
      acc[0] = mfma32(cf[g & 1][2], asbf8(hf[2 * g + 1]), acc[0]);
      acc[1] = mfma32(cf[g & 1][3], asbf8(hf[2 * g + 1]), acc[1]);
      SG(0x008, 1); SG(0x100, 2); SG(0x008, 1); SG(0x100, 2); SG(0x008, 1); SG(0x020, 1); SG(0x008, 1); SG(0x020, 1);
      SB();
    }
    ST(9); CT(15);
#pragma unroll
    for (int i = 0; i < 8; ++i)
      *(LAS f4*)(L + tw[i]) = (f4){acc[i >> 2][4 * (i & 3)], acc[i >> 2][4 * (i & 3) + 1], acc[i >> 2][4 * (i & 3) + 2], acc[i >> 2][4 * (i & 3) + 3]};
    u4 ov[8];
#pragma unroll
    for (int i = 0; i < 8; ++i) ov[i] = *(const LAS u4*)(L + trd[i & 3] + (i >> 2) * 4096);
    SB();
#pragma unroll
    for (int i = 0; i < 8; ++i) __builtin_amdgcn_raw_buffer_store_b128(ov[i], ro, so_, (32 * (i & 1) + 2 * (i >> 1)) * 4096, OUT_AUX);
    ST(10);
  }
}

extern "C" void kernel_launch(void* const* d_in, const int* in_sizes, int n_in, void* d_out, int out_size, void* d_ws, size_t ws_size,
                              hipStream_t stream) {
  (void)hipFuncSetAttribute((const void*)fused_kernel, hipFuncAttributeMaxDynamicSharedMemorySize, LDS_BYTES);
  fused_kernel<<<256, 512, LDS_BYTES, stream>>>((const u16*)d_in[0], (const u16*)d_in[1], (const u16*)d_in[2], (const u16*)d_in[3],
                                               (const u16*)d_in[4], (const u16*)d_in[5], (const u16*)d_in[6], (const u16*)d_in[7],
                                               (const u16*)d_in[8], (const u16*)d_in[9], (const u16*)d_in[10], (const u16*)d_in[11],
                                               (float*)d_out, (char*)d_ws);
}
```

```cpp
#define ROT 8
#include <hip/hip_runtime.h>
#include <hip/hip_bf16.h>

#define DI __device__ __forceinline__
#define LAS __attribute__((address_space(3)))
#define GAS __attribute__((address_space(1)))
typedef unsigned short u16;
typedef unsigned long long u64;
typedef __bf16 bf8 __attribute__((ext_vector_type(8)));
typedef __bf16 bf2 __attribute__((ext_vector_type(2)));
typedef short s4 __attribute__((ext_vector_type(4)));
typedef short s8 __attribute__((ext_vector_type(8)));
typedef float f16v __attribute__((ext_vector_type(16)));
typedef float f4 __attribute__((ext_vector_type(4)));
typedef float f2 __attribute__((ext_vector_type(2)));
typedef unsigned u4 __attribute__((ext_vector_type(4)));
typedef unsigned u2 __attribute__((ext_vector_type(2)));

constexpr unsigned WS_EPOCH = 0;
constexpr unsigned WS_FA = 4096;
constexpr unsigned WS_FB = WS_FA + 65536;
constexpr unsigned WS_EST = WS_FB + 65536;
constexpr unsigned WS_Z = WS_EST + 131072;
constexpr unsigned WS_KV = WS_Z + 32768;
constexpr unsigned WS_O = 1u << 20;
constexpr unsigned WS_H = 3u << 20;
constexpr unsigned WS_END = 7u << 20;
constexpr unsigned WS_STAMP = 8u << 20;
constexpr int LDS_BYTES = 160 * 1024;
constexpr unsigned SPIN_CAP = 200000;
constexpr int SC1 = 16;

#define NC_EARLY 2
#define OUT_AUX 16
#define PAY_AUX 16
#define PULL 0
#define TOUCH 1
#define CPF 10
#define HROT 1
#define XNT 0
#define PD 8
#define FD 0
#define TD 0
#define NODRAIN 0
#define WOX 2
#define OD 6
#define HD 12

#define CT(i) do {} while (0)
#define ST(i) do {} while (0)
#define HFLUSH() do {} while (0)
#define SB() __builtin_amdgcn_sched_barrier(0)
#define SG(m, n) __builtin_amdgcn_sched_group_barrier(m, n, 0)

DI unsigned pk2(float a, float b) { f2 v = {a, b}; return __builtin_bit_cast(unsigned, __builtin_convertvector(v, bf2)); }
DI unsigned pk2(f2 v) { return __builtin_bit_cast(unsigned, __builtin_convertvector(v, bf2)); }
DI float bflo(unsigned v) { return __uint_as_float(v << 16); }
DI float bfhi(unsigned v) { return __uint_as_float(v & 0xffff0000u); }
DI bf8 asbf8(u4 v) { return __builtin_bit_cast(bf8, v); }
DI f16v mfma32(bf8 a, bf8 b, f16v c) { return __builtin_amdgcn_mfma_f32_32x32x16_bf16(a, b, c, 0, 0, 0); }
DI f4 mfma16(bf8 a, bf8 b, f4 c) { return __builtin_amdgcn_mfma_f32_16x16x32_bf16(a, b, c, 0, 0, 0); }
DI f4 mfma16k16(u2 a, u2 b, f4 c) { return __builtin_amdgcn_mfma_f32_16x16x16bf16_1k(__builtin_bit_cast(s4, a), __builtin_bit_cast(s4, b), c, 0, 0, 0); }
DI bf8 ldsr(const char LAS* p) { return *(const LAS bf8*)p; }
DI bf8 ldstr(const char LAS* p0, const char LAS* p1) {
  s4 a = __builtin_amdgcn_ds_read_tr16_b64_v4i16((LAS s4*)p0), b = __builtin_amdgcn_ds_read_tr16_b64_v4i16((LAS s4*)p1);
  return __builtin_bit_cast(bf8, (s8)__builtin_shufflevector(a, b, 0, 1, 2, 3, 4, 5, 6, 7));
}
DI void dma4(unsigned lds, const GAS char* sb, unsigned v0, unsigned v1, unsigned v2, unsigned v3) {
  asm volatile("s_mov_b32 m0, %0\n\ts_nop 0\n\tglobal_load_lds_dwordx4 %1, %5\n\tglobal_load_lds_dwordx4 %2, %5 offset:1024\n\t"
               "global_load_lds_dwordx4 %3, %5 offset:2048\n\tglobal_load_lds_dwordx4 %4, %5 offset:3072"
               :: "s"(lds), "v"(v0), "v"(v1), "v"(v2), "v"(v3), "s"(sb) : "memory");
}
DI void dma4nt(unsigned lds, const GAS char* sb, unsigned v0, unsigned v1, unsigned v2, unsigned v3) {
  asm volatile("s_mov_b32 m0, %0\n\ts_nop 0\n\tglobal_load_lds_dwordx4 %1, %5 nt\n\tglobal_load_lds_dwordx4 %2, %5 offset:1024 nt\n\t"
               "global_load_lds_dwordx4 %3, %5 offset:2048 nt\n\tglobal_load_lds_dwordx4 %4, %5 offset:3072 nt"
               :: "s"(lds), "v"(v0), "v"(v1), "v"(v2), "v"(v3), "s"(sb) : "memory");
}
DI void dmax(unsigned lds, const GAS char* sb, unsigned v0, unsigned v1, unsigned v2, unsigned v3) { if (XNT) dma4nt(lds, sb, v0, v1, v2, v3); else dma4(lds, sb, v0, v1, v2, v3); }
DI void dma2(unsigned lds, const GAS char* sb, unsigned v0, unsigned v1) {
  asm volatile("s_mov_b32 m0, %0\n\ts_nop 0\n\tglobal_load_lds_dwordx4 %1, %3\n\tglobal_load_lds_dwordx4 %2, %3 offset:1024"
               :: "s"(lds), "v"(v0), "v"(v1), "s"(sb) : "memory");
}
DI void lbar() { asm volatile("s_waitcnt lgkmcnt(0)\n\ts_barrier" ::: "memory"); }
template <int N> DI void wait_bar() { asm volatile("s_waitcnt vmcnt(%0) lgkmcnt(0)\n\ts_barrier" :: "i"(N) : "memory"); }
DI void drain() { asm volatile("s_waitcnt vmcnt(0)" ::: "memory"); }
DI unsigned tagv(float x, unsigned t) { return (__float_as_uint(x) & 0xffffff00u) | t; }
DI f2 cmul(f2 a, f2 b) { return (f2){a.x * b.x - a.y * b.y, a.x * b.y + a.y * b.x}; }
DI f2 cfma(f2 a, f2 b, f2 c) { return (f2){fmaf(a.x, b.x, fmaf(-a.y, b.y, c.x)), fmaf(a.x, b.y, fmaf(a.y, b.x, c.y))}; }
DI f2 fma2(f2 a, f2 b, f2 c) { return __builtin_elementwise_fma(a, b, c); }
constexpr float EPS = 1e-6f, LOG2E = 1.44269504088896341f;
DI unsigned phi2(float s0, float s1) {
  const f2 a = fma2((f2){s0, s1}, (f2){LOG2E, LOG2E}, (f2){-EPS * LOG2E, -EPS * LOG2E});
  const f2 e = {__builtin_amdgcn_fmed3f(__builtin_amdgcn_exp2f(a.x), 0.f, 1.f), __builtin_amdgcn_fmed3f(__builtin_amdgcn_exp2f(a.y), 0.f, 1.f)};
  return pk2(e + (f2){__builtin_amdgcn_fmed3f(s0, EPS, __builtin_inff()), __builtin_amdgcn_fmed3f(s1, EPS, __builtin_inff())});
}
DI f2 swap_lo(f2 v, f2& hi) {
  const auto x = __builtin_amdgcn_permlane32_swap(__float_as_uint(v.x), __float_as_uint(v.x), false, false);
  const auto y = __builtin_amdgcn_permlane32_swap(__float_as_uint(v.y), __float_as_uint(v.y), false, false);
  hi = (f2){__uint_as_float(x[1]), __uint_as_float(y[1])};
  return (f2){__uint_as_float(x[0]), __uint_as_float(y[0])};
}
DI void wait_flags(__amdgpu_buffer_rsrc_t ws, unsigned off, unsigned tag) {
#pragma unroll 1
  for (unsigned sp = 0; sp < SPIN_CAP; ++sp) {
      asm volatile("" ::: "memory");
    const u2 f = __builtin_amdgcn_raw_buffer_load_b64(ws, off, 0, SC1);
    if (__all(f.x == tag && f.y == ~tag)) break;
    __builtin_amdgcn_s_sleep(1);
  }
  asm volatile("" ::: "memory");
}

__global__ void __launch_bounds__(512, 2)
fused_kernel(const u16* a0, const u16* a1, const u16* a2, const u16* a3, const u16* a4, const u16* a5, const u16* a6, const u16* a7,
             const u16* a8, const u16* a9, const u16* a10, const u16* a11, float* a12, char* a13) {
  extern __shared__ __attribute__((aligned(16))) char smem_[];
  char LAS* L = (char LAS*)smem_;
  const unsigned L0 = (unsigned)(size_t)L;
  ST(0);
  const int lane = threadIdx.x & 63;
  const int w8 = __builtin_amdgcn_readfirstlane(threadIdx.x >> 6), w = w8 & 3;
  unsigned cpf = 0;
  if (CPF && w8 == 0) {
    u64 pc;
    asm volatile("s_getpc_b64 %0" : "=s"(pc));
    cpf = __builtin_amdgcn_raw_buffer_load_b32(__builtin_amdgcn_make_buffer_rsrc((char*)(pc & ~127ull), 0, CPF * 4096, 0x00020000),
                                               lane < CPF ? ((blockIdx.x >> 3) * CPF + lane) * 128 : 0x80000000u, 0, 0);
  }
  typedef u64 u64x8 __attribute__((ext_vector_type(8)));
  typedef u64 u64x4 __attribute__((ext_vector_type(4)));
  typedef u64 u64x2 __attribute__((ext_vector_type(2)));
  u64x8 ka; u64x4 kb; u64x2 kc_;
  asm volatile("s_load_dwordx16 %0, %3, 0x0\n\ts_load_dwordx8 %1, %3, 0x40\n\ts_load_dwordx4 %2, %3, 0x60\n\ts_waitcnt lgkmcnt(0)"
               : "=&s"(ka), "=&s"(kb), "=&s"(kc_) : "s"(__builtin_amdgcn_kernarg_segment_ptr()) : "memory");
  const GAS u16 *xa = (const GAS u16*)ka[0], *wqkv = (const GAS u16*)ka[1], *wo = (const GAS u16*)ka[2], *xb = (const GAS u16*)ka[3];
  const GAS u16 *lre = (const GAS u16*)ka[4], *lim = (const GAS u16*)ka[5], *bre = (const GAS u16*)ka[6], *bim = (const GAS u16*)ka[7];
  const GAS u16 *cre = (const GAS u16*)kb[0], *cim = (const GAS u16*)kb[1], *ldt = (const GAS u16*)kb[2], *dsk = (const GAS u16*)kb[3];
  float* out = (float*)kc_[0]; char* wsp = (char*)kc_[1];
  const bool helper = w8 >= 4;
  const int bid = blockIdx.x, xcd = bid & 7, jj = bid >> 3;
  const int tile = (xcd >> 1) * 4 + (jj >> 3), cg = jj & 7;
  const int rot = ROT == 1 ? ((cg + 3 * (jj >> 3) + xcd) & 7) : ROT == 7 ? (((cg & 3) * 2 + (xcd >> 1)) & 7) : ROT == 12 ? ((2 * (cg & 3) + 2 * (xcd >> 1)) & 7) : ROT == 16 ? ((2 * (cg & 3) + ((jj >> 3) & 1) + 2 * (xcd >> 1)) & 7) : ((((cg + (jj >> 3)) % 3) * 3 + (xcd >> 1)) & 7);
  auto seq = [&](int t) { const int b = 2 * (jj >> 3), e = t - 1; return t == 0 ? cg : (b + e + (e >= ((cg - b) & 7) ? 1 : 0)) & 7; };
  const __amdgpu_buffer_rsrc_t ws = __builtin_amdgcn_make_buffer_rsrc(wsp, 0, WS_END, 0x00020000);
  const __amdgpu_buffer_rsrc_t ro = __builtin_amdgcn_make_buffer_rsrc(out, 0, 2048 * 1024 * 4, 0x00020000);
  const int r8 = lane >> 3, c8 = lane & 7;
  const int q = lane >> 4, l15 = lane & 15, l31 = lane & 31, hh = lane >> 5;
  unsigned epoch;
#define LOAD_EPOCH() asm volatile("s_load_dword %0, %1, 0x0" : "=s"(epoch) : "s"(wsp) : "memory")

  if ((xcd & 1) == 0) {
    constexpr unsigned WA = 131072, WB = 147456, SCR = 32768, TRB = 81920;
    constexpr unsigned QBUF[4] = {WB, 0, WA, WB}, OBUF[4] = {32768, 49152, 65536, 0};
    constexpr unsigned WBUF[8] = {WA, WB, 7 * 16384, 0, WA, WB, 0, WA};
    if (helper) {
      const int head = cg * 4 + w;
      unsigned vx[4], vw[2], vo[4];
#pragma unroll
      for (int i = 0; i < 4; ++i) vx[i] = (32 * w + 8 * i + r8) * 1024 + ((c8 ^ ((4 * i + (r8 >> 1)) & 7)) << 4) - 1024 * i;
#pragma unroll
      for (int i = 0; i < 2; ++i) vw[i] = (16 * w + 8 * i + r8) * 3072 + ((c8 ^ ((((r8 >> 1) & 1) | (i << 1)) << 1)) << 4) - 1024 * i;
      const GAS char* xbase = (const GAS char*)xa + (size_t)tile * 131072;
      const GAS char* wbase = (const GAS char*)wqkv + cg * 128;
      auto isx = [&](int kk, int slot) { dmax(L0 + slot * 16384 + w * 4096, xbase + ((kk + rot) & 7) * 128, vx[0], vx[1], vx[2], vx[3]); };
      auto isw = [&](unsigned dst, int kk, int seg) { dma2(L0 + dst + w * 2048, wbase + ((kk + rot) & 7) * (64 * 3072) + seg * 1024, vw[0], vw[1]); };
      auto issue = [&](int kk) { isx(kk, kk); isw(WBUF[kk], kk, 1); isw(WBUF[kk] + 8192, kk, 2); };
      auto isq = [&](int p) { isw(QBUF[p], 2 * p + 1, 0); isw(QBUF[p] + 8192, 2 * p + 2, 0); };
      ST(9); issue(0); ST(10); issue(1); ST(11);
      LOAD_EPOCH();
#pragma unroll
      for (int i = 0; i < 4; ++i) vo[i] = (8 * i + r8) * 1024 + ((c8 ^ (((r8 >> 1) & 3) << 1)) << 4) - 1024 * i;
      const GAS char* obase = (const GAS char*)wo + cg * 128;
      asm volatile("s_waitcnt vmcnt(8) lgkmcnt(0)\n\ts_barrier" : "+s"(epoch) :: "memory");
      issue(2);
      wait_bar<8>(); issue(3);
      wait_bar<8>(); issue(4);
      wait_bar<8>(); issue(5);
      wait_bar<8>(); issue(6);
      wait_bar<8>(); issue(7);
      wait_bar<8>();
      unsigned pfo;
      asm volatile("global_load_dword %0, %1, %2" : "=v"(pfo) : "v"(((jj >> 3) * 128 + w * 32 + l31) * 1024), "s"(obase) : "memory");
      isq(0);
      wait_bar<5>(); isq(1);
      wait_bar<4>(); isq(2);
      wait_bar<4>(); isq(3); isx(0, 1);
      asm volatile("s_waitcnt vmcnt(0)\n\ts_barrier" : "+v"(pfo) :: "memory");
      ST(1);
      const unsigned tag = epoch + 1, t = tag & 0xffu;
      const GAS char* kvb_ = (const GAS char*)wsp + WS_KV + head * 16384;
      const GAS char* zb_ = (const GAS char*)wsp + WS_Z + head * 1024;
      const unsigned qr = (jj >> 3) * 4096;
      unsigned vk0 = lane * 16 + qr, vk1 = lane * 16 + ((qr + 4096) & 12288), vk2 = lane * 16 + ((qr + 8192) & 12288), vk3 = lane * 16 + ((qr + 12288) & 12288), vz = l15 * 64 + q * 16;
      u4 g[16], gz;
#define KVL(i, v) asm volatile("global_load_dwordx4 %0, %1, %2 offset:%3 sc1" : "=v"(g[i]) : "v"(v), "s"(kvb_), "i"((i & 3) * 1024) : "memory")
      asm volatile("global_load_dwordx4 %0, %1, %2 sc1" : "=v"(gz) : "v"(vz), "s"(zb_) : "memory");
      KVL(0, vk0); KVL(1, vk0); KVL(2, vk0); KVL(3, vk0); KVL(4, vk1); KVL(5, vk1); KVL(6, vk1); KVL(7, vk1);
      KVL(8, vk2); KVL(9, vk2); KVL(10, vk2); KVL(11, vk2); KVL(12, vk3); KVL(13, vk3); KVL(14, vk3); KVL(15, vk3);
      auto zsum = [&]() {
        f4 z = __builtin_bit_cast(f4, gz);
#pragma unroll
        for (int r = 0; r < 4; ++r) {
          float v = z[r];
          v += __builtin_bit_cast(float, __builtin_amdgcn_mov_dpp(__builtin_bit_cast(int, v), 0x128, 0xf, 0xf, true));
          v += __builtin_bit_cast(float, __builtin_amdgcn_mov_dpp(__builtin_bit_cast(int, v), 0x124, 0xf, 0xf, true));
          v += __builtin_bit_cast(float, __builtin_amdgcn_mov_dpp(__builtin_bit_cast(int, v), 0x122, 0xf, 0xf, true));
          v += __builtin_bit_cast(float, __builtin_amdgcn_mov_dpp(__builtin_bit_cast(int, v), 0x121, 0xf, 0xf, true));
          z[r] = v;
        }
        return z;
      };
      asm volatile("s_waitcnt vmcnt(16)" : "+v"(gz) :: "memory");
      gz.x ^= t; gz.z ^= t;
      unsigned bad = gz.x | gz.z;
      f4 zz = zsum(), kv = {0, 0, 0, 0};
      asm volatile("" : "+v"(zz));
#define KVA(i) asm volatile("s_waitcnt vmcnt(%1)" : "+v"(g[i]) : "i"(15 - i) : "memory"); g[i].x ^= t; g[i].z ^= t; bad |= g[i].x | g[i].z; kv += __builtin_bit_cast(f4, g[i]); asm volatile("" : "+v"(kv), "+v"(bad))
      KVA(0); KVA(1); KVA(2); KVA(3); KVA(4); KVA(5); KVA(6); KVA(7); KVA(8); KVA(9); KVA(10); KVA(11); KVA(12); KVA(13); KVA(14); KVA(15);
      ST(3);
      if (!__all((bad & 0xffu) == 0)) {
#pragma unroll 1
        for (unsigned rep = 0; rep < SPIN_CAP; ++rep) {
          unsigned nw = __all(((gz.x | gz.z) & 0xffu) == 0) ? 0u : 0x10000u;
#pragma unroll
          for (int i = 0; i < 16; ++i) nw |= __all(((g[i].x | g[i].z) & 0xffu) == 0) ? 0u : 1u << i;
          if (nw == 0) break;
          if (nw & 0x10000u) asm volatile("global_load_dwordx4 %0, %1, %2 sc1" : "+v"(gz) : "v"(vz), "s"(zb_) : "memory");
#define KVLD(i, v) if (nw & (1u << i)) asm volatile("global_load_dwordx4 %0, %1, %2 offset:%3 sc1" : "+v"(g[i]) : "v"(v), "s"(kvb_), "i"((i & 3) * 1024) : "memory")
          KVLD(0, vk0); KVLD(1, vk0); KVLD(2, vk0); KVLD(3, vk0); KVLD(4, vk1); KVLD(5, vk1); KVLD(6, vk1); KVLD(7, vk1);
          KVLD(8, vk2); KVLD(9, vk2); KVLD(10, vk2); KVLD(11, vk2); KVLD(12, vk3); KVLD(13, vk3); KVLD(14, vk3); KVLD(15, vk3);
          asm volatile("s_waitcnt vmcnt(0)" : "+v"(g[0]), "+v"(g[1]), "+v"(g[2]), "+v"(g[3]), "+v"(g[4]), "+v"(g[5]), "+v"(g[6]), "+v"(g[7]),
                       "+v"(g[8]), "+v"(g[9]), "+v"(g[10]), "+v"(g[11]), "+v"(g[12]), "+v"(g[13]), "+v"(g[14]), "+v"(g[15]), "+v"(gz) :: "memory");
          if (nw & 0x10000u) { gz.x ^= t; gz.z ^= t; }
#pragma unroll
          for (int i = 0; i < 16; ++i) if (nw & (1u << i)) { g[i].x ^= t; g[i].z ^= t; }
        }
        zz = zsum();
        kv = (f4){0, 0, 0, 0};
#pragma unroll
        for (int i = 0; i < 16; ++i) kv += __builtin_bit_cast(f4, g[i]);
      }
      ST(5);
      *(LAS u4*)(L + SCR + w * 1024 + lane * 16) = (u4){pk2(kv[0], kv[1]), pk2(kv[2], kv[3]), pk2(zz[0], zz[1]), pk2(zz[2], zz[3])};
      ST(6);
      lbar();
      ST(7);
      lbar();
#pragma unroll
      for (int i = 0; i < 4; ++i)
        dma4(L0 + OBUF[i] + w * 4096, obase + (2 * seq(2 * i + (w >> 1)) + (w & 1)) * 32768, vo[0], vo[1], vo[2], vo[3]);
      wait_bar<0>();
      ST(8);
      HFLUSH();
      if (bid == 0 && w == 0) {
#pragma unroll 1
        for (unsigned sp = 0; sp < SPIN_CAP; ++sp) {
          asm volatile("" ::: "memory");
          const unsigned fo = (lane >> 3) * 4096 + (lane & 7) * 256;
          const u2 f0 = __builtin_amdgcn_raw_buffer_load_b64(ws, WS_FA + fo, 0, SC1), f1 = __builtin_amdgcn_raw_buffer_load_b64(ws, WS_FA + 32768 + fo, 0, SC1);
          const u2 f2_ = __builtin_amdgcn_raw_buffer_load_b64(ws, WS_FB + fo, 0, SC1), f3 = __builtin_amdgcn_raw_buffer_load_b64(ws, WS_FB + 32768 + fo, 0, SC1);
          if (__all(f0.x == tag && f0.y == ~tag && f1.x == tag && f1.y == ~tag && f2_.x == tag && f2_.y == ~tag && f3.x == tag && f3.y == ~tag)) break;
          __builtin_amdgcn_s_sleep(8);
        }
        if (lane == 0) __builtin_amdgcn_raw_buffer_store_b32(tag, ws, WS_EPOCH, 0, SC1);
      }
      return;
    }
    LOAD_EPOCH();
    const int hp = w & 1, th = w >> 1;
    unsigned warm0 = 0, warm1 = 0;
    if (TOUCH) {
      if (TD) __builtin_amdgcn_s_sleep(TD);
      warm0 = __builtin_amdgcn_raw_buffer_load_b32(ws, lane < 16 ? WS_FA + ((tile * 2 + th) * 16 + lane) * 128 : lane < 24 ? WS_Z + (cg * 4 + w) * 1024 + (lane - 16) * 128
                                                   : lane == 24 ? WS_KV + (cg * 4 + w) * 16384 : lane == 25 ? WS_O + tile * 131072 : 0x80000000u, 0, SC1);
      warm1 = __builtin_amdgcn_raw_buffer_load_b32(ro, lane < 2 ? (tile * 128 + w * 32 + lane * 16) * 4096 : 0x80000000u, 0, SC1);
    }
    const int qq = (lane >> 2) & 3, pp = lane & 3;
    const unsigned ax0 = l15 * 128 + ((q ^ (l15 >> 1)) << 4), ax1 = l15 * 128 + (((4 + q) ^ (l15 >> 1)) << 4);
    auto waddr = [&](int h) { return (8 * q + qq) * 128 + (((2 * h + (pp >> 1)) ^ ((((qq >> 1) & 1) | ((q & 1) << 1)) << 1)) << 4) + 8 * (pp & 1); };
    const unsigned aw = waddr(w), aq0 = waddr(2 * hp), aq1 = waddr(2 * hp + 1);
    f4 ka[8], va[8];
#pragma unroll
    for (int i = 0; i < 8; ++i) { ka[i] = (f4){EPS, EPS, EPS, EPS}; va[i] = (f4){0, 0, 0, 0}; }
    bf8 X0[4], X1[4], W0[2], W1[2];
    auto rdx = [&](bf8 (&X)[4], int kk, int ks, unsigned off) {
#pragma unroll
      for (int i = 0; i < 4; ++i) X[i] = ldsr(L + kk * 16384 + (ks ? ax1 : ax0) + off + i * 2048);
    };
    auto rdw = [&](bf8 (&W)[2], int kk, int ks) {
#pragma unroll
      for (int i = 0; i < 2; ++i) { const char LAS* p = L + WBUF[kk] + aw + ks * 4096 + i * 8192; W[i] = ldstr(p, p + 512); }
    };
    auto mm = [&](const bf8 (&X)[4], const bf8 (&W)[2], int h) {
#pragma unroll
      for (int i = 0; i < 4; ++i) {
        ka[4 * h + i] = mfma16(X[i], W[0], ka[4 * h + i]);
        va[4 * h + i] = mfma16(X[i], W[1], va[4 * h + i]);
      }
    };
#define ILV(nr, nm) do { _Pragma("unroll") for (int i_ = 0; i_ < nr; ++i_) { SG(0x008, 1); SG(0x100, 1); } if (nm > nr) SG(0x008, nm - nr); } while (0)
    asm volatile("s_waitcnt lgkmcnt(0)\n\ts_barrier" : "+s"(epoch) : "v"(warm0), "v"(warm1), "v"(cpf) : "memory");
    ST(1);
    const unsigned tag = epoch + 1, t = tag & 0xffu;
    rdw(W0, 0, 0); rdx(X0, 0, 0, 0);
    SB();
#pragma unroll
    for (int kk = 0; kk < 8; ++kk) {
      rdx(X1, kk, 0, 8192); mm(X0, W0, 0); ILV(4, 8); SB();
      rdw(W1, kk, 1); rdx(X0, kk, 1, 0); mm(X1, W0, 1); ILV(8, 8); SB();
      rdx(X1, kk, 1, 8192); mm(X0, W1, 0); ILV(4, 8); SB();
      if (kk < 7) { lbar(); rdw(W0, kk + 1, 0); rdx(X0, kk + 1, 0, 0); mm(X1, W1, 1); ILV(8, 8); }
      else mm(X1, W1, 1);
      SB();
    }
    ST(2);
    const unsigned xq = th * 8192;
    auto rdq = [&](bf8 (&X)[4], bf8 (&W)[2], int s) {
      const char LAS* p = L + QBUF[s >> 2] + ((s >> 1) & 1) * 8192 + (s & 1) * 4096;
      W[0] = ldstr(p + aq0, p + aq0 + 512); W[1] = ldstr(p + aq1, p + aq1 + 512);
      rdx(X, s < 14 ? (s >> 1) + 1 : 1, s & 1, xq);
    };
    {
      const int head = cg * 4 + w;
      f4 kv = {0, 0, 0, 0}, zz = {0, 0, 0, 0};
      const u4 ones = {0x3f803f80u, 0x3f803f80u, 0x3f803f80u, 0x3f803f80u};
#pragma unroll
      for (int m = 0; m < 4; ++m) {
        const f4 a = ka[2 * m], b = ka[2 * m + 1], c = va[2 * m], d = va[2 * m + 1];
        const u4 pa = {phi2(a[0], a[1]), phi2(a[2], a[3]), phi2(b[0], b[1]), phi2(b[2], b[3])};
        const u4 pb = {pk2(c[0], c[1]), pk2(c[2], c[3]), pk2(d[0], d[1]), pk2(d[2], d[3])};
        kv = mfma16(asbf8(pa), asbf8(pb), kv);
        zz = mfma16(asbf8(pa), asbf8(ones), zz);
      }
      const u4 skv = {tagv(kv[0], t), __float_as_uint(kv[1]), tagv(kv[2], t), __float_as_uint(kv[3])};
      __builtin_amdgcn_raw_buffer_store_b128(skv, ws, WS_KV + (head * 16 + tile) * 1024 + lane * 16, 0, SC1);
      const u4 sz = {tagv(zz[0], t), __float_as_uint(zz[1]), tagv(zz[2], t), __float_as_uint(zz[3])};
      __builtin_amdgcn_raw_buffer_store_b128(sz, ws, l15 == 0 ? WS_Z + (head * 16 + tile) * 64 + q * 16 : 0x80000000u, 0, SC1);
    }
    SB();
    ST(3);
    lbar();
    rdq(X0, W0, 0);
    SB();
    f4 qa[2][4];
#pragma unroll
    for (int i = 0; i < 8; ++i) qa[i >> 2][i & 3] = (f4){EPS, EPS, EPS, EPS};
    auto mq = [&](const bf8 (&X)[4], const bf8 (&W)[2]) {
#pragma unroll
      for (int i = 0; i < 4; ++i) { qa[0][i] = mfma16(W[0], X[i], qa[0][i]); qa[1][i] = mfma16(W[1], X[i], qa[1][i]); }
    };
#pragma unroll
    for (int s = 0; s < 16; s += 2) {
      rdq(X1, W1, s + 1); mq(X0, W0); ILV(8, 8); SB();
      if (s < 14) { if (s == 2 || s == 6) lbar(); rdq(X0, W0, s + 2); mq(X1, W1); ILV(8, 8); }
      else mq(X1, W1);
      SB();
    }
    ST(4);
    u2 pq[2][4];
#pragma unroll
    for (int i = 0; i < 8; ++i) pq[i >> 2][i & 3] = (u2){phi2(qa[i >> 2][i & 3][0], qa[i >> 2][i & 3][1]), phi2(qa[i >> 2][i & 3][2], qa[i >> 2][i & 3][3])};
    unsigned ao[4], og[4];
#pragma unroll
    for (int i = 0; i < 4; ++i) og[i] = WS_O + ((tile * 8 + th * 4 + i) * 16 + cg * 2 + hp) * 1024 + lane * 16;
#pragma unroll
    for (int nt = 0; nt < 4; ++nt) ao[nt] = (4 * q + qq) * 128 + ((((2 * nt) ^ (((2 * q + (qq >> 1)) & 3) << 1)) + (pp >> 1)) << 4) + 8 * (pp & 1);
    asm volatile("s_barrier" : "+v"(pq[0][0]), "+v"(pq[0][1]), "+v"(pq[0][2]), "+v"(pq[0][3]), "+v"(pq[1][0]), "+v"(pq[1][1]), "+v"(pq[1][2]), "+v"(pq[1][3]),
                 "+v"(og[0]), "+v"(og[1]), "+v"(og[2]), "+v"(og[3]) :: "memory");
    ST(5);
    const unsigned ex = TRB + th * 16384 + hp * 1024 + lane * 16;
    const f4 zero4 = {0, 0, 0, 0}, eps4 = {EPS, EPS, EPS, EPS};
    f4 num[2][4], den[2][4];
#pragma unroll
    for (int h = 0; h < 2; ++h) {
      const u4 kz = *(const LAS u4*)(L + SCR + (2 * hp + h) * 1024 + lane * 16);
#pragma unroll
      for (int i = 0; i < 4; ++i) { den[h][i] = mfma16k16((u2){kz.z, kz.w}, pq[h][i], eps4); num[h][i] = mfma16k16((u2){kz.x, kz.y}, pq[h][i], zero4); }
    }
    SB();
#pragma unroll
    for (int i = 0; i < 4; ++i) {
      u4 so;
#pragma unroll
      for (int h = 0; h < 2; ++h) {
        const float rc = __builtin_amdgcn_rcpf(den[h][i][0]);
        const f4 n = num[h][i];
        so[2 * h] = pk2((f2){n[0], n[1]} * rc);
        so[2 * h + 1] = pk2((f2){n[2], n[3]} * rc);
      }
      __builtin_amdgcn_raw_buffer_store_b128(so, ws, og[i], 0, SC1);
      *(LAS u4*)(L + ex + (i >> 1) * 8192 + (i & 1) * 2048) = so;
    }
    lbar();
    ST(6);
    u4 of[32];
#pragma unroll
    for (int i = 0; i < 4; ++i) of[i] = *(const LAS u4*)(L + TRB + w * 8192 + ((i & 1) * 2 + (i >> 1)) * 1024 + lane * 16);
    if (!NODRAIN) drain();
    __builtin_amdgcn_raw_buffer_store_b64((u2){tag, ~tag}, ws, lane == 0 ? WS_FA + ((tile * 2 + th) * 16 + cg * 2 + hp) * 128 : 0x80000000u, 0, SC1);
    unsigned tw[4], trd[4], so_ = ((tile * 128 + w * 32 + q) * 1024 + cg * 64 + l15 * 4) * 4;
#pragma unroll
    for (int i = 0; i < 4; ++i) {
      tw[i] = TRB + w * 8192 + l15 * 256 + (((4 * i + q) ^ l15) << 4);
      trd[i] = TRB + w * 8192 + (4 * i + q) * 256 + ((l15 ^ (4 * i + q)) << 4);
      asm volatile("" : "+v"(tw[i]), "+v"(trd[i]));
    }
    asm volatile("s_barrier" : "+v"(so_) :: "memory");
    ST(7);
    f4 acc[2][4];
#pragma unroll
    for (int i = 0; i < 8; ++i) acc[i >> 2][i & 3] = zero4;
    const unsigned ob = WS_O + (tile * 8 + w * 2) * 16384 + lane * 16;
    bf8 wf[2][4];
    auto rdo = [&](int k) {
#pragma unroll
      for (int nt = 0; nt < 4; ++nt) { const char LAS* p = L + OBUF[k >> 2] + (k & 3) * 4096 + ao[nt]; wf[k & 1][nt] = ldstr(p, p + 2048); }
    };
    auto ldo = [&](int k) {
      const unsigned so = (2 * seq(k >> 1) + (k & 1)) * 1024;
      of[2 * k] = __builtin_amdgcn_raw_buffer_load_b128(ws, ob, so, PAY_AUX);
      of[2 * k + 1] = __builtin_amdgcn_raw_buffer_load_b128(ws, ob + 16384, so, PAY_AUX);
    };
    auto mo = [&](int k) {
#pragma unroll
      for (int nt = 0; nt < 4; ++nt) {
        acc[0][nt] = mfma16(wf[k & 1][nt], asbf8(of[2 * k]), acc[0][nt]);
        acc[1][nt] = mfma16(wf[k & 1][nt], asbf8(of[2 * k + 1]), acc[1][nt]);
      }
    };
    rdo(0); rdo(1); SB();
    mo(0); rdo(2); SB();
    mo(1); SB();
    if (FD) __builtin_amdgcn_s_sleep(FD);
    wait_flags(ws, WS_FA + ((tile * 2 + th) * 16 + l15) * 128, tag);
    ST(8); CT(14);
#pragma unroll
    for (int k = 2; k < 2 + OD; ++k) ldo(k);
    SB();
#pragma unroll
    for (int k = 2; k < 16; ++k) {
      if (k + OD < 16) ldo(k + OD);
      if (k + 1 < 16) rdo(k + 1);
      mo(k);
#pragma unroll
      for (int i_ = 0; i_ < 8; ++i_) { SG(0x008, 1); SG(0x100, 1); if (i_ == 1 || i_ == 5) SG(0x020, 1); }
      SB();
    }
    ST(9); CT(15);
#pragma unroll
    for (int mt = 0; mt < 2; ++mt)
#pragma unroll
      for (int nt = 0; nt < 4; ++nt) {
        const f4 v = acc[mt][nt];
        const unsigned p0 = pk2(v[0], v[1]), p1 = pk2(v[2], v[3]);
        *(LAS f4*)(L + tw[nt] + mt * 4096) = (f4){bflo(p0), bfhi(p0), bflo(p1), bfhi(p1)};
      }
    u4 ov[8];
#pragma unroll
    for (int i = 0; i < 8; ++i) ov[i] = *(const LAS u4*)(L + trd[i & 3] + (i >> 2) * 4096);
    SB();
#pragma unroll
    for (int i = 0; i < 8; ++i) __builtin_amdgcn_raw_buffer_store_b128(ov[i], ro, so_, i * 16384, OUT_AUX);
    ST(10);
  } else {
    constexpr unsigned SLOT = 32768, SCR = SLOT, PAR = 4 * SLOT;
    const int wp = w & 1, wt = w >> 1;
    const unsigned ecol = WS_EST + (cg * 64 + wp * 32 + l31) * 8;
    if (helper) {
      unsigned vx[4], vw[4], vc[4];
#pragma unroll
      for (int i = 0; i < 4; ++i) {
        const unsigned sw = (c8 ^ ((4 * i + (r8 >> 1)) & 7)) << 4;
        vx[i] = (64 * (w >> 1) + 32 * (r8 >> 2) + 16 * (r8 & 1) + 8 * (w & 1) + ((r8 >> 1) & 1) + 2 * i) * 1024 + sw - 1024 * i;
        vw[i] = (16 * (i >> 1) + 8 * (r8 >> 2) + 4 * (i & 1) + (r8 & 3)) * 1024 + sw - 1024 * i;
        vc[i] = (8 * i + r8) * 1024 + sw - 1024 * i;
      }
      const GAS char* xbase = (const GAS char*)xb + (size_t)tile * 131072;
      const GAS char* wbase = (const GAS char*)((w & 1) ? bim : bre) + (cg * 64 + (w >> 1) * 32) * 1024;
      auto issue = [&](int kk) {
        const int kc = (kk + rot) & 7;
        const unsigned s = L0 + (kk % 5) * SLOT + w * 4096;
        dmax(s, xbase + kc * 128, vx[0], vx[1], vx[2], vx[3]);
        dma4(s + 16384, wbase + kc * 128, vw[0], vw[1], vw[2], vw[3]);
      };
      ST(9); issue(0); ST(10); issue(1); ST(11);
      LOAD_EPOCH();
      const GAS char* cbase = (const GAS char*)((w & 1) ? cim : cre) + cg * 65536;
      auto issue_c = [&](int piece) {
        const unsigned s = L0 + ((piece + 3) % 5) * SLOT + w * 8192;
        const GAS char* cb = cbase + seq(2 * piece + (w >> 1)) * 128;
        dma4(s, cb, vc[0], vc[1], vc[2], vc[3]);
        dma4(s + 4096, cb + 32768, vc[0], vc[1], vc[2], vc[3]);
      };
      asm volatile("s_waitcnt vmcnt(8) lgkmcnt(0)\n\ts_barrier" : "+s"(epoch) :: "memory");
      issue(2);
      const f4 par = *(const LAS f4*)(L + PAR + w * 1024 + lane * 16);
      const f2 l64 = {par[0], par[1]}, l128 = {par[2], par[3]};
      f2 l256 = cmul(l128, l128);
      f2 wgt = hh ? l64 : (f2){1.f, 0.f};
      if (wt) wgt = cmul(wgt, l128);
      const unsigned tag = epoch + 1, t = tag & 0xffu;
      unsigned eo[8], et[8];
#pragma unroll
      for (int m = 0; m < 8; ++m) {
        const int i = 2 * tile - 1 - (2 * wt + hh) - 4 * m;
        eo[m] = i >= 0 ? ecol + i * 4096 : 0x80000000u;
        et[m] = i >= 0 ? t : 0u;
        asm volatile("" : "+v"(eo[m]), "+v"(et[m]));
      }
      asm volatile("" : "+v"(l256), "+v"(wgt));
      wait_bar<8>(); issue(3);
      wait_bar<8>(); issue(4);
      wait_bar<8>(); issue(5);
      wait_bar<8>(); issue(6);
      wait_bar<8>(); issue(7);
      wait_bar<8>(); issue_c(0);
      wait_bar<8>(); ST(1);
      if (NC_EARLY > 1) issue_c(1);
      if (NC_EARLY > 2) issue_c(2);
      ST(2);
      if (PD) { asm volatile("s_barrier" ::: "memory"); __builtin_amdgcn_s_sleep(PD); }
      f2 cin;
#pragma unroll 1
      for (unsigned sp = 0; sp < SPIN_CAP; ++sp) {
      asm volatile("" ::: "memory");
        u2 g[8];
#pragma unroll
        for (int m = 0; m < 8; ++m) g[m] = __builtin_amdgcn_raw_buffer_load_b64(ws, eo[m], 0, SC1);
        unsigned bad = 0;
        cin = (f2){0, 0};
#pragma unroll
        for (int m = 7; m >= 0; --m) {
          const unsigned cx = g[m].x ^ et[m];
          bad |= cx;
          cin = cfma(l256, cin, (f2){__uint_as_float(cx), __uint_as_float(g[m].y)});
        }
        if (__all((bad & 0xffu) == 0)) break;
      }
      ST(5);
      cin = cmul(cin, wgt);
      f2 chi;
      const f2 clo = swap_lo(cin, chi);
      if (hh == 0) *(LAS f2*)(L + SCR + (wp * 2 + wt) * 256 + l31 * 8) = clo + chi;
      ST(6);
      lbar();
      if (WOX & 2) issue_c(2);
      lbar();
      ST(7);
      if (NC_EARLY <= 1) issue_c(1);
      if (NC_EARLY <= 2 && !(WOX & 2)) issue_c(2);
      issue_c(3);
      wait_bar<0>();
      ST(8); HFLUSH();
      return;
    }
    LOAD_EPOCH();
    const int pl = cg * 64 + wp * 32 + ((l31 & 0x13) | ((l31 & 4) << 1) | ((l31 & 8) >> 1));
    const unsigned plr = lre[pl], pli = lim[pl], pdt = ldt[pl];
    u4 dv[4];
#pragma unroll
    for (int i = 0; i < 4; ++i) dv[i] = *(const GAS u4*)(dsk + cg * 64 + 16 * i + 8 * hh);
    unsigned warm0 = 0, warm1 = 0;
    if (TOUCH) {
      warm0 = __builtin_amdgcn_raw_buffer_load_b32(ws, lane < 16 ? WS_FB + ((tile * 2 + (w >> 1)) * 16 + lane) * 128 : lane == 16 ? WS_EST + tile * 8192
                                                   : lane == 17 ? WS_H + (tile * 4 + w) * 65536 : 0x80000000u, 0, SC1);
      warm1 = __builtin_amdgcn_raw_buffer_load_b32(ro, lane < 2 ? (tile * 128 + w * 32 + lane * 16) * 4096 + 2048 : 0x80000000u, 0, SC1);
    }
    SB();
    f2 lam, kap, l16, l32, l64, l128;
    {
      const float lr = bflo(plr), li = bflo(pli);
      const float dt = bflo(pk2(expf(bflo(pdt)), 0.f));
      const float mag = expf(dt * lr);
      float sn, cs;
      sincosf(dt * li, &sn, &cs);
      lam = (f2){mag * cs, mag * sn};
      const float inv = 2.f / (lr * lr + li * li), nr = lam.x - 1.f, ni = lam.y;
      kap = (f2){(nr * lr + ni * li) * inv, (ni * lr - nr * li) * inv};
      f2 t = cmul(lam, lam); t = cmul(t, t); t = cmul(t, t);
      l16 = cmul(t, t); l32 = cmul(l16, l16); l64 = cmul(l32, l32); l128 = cmul(l64, l64);
      *(LAS f4*)(L + PAR + w * 1024 + lane * 16) = (f4){l64.x, l64.y, l128.x, l128.y};
    }
    unsigned ar[4];
#pragma unroll
    for (int s = 0; s < 4; ++s) ar[s] = l31 * 128 + (((2 * s + hh) ^ ((lane >> 1) & 7)) << 4);
    f16v ur[2], ui[2];
#pragma unroll
    for (int i = 0; i < 16; ++i) { ur[0][i] = 0; ur[1][i] = 0; ui[0][i] = 0; ui[1][i] = 0; }
    bf8 F0[4], F1[4];
    auto rd = [&](bf8 (&F)[4], int kk, int s) {
      const char LAS* sx = L + (kk % 5) * SLOT + wt * 8192 + ar[s];
      const char LAS* sw = L + (kk % 5) * SLOT + 16384 + wp * 8192 + ar[s];
      F[0] = ldsr(sx); F[1] = ldsr(sw); F[2] = ldsr(sw + 4096); F[3] = ldsr(sx + 4096);
    };
    auto mm = [&](const bf8 (&F)[4]) {
      ur[0] = mfma32(F[0], F[1], ur[0]); ui[0] = mfma32(F[0], F[2], ui[0]);
      ur[1] = mfma32(F[3], F[1], ur[1]); ui[1] = mfma32(F[3], F[2], ui[1]);
    };
#define ILB() do { SG(0x008, 1); SG(0x100, 2); SG(0x008, 1); SG(0x100, 2); SG(0x008, 2); } while (0)
    asm volatile("s_waitcnt lgkmcnt(0)\n\ts_barrier" : "+s"(epoch), "+v"(lam), "+v"(kap), "+v"(l16), "+v"(l32), "+v"(l64), "+v"(dv[0]), "+v"(dv[1]), "+v"(dv[2]), "+v"(dv[3]) : "v"(warm0), "v"(warm1), "v"(cpf) : "memory");
    ST(1);
    const unsigned tag = epoch + 1, t = tag & 0xffu;
    rd(F0, 0, 0);
    SB();
#pragma unroll
    for (int kk = 0; kk < 8; ++kk) {
      rd(F1, kk, 1); mm(F0); ILB(); SB();
      rd(F0, kk, 2); mm(F1); ILB(); SB();
      rd(F1, kk, 3); mm(F0); ILB(); SB();
      if (kk < 7) { lbar(); if (kk == 6) ST(2); rd(F0, kk + 1, 0); mm(F1); ILB(); }
      else mm(F1);
      SB();
    }
    ST(3);
    f2 gr[16], gi[16];
#pragma unroll
    for (int a = 0; a < 16; ++a) {
      gr[a] = (f2){ur[a >> 3][2 * (a & 7)], ur[a >> 3][2 * (a & 7) + 1]};
      gi[a] = (f2){ui[a >> 3][2 * (a & 7)], ui[a >> 3][2 * (a & 7) + 1]};
    }
    {
      f2 LR = {lam.x, lam.x}, LI = {lam.y, lam.y}, LN = {-lam.y, -lam.y};
      asm volatile("s_nop 11" : "+v"(gr[0]), "+v"(gi[0]), "+v"(gr[8]), "+v"(gi[8]), "+v"(LR), "+v"(LI), "+v"(LN));
#pragma unroll
      for (int a = 1; a < 16; ++a)
        asm volatile("v_pk_fma_f32 %0, %6, %3, %0\n\tv_pk_fma_f32 %1, %5, %2, %1\n\tv_pk_fma_f32 %0, %4, %2, %0\n\tv_pk_fma_f32 %1, %4, %3, %1"
                     : "+v"(gr[a]), "+v"(gi[a]) : "v"(gr[a - 1]), "v"(gi[a - 1]), "v"(LR), "v"(LI), "v"(LN));
    }
    const f2 er = gr[15], ei = gi[15];
    const f2 e0 = {er.x, ei.x};
    const f2 e32 = cfma(l16, e0, (f2){er.y, ei.y});
    f2 ehi;
    const f2 elo = swap_lo(e32, ehi);
    const f2 e64 = cfma(l32, elo, ehi);
    __builtin_amdgcn_raw_buffer_store_b64((u2){tagv(e64.x, t), __float_as_uint(e64.y)}, ws, hh == 0 ? ecol + (tile * 2 + wt) * 4096 : 0x80000000u, 0, SC1);
    if (wt == 0 && hh == 0) *(LAS f2*)(L + SCR + 1024 + wp * 256 + l31 * 8) = e64;
    if (PD) asm volatile("s_barrier" ::: "memory");
    SB();
    ST(4);
    auto tokr = [&](int r) { return tile * 128 + 64 * (w >> 1) + 32 * ((r >> 2) & 1) + 16 * (r & 1) + 8 * (w & 1) + ((r >> 1) & 1) + 2 * (r >> 3); };
    u4 xd[4];
#pragma unroll
    for (int i = 0; i < 4; ++i) xd[i] = *(const GAS u4*)(xb + tokr(l31) * 512 + cg * 64 + 16 * i + 8 * hh);
    SB();
    {
      const f2 KR = {kap.x, kap.x}, KI = {kap.y, kap.y};
#pragma unroll
      for (int a = 0; a < 16; ++a) {
        const f2 xr = gr[a], xi = gi[a];
        gr[a] = fma2(KR, xr, -KI * xi); gi[a] = fma2(-KR, xi, -KI * xr);
        asm volatile("" : "+v"(gr[a]), "+v"(gi[a]));
      }
    }
    f2 pw[16];
    pw[0] = (f2){lam.x, -lam.y};
#pragma unroll
    for (int k = 1; k < 16; ++k) { pw[k] = cmul(pw[k >> 1], pw[(k - 1) >> 1]); asm volatile("" : "+v"(pw[k])); }
    u4 idn[2];
#pragma unroll
    for (int s = 0; s < 2; ++s)
#pragma unroll
      for (int jq = 0; jq < 4; ++jq) {
        const int r0 = 16 * s + 8 * (jq >> 1) + 4 * hh + 2 * (jq & 1);
        idn[s][jq] = (l31 == r0 ? 0x3f80u : 0u) | (l31 == r0 + 1 ? 0x3f800000u : 0u);
      }
    asm volatile("" : "+v"(idn[0]), "+v"(idn[1]));
#pragma unroll
    for (int i = 0; i < 4; ++i) {
#pragma unroll
      for (int j = 0; j < 4; ++j) xd[i][j] = pk2(bflo(xd[i][j]) * bflo(dv[i][j]), bfhi(xd[i][j]) * bfhi(dv[i][j]));
      asm volatile("" : "+v"(xd[i]));
    }
    f2 m0 = hh ? cmul(kap, l32) : kap, b0 = hh ? cmul(kap, elo) : (f2){0.f, 0.f};
    f2 m1 = cmul(l16, m0), b1 = cfma(l16, b0, cmul(kap, e0));
    unsigned ho[2];
    ho[0] = WS_H + (tile * 4 + wt * 2) * 65536 + (cg * 2 + wp) * 4096 + lane * 16; ho[1] = ho[0] + 65536;
    asm volatile("s_waitcnt lgkmcnt(0)\n\ts_barrier" : "+v"(ho[0]), "+v"(ho[1]), "+v"(m0), "+v"(b0), "+v"(m1), "+v"(b1) :: "memory");
    ST(5);
    f2 cin = *(const LAS f2*)(L + SCR + wp * 512 + l31 * 8) + *(const LAS f2*)(L + SCR + wp * 512 + 256 + l31 * 8);
    const f2 esib = *(const LAS f2*)(L + SCR + 1024 + wp * 256 + l31 * 8);
    if (wt) cin = cfma(l64, cin, esib);
    const f2 d0 = cfma(m0, cin, b0), d1 = cfma(m1, cin, b1);
    const f2 DR = {d0.x, d1.x}, DJ = {-d0.y, -d1.y};
    f16v zero16;
#pragma unroll
    for (int i = 0; i < 16; ++i) zero16[i] = 0;
    const unsigned ex = 2 * SLOT + wt * 16384 + wp * 4096 + lane * 16;
    auto fix = [&](int m, int c) {
      u4 a[2];
#pragma unroll
      for (int i = 0; i < 8; ++i) {
        const f2 P = pw[8 * m + i], PR = {P.x, P.x}, PI = {P.y, P.y};
        const f2 h = c ? fma2(PI, DR, fma2(PR, DJ, gi[8 * m + i])) : fma2(-PI, DJ, fma2(PR, DR, gr[8 * m + i]));
        a[i >> 2][i & 3] = pk2(h);
      }
      return mfma32(asbf8(a[1]), asbf8(idn[1]), mfma32(asbf8(a[0]), asbf8(idn[0]), zero16));
    };
    u4 ow[8];
    auto put = [&](const f16v& T, int m, int c) {
      const u4 o0 = ow[4 * m + 2 * c] = (u4){pk2(T[0], T[1]), pk2(T[2], T[3]), pk2(T[4], T[5]), pk2(T[6], T[7])};
      const u4 o1 = ow[4 * m + 2 * c + 1] = (u4){pk2(T[8], T[9]), pk2(T[10], T[11]), pk2(T[12], T[13]), pk2(T[14], T[15])};
      __builtin_amdgcn_raw_buffer_store_b128(o0, ws, ho[m] + c * 2048, 0, SC1);
      __builtin_amdgcn_raw_buffer_store_b128(o1, ws, ho[m] + c * 2048 + 1024, 0, SC1);
    };
    {
      const f16v T0 = fix(0, 0); SB();
      const f16v T1 = fix(0, 1); SB();
      put(T0, 0, 0);
      const f16v T2 = fix(1, 0); SB();
      put(T1, 0, 1);
      const f16v T3 = fix(1, 1); SB();
      put(T2, 1, 0); SB();
      put(T3, 1, 1);
    }
    SB();
#pragma unroll
    for (int i = 0; i < 8; ++i) *(LAS u4*)(L + ex + (i >> 2) * 8192 + (i & 3) * 1024) = ow[i];
    lbar();
    ST(6);
    if (!NODRAIN) drain();
    __builtin_amdgcn_raw_buffer_store_b64((u2){tag, ~tag}, ws, lane == 0 ? WS_FB + ((tile * 2 + wt) * 16 + cg * 2 + wp) * 128 : 0x80000000u, 0, SC1);
    ST(7);
    const unsigned hb = WS_H + (tile * 4 + w) * 65536 + lane * 16;
    u4 hf[64];
    bf8 cf[2][4];
    f16v acc[2];
    auto ldh = [&](int i) { hf[i] = __builtin_amdgcn_raw_buffer_load_b128(ws, hb + (i & 7) * 1024, seq(i >> 3) * 8192, PAY_AUX); };
    auto rdc = [&](int g) {
      const int tt = g >> 2, i = (g & 3) * 2;
      const char LAS* cb = L + (((tt >> 1) + 3) % 5) * SLOT + (tt & 1) * 16384 + ((i >> 1) & 1) * 8192;
      cf[g & 1][0] = ldsr(cb + ar[(i >> 2) * 2]); cf[g & 1][1] = ldsr(cb + ar[(i >> 2) * 2] + 4096);
      cf[g & 1][2] = ldsr(cb + ar[(i >> 2) * 2 + 1]); cf[g & 1][3] = ldsr(cb + ar[(i >> 2) * 2 + 1] + 4096);
    };
    auto mh = [&](int g) {
      acc[0] = mfma32(cf[g & 1][0], asbf8(hf[2 * g]), acc[0]);
      acc[1] = mfma32(cf[g & 1][1], asbf8(hf[2 * g]), acc[1]);
      acc[0] = mfma32(cf[g & 1][2], asbf8(hf[2 * g + 1]), acc[0]);
      acc[1] = mfma32(cf[g & 1][3], asbf8(hf[2 * g + 1]), acc[1]);
    };
#pragma unroll
    for (int i = 0; i < 8; ++i) hf[i] = *(const LAS u4*)(L + 2 * SLOT + w * 8192 + i * 1024 + lane * 16);
    rdc(0);
    {
      u4 ida[2];
#pragma unroll
      for (int s = 0; s < 2; ++s)
#pragma unroll
        for (int jq = 0; jq < 4; ++jq) {
          const int r0 = 16 * s + 8 * hh + 2 * jq;
          ida[s][jq] = (l31 == r0 ? 0x3f80u : 0u) | (l31 == r0 + 1 ? 0x3f800000u : 0u);
        }
      acc[0] = mfma32(asbf8(ida[1]), asbf8(xd[1]), mfma32(asbf8(ida[0]), asbf8(xd[0]), zero16));
      acc[1] = mfma32(asbf8(ida[1]), asbf8(xd[3]), mfma32(asbf8(ida[0]), asbf8(xd[2]), zero16));
    }
    SB();
#pragma unroll
    for (int g = 0; g < 4; ++g) { rdc(g + 1); mh(g); ILB(); SB(); }
    unsigned tw[8], trd[4], so_ = ((tile * 128 + 64 * (w >> 1) + 8 * (w & 1) + 16 * (q & 1) + (q >> 1)) * 1024 + 512 + cg * 64 + l15 * 4) * 4;
#pragma unroll
    for (int i = 0; i < 8; ++i) { tw[i] = 2 * SLOT + w * 8192 + l31 * 256 + (((2 * i + hh) ^ l15) << 4); asm volatile("" : "+v"(tw[i])); }
#pragma unroll
    for (int i = 0; i < 4; ++i) { trd[i] = 2 * SLOT + w * 8192 + (4 * i + q) * 256 + ((l15 ^ (4 * i + q)) << 4); asm volatile("" : "+v"(trd[i])); }
    asm volatile("s_barrier" : "+v"(so_) :: "memory");
    if (FD) __builtin_amdgcn_s_sleep(FD);
    wait_flags(ws, WS_FB + ((tile * 2 + (w >> 1)) * 16 + l15) * 128, tag);
    ST(8); CT(14);
#pragma unroll
    for (int i = 8; i < 8 + HD; ++i) ldh(i);
    SB();
#pragma unroll
    for (int g = 4; g < 32; ++g) {
      if (2 * g + HD < 64) { ldh(2 * g + HD); ldh(2 * g + HD + 1); }
      if (g + 1 < 32) rdc(g + 1);
      mh(g);
      SG(0x008, 1); SG(0x100, 2); SG(0x008, 1); SG(0x100, 2); SG(0x008, 1); SG(0x020, 1); SG(0x008, 1); SG(0x020, 1);
      SB();
    }
    ST(9); CT(15);
#pragma unroll
    for (int i = 0; i < 8; ++i)
      *(LAS f4*)(L + tw[i]) = (f4){acc[i >> 2][4 * (i & 3)], acc[i >> 2][4 * (i & 3) + 1], acc[i >> 2][4 * (i & 3) + 2], acc[i >> 2][4 * (i & 3) + 3]};
    u4 ov[8];
#pragma unroll
    for (int i = 0; i < 8; ++i) ov[i] = *(const LAS u4*)(L + trd[i & 3] + (i >> 2) * 4096);
    SB();
#pragma unroll
    for (int i = 0; i < 8; ++i) __builtin_amdgcn_raw_buffer_store_b128(ov[i], ro, so_, (32 * (i & 1) + 2 * (i >> 1)) * 4096, OUT_AUX);
    ST(10);
  }
}

extern "C" void kernel_launch(void* const* d_in, const int* in_sizes, int n_in, void* d_out, int out_size, void* d_ws, size_t ws_size,
                              hipStream_t stream) {
  (void)hipFuncSetAttribute((const void*)fused_kernel, hipFuncAttributeMaxDynamicSharedMemorySize, LDS_BYTES);
  fused_kernel<<<256, 512, LDS_BYTES, stream>>>((const u16*)d_in[0], (const u16*)d_in[1], (const u16*)d_in[2], (const u16*)d_in[3],
                                               (const u16*)d_in[4], (const u16*)d_in[5], (const u16*)d_in[6], (const u16*)d_in[7],
                                               (const u16*)d_in[8], (const u16*)d_in[9], (const u16*)d_in[10], (const u16*)d_in[11],
                                               (float*)d_out, (char*)d_ws);
}
```

```cpp
#include <hip/hip_runtime.h>
#include <hip/hip_bf16.h>

#define DI __device__ __forceinline__
#define LAS __attribute__((address_space(3)))
#define GAS __attribute__((address_space(1)))
typedef unsigned short u16;
typedef unsigned long long u64;
typedef __bf16 bf8 __attribute__((ext_vector_type(8)));
typedef __bf16 bf2 __attribute__((ext_vector_type(2)));
typedef short s4 __attribute__((ext_vector_type(4)));
typedef short s8 __attribute__((ext_vector_type(8)));
typedef float f16v __attribute__((ext_vector_type(16)));
typedef float f4 __attribute__((ext_vector_type(4)));
typedef float f2 __attribute__((ext_vector_type(2)));
typedef unsigned u4 __attribute__((ext_vector_type(4)));
typedef unsigned u2 __attribute__((ext_vector_type(2)));

constexpr unsigned WS_EPOCH = 0;
constexpr unsigned WS_FA = 4096;
constexpr unsigned WS_FB = WS_FA + 65536;
constexpr unsigned WS_EST = WS_FB + 65536;
constexpr unsigned WS_Z = WS_EST + 131072;
constexpr unsigned WS_KV = WS_Z + 32768;
constexpr unsigned WS_O = 1u << 20;
constexpr unsigned WS_H = 3u << 20;
constexpr unsigned WS_END = 7u << 20;
constexpr unsigned WS_STAMP = 8u << 20;
constexpr int LDS_BYTES = 160 * 1024;
constexpr unsigned SPIN_CAP = 200000;
constexpr int SC1 = 16;

#define ROT 8
#define NC_EARLY 2
#define OUT_AUX 16
#define PAY_AUX 16
#define PULL 0
#define TOUCH 1
#define CPF 9
#define HROT 1
#define XNT 0
#define OD 6
#define HD 20

#define CT(i) do {} while (0)
#define ST(i) do {} while (0)
#define HFLUSH() do {} while (0)
#define SB() __builtin_amdgcn_sched_barrier(0)
#define SG(m, n) __builtin_amdgcn_sched_group_barrier(m, n, 0)

DI unsigned pk2(float a, float b) { f2 v = {a, b}; return __builtin_bit_cast(unsigned, __builtin_convertvector(v, bf2)); }
DI unsigned pk2(f2 v) { return __builtin_bit_cast(unsigned, __builtin_convertvector(v, bf2)); }
DI float bflo(unsigned v) { return __uint_as_float(v << 16); }
DI float bfhi(unsigned v) { return __uint_as_float(v & 0xffff0000u); }
DI bf8 asbf8(u4 v) { return __builtin_bit_cast(bf8, v); }
DI f16v mfma32(bf8 a, bf8 b, f16v c) { return __builtin_amdgcn_mfma_f32_32x32x16_bf16(a, b, c, 0, 0, 0); }
DI f4 mfma16(bf8 a, bf8 b, f4 c) { return __builtin_amdgcn_mfma_f32_16x16x32_bf16(a, b, c, 0, 0, 0); }
DI f4 mfma16k16(u2 a, u2 b, f4 c) { return __builtin_amdgcn_mfma_f32_16x16x16bf16_1k(__builtin_bit_cast(s4, a), __builtin_bit_cast(s4, b), c, 0, 0, 0); }
DI bf8 ldsr(const char LAS* p) { return *(const LAS bf8*)p; }
DI bf8 ldstr(const char LAS* p0, const char LAS* p1) {
  s4 a = __builtin_amdgcn_ds_read_tr16_b64_v4i16((LAS s4*)p0), b = __builtin_amdgcn_ds_read_tr16_b64_v4i16((LAS s4*)p1);
  return __builtin_bit_cast(bf8, (s8)__builtin_shufflevector(a, b, 0, 1, 2, 3, 4, 5, 6, 7));
}
DI void dma4(unsigned lds, const GAS char* sb, unsigned v0, unsigned v1, unsigned v2, unsigned v3) {
  asm volatile("s_mov_b32 m0, %0\n\ts_nop 0\n\tglobal_load_lds_dwordx4 %1, %5\n\tglobal_load_lds_dwordx4 %2, %5 offset:1024\n\t"
               "global_load_lds_dwordx4 %3, %5 offset:2048\n\tglobal_load_lds_dwordx4 %4, %5 offset:3072"
               :: "s"(lds), "v"(v0), "v"(v1), "v"(v2), "v"(v3), "s"(sb) : "memory");
}
DI void dma4nt(unsigned lds, const GAS char* sb, unsigned v0, unsigned v1, unsigned v2, unsigned v3) {
  asm volatile("s_mov_b32 m0, %0\n\ts_nop 0\n\tglobal_load_lds_dwordx4 %1, %5 nt\n\tglobal_load_lds_dwordx4 %2, %5 offset:1024 nt\n\t"
               "global_load_lds_dwordx4 %3, %5 offset:2048 nt\n\tglobal_load_lds_dwordx4 %4, %5 offset:3072 nt"
               :: "s"(lds), "v"(v0), "v"(v1), "v"(v2), "v"(v3), "s"(sb) : "memory");
}
DI void dmax(unsigned lds, const GAS char* sb, unsigned v0, unsigned v1, unsigned v2, unsigned v3) { if (XNT) dma4nt(lds, sb, v0, v1, v2, v3); else dma4(lds, sb, v0, v1, v2, v3); }
DI void dma2(unsigned lds, const GAS char* sb, unsigned v0, unsigned v1) {
  asm volatile("s_mov_b32 m0, %0\n\ts_nop 0\n\tglobal_load_lds_dwordx4 %1, %3\n\tglobal_load_lds_dwordx4 %2, %3 offset:1024"
               :: "s"(lds), "v"(v0), "v"(v1), "s"(sb) : "memory");
}
DI void lbar() { asm volatile("s_waitcnt lgkmcnt(0)\n\ts_barrier" ::: "memory"); }
template <int N> DI void wait_bar() { asm volatile("s_waitcnt vmcnt(%0) lgkmcnt(0)\n\ts_barrier" :: "i"(N) : "memory"); }
DI void drain() { asm volatile("s_waitcnt vmcnt(0)" ::: "memory"); }
DI unsigned tagv(float x, unsigned t) { return (__float_as_uint(x) & 0xffffff00u) | t; }
DI f2 cmul(f2 a, f2 b) { return (f2){a.x * b.x - a.y * b.y, a.x * b.y + a.y * b.x}; }
DI f2 cfma(f2 a, f2 b, f2 c) { return (f2){fmaf(a.x, b.x, fmaf(-a.y, b.y, c.x)), fmaf(a.x, b.y, fmaf(a.y, b.x, c.y))}; }
DI f2 fma2(f2 a, f2 b, f2 c) { return __builtin_elementwise_fma(a, b, c); }
constexpr float EPS = 1e-6f, LOG2E = 1.44269504088896341f;
DI unsigned phi2(float s0, float s1) {
  const f2 a = fma2((f2){s0, s1}, (f2){LOG2E, LOG2E}, (f2){-EPS * LOG2E, -EPS * LOG2E});
  const f2 e = {__builtin_amdgcn_fmed3f(__builtin_amdgcn_exp2f(a.x), 0.f, 1.f), __builtin_amdgcn_fmed3f(__builtin_amdgcn_exp2f(a.y), 0.f, 1.f)};
  return pk2(e + (f2){__builtin_amdgcn_fmed3f(s0, EPS, __builtin_inff()), __builtin_amdgcn_fmed3f(s1, EPS, __builtin_inff())});
}
DI f2 swap_lo(f2 v, f2& hi) {
  const auto x = __builtin_amdgcn_permlane32_swap(__float_as_uint(v.x), __float_as_uint(v.x), false, false);
  const auto y = __builtin_amdgcn_permlane32_swap(__float_as_uint(v.y), __float_as_uint(v.y), false, false);
  hi = (f2){__uint_as_float(x[1]), __uint_as_float(y[1])};
  return (f2){__uint_as_float(x[0]), __uint_as_float(y[0])};
}
DI void wait_flags(__amdgpu_buffer_rsrc_t ws, unsigned off, unsigned tag) {
#pragma unroll 1
  for (unsigned sp = 0; sp < SPIN_CAP; ++sp) {
      asm volatile("" ::: "memory");
    const u2 f = __builtin_amdgcn_raw_buffer_load_b64(ws, off, 0, SC1);
    if (__all(f.x == tag && f.y == ~tag)) break;
    __builtin_amdgcn_s_sleep(1);
  }
  asm volatile("" ::: "memory");
}

__global__ void __launch_bounds__(512, 2)
fused_kernel(const u16* a0, const u16* a1, const u16* a2, const u16* a3, const u16* a4, const u16* a5, const u16* a6, const u16* a7,
             const u16* a8, const u16* a9, const u16* a10, const u16* a11, float* a12, char* a13) {
  extern __shared__ __attribute__((aligned(16))) char smem_[];
  char LAS* L = (char LAS*)smem_;
  const unsigned L0 = (unsigned)(size_t)L;
  ST(0);
  const int lane = threadIdx.x & 63;
  const int w8 = __builtin_amdgcn_readfirstlane(threadIdx.x >> 6), w = w8 & 3;
  unsigned cpf = 0;
  if (CPF && w8 == 0) {
    u64 pc;
    asm volatile("s_getpc_b64 %0" : "=s"(pc));
    cpf = __builtin_amdgcn_raw_buffer_load_b32(__builtin_amdgcn_make_buffer_rsrc((char*)(pc & ~127ull), 0, CPF * 4096, 0x00020000),
                                               lane < CPF ? ((blockIdx.x >> 3) * CPF + lane) * 128 : 0x80000000u, 0, 0);
  }
  typedef u64 u64x8 __attribute__((ext_vector_type(8)));
  typedef u64 u64x4 __attribute__((ext_vector_type(4)));
  typedef u64 u64x2 __attribute__((ext_vector_type(2)));
  u64x8 ka; u64x4 kb; u64x2 kc_;
  asm volatile("s_load_dwordx16 %0, %3, 0x0\n\ts_load_dwordx8 %1, %3, 0x40\n\ts_load_dwordx4 %2, %3, 0x60\n\ts_waitcnt lgkmcnt(0)"
               : "=&s"(ka), "=&s"(kb), "=&s"(kc_) : "s"(__builtin_amdgcn_kernarg_segment_ptr()) : "memory");
  const GAS u16 *xa = (const GAS u16*)ka[0], *wqkv = (const GAS u16*)ka[1], *wo = (const GAS u16*)ka[2], *xb = (const GAS u16*)ka[3];
  const GAS u16 *lre = (const GAS u16*)ka[4], *lim = (const GAS u16*)ka[5], *bre = (const GAS u16*)ka[6], *bim = (const GAS u16*)ka[7];
  const GAS u16 *cre = (const GAS u16*)kb[0], *cim = (const GAS u16*)kb[1], *ldt = (const GAS u16*)kb[2], *dsk = (const GAS u16*)kb[3];
  float* out = (float*)kc_[0]; char* wsp = (char*)kc_[1];
  const bool helper = w8 >= 4;
  const int bid = blockIdx.x, xcd = bid & 7, jj = bid >> 3;
  const int tile = (xcd >> 1) * 4 + (jj >> 3), cg = jj & 7;
  const int rot = ROT == 1 ? ((cg + 3 * (jj >> 3) + xcd) & 7) : ROT == 7 ? (((cg & 3) * 2 + (xcd >> 1)) & 7) : ((((cg + (jj >> 3)) % 3) * 3 + (xcd >> 1)) & 7);
  const int hr = HROT == 8 ? cg : HROT == 4 ? 2 * (cg & 3) : HROT == 2 ? 4 * (cg & 1) : 0;
  const int orot = 2 * hr + 4 * (jj >> 3);
  const __amdgpu_buffer_rsrc_t ws = __builtin_amdgcn_make_buffer_rsrc(wsp, 0, WS_END, 0x00020000);
  const __amdgpu_buffer_rsrc_t ro = __builtin_amdgcn_make_buffer_rsrc(out, 0, 2048 * 1024 * 4, 0x00020000);
  const int r8 = lane >> 3, c8 = lane & 7;
  const int q = lane >> 4, l15 = lane & 15, l31 = lane & 31, hh = lane >> 5;
  unsigned epoch;
#define LOAD_EPOCH() asm volatile("s_load_dword %0, %1, 0x0" : "=s"(epoch) : "s"(wsp) : "memory")

  if ((xcd & 1) == 0) {
    constexpr unsigned WA = 131072, WB = 147456, SCR = WA, TRB = 81920;
    constexpr unsigned QBUF[4] = {WB, 0, WA, WB}, OBUF[4] = {32768, 49152, 65536, 0};
    constexpr unsigned WBUF[8] = {WA, WB, 7 * 16384, 0, WA, WB, 0, WA};
    if (helper) {
      const int head = cg * 4 + w;
      unsigned vx[4], vw[2], vo[4];
#pragma unroll
      for (int i = 0; i < 4; ++i) vx[i] = (32 * w + 8 * i + r8) * 1024 + ((c8 ^ ((4 * i + (r8 >> 1)) & 7)) << 4) - 1024 * i;
#pragma unroll
      for (int i = 0; i < 2; ++i) vw[i] = (16 * w + 8 * i + r8) * 3072 + ((c8 ^ ((((r8 >> 1) & 1) | (i << 1)) << 1)) << 4) - 1024 * i;
      const GAS char* xbase = (const GAS char*)xa + (size_t)tile * 131072;
      const GAS char* wbase = (const GAS char*)wqkv + cg * 128;
      auto isx = [&](int kk, int slot) { dmax(L0 + slot * 16384 + w * 4096, xbase + ((kk + rot) & 7) * 128, vx[0], vx[1], vx[2], vx[3]); };
      auto isw = [&](unsigned dst, int kk, int seg) { dma2(L0 + dst + w * 2048, wbase + ((kk + rot) & 7) * (64 * 3072) + seg * 1024, vw[0], vw[1]); };
      auto issue = [&](int kk) { isx(kk, kk); isw(WBUF[kk], kk, 1); isw(WBUF[kk] + 8192, kk, 2); };
      auto isq = [&](int p) { isw(QBUF[p], 2 * p + 1, 0); isw(QBUF[p] + 8192, 2 * p + 2, 0); };
      ST(9); issue(0); ST(10); issue(1); ST(11);
      LOAD_EPOCH();
#pragma unroll
      for (int i = 0; i < 4; ++i) vo[i] = (8 * i + r8) * 1024 + ((c8 ^ (((r8 >> 1) & 3) << 1)) << 4) - 1024 * i;
      const GAS char* obase = (const GAS char*)wo + cg * 128;
      asm volatile("s_waitcnt vmcnt(8) lgkmcnt(0)\n\ts_barrier" : "+s"(epoch) :: "memory");
      issue(2);
      wait_bar<8>(); issue(3);
      wait_bar<8>(); issue(4);
      wait_bar<8>(); issue(5);
      wait_bar<8>(); issue(6);
      wait_bar<8>(); issue(7);
      wait_bar<8>(); isq(0);
      wait_bar<4>(); isq(1);
      wait_bar<4>(); isq(2);
      wait_bar<4>(); isq(3); isx(0, 1);
      wait_bar<8>();
      ST(1);
      const unsigned tag = epoch + 1, t = tag & 0xffu;
      const GAS char* kvb_ = (const GAS char*)wsp + WS_KV + head * 16384;
      const unsigned vk0 = lane * 16, vk1 = vk0 + 4096, vk2 = vk0 + 8192, vk3 = vk0 + 12288;
      u4 g[16], gz;
#pragma unroll
      for (int i = 0; i < 16; ++i) g[i] = (u4){0, 0, 0, 0};
      unsigned nw = 0xffffu;
      const GAS char* zb_ = (const GAS char*)wsp + WS_Z + head * 1024;
      const unsigned vz = l15 * 64 + q * 16;
      gz = (u4){0, 0, 0, 0};
#pragma unroll 1
      for (unsigned rep = 0; rep < SPIN_CAP; ++rep) {
        asm volatile("global_load_dwordx4 %0, %1, %2 sc1" : "+v"(gz) : "v"(vz), "s"(zb_) : "memory");
#define KVLD(i, v) if (nw & (1u << i)) asm volatile("global_load_dwordx4 %0, %1, %2 offset:%3 sc1" : "+v"(g[i]) : "v"(v), "s"(kvb_), "i"((i & 3) * 1024) : "memory")
        KVLD(0, vk0); KVLD(1, vk0); KVLD(2, vk0); KVLD(3, vk0); KVLD(4, vk1); KVLD(5, vk1); KVLD(6, vk1); KVLD(7, vk1);
        KVLD(8, vk2); KVLD(9, vk2); KVLD(10, vk2); KVLD(11, vk2); KVLD(12, vk3); KVLD(13, vk3); KVLD(14, vk3); KVLD(15, vk3);
        if (rep == 0) {
          asm volatile("s_waitcnt vmcnt(17)\n\ts_barrier" ::: "memory");
        }
        drain();
        asm volatile("" : "+v"(g[0]), "+v"(g[1]), "+v"(g[2]), "+v"(g[3]), "+v"(g[4]), "+v"(g[5]), "+v"(g[6]), "+v"(g[7]),
                     "+v"(g[8]), "+v"(g[9]), "+v"(g[10]), "+v"(g[11]), "+v"(g[12]), "+v"(g[13]), "+v"(g[14]), "+v"(g[15]), "+v"(gz) :: "memory");
        unsigned bad = (gz.x ^ t) | (gz.z ^ t);
#pragma unroll
        for (int i = 0; i < 16; ++i) bad |= (g[i].x ^ t) | (g[i].z ^ t);
        if (__all((bad & 0xffu) == 0)) break;
        nw = 0;
#pragma unroll
        for (int i = 0; i < 16; ++i) nw |= __all((((g[i].x ^ t) | (g[i].z ^ t)) & 0xffu) == 0) ? 0u : 1u << i;
      }
      ST(5);
      f4 kv = {0, 0, 0, 0};
#pragma unroll
      for (int i = 0; i < 16; ++i) { g[i].x ^= t; g[i].z ^= t; kv += __builtin_bit_cast(f4, g[i]); }
      gz.x ^= t; gz.z ^= t;
      f4 zz = __builtin_bit_cast(f4, gz);
#pragma unroll
      for (int r = 0; r < 4; ++r) {
        float v = zz[r];
        v += __builtin_bit_cast(float, __builtin_amdgcn_mov_dpp(__builtin_bit_cast(int, v), 0x128, 0xf, 0xf, true));
        v += __builtin_bit_cast(float, __builtin_amdgcn_mov_dpp(__builtin_bit_cast(int, v), 0x124, 0xf, 0xf, true));
        v += __builtin_bit_cast(float, __builtin_amdgcn_mov_dpp(__builtin_bit_cast(int, v), 0x122, 0xf, 0xf, true));
        v += __builtin_bit_cast(float, __builtin_amdgcn_mov_dpp(__builtin_bit_cast(int, v), 0x121, 0xf, 0xf, true));
        zz[r] = v;
      }
      *(LAS u4*)(L + SCR + w * 1024 + lane * 16) = (u4){pk2(kv[0], kv[1]), pk2(kv[2], kv[3]), pk2(zz[0], zz[1]), pk2(zz[2], zz[3])};
      ST(6);
      lbar();
      ST(7);
      lbar();
#pragma unroll
      for (int i = 0; i < 4; ++i)
        dma4(L0 + OBUF[i] + w * 4096, obase + ((4 * i + w + orot) & 15) * 32768, vo[0], vo[1], vo[2], vo[3]);
      wait_bar<0>();
      ST(8);
      HFLUSH();
      if (bid == 0 && w == 0) {
#pragma unroll 1
        for (unsigned sp = 0; sp < SPIN_CAP; ++sp) {
          asm volatile("" ::: "memory");
          const unsigned fo = (lane >> 3) * 4096 + (lane & 7) * 256;
          const u2 f0 = __builtin_amdgcn_raw_buffer_load_b64(ws, WS_FA + fo, 0, SC1), f1 = __builtin_amdgcn_raw_buffer_load_b64(ws, WS_FA + 32768 + fo, 0, SC1);
          const u2 f2_ = __builtin_amdgcn_raw_buffer_load_b64(ws, WS_FB + fo, 0, SC1), f3 = __builtin_amdgcn_raw_buffer_load_b64(ws, WS_FB + 32768 + fo, 0, SC1);
          if (__all(f0.x == tag && f0.y == ~tag && f1.x == tag && f1.y == ~tag && f2_.x == tag && f2_.y == ~tag && f3.x == tag && f3.y == ~tag)) break;
          __builtin_amdgcn_s_sleep(8);
        }
        if (lane == 0) __builtin_amdgcn_raw_buffer_store_b32(tag, ws, WS_EPOCH, 0, SC1);
      }
      return;
    }
    LOAD_EPOCH();
    const int hp = w & 1, th = w >> 1;
    unsigned warm0 = 0, warm1 = 0;
    if (TOUCH) {
      warm0 = __builtin_amdgcn_raw_buffer_load_b32(ws, lane < 16 ? WS_FA + ((tile * 2 + th) * 16 + lane) * 128 : lane < 24 ? WS_Z + (cg * 4 + w) * 1024 + (lane - 16) * 128
                                                   : lane == 24 ? WS_KV + (cg * 4 + w) * 16384 : lane == 25 ? WS_O + tile * 131072 : 0x80000000u, 0, SC1);
      warm1 = __builtin_amdgcn_raw_buffer_load_b32(ro, lane < 2 ? (tile * 128 + w * 32 + lane * 16) * 4096 : 0x80000000u, 0, SC1);
    }
    const int qq = (lane >> 2) & 3, pp = lane & 3;
    const unsigned ax0 = l15 * 128 + ((q ^ (l15 >> 1)) << 4), ax1 = l15 * 128 + (((4 + q) ^ (l15 >> 1)) << 4);
    auto waddr = [&](int h) { return (8 * q + qq) * 128 + (((2 * h + (pp >> 1)) ^ ((((qq >> 1) & 1) | ((q & 1) << 1)) << 1)) << 4) + 8 * (pp & 1); };
    const unsigned aw = waddr(w), aq0 = waddr(2 * hp), aq1 = waddr(2 * hp + 1);
    f4 ka[8], va[8];
#pragma unroll
    for (int i = 0; i < 8; ++i) { ka[i] = (f4){EPS, EPS, EPS, EPS}; va[i] = (f4){0, 0, 0, 0}; }
    bf8 X0[4], X1[4], W0[2], W1[2];
    auto rdx = [&](bf8 (&X)[4], int kk, int ks, unsigned off) {
#pragma unroll
      for (int i = 0; i < 4; ++i) X[i] = ldsr(L + kk * 16384 + (ks ? ax1 : ax0) + off + i * 2048);
    };
    auto rdw = [&](bf8 (&W)[2], int kk, int ks) {
#pragma unroll
      for (int i = 0; i < 2; ++i) { const char LAS* p = L + WBUF[kk] + aw + ks * 4096 + i * 8192; W[i] = ldstr(p, p + 512); }
    };
    auto mm = [&](const bf8 (&X)[4], const bf8 (&W)[2], int h) {
#pragma unroll
      for (int i = 0; i < 4; ++i) {
        ka[4 * h + i] = mfma16(X[i], W[0], ka[4 * h + i]);
        va[4 * h + i] = mfma16(X[i], W[1], va[4 * h + i]);
      }
    };
#define ILV(nr, nm) do { _Pragma("unroll") for (int i_ = 0; i_ < nr; ++i_) { SG(0x008, 1); SG(0x100, 1); } if (nm > nr) SG(0x008, nm - nr); } while (0)
    asm volatile("s_waitcnt lgkmcnt(0)\n\ts_barrier" : "+s"(epoch) : "v"(warm0), "v"(warm1), "v"(cpf) : "memory");
    ST(1);
    const unsigned tag = epoch + 1, t = tag & 0xffu;
    rdw(W0, 0, 0); rdx(X0, 0, 0, 0);
    SB();
#pragma unroll
    for (int kk = 0; kk < 8; ++kk) {
      rdx(X1, kk, 0, 8192); mm(X0, W0, 0); ILV(4, 8); SB();
      rdw(W1, kk, 1); rdx(X0, kk, 1, 0); mm(X1, W0, 1); ILV(8, 8); SB();
      rdx(X1, kk, 1, 8192); mm(X0, W1, 0); ILV(4, 8); SB();
      if (kk < 7) { lbar(); rdw(W0, kk + 1, 0); rdx(X0, kk + 1, 0, 0); mm(X1, W1, 1); ILV(8, 8); }
      else mm(X1, W1, 1);
      SB();
    }
    ST(2);
    const unsigned xq = th * 8192;
    auto rdq = [&](bf8 (&X)[4], bf8 (&W)[2], int s) {
      const char LAS* p = L + QBUF[s >> 2] + ((s >> 1) & 1) * 8192 + (s & 1) * 4096;
      W[0] = ldstr(p + aq0, p + aq0 + 512); W[1] = ldstr(p + aq1, p + aq1 + 512);
      rdx(X, s < 14 ? (s >> 1) + 1 : 1, s & 1, xq);
    };
    {
      const int head = cg * 4 + w;
      f4 kv = {0, 0, 0, 0}, zz = {0, 0, 0, 0};
      const u4 ones = {0x3f803f80u, 0x3f803f80u, 0x3f803f80u, 0x3f803f80u};
#pragma unroll
      for (int m = 0; m < 4; ++m) {
        const f4 a = ka[2 * m], b = ka[2 * m + 1], c = va[2 * m], d = va[2 * m + 1];
        const u4 pa = {phi2(a[0], a[1]), phi2(a[2], a[3]), phi2(b[0], b[1]), phi2(b[2], b[3])};
        const u4 pb = {pk2(c[0], c[1]), pk2(c[2], c[3]), pk2(d[0], d[1]), pk2(d[2], d[3])};
        kv = mfma16(asbf8(pa), asbf8(pb), kv);
        zz = mfma16(asbf8(pa), asbf8(ones), zz);
      }
      const u4 skv = {tagv(kv[0], t), __float_as_uint(kv[1]), tagv(kv[2], t), __float_as_uint(kv[3])};
      __builtin_amdgcn_raw_buffer_store_b128(skv, ws, WS_KV + (head * 16 + tile) * 1024 + lane * 16, 0, SC1);
      const u4 sz = {tagv(zz[0], t), __float_as_uint(zz[1]), tagv(zz[2], t), __float_as_uint(zz[3])};
      __builtin_amdgcn_raw_buffer_store_b128(sz, ws, l15 == 0 ? WS_Z + (head * 16 + tile) * 64 + q * 16 : 0x80000000u, 0, SC1);
    }
    SB();
    ST(3);
    lbar();
    rdq(X0, W0, 0);
    SB();
    f4 qa[2][4];
#pragma unroll
    for (int i = 0; i < 8; ++i) qa[i >> 2][i & 3] = (f4){EPS, EPS, EPS, EPS};
    auto mq = [&](const bf8 (&X)[4], const bf8 (&W)[2]) {
#pragma unroll
      for (int i = 0; i < 4; ++i) { qa[0][i] = mfma16(W[0], X[i], qa[0][i]); qa[1][i] = mfma16(W[1], X[i], qa[1][i]); }
    };
#pragma unroll
    for (int s = 0; s < 16; s += 2) {
      rdq(X1, W1, s + 1); mq(X0, W0); ILV(8, 8); SB();
      if (s < 14) { if ((s & 3) == 2) lbar(); rdq(X0, W0, s + 2); mq(X1, W1); ILV(8, 8); }
      else mq(X1, W1);
      SB();
    }
    ST(4);
    u2 pq[2][4];
#pragma unroll
    for (int i = 0; i < 8; ++i) pq[i >> 2][i & 3] = (u2){phi2(qa[i >> 2][i & 3][0], qa[i >> 2][i & 3][1]), phi2(qa[i >> 2][i & 3][2], qa[i >> 2][i & 3][3])};
    unsigned ao[4], og[4];
#pragma unroll
    for (int i = 0; i < 4; ++i) og[i] = WS_O + ((tile * 8 + th * 4 + i) * 16 + cg * 2 + hp) * 1024 + lane * 16;
#pragma unroll
    for (int nt = 0; nt < 4; ++nt) ao[nt] = (4 * q + qq) * 128 + ((((2 * nt) ^ (((2 * q + (qq >> 1)) & 3) << 1)) + (pp >> 1)) << 4) + 8 * (pp & 1);
    asm volatile("s_barrier" : "+v"(pq[0][0]), "+v"(pq[0][1]), "+v"(pq[0][2]), "+v"(pq[0][3]), "+v"(pq[1][0]), "+v"(pq[1][1]), "+v"(pq[1][2]), "+v"(pq[1][3]),
                 "+v"(og[0]), "+v"(og[1]), "+v"(og[2]), "+v"(og[3]) :: "memory");
    ST(5);
    const f4 zero4 = {0, 0, 0, 0}, eps4 = {EPS, EPS, EPS, EPS};
    f4 num[2][4], den[2][4];
#pragma unroll
    for (int h = 0; h < 2; ++h) {
      const u4 kz = *(const LAS u4*)(L + SCR + (2 * hp + h) * 1024 + lane * 16);
#pragma unroll
      for (int i = 0; i < 4; ++i) { den[h][i] = mfma16k16((u2){kz.z, kz.w}, pq[h][i], eps4); num[h][i] = mfma16k16((u2){kz.x, kz.y}, pq[h][i], zero4); }
    }
    SB();
#pragma unroll
    for (int i = 0; i < 4; ++i) {
      u4 so;
#pragma unroll
      for (int h = 0; h < 2; ++h) {
        const float rc = __builtin_amdgcn_rcpf(den[h][i][0]);
        const f4 n = num[h][i];
        so[2 * h] = pk2((f2){n[0], n[1]} * rc);
        so[2 * h + 1] = pk2((f2){n[2], n[3]} * rc);
      }
      __builtin_amdgcn_raw_buffer_store_b128(so, ws, og[i], 0, SC1);
    }
    asm volatile("s_barrier" ::: "memory");
    ST(6);
    drain();
    __builtin_amdgcn_raw_buffer_store_b64((u2){tag, ~tag}, ws, lane == 0 ? WS_FA + ((tile * 2 + th) * 16 + cg * 2 + hp) * 128 : 0x80000000u, 0, SC1);
    unsigned tw[4], trd[4], so_ = ((tile * 128 + w * 32 + q) * 1024 + cg * 64 + l15 * 4) * 4;
#pragma unroll
    for (int i = 0; i < 4; ++i) {
      tw[i] = TRB + w * 8192 + l15 * 256 + (((4 * i + q) ^ l15) << 4);
      trd[i] = TRB + w * 8192 + (4 * i + q) * 256 + ((l15 ^ (4 * i + q)) << 4);
      asm volatile("" : "+v"(tw[i]), "+v"(trd[i]));
    }
    asm volatile("s_barrier" : "+v"(so_) :: "memory");
    ST(7);
    f4 acc[2][4];
#pragma unroll
    for (int i = 0; i < 8; ++i) acc[i >> 2][i & 3] = zero4;
    const unsigned ob = WS_O + (tile * 8 + w * 2) * 16384 + lane * 16;
    u4 of[32];
    bf8 wf[2][4];
    auto rdo = [&](int k) {
#pragma unroll
      for (int nt = 0; nt < 4; ++nt) { const char LAS* p = L + OBUF[k >> 2] + (k & 3) * 4096 + ao[nt]; wf[k & 1][nt] = ldstr(p, p + 2048); }
    };
    auto ldo = [&](int k) {
      const unsigned so = ((k + orot) & 15) * 1024;
      of[2 * k] = __builtin_amdgcn_raw_buffer_load_b128(ws, ob, so, PAY_AUX);
      of[2 * k + 1] = __builtin_amdgcn_raw_buffer_load_b128(ws, ob + 16384, so, PAY_AUX);
    };
    rdo(0);
    wait_flags(ws, WS_FA + ((tile * 2 + th) * 16 + l15) * 128, tag);
    ST(8); CT(14);
#pragma unroll
    for (int k = 0; k < OD; ++k) ldo(k);
    SB();
#pragma unroll
    for (int k = 0; k < 16; ++k) {
      if (k + OD < 16) ldo(k + OD);
      if (k + 1 < 16) rdo(k + 1);
#pragma unroll
      for (int nt = 0; nt < 4; ++nt) {
        acc[0][nt] = mfma16(wf[k & 1][nt], asbf8(of[2 * k]), acc[0][nt]);
        acc[1][nt] = mfma16(wf[k & 1][nt], asbf8(of[2 * k + 1]), acc[1][nt]);
      }
#pragma unroll
      for (int i_ = 0; i_ < 8; ++i_) { SG(0x008, 1); SG(0x100, 1); if (i_ == 1 || i_ == 5) SG(0x020, 1); }
      SB();
    }
    ST(9); CT(15);
#pragma unroll
    for (int mt = 0; mt < 2; ++mt)
#pragma unroll
      for (int nt = 0; nt < 4; ++nt) {
        const f4 v = acc[mt][nt];
        const unsigned p0 = pk2(v[0], v[1]), p1 = pk2(v[2], v[3]);
        *(LAS f4*)(L + tw[nt] + mt * 4096) = (f4){bflo(p0), bfhi(p0), bflo(p1), bfhi(p1)};
      }
    u4 ov[8];
#pragma unroll
    for (int i = 0; i < 8; ++i) ov[i] = *(const LAS u4*)(L + trd[i & 3] + (i >> 2) * 4096);
    SB();
#pragma unroll
    for (int i = 0; i < 8; ++i) __builtin_amdgcn_raw_buffer_store_b128(ov[i], ro, so_, i * 16384, OUT_AUX);
    ST(10);
  } else {
    constexpr unsigned SLOT = 32768, SCR = SLOT, PAR = 4 * SLOT;
    const int crot = hr + 2 * (jj >> 3);
    const int wp = w & 1, wt = w >> 1;
    const unsigned ecol = WS_EST + (cg * 64 + wp * 32 + l31) * 8;
    if (helper) {
      unsigned vx[4], vw[4], vc[4];
#pragma unroll
      for (int i = 0; i < 4; ++i) {
        const unsigned sw = (c8 ^ ((4 * i + (r8 >> 1)) & 7)) << 4;
        vx[i] = (64 * (w >> 1) + 32 * (r8 >> 2) + 16 * (r8 & 1) + 8 * (w & 1) + ((r8 >> 1) & 1) + 2 * i) * 1024 + sw - 1024 * i;
        vw[i] = (16 * (i >> 1) + 8 * (r8 >> 2) + 4 * (i & 1) + (r8 & 3)) * 1024 + sw - 1024 * i;
        vc[i] = (8 * i + r8) * 1024 + sw - 1024 * i;
      }
      const GAS char* xbase = (const GAS char*)xb + (size_t)tile * 131072;
      const GAS char* wbase = (const GAS char*)((w & 1) ? bim : bre) + (cg * 64 + (w >> 1) * 32) * 1024;
      auto issue = [&](int kk) {
        const int kc = (kk + rot) & 7;
        const unsigned s = L0 + (kk % 5) * SLOT + w * 4096;
        dmax(s, xbase + kc * 128, vx[0], vx[1], vx[2], vx[3]);
        dma4(s + 16384, wbase + kc * 128, vw[0], vw[1], vw[2], vw[3]);
      };
      ST(9); issue(0); ST(10); issue(1); ST(11);
      LOAD_EPOCH();
      const GAS char* cbase = (const GAS char*)((w & 1) ? cim : cre) + cg * 65536;
      auto issue_c = [&](int piece) {
        const unsigned s = L0 + ((piece + 3) % 5) * SLOT + w * 8192;
        const GAS char* cb = cbase + ((crot + 2 * piece + (w >> 1)) & 7) * 128;
        dma4(s, cb, vc[0], vc[1], vc[2], vc[3]);
        dma4(s + 4096, cb + 32768, vc[0], vc[1], vc[2], vc[3]);
      };
      asm volatile("s_waitcnt vmcnt(8) lgkmcnt(0)\n\ts_barrier" : "+s"(epoch) :: "memory");
      issue(2);
      const f4 par = *(const LAS f4*)(L + PAR + w * 1024 + lane * 16);
      const f2 l64 = {par[0], par[1]}, l128 = {par[2], par[3]};
      f2 l256 = cmul(l128, l128);
      f2 wgt = hh ? l64 : (f2){1.f, 0.f};
      if (wt) wgt = cmul(wgt, l128);
      const unsigned tag = epoch + 1, t = tag & 0xffu;
      unsigned eo[8], et[8];
#pragma unroll
      for (int m = 0; m < 8; ++m) {
        const int i = 2 * tile - 1 - (2 * wt + hh) - 4 * m;
        eo[m] = i >= 0 ? ecol + i * 4096 : 0x80000000u;
        et[m] = i >= 0 ? t : 0u;
        asm volatile("" : "+v"(eo[m]), "+v"(et[m]));
      }
      asm volatile("" : "+v"(l256), "+v"(wgt));
      wait_bar<8>(); issue(3);
      wait_bar<8>(); issue(4);
      wait_bar<8>(); issue(5);
      wait_bar<8>(); issue(6);
      wait_bar<8>(); issue(7);
      wait_bar<8>(); issue_c(0);
      wait_bar<8>(); ST(1);
      if (NC_EARLY > 1) issue_c(1);
      if (NC_EARLY > 2) issue_c(2);
      ST(2);
      f2 cin;
#pragma unroll 1
      for (unsigned sp = 0; sp < SPIN_CAP; ++sp) {
      asm volatile("" ::: "memory");
        u2 g[8];
#pragma unroll
        for (int m = 0; m < 8; ++m) g[m] = __builtin_amdgcn_raw_buffer_load_b64(ws, eo[m], 0, SC1);
        unsigned bad = 0;
        cin = (f2){0, 0};
#pragma unroll
        for (int m = 7; m >= 0; --m) {
          const unsigned cx = g[m].x ^ et[m];
          bad |= cx;
          cin = cfma(l256, cin, (f2){__uint_as_float(cx), __uint_as_float(g[m].y)});
        }
        if (__all((bad & 0xffu) == 0)) break;
      }
      ST(5);
      cin = cmul(cin, wgt);
      f2 chi;
      const f2 clo = swap_lo(cin, chi);
      if (hh == 0) *(LAS f2*)(L + SCR + (wp * 2 + wt) * 256 + l31 * 8) = clo + chi;
      ST(6);
      lbar();
      lbar();
      ST(7);
      if (NC_EARLY <= 1) issue_c(1);
      if (NC_EARLY <= 2) issue_c(2);
      issue_c(3);
      wait_bar<0>();
      ST(8); HFLUSH();
      return;
    }
    LOAD_EPOCH();
    const int pl = cg * 64 + wp * 32 + ((l31 & 0x13) | ((l31 & 4) << 1) | ((l31 & 8) >> 1));
    const unsigned plr = lre[pl], pli = lim[pl], pdt = ldt[pl];
    unsigned warm0 = 0, warm1 = 0;
    if (TOUCH) {
      warm0 = __builtin_amdgcn_raw_buffer_load_b32(ws, lane < 16 ? WS_FB + ((tile * 2 + (w >> 1)) * 16 + lane) * 128 : lane == 16 ? WS_EST + tile * 8192
                                                   : lane == 17 ? WS_H + (tile * 4 + w) * 65536 : 0x80000000u, 0, SC1);
      warm1 = __builtin_amdgcn_raw_buffer_load_b32(ro, lane < 2 ? (tile * 128 + w * 32 + lane * 16) * 4096 + 2048 : 0x80000000u, 0, SC1);
    }
    f2 lam, kap, l16, l32, l64, l128;
    {
      const float lr = bflo(plr), li = bflo(pli);
      const float dt = bflo(pk2(expf(bflo(pdt)), 0.f));
      const float mag = expf(dt * lr);
      float sn, cs;
      sincosf(dt * li, &sn, &cs);
      lam = (f2){mag * cs, mag * sn};
      const float inv = 2.f / (lr * lr + li * li), nr = lam.x - 1.f, ni = lam.y;
      kap = (f2){(nr * lr + ni * li) * inv, (ni * lr - nr * li) * inv};
      f2 t = cmul(lam, lam); t = cmul(t, t); t = cmul(t, t);
      l16 = cmul(t, t); l32 = cmul(l16, l16); l64 = cmul(l32, l32); l128 = cmul(l64, l64);
      *(LAS f4*)(L + PAR + w * 1024 + lane * 16) = (f4){l64.x, l64.y, l128.x, l128.y};
    }
    unsigned ar[4];
#pragma unroll
    for (int s = 0; s < 4; ++s) ar[s] = l31 * 128 + (((2 * s + hh) ^ ((lane >> 1) & 7)) << 4);
    f16v ur[2], ui[2];
#pragma unroll
    for (int i = 0; i < 16; ++i) { ur[0][i] = 0; ur[1][i] = 0; ui[0][i] = 0; ui[1][i] = 0; }
    bf8 F0[4], F1[4];
    auto rd = [&](bf8 (&F)[4], int kk, int s) {
      const char LAS* sx = L + (kk % 5) * SLOT + wt * 8192 + ar[s];
      const char LAS* sw = L + (kk % 5) * SLOT + 16384 + wp * 8192 + ar[s];
      F[0] = ldsr(sx); F[1] = ldsr(sw); F[2] = ldsr(sw + 4096); F[3] = ldsr(sx + 4096);
    };
    auto mm = [&](const bf8 (&F)[4]) {
      ur[0] = mfma32(F[0], F[1], ur[0]); ui[0] = mfma32(F[0], F[2], ui[0]);
      ur[1] = mfma32(F[3], F[1], ur[1]); ui[1] = mfma32(F[3], F[2], ui[1]);
    };
#define ILB() do { SG(0x008, 1); SG(0x100, 2); SG(0x008, 1); SG(0x100, 2); SG(0x008, 2); } while (0)
    asm volatile("s_waitcnt lgkmcnt(0)\n\ts_barrier" : "+s"(epoch), "+v"(lam), "+v"(kap), "+v"(l16), "+v"(l32), "+v"(l64) : "v"(warm0), "v"(warm1), "v"(cpf) : "memory");
    ST(1);
    const unsigned tag = epoch + 1, t = tag & 0xffu;
    rd(F0, 0, 0);
    SB();
#pragma unroll
    for (int kk = 0; kk < 8; ++kk) {
      rd(F1, kk, 1); mm(F0); ILB(); SB();
      rd(F0, kk, 2); mm(F1); ILB(); SB();
      rd(F1, kk, 3); mm(F0); ILB(); SB();
      if (kk < 7) { lbar(); if (kk == 6) ST(2); rd(F0, kk + 1, 0); mm(F1); ILB(); }
      else mm(F1);
      SB();
    }
    ST(3);
    f2 gr[16], gi[16];
#pragma unroll
    for (int a = 0; a < 16; ++a) {
      gr[a] = (f2){ur[a >> 3][2 * (a & 7)], ur[a >> 3][2 * (a & 7) + 1]};
      gi[a] = (f2){ui[a >> 3][2 * (a & 7)], ui[a >> 3][2 * (a & 7) + 1]};
    }
    {
      f2 LR = {lam.x, lam.x}, LI = {lam.y, lam.y}, LN = {-lam.y, -lam.y};
      asm volatile("s_nop 11" : "+v"(gr[0]), "+v"(gi[0]), "+v"(gr[8]), "+v"(gi[8]), "+v"(LR), "+v"(LI), "+v"(LN));
#pragma unroll
      for (int a = 1; a < 16; ++a)
        asm volatile("v_pk_fma_f32 %0, %6, %3, %0\n\tv_pk_fma_f32 %1, %5, %2, %1\n\tv_pk_fma_f32 %0, %4, %2, %0\n\tv_pk_fma_f32 %1, %4, %3, %1"
                     : "+v"(gr[a]), "+v"(gi[a]) : "v"(gr[a - 1]), "v"(gi[a - 1]), "v"(LR), "v"(LI), "v"(LN));
    }
    const f2 er = gr[15], ei = gi[15];
    const f2 e0 = {er.x, ei.x};
    const f2 e32 = cfma(l16, e0, (f2){er.y, ei.y});
    f2 ehi;
    const f2 elo = swap_lo(e32, ehi);
    const f2 e64 = cfma(l32, elo, ehi);
    __builtin_amdgcn_raw_buffer_store_b64((u2){tagv(e64.x, t), __float_as_uint(e64.y)}, ws, hh == 0 ? ecol + (tile * 2 + wt) * 4096 : 0x80000000u, 0, SC1);
    if (wt == 0 && hh == 0) *(LAS f2*)(L + SCR + 1024 + wp * 256 + l31 * 8) = e64;
    SB();
    ST(4);
    auto tokr = [&](int r) { return tile * 128 + 64 * (w >> 1) + 32 * ((r >> 2) & 1) + 16 * (r & 1) + 8 * (w & 1) + ((r >> 1) & 1) + 2 * (r >> 3); };
    u2 xd[8];
    const u2 dv = *(const GAS u2*)(dsk + cg * 64 + l15 * 4);
#pragma unroll
    for (int i = 0; i < 8; ++i) xd[i] = *(const GAS u2*)(xb + tokr(4 * i + q) * 512 + cg * 64 + l15 * 4);
    SB();
    {
      const f2 KR = {kap.x, kap.x}, KI = {kap.y, kap.y};
#pragma unroll
      for (int a = 0; a < 16; ++a) {
        const f2 xr = gr[a], xi = gi[a];
        gr[a] = fma2(KR, xr, -KI * xi); gi[a] = fma2(-KR, xi, -KI * xr);
        asm volatile("" : "+v"(gr[a]), "+v"(gi[a]));
      }
    }
    f2 pw[16];
    pw[0] = (f2){lam.x, -lam.y};
#pragma unroll
    for (int k = 1; k < 16; ++k) { pw[k] = cmul(pw[k >> 1], pw[(k - 1) >> 1]); asm volatile("" : "+v"(pw[k])); }
    u4 idn[2];
#pragma unroll
    for (int s = 0; s < 2; ++s)
#pragma unroll
      for (int jq = 0; jq < 4; ++jq) {
        const int r0 = 16 * s + 8 * (jq >> 1) + 4 * hh + 2 * (jq & 1);
        idn[s][jq] = (l31 == r0 ? 0x3f80u : 0u) | (l31 == r0 + 1 ? 0x3f800000u : 0u);
      }
    asm volatile("" : "+v"(idn[0]), "+v"(idn[1]));
    f2 m0 = hh ? cmul(kap, l32) : kap, b0 = hh ? cmul(kap, elo) : (f2){0.f, 0.f};
    f2 m1 = cmul(l16, m0), b1 = cfma(l16, b0, cmul(kap, e0));
    unsigned ho[2];
    ho[0] = WS_H + (tile * 4 + wt * 2) * 65536 + (cg * 2 + wp) * 4096 + lane * 16; ho[1] = ho[0] + 65536;
    asm volatile("s_waitcnt lgkmcnt(0)\n\ts_barrier" : "+v"(ho[0]), "+v"(ho[1]), "+v"(m0), "+v"(b0), "+v"(m1), "+v"(b1) :: "memory");
    ST(5);
    f2 cin = *(const LAS f2*)(L + SCR + wp * 512 + l31 * 8) + *(const LAS f2*)(L + SCR + wp * 512 + 256 + l31 * 8);
    const f2 esib = *(const LAS f2*)(L + SCR + 1024 + wp * 256 + l31 * 8);
    if (wt) cin = cfma(l64, cin, esib);
    const f2 d0 = cfma(m0, cin, b0), d1 = cfma(m1, cin, b1);
    const f2 DR = {d0.x, d1.x}, DJ = {-d0.y, -d1.y};
    f16v zero16;
#pragma unroll
    for (int i = 0; i < 16; ++i) zero16[i] = 0;
    auto fix = [&](int m, int c) {
      u4 a[2];
#pragma unroll
      for (int i = 0; i < 8; ++i) {
        const f2 P = pw[8 * m + i], PR = {P.x, P.x}, PI = {P.y, P.y};
        const f2 h = c ? fma2(PI, DR, fma2(PR, DJ, gi[8 * m + i])) : fma2(-PI, DJ, fma2(PR, DR, gr[8 * m + i]));
        a[i >> 2][i & 3] = pk2(h);
      }
      return mfma32(asbf8(a[1]), asbf8(idn[1]), mfma32(asbf8(a[0]), asbf8(idn[0]), zero16));
    };
    auto put = [&](const f16v& T, int m, int c) {
      const u4 o0 = {pk2(T[0], T[1]), pk2(T[2], T[3]), pk2(T[4], T[5]), pk2(T[6], T[7])};
      const u4 o1 = {pk2(T[8], T[9]), pk2(T[10], T[11]), pk2(T[12], T[13]), pk2(T[14], T[15])};
      __builtin_amdgcn_raw_buffer_store_b128(o0, ws, ho[m] + c * 2048, 0, SC1);
      __builtin_amdgcn_raw_buffer_store_b128(o1, ws, ho[m] + c * 2048 + 1024, 0, SC1);
    };
    {
      const f16v T0 = fix(0, 0); SB();
      const f16v T1 = fix(0, 1); SB();
      put(T0, 0, 0);
      const f16v T2 = fix(1, 0); SB();
      put(T1, 0, 1);
      const f16v T3 = fix(1, 1); SB();
      put(T2, 1, 0); SB();
      put(T3, 1, 1);
    }
    asm volatile("s_barrier" ::: "memory");
    ST(6);
    drain();
    __builtin_amdgcn_raw_buffer_store_b64((u2){tag, ~tag}, ws, lane == 0 ? WS_FB + ((tile * 2 + wt) * 16 + cg * 2 + wp) * 128 : 0x80000000u, 0, SC1);
    ST(7);
    unsigned pull = 0;
    if (PULL) pull = __builtin_amdgcn_raw_buffer_load_b32(ws, WS_H + (tile * 4 + wt * 2 + hh) * 65536 + (cg * 2 + wp) * 4096 + l31 * 128, 0, SC1);
    unsigned tw[8], trd[4], so_ = ((tile * 128 + 64 * (w >> 1) + 8 * (w & 1) + 16 * (q & 1) + (q >> 1)) * 1024 + 512 + cg * 64 + l15 * 4) * 4;
#pragma unroll
    for (int i = 0; i < 8; ++i) { tw[i] = 2 * SLOT + w * 8192 + l31 * 256 + (((2 * i + hh) ^ l15) << 4); asm volatile("" : "+v"(tw[i])); }
#pragma unroll
    for (int i = 0; i < 4; ++i) { trd[i] = 2 * SLOT + w * 8192 + (4 * i + q) * 256 + ((l15 ^ (4 * i + q)) << 4); asm volatile("" : "+v"(trd[i])); }
    asm volatile("" : "+v"(so_));
#pragma unroll
    for (int i = 0; i < 8; ++i) {
      const unsigned p0 = pk2(bflo(xd[i].x) * bflo(dv.x), bfhi(xd[i].x) * bfhi(dv.x)), p1 = pk2(bflo(xd[i].y) * bflo(dv.y), bfhi(xd[i].y) * bfhi(dv.y));
      *(LAS f4*)(L + trd[i & 3] + (i >> 2) * 4096) = (f4){bflo(p0), bfhi(p0), bflo(p1), bfhi(p1)};
    }
    f16v acc[2];
#pragma unroll
    for (int i = 0; i < 8; ++i) {
      const f4 v = *(const LAS f4*)(L + tw[i]);
#pragma unroll
      for (int j = 0; j < 4; ++j) acc[i >> 2][4 * (i & 3) + j] = v[j];
    }
    asm volatile("s_barrier" : "+v"(acc[0]), "+v"(acc[1]) :: "memory");
    const unsigned hb = WS_H + (tile * 4 + w) * 65536 + lane * 16;
    u4 hf[64];
    bf8 cf[2][4];
    auto ldh = [&](int i) { hf[i] = __builtin_amdgcn_raw_buffer_load_b128(ws, hb + (i & 7) * 1024, ((crot + (i >> 3)) & 7) * 8192, PAY_AUX); };
    auto rdc = [&](int g) {
      const int tt = g >> 2, i = (g & 3) * 2;
      const char LAS* cb = L + (((tt >> 1) + 3) % 5) * SLOT + (tt & 1) * 16384 + ((i >> 1) & 1) * 8192;
      cf[g & 1][0] = ldsr(cb + ar[(i >> 2) * 2]); cf[g & 1][1] = ldsr(cb + ar[(i >> 2) * 2] + 4096);
      cf[g & 1][2] = ldsr(cb + ar[(i >> 2) * 2 + 1]); cf[g & 1][3] = ldsr(cb + ar[(i >> 2) * 2 + 1] + 4096);
    };
    rdc(0);
    wait_flags(ws, WS_FB + ((tile * 2 + (w >> 1)) * 16 + l15) * 128, tag);
    asm volatile("" :: "v"(pull));
    ST(8); CT(14);
#pragma unroll
    for (int i = 0; i < HD; ++i) ldh(i);
    SB();
#pragma unroll
    for (int g = 0; g < 32; ++g) {
      if (2 * g + HD < 64) { ldh(2 * g + HD); ldh(2 * g + HD + 1); }
      if (g + 1 < 32) rdc(g + 1);
      acc[0] = mfma32(cf[g & 1][0], asbf8(hf[2 * g]), acc[0]);
      acc[1] = mfma32(cf[g & 1][1], asbf8(hf[2 * g]), acc[1]);
      acc[0] = mfma32(cf[g & 1][2], asbf8(hf[2 * g + 1]), acc[0]);
      acc[1] = mfma32(cf[g & 1][3], asbf8(hf[2 * g + 1]), acc[1]);
      SG(0x008, 1); SG(0x100, 2); SG(0x008, 1); SG(0x100, 2); SG(0x008, 1); SG(0x020, 1); SG(0x008, 1); SG(0x020, 1);
      SB();
    }
    ST(9); CT(15);
#pragma unroll
    for (int i = 0; i < 8; ++i)
      *(LAS f4*)(L + tw[i]) = (f4){acc[i >> 2][4 * (i & 3)], acc[i >> 2][4 * (i & 3) + 1], acc[i >> 2][4 * (i & 3) + 2], acc[i >> 2][4 * (i & 3) + 3]};
    u4 ov[8];
#pragma unroll
    for (int i = 0; i < 8; ++i) ov[i] = *(const LAS u4*)(L + trd[i & 3] + (i >> 2) * 4096);
    SB();
#pragma unroll
    for (int i = 0; i < 8; ++i) __builtin_amdgcn_raw_buffer_store_b128(ov[i], ro, so_, (32 * (i & 1) + 2 * (i >> 1)) * 4096, OUT_AUX);
    ST(10);
  }
}

extern "C" void kernel_launch(void* const* d_in, const int* in_sizes, int n_in, void* d_out, int out_size, void* d_ws, size_t ws_size,
                              hipStream_t stream) {
  (void)hipFuncSetAttribute((const void*)fused_kernel, hipFuncAttributeMaxDynamicSharedMemorySize, LDS_BYTES);
  fused_kernel<<<256, 512, LDS_BYTES, stream>>>((const u16*)d_in[0], (const u16*)d_in[1], (const u16*)d_in[2], (const u16*)d_in[3],
                                               (const u16*)d_in[4], (const u16*)d_in[5], (const u16*)d_in[6], (const u16*)d_in[7],
                                               (const u16*)d_in[8], (const u16*)d_in[9], (const u16*)d_in[10], (const u16*)d_in[11],
                                               (float*)d_out, (char*)d_ws);
}
```

```cpp
#define PD 4
#define AMP_A 0
#define AMP_B 0
#define AMP_BID 254
#define AMP_K 0
#include <hip/hip_runtime.h>
#include <hip/hip_bf16.h>

#define DI __device__ __forceinline__
#define LAS __attribute__((address_space(3)))
#define GAS __attribute__((address_space(1)))
typedef unsigned short u16;
typedef unsigned long long u64;
typedef __bf16 bf8 __attribute__((ext_vector_type(8)));
typedef __bf16 bf2 __attribute__((ext_vector_type(2)));
typedef short s4 __attribute__((ext_vector_type(4)));
typedef short s8 __attribute__((ext_vector_type(8)));
typedef float f16v __attribute__((ext_vector_type(16)));
typedef float f4 __attribute__((ext_vector_type(4)));
typedef float f2 __attribute__((ext_vector_type(2)));
typedef unsigned u4 __attribute__((ext_vector_type(4)));
typedef unsigned u2 __attribute__((ext_vector_type(2)));

constexpr unsigned WS_EPOCH = 0;
constexpr unsigned WS_FA = 4096;
constexpr unsigned WS_FB = WS_FA + 65536;
constexpr unsigned WS_EST = WS_FB + 65536;
constexpr unsigned WS_Z = WS_EST + 131072;
constexpr unsigned WS_KV = WS_Z + 32768;
constexpr unsigned WS_O = 1u << 20;
constexpr unsigned WS_H = 3u << 20;
constexpr unsigned WS_END = 7u << 20;
constexpr unsigned WS_STAMP = 8u << 20;
constexpr int LDS_BYTES = 160 * 1024;
constexpr unsigned SPIN_CAP = 200000;
constexpr int SC1 = 16;

#define ROT 8
#define NC_EARLY 2
#define OUT_AUX 16
#define PAY_AUX 16
#define PULL 0
#define TOUCH 1
#define CPF 10
#define HROT 1
#define XNT 0
#define FD 0
#define TD 0
#define NODRAIN 0
#define WOX 2
#define OD 6
#define HD 12

#define ST(i) do { __builtin_amdgcn_sched_barrier(0); asm volatile("s_memrealtime %0\n\ts_waitcnt lgkmcnt(0)" : "=s"(st[i]) :: "memory"); __builtin_amdgcn_sched_barrier(0); } while (0)
#define CT(i) do { __builtin_amdgcn_sched_barrier(0); asm volatile("s_memtime %0\n\ts_waitcnt lgkmcnt(0)" : "=s"(st[i]) :: "memory"); __builtin_amdgcn_sched_barrier(0); } while (0)
#define HFLUSH() do {} while (0)
#define SB() __builtin_amdgcn_sched_barrier(0)
#define SG(m, n) __builtin_amdgcn_sched_group_barrier(m, n, 0)

DI unsigned pk2(float a, float b) { f2 v = {a, b}; return __builtin_bit_cast(unsigned, __builtin_convertvector(v, bf2)); }
DI unsigned pk2(f2 v) { return __builtin_bit_cast(unsigned, __builtin_convertvector(v, bf2)); }
DI float bflo(unsigned v) { return __uint_as_float(v << 16); }
DI float bfhi(unsigned v) { return __uint_as_float(v & 0xffff0000u); }
DI bf8 asbf8(u4 v) { return __builtin_bit_cast(bf8, v); }
DI f16v mfma32(bf8 a, bf8 b, f16v c) { return __builtin_amdgcn_mfma_f32_32x32x16_bf16(a, b, c, 0, 0, 0); }
DI f4 mfma16(bf8 a, bf8 b, f4 c) { return __builtin_amdgcn_mfma_f32_16x16x32_bf16(a, b, c, 0, 0, 0); }
DI f4 mfma16k16(u2 a, u2 b, f4 c) { return __builtin_amdgcn_mfma_f32_16x16x16bf16_1k(__builtin_bit_cast(s4, a), __builtin_bit_cast(s4, b), c, 0, 0, 0); }
DI bf8 ldsr(const char LAS* p) { return *(const LAS bf8*)p; }
DI bf8 ldstr(const char LAS* p0, const char LAS* p1) {
  s4 a = __builtin_amdgcn_ds_read_tr16_b64_v4i16((LAS s4*)p0), b = __builtin_amdgcn_ds_read_tr16_b64_v4i16((LAS s4*)p1);
  return __builtin_bit_cast(bf8, (s8)__builtin_shufflevector(a, b, 0, 1, 2, 3, 4, 5, 6, 7));
}
DI void dma4(unsigned lds, const GAS char* sb, unsigned v0, unsigned v1, unsigned v2, unsigned v3) {
  asm volatile("s_mov_b32 m0, %0\n\ts_nop 0\n\tglobal_load_lds_dwordx4 %1, %5\n\tglobal_load_lds_dwordx4 %2, %5 offset:1024\n\t"
               "global_load_lds_dwordx4 %3, %5 offset:2048\n\tglobal_load_lds_dwordx4 %4, %5 offset:3072"
               :: "s"(lds), "v"(v0), "v"(v1), "v"(v2), "v"(v3), "s"(sb) : "memory");
}
DI void dma4nt(unsigned lds, const GAS char* sb, unsigned v0, unsigned v1, unsigned v2, unsigned v3) {
  asm volatile("s_mov_b32 m0, %0\n\ts_nop 0\n\tglobal_load_lds_dwordx4 %1, %5 nt\n\tglobal_load_lds_dwordx4 %2, %5 offset:1024 nt\n\t"
               "global_load_lds_dwordx4 %3, %5 offset:2048 nt\n\tglobal_load_lds_dwordx4 %4, %5 offset:3072 nt"
               :: "s"(lds), "v"(v0), "v"(v1), "v"(v2), "v"(v3), "s"(sb) : "memory");
}
DI void dmax(unsigned lds, const GAS char* sb, unsigned v0, unsigned v1, unsigned v2, unsigned v3) { if (XNT) dma4nt(lds, sb, v0, v1, v2, v3); else dma4(lds, sb, v0, v1, v2, v3); }
DI void dma2(unsigned lds, const GAS char* sb, unsigned v0, unsigned v1) {
  asm volatile("s_mov_b32 m0, %0\n\ts_nop 0\n\tglobal_load_lds_dwordx4 %1, %3\n\tglobal_load_lds_dwordx4 %2, %3 offset:1024"
               :: "s"(lds), "v"(v0), "v"(v1), "s"(sb) : "memory");
}
DI void lbar() { asm volatile("s_waitcnt lgkmcnt(0)\n\ts_barrier" ::: "memory"); }
template <int N> DI void wait_bar() { asm volatile("s_waitcnt vmcnt(%0) lgkmcnt(0)\n\ts_barrier" :: "i"(N) : "memory"); }
DI void drain() { asm volatile("s_waitcnt vmcnt(0)" ::: "memory"); }
DI unsigned tagv(float x, unsigned t) { return (__float_as_uint(x) & 0xffffff00u) | t; }
DI f2 cmul(f2 a, f2 b) { return (f2){a.x * b.x - a.y * b.y, a.x * b.y + a.y * b.x}; }
DI f2 cfma(f2 a, f2 b, f2 c) { return (f2){fmaf(a.x, b.x, fmaf(-a.y, b.y, c.x)), fmaf(a.x, b.y, fmaf(a.y, b.x, c.y))}; }
DI f2 fma2(f2 a, f2 b, f2 c) { return __builtin_elementwise_fma(a, b, c); }
constexpr float EPS = 1e-6f, LOG2E = 1.44269504088896341f;
DI unsigned phi2(float s0, float s1) {
  const f2 a = fma2((f2){s0, s1}, (f2){LOG2E, LOG2E}, (f2){-EPS * LOG2E, -EPS * LOG2E});
  const f2 e = {__builtin_amdgcn_fmed3f(__builtin_amdgcn_exp2f(a.x), 0.f, 1.f), __builtin_amdgcn_fmed3f(__builtin_amdgcn_exp2f(a.y), 0.f, 1.f)};
  return pk2(e + (f2){__builtin_amdgcn_fmed3f(s0, EPS, __builtin_inff()), __builtin_amdgcn_fmed3f(s1, EPS, __builtin_inff())});
}
DI f2 swap_lo(f2 v, f2& hi) {
  const auto x = __builtin_amdgcn_permlane32_swap(__float_as_uint(v.x), __float_as_uint(v.x), false, false);
  const auto y = __builtin_amdgcn_permlane32_swap(__float_as_uint(v.y), __float_as_uint(v.y), false, false);
  hi = (f2){__uint_as_float(x[1]), __uint_as_float(y[1])};
  return (f2){__uint_as_float(x[0]), __uint_as_float(y[0])};
}
DI void wait_flags(__amdgpu_buffer_rsrc_t ws, unsigned off, unsigned tag) {
#pragma unroll 1
  for (unsigned sp = 0; sp < SPIN_CAP; ++sp) {
      asm volatile("" ::: "memory");
    const u2 f = __builtin_amdgcn_raw_buffer_load_b64(ws, off, 0, SC1);
    if (__all(f.x == tag && f.y == ~tag)) break;
    __builtin_amdgcn_s_sleep(1);
  }
  asm volatile("" ::: "memory");
}

__global__ void __launch_bounds__(512, 2)
fused_kernel(const u16* a0, const u16* a1, const u16* a2, const u16* a3, const u16* a4, const u16* a5, const u16* a6, const u16* a7,
             const u16* a8, const u16* a9, const u16* a10, const u16* a11, float* a12, char* a13) {
  extern __shared__ __attribute__((aligned(16))) char smem_[];
  char LAS* L = (char LAS*)smem_;
  const unsigned L0 = (unsigned)(size_t)L;
  u64 st[16];
  for (int i = 0; i < 16; ++i) st[i] = 0;
  ST(0);
  const int lane = threadIdx.x & 63;
  const int w8 = __builtin_amdgcn_readfirstlane(threadIdx.x >> 6), w = w8 & 3;
  unsigned cpf = 0;
  if (CPF && w8 == 0) {
    u64 pc;
    asm volatile("s_getpc_b64 %0" : "=s"(pc));
    cpf = __builtin_amdgcn_raw_buffer_load_b32(__builtin_amdgcn_make_buffer_rsrc((char*)(pc & ~127ull), 0, CPF * 4096, 0x00020000),
                                               lane < CPF ? ((blockIdx.x >> 3) * CPF + lane) * 128 : 0x80000000u, 0, 0);
  }
  typedef u64 u64x8 __attribute__((ext_vector_type(8)));
  typedef u64 u64x4 __attribute__((ext_vector_type(4)));
  typedef u64 u64x2 __attribute__((ext_vector_type(2)));
  u64x8 ka; u64x4 kb; u64x2 kc_;
  asm volatile("s_load_dwordx16 %0, %3, 0x0\n\ts_load_dwordx8 %1, %3, 0x40\n\ts_load_dwordx4 %2, %3, 0x60\n\ts_waitcnt lgkmcnt(0)"
               : "=&s"(ka), "=&s"(kb), "=&s"(kc_) : "s"(__builtin_amdgcn_kernarg_segment_ptr()) : "memory");
  const GAS u16 *xa = (const GAS u16*)ka[0], *wqkv = (const GAS u16*)ka[1], *wo = (const GAS u16*)ka[2], *xb = (const GAS u16*)ka[3];
  const GAS u16 *lre = (const GAS u16*)ka[4], *lim = (const GAS u16*)ka[5], *bre = (const GAS u16*)ka[6], *bim = (const GAS u16*)ka[7];
  const GAS u16 *cre = (const GAS u16*)kb[0], *cim = (const GAS u16*)kb[1], *ldt = (const GAS u16*)kb[2], *dsk = (const GAS u16*)kb[3];
  float* out = (float*)kc_[0]; char* wsp = (char*)kc_[1];
  const bool helper = w8 >= 4;
  const int bid = blockIdx.x, xcd = bid & 7, jj = bid >> 3;
  const int tile = (xcd >> 1) * 4 + (jj >> 3), cg = jj & 7;
  const int rot = ROT == 1 ? ((cg + 3 * (jj >> 3) + xcd) & 7) : ROT == 7 ? (((cg & 3) * 2 + (xcd >> 1)) & 7) : ROT == 12 ? ((2 * (cg & 3) + 2 * (xcd >> 1)) & 7) : ROT == 16 ? ((2 * (cg & 3) + ((jj >> 3) & 1) + 2 * (xcd >> 1)) & 7) : ((((cg + (jj >> 3)) % 3) * 3 + (xcd >> 1)) & 7);
  auto seq = [&](int t) { const int b = 2 * (jj >> 3), e = t - 1; return t == 0 ? cg : (b + e + (e >= ((cg - b) & 7) ? 1 : 0)) & 7; };
  const __amdgpu_buffer_rsrc_t ws = __builtin_amdgcn_make_buffer_rsrc(wsp, 0, WS_END, 0x00020000);
  const __amdgpu_buffer_rsrc_t ro = __builtin_amdgcn_make_buffer_rsrc(out, 0, 2048 * 1024 * 4, 0x00020000);
  const int r8 = lane >> 3, c8 = lane & 7;
  const int q = lane >> 4, l15 = lane & 15, l31 = lane & 31, hh = lane >> 5;
  unsigned epoch;
#define LOAD_EPOCH() asm volatile("s_load_dword %0, %1, 0x0" : "=s"(epoch) : "s"(wsp) : "memory")

  if ((xcd & 1) == 0) {
    constexpr unsigned WA = 131072, WB = 147456, SCR = 32768, TRB = 81920;
    constexpr unsigned QBUF[4] = {WB, 0, WA, WB}, OBUF[4] = {32768, 49152, 65536, 0};
    constexpr unsigned WBUF[8] = {WA, WB, 7 * 16384, 0, WA, WB, 0, WA};
    if (helper) {
      const int head = cg * 4 + w;
      unsigned vx[4], vw[2], vo[4];
#pragma unroll
      for (int i = 0; i < 4; ++i) vx[i] = (32 * w + 8 * i + r8) * 1024 + ((c8 ^ ((4 * i + (r8 >> 1)) & 7)) << 4) - 1024 * i;
#pragma unroll
      for (int i = 0; i < 2; ++i) vw[i] = (16 * w + 8 * i + r8) * 3072 + ((c8 ^ ((((r8 >> 1) & 1) | (i << 1)) << 1)) << 4) - 1024 * i;
      const GAS char* xbase = (const GAS char*)xa + (size_t)tile * 131072;
      const GAS char* wbase = (const GAS char*)wqkv + cg * 128;
      auto isx = [&](int kk, int slot) { dmax(L0 + slot * 16384 + w * 4096, xbase + ((kk + rot) & 7) * 128, vx[0], vx[1], vx[2], vx[3]); };
      auto isw = [&](unsigned dst, int kk, int seg) { dma2(L0 + dst + w * 2048, wbase + ((kk + rot) & 7) * (64 * 3072) + seg * 1024, vw[0], vw[1]); };
      auto issue = [&](int kk) { isx(kk, kk); isw(WBUF[kk], kk, 1); isw(WBUF[kk] + 8192, kk, 2); };
      auto isq = [&](int p) { isw(QBUF[p], 2 * p + 1, 0); isw(QBUF[p] + 8192, 2 * p + 2, 0); };
      ST(9); issue(0); ST(10); issue(1); ST(11);
      LOAD_EPOCH();
#pragma unroll
      for (int i = 0; i < 4; ++i) vo[i] = (8 * i + r8) * 1024 + ((c8 ^ (((r8 >> 1) & 3) << 1)) << 4) - 1024 * i;
      const GAS char* obase = (const GAS char*)wo + cg * 128;
      asm volatile("s_waitcnt vmcnt(8) lgkmcnt(0)\n\ts_barrier" : "+s"(epoch) :: "memory");
      issue(2);
      wait_bar<8>(); issue(3);
      wait_bar<8>(); issue(4);
      wait_bar<8>(); issue(5);
      wait_bar<8>(); issue(6);
      wait_bar<8>(); issue(7);
      wait_bar<8>();
      unsigned pfo;
      asm volatile("global_load_dword %0, %1, %2" : "=v"(pfo) : "v"(((jj >> 3) * 128 + w * 32 + l31) * 1024), "s"(obase) : "memory");
      isq(0);
      wait_bar<5>(); isq(1);
      wait_bar<4>(); isq(2);
      wait_bar<4>(); isq(3); isx(0, 1);
      asm volatile("s_waitcnt vmcnt(0)\n\ts_barrier" : "+v"(pfo) :: "memory");
      ST(1);
      const unsigned tag = epoch + 1, t = tag & 0xffu;
      const GAS char* kvb_ = (const GAS char*)wsp + WS_KV + head * 16384;
      const GAS char* zb_ = (const GAS char*)wsp + WS_Z + head * 1024;
      const unsigned qr = (jj >> 3) * 4096;
      unsigned vk0 = lane * 16 + qr, vk1 = lane * 16 + ((qr + 4096) & 12288), vk2 = lane * 16 + ((qr + 8192) & 12288), vk3 = lane * 16 + ((qr + 12288) & 12288), vz = l15 * 64 + q * 16;
      u4 g[16], gz;
#define KVL(i, v) asm volatile("global_load_dwordx4 %0, %1, %2 offset:%3 sc1" : "=v"(g[i]) : "v"(v), "s"(kvb_), "i"((i & 3) * 1024) : "memory")
      asm volatile("global_load_dwordx4 %0, %1, %2 sc1" : "=v"(gz) : "v"(vz), "s"(zb_) : "memory");
      KVL(0, vk0); KVL(1, vk0); KVL(2, vk0); KVL(3, vk0); KVL(4, vk1); KVL(5, vk1); KVL(6, vk1); KVL(7, vk1);
      KVL(8, vk2); KVL(9, vk2); KVL(10, vk2); KVL(11, vk2); KVL(12, vk3); KVL(13, vk3); KVL(14, vk3); KVL(15, vk3);
      auto zsum = [&]() {
        f4 z = __builtin_bit_cast(f4, gz);
#pragma unroll
        for (int r = 0; r < 4; ++r) {
          float v = z[r];
          v += __builtin_bit_cast(float, __builtin_amdgcn_mov_dpp(__builtin_bit_cast(int, v), 0x128, 0xf, 0xf, true));
          v += __builtin_bit_cast(float, __builtin_amdgcn_mov_dpp(__builtin_bit_cast(int, v), 0x124, 0xf, 0xf, true));
          v += __builtin_bit_cast(float, __builtin_amdgcn_mov_dpp(__builtin_bit_cast(int, v), 0x122, 0xf, 0xf, true));
          v += __builtin_bit_cast(float, __builtin_amdgcn_mov_dpp(__builtin_bit_cast(int, v), 0x121, 0xf, 0xf, true));
          z[r] = v;
        }
        return z;
      };
      asm volatile("s_waitcnt vmcnt(16)" : "+v"(gz) :: "memory");
      gz.x ^= t; gz.z ^= t;
      unsigned bad = gz.x | gz.z;
      f4 zz = zsum(), kv = {0, 0, 0, 0};
      asm volatile("" : "+v"(zz));
#define KVA(i) asm volatile("s_waitcnt vmcnt(%1)" : "+v"(g[i]) : "i"(15 - i) : "memory"); g[i].x ^= t; g[i].z ^= t; bad |= g[i].x | g[i].z; kv += __builtin_bit_cast(f4, g[i]); asm volatile("" : "+v"(kv), "+v"(bad))
      KVA(0); KVA(1); KVA(2); KVA(3); KVA(4); KVA(5); KVA(6); KVA(7); KVA(8); KVA(9); KVA(10); KVA(11); KVA(12); KVA(13); KVA(14); KVA(15);
      ST(3);
      if (!__all((bad & 0xffu) == 0)) {
#pragma unroll 1
        for (unsigned rep = 0; rep < SPIN_CAP; ++rep) {
          unsigned nw = __all(((gz.x | gz.z) & 0xffu) == 0) ? 0u : 0x10000u;
#pragma unroll
          for (int i = 0; i < 16; ++i) nw |= __all(((g[i].x | g[i].z) & 0xffu) == 0) ? 0u : 1u << i;
          if (nw == 0) break;
          if (nw & 0x10000u) asm volatile("global_load_dwordx4 %0, %1, %2 sc1" : "+v"(gz) : "v"(vz), "s"(zb_) : "memory");
#define KVLD(i, v) if (nw & (1u << i)) asm volatile("global_load_dwordx4 %0, %1, %2 offset:%3 sc1" : "+v"(g[i]) : "v"(v), "s"(kvb_), "i"((i & 3) * 1024) : "memory")
          KVLD(0, vk0); KVLD(1, vk0); KVLD(2, vk0); KVLD(3, vk0); KVLD(4, vk1); KVLD(5, vk1); KVLD(6, vk1); KVLD(7, vk1);
          KVLD(8, vk2); KVLD(9, vk2); KVLD(10, vk2); KVLD(11, vk2); KVLD(12, vk3); KVLD(13, vk3); KVLD(14, vk3); KVLD(15, vk3);
          asm volatile("s_waitcnt vmcnt(0)" : "+v"(g[0]), "+v"(g[1]), "+v"(g[2]), "+v"(g[3]), "+v"(g[4]), "+v"(g[5]), "+v"(g[6]), "+v"(g[7]),
                       "+v"(g[8]), "+v"(g[9]), "+v"(g[10]), "+v"(g[11]), "+v"(g[12]), "+v"(g[13]), "+v"(g[14]), "+v"(g[15]), "+v"(gz) :: "memory");
          if (nw & 0x10000u) { gz.x ^= t; gz.z ^= t; }
#pragma unroll
          for (int i = 0; i < 16; ++i) if (nw & (1u << i)) { g[i].x ^= t; g[i].z ^= t; }
        }
        zz = zsum();
        kv = (f4){0, 0, 0, 0};
#pragma unroll
        for (int i = 0; i < 16; ++i) kv += __builtin_bit_cast(f4, g[i]);
      }
      ST(5);
      *(LAS u4*)(L + SCR + w * 1024 + lane * 16) = (u4){pk2(kv[0], kv[1]), pk2(kv[2], kv[3]), pk2(zz[0], zz[1]), pk2(zz[2], zz[3])};
      ST(6);
      lbar();
      ST(7);
      lbar();
#pragma unroll
      for (int i = 0; i < 4; ++i)
        dma4(L0 + OBUF[i] + w * 4096, obase + (2 * seq(2 * i + (w >> 1)) + (w & 1)) * 32768, vo[0], vo[1], vo[2], vo[3]);
      wait_bar<0>();
      ST(8);
      HFLUSH();
      if (bid == 0 && w == 0) {
#pragma unroll 1
        for (unsigned sp = 0; sp < SPIN_CAP; ++sp) {
          asm volatile("" ::: "memory");
          const unsigned fo = (lane >> 3) * 4096 + (lane & 7) * 256;
          const u2 f0 = __builtin_amdgcn_raw_buffer_load_b64(ws, WS_FA + fo, 0, SC1), f1 = __builtin_amdgcn_raw_buffer_load_b64(ws, WS_FA + 32768 + fo, 0, SC1);
          const u2 f2_ = __builtin_amdgcn_raw_buffer_load_b64(ws, WS_FB + fo, 0, SC1), f3 = __builtin_amdgcn_raw_buffer_load_b64(ws, WS_FB + 32768 + fo, 0, SC1);
          if (__all(f0.x == tag && f0.y == ~tag && f1.x == tag && f1.y == ~tag && f2_.x == tag && f2_.y == ~tag && f3.x == tag && f3.y == ~tag)) break;
          __builtin_amdgcn_s_sleep(8);
        }
        if (lane == 0) __builtin_amdgcn_raw_buffer_store_b32(tag, ws, WS_EPOCH, 0, SC1);
      }
      return;
    }
    LOAD_EPOCH();
    const int hp = w & 1, th = w >> 1;
    unsigned warm0 = 0, warm1 = 0;
    if (TOUCH) {
      if (TD) __builtin_amdgcn_s_sleep(TD);
      warm0 = __builtin_amdgcn_raw_buffer_load_b32(ws, lane < 16 ? WS_FA + ((tile * 2 + th) * 16 + lane) * 128 : lane < 24 ? WS_Z + (cg * 4 + w) * 1024 + (lane - 16) * 128
                                                   : lane == 24 ? WS_KV + (cg * 4 + w) * 16384 : lane == 25 ? WS_O + tile * 131072 : 0x80000000u, 0, SC1);
      warm1 = __builtin_amdgcn_raw_buffer_load_b32(ro, lane < 2 ? (tile * 128 + w * 32 + lane * 16) * 4096 : 0x80000000u, 0, SC1);
    }
    const int qq = (lane >> 2) & 3, pp = lane & 3;
    const unsigned ax0 = l15 * 128 + ((q ^ (l15 >> 1)) << 4), ax1 = l15 * 128 + (((4 + q) ^ (l15 >> 1)) << 4);
    auto waddr = [&](int h) { return (8 * q + qq) * 128 + (((2 * h + (pp >> 1)) ^ ((((qq >> 1) & 1) | ((q & 1) << 1)) << 1)) << 4) + 8 * (pp & 1); };
    const unsigned aw = waddr(w), aq0 = waddr(2 * hp), aq1 = waddr(2 * hp + 1);
    f4 ka[8], va[8];
#pragma unroll
    for (int i = 0; i < 8; ++i) { ka[i] = (f4){EPS, EPS, EPS, EPS}; va[i] = (f4){0, 0, 0, 0}; }
    bf8 X0[4], X1[4], W0[2], W1[2];
    auto rdx = [&](bf8 (&X)[4], int kk, int ks, unsigned off) {
#pragma unroll
      for (int i = 0; i < 4; ++i) X[i] = ldsr(L + kk * 16384 + (ks ? ax1 : ax0) + off + i * 2048);
    };
    auto rdw = [&](bf8 (&W)[2], int kk, int ks) {
#pragma unroll
      for (int i = 0; i < 2; ++i) { const char LAS* p = L + WBUF[kk] + aw + ks * 4096 + i * 8192; W[i] = ldstr(p, p + 512); }
    };
    auto mm = [&](const bf8 (&X)[4], const bf8 (&W)[2], int h) {
#pragma unroll
      for (int i = 0; i < 4; ++i) {
        ka[4 * h + i] = mfma16(X[i], W[0], ka[4 * h + i]);
        va[4 * h + i] = mfma16(X[i], W[1], va[4 * h + i]);
      }
    };
#define ILV(nr, nm) do { _Pragma("unroll") for (int i_ = 0; i_ < nr; ++i_) { SG(0x008, 1); SG(0x100, 1); } if (nm > nr) SG(0x008, nm - nr); } while (0)
    asm volatile("s_waitcnt lgkmcnt(0)\n\ts_barrier" : "+s"(epoch) : "v"(warm0), "v"(warm1), "v"(cpf) : "memory");
    ST(1);
    const unsigned tag = epoch + 1, t = tag & 0xffu;
    rdw(W0, 0, 0); rdx(X0, 0, 0, 0);
    SB();
#pragma unroll
    for (int kk = 0; kk < 8; ++kk) {
      rdx(X1, kk, 0, 8192); mm(X0, W0, 0); ILV(4, 8); SB();
      rdw(W1, kk, 1); rdx(X0, kk, 1, 0); mm(X1, W0, 1); ILV(8, 8); SB();
      rdx(X1, kk, 1, 8192); mm(X0, W1, 0); ILV(4, 8); SB();
      if (kk < 7) { lbar(); rdw(W0, kk + 1, 0); rdx(X0, kk + 1, 0, 0); mm(X1, W1, 1); ILV(8, 8); }
      else mm(X1, W1, 1);
      SB();
    }
    ST(2);
    const unsigned xq = th * 8192;
    auto rdq = [&](bf8 (&X)[4], bf8 (&W)[2], int s) {
      const char LAS* p = L + QBUF[s >> 2] + ((s >> 1) & 1) * 8192 + (s & 1) * 4096;
      W[0] = ldstr(p + aq0, p + aq0 + 512); W[1] = ldstr(p + aq1, p + aq1 + 512);
      rdx(X, s < 14 ? (s >> 1) + 1 : 1, s & 1, xq);
    };
    {
      const int head = cg * 4 + w;
      f4 kv = {0, 0, 0, 0}, zz = {0, 0, 0, 0};
      const u4 ones = {0x3f803f80u, 0x3f803f80u, 0x3f803f80u, 0x3f803f80u};
#pragma unroll
      for (int m = 0; m < 4; ++m) {
        const f4 a = ka[2 * m], b = ka[2 * m + 1], c = va[2 * m], d = va[2 * m + 1];
        const u4 pa = {phi2(a[0], a[1]), phi2(a[2], a[3]), phi2(b[0], b[1]), phi2(b[2], b[3])};
        const u4 pb = {pk2(c[0], c[1]), pk2(c[2], c[3]), pk2(d[0], d[1]), pk2(d[2], d[3])};
        kv = mfma16(asbf8(pa), asbf8(pb), kv);
        zz = mfma16(asbf8(pa), asbf8(ones), zz);
      }
      const u4 skv = {tagv(kv[0], t), __float_as_uint(kv[1]), tagv(kv[2], t), __float_as_uint(kv[3])};
      __builtin_amdgcn_raw_buffer_store_b128(skv, ws, WS_KV + (head * 16 + tile) * 1024 + lane * 16, 0, SC1);
      const u4 sz = {tagv(zz[0], t), __float_as_uint(zz[1]), tagv(zz[2], t), __float_as_uint(zz[3])};
      __builtin_amdgcn_raw_buffer_store_b128(sz, ws, l15 == 0 ? WS_Z + (head * 16 + tile) * 64 + q * 16 : 0x80000000u, 0, SC1);
    }
    SB();
    ST(3);
    lbar();
    rdq(X0, W0, 0);
    SB();
    f4 qa[2][4];
#pragma unroll
    for (int i = 0; i < 8; ++i) qa[i >> 2][i & 3] = (f4){EPS, EPS, EPS, EPS};
    auto mq = [&](const bf8 (&X)[4], const bf8 (&W)[2]) {
#pragma unroll
      for (int i = 0; i < 4; ++i) { qa[0][i] = mfma16(W[0], X[i], qa[0][i]); qa[1][i] = mfma16(W[1], X[i], qa[1][i]); }
    };
#pragma unroll
    for (int s = 0; s < 16; s += 2) {
      rdq(X1, W1, s + 1); mq(X0, W0); ILV(8, 8); SB();
      if (s < 14) { if (s == 2 || s == 6) lbar(); rdq(X0, W0, s + 2); mq(X1, W1); ILV(8, 8); }
      else mq(X1, W1);
      SB();
    }
    ST(4);
    u2 pq[2][4];
#pragma unroll
    for (int i = 0; i < 8; ++i) pq[i >> 2][i & 3] = (u2){phi2(qa[i >> 2][i & 3][0], qa[i >> 2][i & 3][1]), phi2(qa[i >> 2][i & 3][2], qa[i >> 2][i & 3][3])};
    unsigned ao[4], og[4];
#pragma unroll
    for (int i = 0; i < 4; ++i) og[i] = WS_O + ((tile * 8 + th * 4 + i) * 16 + cg * 2 + hp) * 1024 + lane * 16;
#pragma unroll
    for (int nt = 0; nt < 4; ++nt) ao[nt] = (4 * q + qq) * 128 + ((((2 * nt) ^ (((2 * q + (qq >> 1)) & 3) << 1)) + (pp >> 1)) << 4) + 8 * (pp & 1);
    asm volatile("s_barrier" : "+v"(pq[0][0]), "+v"(pq[0][1]), "+v"(pq[0][2]), "+v"(pq[0][3]), "+v"(pq[1][0]), "+v"(pq[1][1]), "+v"(pq[1][2]), "+v"(pq[1][3]),
                 "+v"(og[0]), "+v"(og[1]), "+v"(og[2]), "+v"(og[3]) :: "memory");
    ST(5);
    const unsigned ex = TRB + th * 16384 + hp * 1024 + lane * 16;
    const f4 zero4 = {0, 0, 0, 0}, eps4 = {EPS, EPS, EPS, EPS};
    f4 num[2][4], den[2][4];
#pragma unroll
    for (int h = 0; h < 2; ++h) {
      const u4 kz = *(const LAS u4*)(L + SCR + (2 * hp + h) * 1024 + lane * 16);
#pragma unroll
      for (int i = 0; i < 4; ++i) { den[h][i] = mfma16k16((u2){kz.z, kz.w}, pq[h][i], eps4); num[h][i] = mfma16k16((u2){kz.x, kz.y}, pq[h][i], zero4); }
    }
    SB();
#pragma unroll
    for (int i = 0; i < 4; ++i) {
      u4 so;
#pragma unroll
      for (int h = 0; h < 2; ++h) {
        const float rc = __builtin_amdgcn_rcpf(den[h][i][0]);
        const f4 n = num[h][i];
        so[2 * h] = pk2((f2){n[0], n[1]} * rc);
        so[2 * h + 1] = pk2((f2){n[2], n[3]} * rc);
      }
      __builtin_amdgcn_raw_buffer_store_b128(so, ws, og[i], 0, SC1);
      *(LAS u4*)(L + ex + (i >> 1) * 8192 + (i & 1) * 2048) = so;
    }
    lbar();
    ST(6);
    u4 of[32];
#pragma unroll
    for (int i = 0; i < 4; ++i) of[i] = *(const LAS u4*)(L + TRB + w * 8192 + ((i & 1) * 2 + (i >> 1)) * 1024 + lane * 16);
    if (!NODRAIN) drain();
    __builtin_amdgcn_raw_buffer_store_b64((u2){tag, ~tag}, ws, lane == 0 ? WS_FA + ((tile * 2 + th) * 16 + cg * 2 + hp) * 128 : 0x80000000u, 0, SC1);
    unsigned tw[4], trd[4], so_ = ((tile * 128 + w * 32 + q) * 1024 + cg * 64 + l15 * 4) * 4;
#pragma unroll
    for (int i = 0; i < 4; ++i) {
      tw[i] = TRB + w * 8192 + l15 * 256 + (((4 * i + q) ^ l15) << 4);
      trd[i] = TRB + w * 8192 + (4 * i + q) * 256 + ((l15 ^ (4 * i + q)) << 4);
      asm volatile("" : "+v"(tw[i]), "+v"(trd[i]));
    }
    asm volatile("s_barrier" : "+v"(so_) :: "memory");
    ST(7);
    f4 acc[2][4];
#pragma unroll
    for (int i = 0; i < 8; ++i) acc[i >> 2][i & 3] = zero4;
    const unsigned ob = WS_O + (tile * 8 + w * 2) * 16384 + lane * 16;
    bf8 wf[2][4];
    auto rdo = [&](int k) {
#pragma unroll
      for (int nt = 0; nt < 4; ++nt) { const char LAS* p = L + OBUF[k >> 2] + (k & 3) * 4096 + ao[nt]; wf[k & 1][nt] = ldstr(p, p + 2048); }
    };
    auto ldo = [&](int k) {
      const unsigned so = (2 * seq(k >> 1) + (k & 1)) * 1024;
      of[2 * k] = __builtin_amdgcn_raw_buffer_load_b128(ws, ob, so, PAY_AUX);
      of[2 * k + 1] = __builtin_amdgcn_raw_buffer_load_b128(ws, ob + 16384, so, PAY_AUX);
    };
    auto mo = [&](int k) {
#pragma unroll
      for (int nt = 0; nt < 4; ++nt) {
        acc[0][nt] = mfma16(wf[k & 1][nt], asbf8(of[2 * k]), acc[0][nt]);
        acc[1][nt] = mfma16(wf[k & 1][nt], asbf8(of[2 * k + 1]), acc[1][nt]);
      }
    };
    rdo(0); rdo(1); SB();
    mo(0); rdo(2); SB();
    mo(1); SB();
    if (FD) __builtin_amdgcn_s_sleep(FD);
    wait_flags(ws, WS_FA + ((tile * 2 + th) * 16 + l15) * 128, tag);
    ST(8); CT(14);
#pragma unroll
    for (int k = 2; k < 2 + OD; ++k) ldo(k);
    SB();
#pragma unroll
    for (int k = 2; k < 16; ++k) {
      if (k + OD < 16) ldo(k + OD);
      if (k + 1 < 16) rdo(k + 1);
      mo(k);
#pragma unroll
      for (int i_ = 0; i_ < 8; ++i_) { SG(0x008, 1); SG(0x100, 1); if (i_ == 1 || i_ == 5) SG(0x020, 1); }
      SB();
    }
    ST(9); CT(15);
#pragma unroll
    for (int mt = 0; mt < 2; ++mt)
#pragma unroll
      for (int nt = 0; nt < 4; ++nt) {
        const f4 v = acc[mt][nt];
        const unsigned p0 = pk2(v[0], v[1]), p1 = pk2(v[2], v[3]);
        *(LAS f4*)(L + tw[nt] + mt * 4096) = (f4){bflo(p0), bfhi(p0), bflo(p1), bfhi(p1)};
      }
    u4 ov[8];
#pragma unroll
    for (int i = 0; i < 8; ++i) ov[i] = *(const LAS u4*)(L + trd[i & 3] + (i >> 2) * 4096);
    SB();
#pragma unroll
    for (int i = 0; i < 8; ++i) __builtin_amdgcn_raw_buffer_store_b128(ov[i], ro, so_, i * 16384, OUT_AUX);
    ST(10);
  } else {
    constexpr unsigned SLOT = 32768, SCR = SLOT, PAR = 4 * SLOT;
    const int wp = w & 1, wt = w >> 1;
    const unsigned ecol = WS_EST + (cg * 64 + wp * 32 + l31) * 8;
    if (helper) {
      unsigned vx[4], vw[4], vc[4];
#pragma unroll
      for (int i = 0; i < 4; ++i) {
        const unsigned sw = (c8 ^ ((4 * i + (r8 >> 1)) & 7)) << 4;
        vx[i] = (64 * (w >> 1) + 32 * (r8 >> 2) + 16 * (r8 & 1) + 8 * (w & 1) + ((r8 >> 1) & 1) + 2 * i) * 1024 + sw - 1024 * i;
        vw[i] = (16 * (i >> 1) + 8 * (r8 >> 2) + 4 * (i & 1) + (r8 & 3)) * 1024 + sw - 1024 * i;
        vc[i] = (8 * i + r8) * 1024 + sw - 1024 * i;
      }
      const GAS char* xbase = (const GAS char*)xb + (size_t)tile * 131072;
      const GAS char* wbase = (const GAS char*)((w & 1) ? bim : bre) + (cg * 64 + (w >> 1) * 32) * 1024;
      auto issue = [&](int kk) {
        const int kc = (kk + rot) & 7;
        const unsigned s = L0 + (kk % 5) * SLOT + w * 4096;
        dmax(s, xbase + kc * 128, vx[0], vx[1], vx[2], vx[3]);
        dma4(s + 16384, wbase + kc * 128, vw[0], vw[1], vw[2], vw[3]);
      };
      ST(9); issue(0); ST(10); issue(1); ST(11);
      LOAD_EPOCH();
      const GAS char* cbase = (const GAS char*)((w & 1) ? cim : cre) + cg * 65536;
      auto issue_c = [&](int piece) {
        const unsigned s = L0 + ((piece + 3) % 5) * SLOT + w * 8192;
        const GAS char* cb = cbase + seq(2 * piece + (w >> 1)) * 128;
        dma4(s, cb, vc[0], vc[1], vc[2], vc[3]);
        dma4(s + 4096, cb + 32768, vc[0], vc[1], vc[2], vc[3]);
      };
      asm volatile("s_waitcnt vmcnt(8) lgkmcnt(0)\n\ts_barrier" : "+s"(epoch) :: "memory");
      issue(2);
      const f4 par = *(const LAS f4*)(L + PAR + w * 1024 + lane * 16);
      const f2 l64 = {par[0], par[1]}, l128 = {par[2], par[3]};
      f2 l256 = cmul(l128, l128);
      f2 wgt = hh ? l64 : (f2){1.f, 0.f};
      if (wt) wgt = cmul(wgt, l128);
      const unsigned tag = epoch + 1, t = tag & 0xffu;
      unsigned eo[8], et[8];
#pragma unroll
      for (int m = 0; m < 8; ++m) {
        const int i = 2 * tile - 1 - (2 * wt + hh) - 4 * m;
        eo[m] = i >= 0 ? ecol + i * 4096 : 0x80000000u;
        et[m] = i >= 0 ? t : 0u;
        asm volatile("" : "+v"(eo[m]), "+v"(et[m]));
      }
      asm volatile("" : "+v"(l256), "+v"(wgt));
      wait_bar<8>(); issue(3);
      wait_bar<8>(); issue(4);
      wait_bar<8>(); issue(5);
      wait_bar<8>(); issue(6);
      wait_bar<8>(); issue(7);
      wait_bar<8>(); issue_c(0);
      wait_bar<8>(); ST(1);
      if (NC_EARLY > 1) issue_c(1);
      if (NC_EARLY > 2) issue_c(2);
      ST(2);
      if (PD) { asm volatile("s_barrier" ::: "memory"); __builtin_amdgcn_s_sleep(PD); }
      f2 cin;
#pragma unroll 1
      for (unsigned sp = 0; sp < SPIN_CAP; ++sp) {
      asm volatile("" ::: "memory");
        u2 g[8];
#pragma unroll
        for (int m = 0; m < 8; ++m) g[m] = __builtin_amdgcn_raw_buffer_load_b64(ws, eo[m], 0, SC1);
        unsigned bad = 0;
        cin = (f2){0, 0};
#pragma unroll
        for (int m = 7; m >= 0; --m) {
          const unsigned cx = g[m].x ^ et[m];
          bad |= cx;
          cin = cfma(l256, cin, (f2){__uint_as_float(cx), __uint_as_float(g[m].y)});
        }
        if (__all((bad & 0xffu) == 0)) break;
      }
      ST(5);
      cin = cmul(cin, wgt);
      f2 chi;
      const f2 clo = swap_lo(cin, chi);
      if (hh == 0) *(LAS f2*)(L + SCR + (wp * 2 + wt) * 256 + l31 * 8) = clo + chi;
      ST(6);
      lbar();
      if (WOX & 2) issue_c(2);
      lbar();
      ST(7);
      if (NC_EARLY <= 1) issue_c(1);
      if (NC_EARLY <= 2 && !(WOX & 2)) issue_c(2);
      issue_c(3);
      wait_bar<0>();
      ST(8); HFLUSH();
      return;
    }
    LOAD_EPOCH();
    const int pl = cg * 64 + wp * 32 + ((l31 & 0x13) | ((l31 & 4) << 1) | ((l31 & 8) >> 1));
    const unsigned plr = lre[pl], pli = lim[pl], pdt = ldt[pl];
    u4 dv[4];
#pragma unroll
    for (int i = 0; i < 4; ++i) dv[i] = *(const GAS u4*)(dsk + cg * 64 + 16 * i + 8 * hh);
    unsigned warm0 = 0, warm1 = 0;
    if (TOUCH) {
      warm0 = __builtin_amdgcn_raw_buffer_load_b32(ws, lane < 16 ? WS_FB + ((tile * 2 + (w >> 1)) * 16 + lane) * 128 : lane == 16 ? WS_EST + tile * 8192
                                                   : lane == 17 ? WS_H + (tile * 4 + w) * 65536 : 0x80000000u, 0, SC1);
      warm1 = __builtin_amdgcn_raw_buffer_load_b32(ro, lane < 2 ? (tile * 128 + w * 32 + lane * 16) * 4096 + 2048 : 0x80000000u, 0, SC1);
    }
    SB();
    f2 lam, kap, l16, l32, l64, l128;
    {
      const float lr = bflo(plr), li = bflo(pli);
      const float dt = bflo(pk2(expf(bflo(pdt)), 0.f));
      const float mag = expf(dt * lr);
      float sn, cs;
      sincosf(dt * li, &sn, &cs);
      lam = (f2){mag * cs, mag * sn};
      const float inv = 2.f / (lr * lr + li * li), nr = lam.x - 1.f, ni = lam.y;
      kap = (f2){(nr * lr + ni * li) * inv, (ni * lr - nr * li) * inv};
      f2 t = cmul(lam, lam); t = cmul(t, t); t = cmul(t, t);
      l16 = cmul(t, t); l32 = cmul(l16, l16); l64 = cmul(l32, l32); l128 = cmul(l64, l64);
      *(LAS f4*)(L + PAR + w * 1024 + lane * 16) = (f4){l64.x, l64.y, l128.x, l128.y};
    }
    unsigned ar[4];
#pragma unroll
    for (int s = 0; s < 4; ++s) ar[s] = l31 * 128 + (((2 * s + hh) ^ ((lane >> 1) & 7)) << 4);
    f16v ur[2], ui[2];
#pragma unroll
    for (int i = 0; i < 16; ++i) { ur[0][i] = 0; ur[1][i] = 0; ui[0][i] = 0; ui[1][i] = 0; }
    bf8 F0[4], F1[4];
    auto rd = [&](bf8 (&F)[4], int kk, int s) {
      const char LAS* sx = L + (kk % 5) * SLOT + wt * 8192 + ar[s];
      const char LAS* sw = L + (kk % 5) * SLOT + 16384 + wp * 8192 + ar[s];
      F[0] = ldsr(sx); F[1] = ldsr(sw); F[2] = ldsr(sw + 4096); F[3] = ldsr(sx + 4096);
    };
    auto mm = [&](const bf8 (&F)[4]) {
      ur[0] = mfma32(F[0], F[1], ur[0]); ui[0] = mfma32(F[0], F[2], ui[0]);
      ur[1] = mfma32(F[3], F[1], ur[1]); ui[1] = mfma32(F[3], F[2], ui[1]);
    };
#define ILB() do { SG(0x008, 1); SG(0x100, 2); SG(0x008, 1); SG(0x100, 2); SG(0x008, 2); } while (0)
    asm volatile("s_waitcnt lgkmcnt(0)\n\ts_barrier" : "+s"(epoch), "+v"(lam), "+v"(kap), "+v"(l16), "+v"(l32), "+v"(l64), "+v"(dv[0]), "+v"(dv[1]), "+v"(dv[2]), "+v"(dv[3]) : "v"(warm0), "v"(warm1), "v"(cpf) : "memory");
    ST(1);
    const unsigned tag = epoch + 1, t = tag & 0xffu;
    rd(F0, 0, 0);
    SB();
#pragma unroll
    for (int kk = 0; kk < 8; ++kk) {
      rd(F1, kk, 1); mm(F0); ILB(); SB();
      rd(F0, kk, 2); mm(F1); ILB(); SB();
      rd(F1, kk, 3); mm(F0); ILB(); SB();
      if (kk < 7) { lbar(); if (kk == 6) ST(2); rd(F0, kk + 1, 0); mm(F1); ILB(); }
      else mm(F1);
      SB();
    }
    ST(3);
    f2 gr[16], gi[16];
#pragma unroll
    for (int a = 0; a < 16; ++a) {
      gr[a] = (f2){ur[a >> 3][2 * (a & 7)], ur[a >> 3][2 * (a & 7) + 1]};
      gi[a] = (f2){ui[a >> 3][2 * (a & 7)], ui[a >> 3][2 * (a & 7) + 1]};
    }
    {
      f2 LR = {lam.x, lam.x}, LI = {lam.y, lam.y}, LN = {-lam.y, -lam.y};
      asm volatile("s_nop 11" : "+v"(gr[0]), "+v"(gi[0]), "+v"(gr[8]), "+v"(gi[8]), "+v"(LR), "+v"(LI), "+v"(LN));
#pragma unroll
      for (int a = 1; a < 16; ++a)
        asm volatile("v_pk_fma_f32 %0, %6, %3, %0\n\tv_pk_fma_f32 %1, %5, %2, %1\n\tv_pk_fma_f32 %0, %4, %2, %0\n\tv_pk_fma_f32 %1, %4, %3, %1"
                     : "+v"(gr[a]), "+v"(gi[a]) : "v"(gr[a - 1]), "v"(gi[a - 1]), "v"(LR), "v"(LI), "v"(LN));
    }
    const f2 er = gr[15], ei = gi[15];
    const f2 e0 = {er.x, ei.x};
    const f2 e32 = cfma(l16, e0, (f2){er.y, ei.y});
    f2 ehi;
    const f2 elo = swap_lo(e32, ehi);
    const f2 e64 = cfma(l32, elo, ehi);
    __builtin_amdgcn_raw_buffer_store_b64((u2){tagv(e64.x, t), __float_as_uint(e64.y)}, ws, hh == 0 ? ecol + (tile * 2 + wt) * 4096 : 0x80000000u, 0, SC1);
    if (wt == 0 && hh == 0) *(LAS f2*)(L + SCR + 1024 + wp * 256 + l31 * 8) = e64;
    if (PD) asm volatile("s_barrier" ::: "memory");
    SB();
    ST(4);
    auto tokr = [&](int r) { return tile * 128 + 64 * (w >> 1) + 32 * ((r >> 2) & 1) + 16 * (r & 1) + 8 * (w & 1) + ((r >> 1) & 1) + 2 * (r >> 3); };
    u4 xd[4];
#pragma unroll
    for (int i = 0; i < 4; ++i) xd[i] = *(const GAS u4*)(xb + tokr(l31) * 512 + cg * 64 + 16 * i + 8 * hh);
    SB();
    {
      const f2 KR = {kap.x, kap.x}, KI = {kap.y, kap.y};
#pragma unroll
      for (int a = 0; a < 16; ++a) {
        const f2 xr = gr[a], xi = gi[a];
        gr[a] = fma2(KR, xr, -KI * xi); gi[a] = fma2(-KR, xi, -KI * xr);
        asm volatile("" : "+v"(gr[a]), "+v"(gi[a]));
      }
    }
    f2 pw[16];
    pw[0] = (f2){lam.x, -lam.y};
#pragma unroll
    for (int k = 1; k < 16; ++k) { pw[k] = cmul(pw[k >> 1], pw[(k - 1) >> 1]); asm volatile("" : "+v"(pw[k])); }
    u4 idn[2];
#pragma unroll
    for (int s = 0; s < 2; ++s)
#pragma unroll
      for (int jq = 0; jq < 4; ++jq) {
        const int r0 = 16 * s + 8 * (jq >> 1) + 4 * hh + 2 * (jq & 1);
        idn[s][jq] = (l31 == r0 ? 0x3f80u : 0u) | (l31 == r0 + 1 ? 0x3f800000u : 0u);
      }
    asm volatile("" : "+v"(idn[0]), "+v"(idn[1]));
#pragma unroll
    for (int i = 0; i < 4; ++i) {
#pragma unroll
      for (int j = 0; j < 4; ++j) xd[i][j] = pk2(bflo(xd[i][j]) * bflo(dv[i][j]), bfhi(xd[i][j]) * bfhi(dv[i][j]));
      asm volatile("" : "+v"(xd[i]));
    }
    f2 m0 = hh ? cmul(kap, l32) : kap, b0 = hh ? cmul(kap, elo) : (f2){0.f, 0.f};
    f2 m1 = cmul(l16, m0), b1 = cfma(l16, b0, cmul(kap, e0));
    unsigned ho[2];
    ho[0] = WS_H + (tile * 4 + wt * 2) * 65536 + (cg * 2 + wp) * 4096 + lane * 16; ho[1] = ho[0] + 65536;
    asm volatile("s_waitcnt lgkmcnt(0)\n\ts_barrier" : "+v"(ho[0]), "+v"(ho[1]), "+v"(m0), "+v"(b0), "+v"(m1), "+v"(b1) :: "memory");
    ST(5);
    f2 cin = *(const LAS f2*)(L + SCR + wp * 512 + l31 * 8) + *(const LAS f2*)(L + SCR + wp * 512 + 256 + l31 * 8);
    const f2 esib = *(const LAS f2*)(L + SCR + 1024 + wp * 256 + l31 * 8);
    if (wt) cin = cfma(l64, cin, esib);
    const f2 d0 = cfma(m0, cin, b0), d1 = cfma(m1, cin, b1);
    const f2 DR = {d0.x, d1.x}, DJ = {-d0.y, -d1.y};
    f16v zero16;
#pragma unroll
    for (int i = 0; i < 16; ++i) zero16[i] = 0;
    const unsigned ex = 2 * SLOT + wt * 16384 + wp * 4096 + lane * 16;
    auto fix = [&](int m, int c) {
      u4 a[2];
#pragma unroll
      for (int i = 0; i < 8; ++i) {
        const f2 P = pw[8 * m + i], PR = {P.x, P.x}, PI = {P.y, P.y};
        const f2 h = c ? fma2(PI, DR, fma2(PR, DJ, gi[8 * m + i])) : fma2(-PI, DJ, fma2(PR, DR, gr[8 * m + i]));
        a[i >> 2][i & 3] = pk2(h);
      }
      return mfma32(asbf8(a[1]), asbf8(idn[1]), mfma32(asbf8(a[0]), asbf8(idn[0]), zero16));
    };
    u4 ow[8];
    auto put = [&](const f16v& T, int m, int c) {
      const u4 o0 = ow[4 * m + 2 * c] = (u4){pk2(T[0], T[1]), pk2(T[2], T[3]), pk2(T[4], T[5]), pk2(T[6], T[7])};
      const u4 o1 = ow[4 * m + 2 * c + 1] = (u4){pk2(T[8], T[9]), pk2(T[10], T[11]), pk2(T[12], T[13]), pk2(T[14], T[15])};
      __builtin_amdgcn_raw_buffer_store_b128(o0, ws, ho[m] + c * 2048, 0, SC1);
      __builtin_amdgcn_raw_buffer_store_b128(o1, ws, ho[m] + c * 2048 + 1024, 0, SC1);
    };
    {
      const f16v T0 = fix(0, 0); SB();
      const f16v T1 = fix(0, 1); SB();
      put(T0, 0, 0);
      const f16v T2 = fix(1, 0); SB();
      put(T1, 0, 1);
      const f16v T3 = fix(1, 1); SB();
      put(T2, 1, 0); SB();
      put(T3, 1, 1);
    }
    SB();
#pragma unroll
    for (int i = 0; i < 8; ++i) *(LAS u4*)(L + ex + (i >> 2) * 8192 + (i & 3) * 1024) = ow[i];
    lbar();
    ST(6);
    if (!NODRAIN) drain();
    __builtin_amdgcn_raw_buffer_store_b64((u2){tag, ~tag}, ws, lane == 0 ? WS_FB + ((tile * 2 + wt) * 16 + cg * 2 + wp) * 128 : 0x80000000u, 0, SC1);
    ST(7);
    const unsigned hb = WS_H + (tile * 4 + w) * 65536 + lane * 16;
    u4 hf[64];
    bf8 cf[2][4];
    f16v acc[2];
    auto ldh = [&](int i) { hf[i] = __builtin_amdgcn_raw_buffer_load_b128(ws, hb + (i & 7) * 1024, seq(i >> 3) * 8192, PAY_AUX); };
    auto rdc = [&](int g) {
      const int tt = g >> 2, i = (g & 3) * 2;
      const char LAS* cb = L + (((tt >> 1) + 3) % 5) * SLOT + (tt & 1) * 16384 + ((i >> 1) & 1) * 8192;
      cf[g & 1][0] = ldsr(cb + ar[(i >> 2) * 2]); cf[g & 1][1] = ldsr(cb + ar[(i >> 2) * 2] + 4096);
      cf[g & 1][2] = ldsr(cb + ar[(i >> 2) * 2 + 1]); cf[g & 1][3] = ldsr(cb + ar[(i >> 2) * 2 + 1] + 4096);
    };
    auto mh = [&](int g) {
      acc[0] = mfma32(cf[g & 1][0], asbf8(hf[2 * g]), acc[0]);
      acc[1] = mfma32(cf[g & 1][1], asbf8(hf[2 * g]), acc[1]);
      acc[0] = mfma32(cf[g & 1][2], asbf8(hf[2 * g + 1]), acc[0]);
      acc[1] = mfma32(cf[g & 1][3], asbf8(hf[2 * g + 1]), acc[1]);
    };
#pragma unroll
    for (int i = 0; i < 8; ++i) hf[i] = *(const LAS u4*)(L + 2 * SLOT + w * 8192 + i * 1024 + lane * 16);
    rdc(0);
    {
      u4 ida[2];
#pragma unroll
      for (int s = 0; s < 2; ++s)
#pragma unroll
        for (int jq = 0; jq < 4; ++jq) {
          const int r0 = 16 * s + 8 * hh + 2 * jq;
          ida[s][jq] = (l31 == r0 ? 0x3f80u : 0u) | (l31 == r0 + 1 ? 0x3f800000u : 0u);
        }
      acc[0] = mfma32(asbf8(ida[1]), asbf8(xd[1]), mfma32(asbf8(ida[0]), asbf8(xd[0]), zero16));
      acc[1] = mfma32(asbf8(ida[1]), asbf8(xd[3]), mfma32(asbf8(ida[0]), asbf8(xd[2]), zero16));
    }
    SB();
#pragma unroll
    for (int g = 0; g < 4; ++g) { rdc(g + 1); mh(g); ILB(); SB(); }
    unsigned tw[8], trd[4], so_ = ((tile * 128 + 64 * (w >> 1) + 8 * (w & 1) + 16 * (q & 1) + (q >> 1)) * 1024 + 512 + cg * 64 + l15 * 4) * 4;
#pragma unroll
    for (int i = 0; i < 8; ++i) { tw[i] = 2 * SLOT + w * 8192 + l31 * 256 + (((2 * i + hh) ^ l15) << 4); asm volatile("" : "+v"(tw[i])); }
#pragma unroll
    for (int i = 0; i < 4; ++i) { trd[i] = 2 * SLOT + w * 8192 + (4 * i + q) * 256 + ((l15 ^ (4 * i + q)) << 4); asm volatile("" : "+v"(trd[i])); }
    asm volatile("s_barrier" : "+v"(so_) :: "memory");
    if (FD) __builtin_amdgcn_s_sleep(FD);
    wait_flags(ws, WS_FB + ((tile * 2 + (w >> 1)) * 16 + l15) * 128, tag);
    ST(8); CT(14);
#pragma unroll
    for (int i = 8; i < 8 + HD; ++i) ldh(i);
    SB();
#pragma unroll
    for (int g = 4; g < 32; ++g) {
      if (2 * g + HD < 64) { ldh(2 * g + HD); ldh(2 * g + HD + 1); }
      if (g + 1 < 32) rdc(g + 1);
      mh(g);
      SG(0x008, 1); SG(0x100, 2); SG(0x008, 1); SG(0x100, 2); SG(0x008, 1); SG(0x020, 1); SG(0x008, 1); SG(0x020, 1);
      SB();
    }
    ST(9); CT(15);
#pragma unroll
    for (int i = 0; i < 8; ++i)
      *(LAS f4*)(L + tw[i]) = (f4){acc[i >> 2][4 * (i & 3)], acc[i >> 2][4 * (i & 3) + 1], acc[i >> 2][4 * (i & 3) + 2], acc[i >> 2][4 * (i & 3) + 3]};
    u4 ov[8];
#pragma unroll
    for (int i = 0; i < 8; ++i) ov[i] = *(const LAS u4*)(L + trd[i & 3] + (i >> 2) * 4096);
    SB();
#pragma unroll
    for (int i = 0; i < 8; ++i) __builtin_amdgcn_raw_buffer_store_b128(ov[i], ro, so_, (32 * (i & 1) + 2 * (i >> 1)) * 4096, OUT_AUX);
    ST(10);
  }
  drain();
  ST(11);
  if (bid == AMP_BID && w == 0) {
    const u64 until = st[11] + AMP_K * (st[AMP_B] - st[AMP_A]);
    while (__builtin_amdgcn_s_memrealtime() < until) __builtin_amdgcn_s_sleep(2);
  }
}

extern "C" void kernel_launch(void* const* d_in, const int* in_sizes, int n_in, void* d_out, int out_size, void* d_ws, size_t ws_size,
                              hipStream_t stream) {
  (void)hipFuncSetAttribute((const void*)fused_kernel, hipFuncAttributeMaxDynamicSharedMemorySize, LDS_BYTES);
  fused_kernel<<<256, 512, LDS_BYTES, stream>>>((const u16*)d_in[0], (const u16*)d_in[1], (const u16*)d_in[2], (const u16*)d_in[3],
                                               (const u16*)d_in[4], (const u16*)d_in[5], (const u16*)d_in[6], (const u16*)d_in[7],
                                               (const u16*)d_in[8], (const u16*)d_in[9], (const u16*)d_in[10], (const u16*)d_in[11],
                                               (float*)d_out, (char*)d_ws);
}
```

```cpp
#include <hip/hip_runtime.h>
#include <hip/hip_bf16.h>

#define DI __device__ __forceinline__
#define LAS __attribute__((address_space(3)))
#define GAS __attribute__((address_space(1)))
typedef unsigned short u16;
typedef unsigned long long u64;
typedef __bf16 bf8 __attribute__((ext_vector_type(8)));
typedef __bf16 bf2 __attribute__((ext_vector_type(2)));
typedef short s4 __attribute__((ext_vector_type(4)));
typedef short s8 __attribute__((ext_vector_type(8)));
typedef float f16v __attribute__((ext_vector_type(16)));
typedef float f4 __attribute__((ext_vector_type(4)));
typedef float f2 __attribute__((ext_vector_type(2)));
typedef unsigned u4 __attribute__((ext_vector_type(4)));
typedef unsigned u2 __attribute__((ext_vector_type(2)));

constexpr unsigned WS_EPOCH = 0;
constexpr unsigned WS_FA = 4096;
constexpr unsigned WS_FB = WS_FA + 65536;
constexpr unsigned WS_EST = WS_FB + 65536;
constexpr unsigned WS_Z = WS_EST + 131072;
constexpr unsigned WS_KV = WS_Z + 32768;
constexpr unsigned WS_O = 1u << 20;
constexpr unsigned WS_H = 3u << 20;
constexpr unsigned WS_END = 7u << 20;
constexpr unsigned WS_STAMP = 8u << 20;
constexpr int LDS_BYTES = 160 * 1024;
constexpr unsigned SPIN_CAP = 200000;
constexpr int SC1 = 16;

#define ROT 8
#define NC_EARLY 2
#define OUT_AUX 16
#define PAY_AUX 16
#define PULL 0
#define TOUCH 1
#define CPF 10
#define HROT 1
#define XNT 0
#define PD 4
#define FD 0
#define TD 0
#define NODRAIN 0
#define WOX 2
#define DLA 0
#define DLB 0
#define OD 6
#define HD 12

#define CT(i) do {} while (0)
#define ST(i) do {} while (0)
#define HFLUSH() do {} while (0)
#define SB() __builtin_amdgcn_sched_barrier(0)
#define SG(m, n) __builtin_amdgcn_sched_group_barrier(m, n, 0)

DI unsigned pk2(float a, float b) { f2 v = {a, b}; return __builtin_bit_cast(unsigned, __builtin_convertvector(v, bf2)); }
DI unsigned pk2(f2 v) { return __builtin_bit_cast(unsigned, __builtin_convertvector(v, bf2)); }
DI float bflo(unsigned v) { return __uint_as_float(v << 16); }
DI float bfhi(unsigned v) { return __uint_as_float(v & 0xffff0000u); }
DI bf8 asbf8(u4 v) { return __builtin_bit_cast(bf8, v); }
DI f16v mfma32(bf8 a, bf8 b, f16v c) { return __builtin_amdgcn_mfma_f32_32x32x16_bf16(a, b, c, 0, 0, 0); }
DI f4 mfma16(bf8 a, bf8 b, f4 c) { return __builtin_amdgcn_mfma_f32_16x16x32_bf16(a, b, c, 0, 0, 0); }
DI f4 mfma16k16(u2 a, u2 b, f4 c) { return __builtin_amdgcn_mfma_f32_16x16x16bf16_1k(__builtin_bit_cast(s4, a), __builtin_bit_cast(s4, b), c, 0, 0, 0); }
DI bf8 ldsr(const char LAS* p) { return *(const LAS bf8*)p; }
DI bf8 ldstr(const char LAS* p0, const char LAS* p1) {
  s4 a = __builtin_amdgcn_ds_read_tr16_b64_v4i16((LAS s4*)p0), b = __builtin_amdgcn_ds_read_tr16_b64_v4i16((LAS s4*)p1);
  return __builtin_bit_cast(bf8, (s8)__builtin_shufflevector(a, b, 0, 1, 2, 3, 4, 5, 6, 7));
}
DI void dma4(unsigned lds, const GAS char* sb, unsigned v0, unsigned v1, unsigned v2, unsigned v3) {
  asm volatile("s_mov_b32 m0, %0\n\ts_nop 0\n\tglobal_load_lds_dwordx4 %1, %5\n\tglobal_load_lds_dwordx4 %2, %5 offset:1024\n\t"
               "global_load_lds_dwordx4 %3, %5 offset:2048\n\tglobal_load_lds_dwordx4 %4, %5 offset:3072"
               :: "s"(lds), "v"(v0), "v"(v1), "v"(v2), "v"(v3), "s"(sb) : "memory");
}
DI void dma4nt(unsigned lds, const GAS char* sb, unsigned v0, unsigned v1, unsigned v2, unsigned v3) {
  asm volatile("s_mov_b32 m0, %0\n\ts_nop 0\n\tglobal_load_lds_dwordx4 %1, %5 nt\n\tglobal_load_lds_dwordx4 %2, %5 offset:1024 nt\n\t"
               "global_load_lds_dwordx4 %3, %5 offset:2048 nt\n\tglobal_load_lds_dwordx4 %4, %5 offset:3072 nt"
               :: "s"(lds), "v"(v0), "v"(v1), "v"(v2), "v"(v3), "s"(sb) : "memory");
}
DI void dmax(unsigned lds, const GAS char* sb, unsigned v0, unsigned v1, unsigned v2, unsigned v3) { if (XNT) dma4nt(lds, sb, v0, v1, v2, v3); else dma4(lds, sb, v0, v1, v2, v3); }
DI void dma2(unsigned lds, const GAS char* sb, unsigned v0, unsigned v1) {
  asm volatile("s_mov_b32 m0, %0\n\ts_nop 0\n\tglobal_load_lds_dwordx4 %1, %3\n\tglobal_load_lds_dwordx4 %2, %3 offset:1024"
               :: "s"(lds), "v"(v0), "v"(v1), "s"(sb) : "memory");
}
DI void lbar() { asm volatile("s_waitcnt lgkmcnt(0)\n\ts_barrier" ::: "memory"); }
template <int N> DI void wait_bar() { asm volatile("s_waitcnt vmcnt(%0) lgkmcnt(0)\n\ts_barrier" :: "i"(N) : "memory"); }
DI void drain() { asm volatile("s_waitcnt vmcnt(0)" ::: "memory"); }
DI unsigned tagv(float x, unsigned t) { return (__float_as_uint(x) & 0xffffff00u) | t; }
DI f2 cmul(f2 a, f2 b) { return (f2){a.x * b.x - a.y * b.y, a.x * b.y + a.y * b.x}; }
DI f2 cfma(f2 a, f2 b, f2 c) { return (f2){fmaf(a.x, b.x, fmaf(-a.y, b.y, c.x)), fmaf(a.x, b.y, fmaf(a.y, b.x, c.y))}; }
DI f2 fma2(f2 a, f2 b, f2 c) { return __builtin_elementwise_fma(a, b, c); }
constexpr float EPS = 1e-6f, LOG2E = 1.44269504088896341f;
DI unsigned phi2(float s0, float s1) {
  const f2 a = fma2((f2){s0, s1}, (f2){LOG2E, LOG2E}, (f2){-EPS * LOG2E, -EPS * LOG2E});
  const f2 e = {__builtin_amdgcn_fmed3f(__builtin_amdgcn_exp2f(a.x), 0.f, 1.f), __builtin_amdgcn_fmed3f(__builtin_amdgcn_exp2f(a.y), 0.f, 1.f)};
  return pk2(e + (f2){__builtin_amdgcn_fmed3f(s0, EPS, __builtin_inff()), __builtin_amdgcn_fmed3f(s1, EPS, __builtin_inff())});
}
DI f2 swap_lo(f2 v, f2& hi) {
  const auto x = __builtin_amdgcn_permlane32_swap(__float_as_uint(v.x), __float_as_uint(v.x), false, false);
  const auto y = __builtin_amdgcn_permlane32_swap(__float_as_uint(v.y), __float_as_uint(v.y), false, false);
  hi = (f2){__uint_as_float(x[1]), __uint_as_float(y[1])};
  return (f2){__uint_as_float(x[0]), __uint_as_float(y[0])};
}
DI void wait_flags(__amdgpu_buffer_rsrc_t ws, unsigned off, unsigned tag) {
#pragma unroll 1
  for (unsigned sp = 0; sp < SPIN_CAP; ++sp) {
      asm volatile("" ::: "memory");
    const u2 f = __builtin_amdgcn_raw_buffer_load_b64(ws, off, 0, SC1);
    if (__all(f.x == tag && f.y == ~tag)) break;
    __builtin_amdgcn_s_sleep(1);
  }
  asm volatile("" ::: "memory");
}

__global__ void __launch_bounds__(512, 2)
fused_kernel(const u16* a0, const u16* a1, const u16* a2, const u16* a3, const u16* a4, const u16* a5, const u16* a6, const u16* a7,
             const u16* a8, const u16* a9, const u16* a10, const u16* a11, float* a12, char* a13) {
  extern __shared__ __attribute__((aligned(16))) char smem_[];
  char LAS* L = (char LAS*)smem_;
  const unsigned L0 = (unsigned)(size_t)L;
  ST(0);
  const int lane = threadIdx.x & 63;
  const int w8 = __builtin_amdgcn_readfirstlane(threadIdx.x >> 6), w = w8 & 3;
  unsigned cpf = 0;
  if (CPF && w8 == 0) {
    u64 pc;
    asm volatile("s_getpc_b64 %0" : "=s"(pc));
    cpf = __builtin_amdgcn_raw_buffer_load_b32(__builtin_amdgcn_make_buffer_rsrc((char*)(pc & ~127ull), 0, CPF * 4096, 0x00020000),
                                               lane < CPF ? ((blockIdx.x >> 3) * CPF + lane) * 128 : 0x80000000u, 0, 0);
  }
  typedef u64 u64x8 __attribute__((ext_vector_type(8)));
  typedef u64 u64x4 __attribute__((ext_vector_type(4)));
  typedef u64 u64x2 __attribute__((ext_vector_type(2)));
  u64x8 ka; u64x4 kb; u64x2 kc_;
  asm volatile("s_load_dwordx16 %0, %3, 0x0\n\ts_load_dwordx8 %1, %3, 0x40\n\ts_load_dwordx4 %2, %3, 0x60\n\ts_waitcnt lgkmcnt(0)"
               : "=&s"(ka), "=&s"(kb), "=&s"(kc_) : "s"(__builtin_amdgcn_kernarg_segment_ptr()) : "memory");
  const GAS u16 *xa = (const GAS u16*)ka[0], *wqkv = (const GAS u16*)ka[1], *wo = (const GAS u16*)ka[2], *xb = (const GAS u16*)ka[3];
  const GAS u16 *lre = (const GAS u16*)ka[4], *lim = (const GAS u16*)ka[5], *bre = (const GAS u16*)ka[6], *bim = (const GAS u16*)ka[7];
  const GAS u16 *cre = (const GAS u16*)kb[0], *cim = (const GAS u16*)kb[1], *ldt = (const GAS u16*)kb[2], *dsk = (const GAS u16*)kb[3];
  float* out = (float*)kc_[0]; char* wsp = (char*)kc_[1];
  const bool helper = w8 >= 4;
  const int bid = blockIdx.x, xcd = bid & 7, jj = bid >> 3;
  const int tile = (xcd >> 1) * 4 + (jj >> 3), cg = jj & 7;
  const int rot = ROT == 1 ? ((cg + 3 * (jj >> 3) + xcd) & 7) : ROT == 7 ? (((cg & 3) * 2 + (xcd >> 1)) & 7) : ROT == 12 ? ((2 * (cg & 3) + 2 * (xcd >> 1)) & 7) : ROT == 16 ? ((2 * (cg & 3) + ((jj >> 3) & 1) + 2 * (xcd >> 1)) & 7) : ((((cg + (jj >> 3)) % 3) * 3 + (xcd >> 1)) & 7);
  auto seq = [&](int t) { const int b = 2 * (jj >> 3), e = t - 1; return t == 0 ? cg : (b + e + (e >= ((cg - b) & 7) ? 1 : 0)) & 7; };
  const __amdgpu_buffer_rsrc_t ws = __builtin_amdgcn_make_buffer_rsrc(wsp, 0, WS_END, 0x00020000);
  const __amdgpu_buffer_rsrc_t ro = __builtin_amdgcn_make_buffer_rsrc(out, 0, 2048 * 1024 * 4, 0x00020000);
  const int r8 = lane >> 3, c8 = lane & 7;
  const int q = lane >> 4, l15 = lane & 15, l31 = lane & 31, hh = lane >> 5;
  unsigned epoch;
#define LOAD_EPOCH() asm volatile("s_load_dword %0, %1, 0x0" : "=s"(epoch) : "s"(wsp) : "memory")

  if ((xcd & 1) == 0) {
    constexpr unsigned WA = 131072, WB = 147456, SCR = 32768, TRB = 81920;
    constexpr unsigned QBUF[4] = {WB, 0, WA, WB}, OBUF[4] = {32768, 49152, 65536, 0};
    constexpr unsigned WBUF[8] = {WA, WB, 7 * 16384, 0, WA, WB, 0, WA};
    if (helper) {
      const int head = cg * 4 + w;
      unsigned vx[4], vw[2], vo[4];
#pragma unroll
      for (int i = 0; i < 4; ++i) vx[i] = (32 * w + 8 * i + r8) * 1024 + ((c8 ^ ((4 * i + (r8 >> 1)) & 7)) << 4) - 1024 * i;
#pragma unroll
      for (int i = 0; i < 2; ++i) vw[i] = (16 * w + 8 * i + r8) * 3072 + ((c8 ^ ((((r8 >> 1) & 1) | (i << 1)) << 1)) << 4) - 1024 * i;
      const GAS char* xbase = (const GAS char*)xa + (size_t)tile * 131072;
      const GAS char* wbase = (const GAS char*)wqkv + cg * 128;
      auto isx = [&](int kk, int slot) { dmax(L0 + slot * 16384 + w * 4096, xbase + ((kk + rot) & 7) * 128, vx[0], vx[1], vx[2], vx[3]); };
      auto isw = [&](unsigned dst, int kk, int seg) { dma2(L0 + dst + w * 2048, wbase + ((kk + rot) & 7) * (64 * 3072) + seg * 1024, vw[0], vw[1]); };
      auto issue = [&](int kk) { isx(kk, kk); isw(WBUF[kk], kk, 1); isw(WBUF[kk] + 8192, kk, 2); };
      auto isq = [&](int p) { isw(QBUF[p], 2 * p + 1, 0); isw(QBUF[p] + 8192, 2 * p + 2, 0); };
      ST(9); issue(0); ST(10); issue(1); ST(11);
      LOAD_EPOCH();
#pragma unroll
      for (int i = 0; i < 4; ++i) vo[i] = (8 * i + r8) * 1024 + ((c8 ^ (((r8 >> 1) & 3) << 1)) << 4) - 1024 * i;
      const GAS char* obase = (const GAS char*)wo + cg * 128;
      asm volatile("s_waitcnt vmcnt(8) lgkmcnt(0)\n\ts_barrier" : "+s"(epoch) :: "memory");
      issue(2);
      wait_bar<8>(); issue(3);
      wait_bar<8>(); issue(4);
      wait_bar<8>(); issue(5);
      wait_bar<8>(); issue(6);
      wait_bar<8>(); issue(7);
      wait_bar<8>();
      unsigned pfo;
      asm volatile("global_load_dword %0, %1, %2" : "=v"(pfo) : "v"(((jj >> 3) * 128 + w * 32 + l31) * 1024), "s"(obase) : "memory");
      isq(0);
      wait_bar<5>(); isq(1);
      wait_bar<4>(); isq(2);
      wait_bar<4>(); isq(3); isx(0, 1);
      asm volatile("s_waitcnt vmcnt(0)\n\ts_barrier" : "+v"(pfo) :: "memory");
      ST(1);
      const unsigned tag = epoch + 1, t = tag & 0xffu;
      const GAS char* kvb_ = (const GAS char*)wsp + WS_KV + head * 16384;
      const GAS char* zb_ = (const GAS char*)wsp + WS_Z + head * 1024;
      const unsigned qr = (jj >> 3) * 4096;
      const unsigned vk0 = lane * 16 + qr, vk1 = lane * 16 + ((qr + 4096) & 12288), vz = l15 * 64 + q * 16;
      u4 g[8], gz;
#define KVL(i, v) asm volatile("global_load_dwordx4 %0, %1, %2 offset:%3 sc1" : "=v"(g[i]) : "v"(v), "s"(kvb_), "i"((i & 3) * 1024) : "memory")
      asm volatile("global_load_dwordx4 %0, %1, %2 sc1" : "=v"(gz) : "v"(vz), "s"(zb_) : "memory");
      KVL(0, vk0); KVL(1, vk0); KVL(2, vk0); KVL(3, vk0); KVL(4, vk1); KVL(5, vk1); KVL(6, vk1); KVL(7, vk1);
      auto zsum = [&]() {
        f4 z = __builtin_bit_cast(f4, gz);
#pragma unroll
        for (int r = 0; r < 4; ++r) {
          float v = z[r];
          v += __builtin_bit_cast(float, __builtin_amdgcn_mov_dpp(__builtin_bit_cast(int, v), 0x128, 0xf, 0xf, true));
          v += __builtin_bit_cast(float, __builtin_amdgcn_mov_dpp(__builtin_bit_cast(int, v), 0x124, 0xf, 0xf, true));
          v += __builtin_bit_cast(float, __builtin_amdgcn_mov_dpp(__builtin_bit_cast(int, v), 0x122, 0xf, 0xf, true));
          v += __builtin_bit_cast(float, __builtin_amdgcn_mov_dpp(__builtin_bit_cast(int, v), 0x121, 0xf, 0xf, true));
          z[r] = v;
        }
        return z;
      };
      asm volatile("s_waitcnt vmcnt(8)" : "+v"(gz) :: "memory");
      gz.x ^= t; gz.z ^= t;
      unsigned bad = gz.x | gz.z;
      f4 zz = zsum(), kv = {0, 0, 0, 0};
      asm volatile("" : "+v"(zz));
#define KVA(i) asm volatile("s_waitcnt vmcnt(%1)" : "+v"(g[i]) : "i"(7 - i) : "memory"); g[i].x ^= t; g[i].z ^= t; bad |= g[i].x | g[i].z; kv += __builtin_bit_cast(f4, g[i]); asm volatile("" : "+v"(kv), "+v"(bad))
      KVA(0); KVA(1); KVA(2); KVA(3); KVA(4); KVA(5); KVA(6); KVA(7);
      ST(3);
      if (!__all((bad & 0xffu) == 0)) {
#pragma unroll 1
        for (unsigned rep = 0; rep < SPIN_CAP; ++rep) {
          asm volatile("" ::: "memory");
          gz = __builtin_amdgcn_raw_buffer_load_b128(ws, WS_Z + head * 1024 + vz, 0, SC1);
#pragma unroll
          for (int i = 0; i < 8; ++i) g[i] = __builtin_amdgcn_raw_buffer_load_b128(ws, WS_KV + head * 16384 + (i < 4 ? vk0 : vk1) + (i & 3) * 1024, 0, SC1);
          gz.x ^= t; gz.z ^= t;
          bad = gz.x | gz.z;
#pragma unroll
          for (int i = 0; i < 8; ++i) { g[i].x ^= t; g[i].z ^= t; bad |= g[i].x | g[i].z; }
          if (__all((bad & 0xffu) == 0)) break;
        }
        zz = zsum();
        kv = (f4){0, 0, 0, 0};
#pragma unroll
        for (int i = 0; i < 8; ++i) kv += __builtin_bit_cast(f4, g[i]);
      }
      ST(5);
      *(LAS f4*)(L + SCR + w * 1024 + lane * 16) = kv;
      *(LAS u2*)(L + SCR + 8192 + w * 512 + lane * 8) = (u2){pk2(zz[0], zz[1]), pk2(zz[2], zz[3])};
      ST(6);
      lbar();
      ST(7);
      lbar();
#pragma unroll
      for (int i = 0; i < 4; ++i)
        dma4(L0 + OBUF[i] + w * 4096, obase + (2 * seq(2 * i + (w >> 1)) + (w & 1)) * 32768, vo[0], vo[1], vo[2], vo[3]);
      wait_bar<0>();
      ST(8);
      HFLUSH();
      if (bid == 0 && w == 0) {
#pragma unroll 1
        for (unsigned sp = 0; sp < SPIN_CAP; ++sp) {
          asm volatile("" ::: "memory");
          const unsigned fo = (lane >> 3) * 4096 + (lane & 7) * 256;
          const u2 f0 = __builtin_amdgcn_raw_buffer_load_b64(ws, WS_FA + fo, 0, SC1), f1 = __builtin_amdgcn_raw_buffer_load_b64(ws, WS_FA + 32768 + fo, 0, SC1);
          const u2 f2_ = __builtin_amdgcn_raw_buffer_load_b64(ws, WS_FB + fo, 0, SC1), f3 = __builtin_amdgcn_raw_buffer_load_b64(ws, WS_FB + 32768 + fo, 0, SC1);
          if (__all(f0.x == tag && f0.y == ~tag && f1.x == tag && f1.y == ~tag && f2_.x == tag && f2_.y == ~tag && f3.x == tag && f3.y == ~tag)) break;
          __builtin_amdgcn_s_sleep(8);
        }
        if (lane == 0) __builtin_amdgcn_raw_buffer_store_b32(tag, ws, WS_EPOCH, 0, SC1);
      }
      return;
    }
    LOAD_EPOCH();
    const int hp = w & 1, th = w >> 1;
    unsigned warm0 = 0, warm1 = 0;
    if (TOUCH) {
      if (TD) __builtin_amdgcn_s_sleep(TD);
      warm0 = __builtin_amdgcn_raw_buffer_load_b32(ws, lane < 16 ? WS_FA + ((tile * 2 + th) * 16 + lane) * 128 : lane < 24 ? WS_Z + (cg * 4 + w) * 1024 + (lane - 16) * 128
                                                   : lane == 24 ? WS_KV + (cg * 4 + w) * 16384 : lane == 25 ? WS_O + tile * 131072 : 0x80000000u, 0, SC1);
      warm1 = __builtin_amdgcn_raw_buffer_load_b32(ro, lane < 2 ? (tile * 128 + w * 32 + lane * 16) * 4096 : 0x80000000u, 0, SC1);
    }
    const int qq = (lane >> 2) & 3, pp = lane & 3;
    const unsigned ax0 = l15 * 128 + ((q ^ (l15 >> 1)) << 4), ax1 = l15 * 128 + (((4 + q) ^ (l15 >> 1)) << 4);
    auto waddr = [&](int h) { return (8 * q + qq) * 128 + (((2 * h + (pp >> 1)) ^ ((((qq >> 1) & 1) | ((q & 1) << 1)) << 1)) << 4) + 8 * (pp & 1); };
    const unsigned aw = waddr(w), aq0 = waddr(2 * hp), aq1 = waddr(2 * hp + 1);
    f4 ka[8], va[8];
#pragma unroll
    for (int i = 0; i < 8; ++i) { ka[i] = (f4){EPS, EPS, EPS, EPS}; va[i] = (f4){0, 0, 0, 0}; }
    bf8 X0[4], X1[4], W0[2], W1[2];
    auto rdx = [&](bf8 (&X)[4], int kk, int ks, unsigned off) {
#pragma unroll
      for (int i = 0; i < 4; ++i) X[i] = ldsr(L + kk * 16384 + (ks ? ax1 : ax0) + off + i * 2048);
    };
    auto rdw = [&](bf8 (&W)[2], int kk, int ks) {
#pragma unroll
      for (int i = 0; i < 2; ++i) { const char LAS* p = L + WBUF[kk] + aw + ks * 4096 + i * 8192; W[i] = ldstr(p, p + 512); }
    };
    auto mm = [&](const bf8 (&X)[4], const bf8 (&W)[2], int h) {
#pragma unroll
      for (int i = 0; i < 4; ++i) {
        ka[4 * h + i] = mfma16(X[i], W[0], ka[4 * h + i]);
        va[4 * h + i] = mfma16(X[i], W[1], va[4 * h + i]);
      }
    };
#define ILV(nr, nm) do { _Pragma("unroll") for (int i_ = 0; i_ < nr; ++i_) { SG(0x008, 1); SG(0x100, 1); } if (nm > nr) SG(0x008, nm - nr); } while (0)
    asm volatile("s_waitcnt lgkmcnt(0)\n\ts_barrier" : "+s"(epoch) : "v"(warm0), "v"(warm1), "v"(cpf) : "memory");
    ST(1);
    const unsigned tag = epoch + 1, t = tag & 0xffu;
    rdw(W0, 0, 0); rdx(X0, 0, 0, 0);
    SB();
#pragma unroll
    for (int kk = 0; kk < 8; ++kk) {
      rdx(X1, kk, 0, 8192); mm(X0, W0, 0); ILV(4, 8); SB();
      rdw(W1, kk, 1); rdx(X0, kk, 1, 0); mm(X1, W0, 1); ILV(8, 8); SB();
      rdx(X1, kk, 1, 8192); mm(X0, W1, 0); ILV(4, 8); SB();
      if (kk < 7) { lbar(); rdw(W0, kk + 1, 0); rdx(X0, kk + 1, 0, 0); mm(X1, W1, 1); ILV(8, 8); }
      else mm(X1, W1, 1);
      SB();
    }
    ST(2);
    const unsigned xq = th * 8192;
    auto rdq = [&](bf8 (&X)[4], bf8 (&W)[2], int s) {
      const char LAS* p = L + QBUF[s >> 2] + ((s >> 1) & 1) * 8192 + (s & 1) * 4096;
      W[0] = ldstr(p + aq0, p + aq0 + 512); W[1] = ldstr(p + aq1, p + aq1 + 512);
      rdx(X, s < 14 ? (s >> 1) + 1 : 1, s & 1, xq);
    };
    {
      const int head = cg * 4 + w;
      f4 kv = {0, 0, 0, 0}, zz = {0, 0, 0, 0};
      const u4 ones = {0x3f803f80u, 0x3f803f80u, 0x3f803f80u, 0x3f803f80u};
#pragma unroll
      for (int m = 0; m < 4; ++m) {
        const f4 a = ka[2 * m], b = ka[2 * m + 1], c = va[2 * m], d = va[2 * m + 1];
        const u4 pa = {phi2(a[0], a[1]), phi2(a[2], a[3]), phi2(b[0], b[1]), phi2(b[2], b[3])};
        const u4 pb = {pk2(c[0], c[1]), pk2(c[2], c[3]), pk2(d[0], d[1]), pk2(d[2], d[3])};
        kv = mfma16(asbf8(pa), asbf8(pb), kv);
        zz = mfma16(asbf8(pa), asbf8(ones), zz);
      }
      const u4 skv = {tagv(kv[0], t), __float_as_uint(kv[1]), tagv(kv[2], t), __float_as_uint(kv[3])};
      __builtin_amdgcn_raw_buffer_store_b128(skv, ws, WS_KV + (head * 16 + tile) * 1024 + lane * 16, 0, SC1);
      const u4 sz = {tagv(zz[0], t), __float_as_uint(zz[1]), tagv(zz[2], t), __float_as_uint(zz[3])};
      __builtin_amdgcn_raw_buffer_store_b128(sz, ws, l15 == 0 ? WS_Z + (head * 16 + tile) * 64 + q * 16 : 0x80000000u, 0, SC1);
    }
    SB();
    ST(3);
    lbar();
    rdq(X0, W0, 0);
    SB();
    f4 qa[2][4];
#pragma unroll
    for (int i = 0; i < 8; ++i) qa[i >> 2][i & 3] = (f4){EPS, EPS, EPS, EPS};
    u4 gc[8];
    const unsigned gb = WS_KV + (cg * 4 + w) * 16384 + lane * 16, g2 = (((jj >> 3) + 2) & 3) * 4096, g3 = (((jj >> 3) + 3) & 3) * 4096;
    auto ldg = [&]() {
#pragma unroll
      for (int i = 0; i < 8; ++i) gc[i] = __builtin_amdgcn_raw_buffer_load_b128(ws, gb + (i & 3) * 1024, i < 4 ? g2 : g3, SC1);
    };
    auto mq = [&](const bf8 (&X)[4], const bf8 (&W)[2]) {
#pragma unroll
      for (int i = 0; i < 4; ++i) { qa[0][i] = mfma16(W[0], X[i], qa[0][i]); qa[1][i] = mfma16(W[1], X[i], qa[1][i]); }
    };
#pragma unroll
    for (int s = 0; s < 16; s += 2) {
      rdq(X1, W1, s + 1); mq(X0, W0); ILV(8, 8); SB();
      if (s == 2 || s == 6) lbar();
      if (s == 6) { ldg(); SB(); }
      if (s < 14) { rdq(X0, W0, s + 2); mq(X1, W1); ILV(8, 8); }
      else mq(X1, W1);
      SB();
    }
    ST(4);
    u2 pq[2][4];
#pragma unroll
    for (int i = 0; i < 8; ++i) pq[i >> 2][i & 3] = (u2){phi2(qa[i >> 2][i & 3][0], qa[i >> 2][i & 3][1]), phi2(qa[i >> 2][i & 3][2], qa[i >> 2][i & 3][3])};
    unsigned ao[4], og[4];
#pragma unroll
    for (int i = 0; i < 4; ++i) og[i] = WS_O + ((tile * 8 + th * 4 + i) * 16 + cg * 2 + hp) * 1024 + lane * 16;
#pragma unroll
    for (int nt = 0; nt < 4; ++nt) ao[nt] = (4 * q + qq) * 128 + ((((2 * nt) ^ (((2 * q + (qq >> 1)) & 3) << 1)) + (pp >> 1)) << 4) + 8 * (pp & 1);
    SB();
    {
      f4 kvp;
#pragma unroll 1
      for (unsigned rep = 0; rep < SPIN_CAP; ++rep) {
        unsigned bad = 0;
        kvp = (f4){0, 0, 0, 0};
#pragma unroll
        for (int i = 0; i < 8; ++i) { gc[i].x ^= t; gc[i].z ^= t; bad |= gc[i].x | gc[i].z; kvp += __builtin_bit_cast(f4, gc[i]); }
        if (__all((bad & 0xffu) == 0)) break;
        asm volatile("" ::: "memory");
        ldg();
      }
      *(LAS f4*)(L + SCR + 4096 + w * 1024 + lane * 16) = kvp;
    }
    asm volatile("s_waitcnt lgkmcnt(0)\n\ts_barrier" : "+v"(pq[0][0]), "+v"(pq[0][1]), "+v"(pq[0][2]), "+v"(pq[0][3]), "+v"(pq[1][0]), "+v"(pq[1][1]), "+v"(pq[1][2]), "+v"(pq[1][3]),
                 "+v"(og[0]), "+v"(og[1]), "+v"(og[2]), "+v"(og[3]) :: "memory");
    ST(5);
    const unsigned ex = TRB + th * 16384 + hp * 1024 + lane * 16;
    const f4 zero4 = {0, 0, 0, 0}, eps4 = {EPS, EPS, EPS, EPS};
    f4 num[2][4], den[2][4];
#pragma unroll
    for (int h = 0; h < 2; ++h) {
      const f4 kv = *(const LAS f4*)(L + SCR + (2 * hp + h) * 1024 + lane * 16) + *(const LAS f4*)(L + SCR + 4096 + (2 * hp + h) * 1024 + lane * 16);
      const u2 zb = *(const LAS u2*)(L + SCR + 8192 + (2 * hp + h) * 512 + lane * 8), kb2 = {pk2(kv[0], kv[1]), pk2(kv[2], kv[3])};
#pragma unroll
      for (int i = 0; i < 4; ++i) { den[h][i] = mfma16k16(zb, pq[h][i], eps4); num[h][i] = mfma16k16(kb2, pq[h][i], zero4); }
    }
    SB();
#pragma unroll
    for (int i = 0; i < 4; ++i) {
      u4 so;
#pragma unroll
      for (int h = 0; h < 2; ++h) {
        const float rc = __builtin_amdgcn_rcpf(den[h][i][0]);
        const f4 n = num[h][i];
        so[2 * h] = pk2((f2){n[0], n[1]} * rc);
        so[2 * h + 1] = pk2((f2){n[2], n[3]} * rc);
      }
      __builtin_amdgcn_raw_buffer_store_b128(so, ws, og[i], 0, SC1);
      *(LAS u4*)(L + ex + (i >> 1) * 8192 + (i & 1) * 2048) = so;
    }
    lbar();
    ST(6);
    u4 of[32];
#pragma unroll
    for (int i = 0; i < 4; ++i) of[i] = *(const LAS u4*)(L + TRB + w * 8192 + ((i & 1) * 2 + (i >> 1)) * 1024 + lane * 16);
    if (!NODRAIN) drain();
    __builtin_amdgcn_raw_buffer_store_b64((u2){tag, ~tag}, ws, lane == 0 ? WS_FA + ((tile * 2 + th) * 16 + cg * 2 + hp) * 128 : 0x80000000u, 0, SC1);
    unsigned tw[4], trd[4], so_ = ((tile * 128 + w * 32 + q) * 1024 + cg * 64 + l15 * 4) * 4;
#pragma unroll
    for (int i = 0; i < 4; ++i) {
      tw[i] = TRB + w * 8192 + l15 * 256 + (((4 * i + q) ^ l15) << 4);
      trd[i] = TRB + w * 8192 + (4 * i + q) * 256 + ((l15 ^ (4 * i + q)) << 4);
      asm volatile("" : "+v"(tw[i]), "+v"(trd[i]));
    }
    asm volatile("s_barrier" : "+v"(so_) :: "memory");
    ST(7);
    f4 acc[2][4];
#pragma unroll
    for (int i = 0; i < 8; ++i) acc[i >> 2][i & 3] = zero4;
    const unsigned ob = WS_O + (tile * 8 + w * 2) * 16384 + lane * 16;
    bf8 wf[2][4];
    auto rdo = [&](int k) {
#pragma unroll
      for (int nt = 0; nt < 4; ++nt) { const char LAS* p = L + OBUF[k >> 2] + (k & 3) * 4096 + ao[nt]; wf[k & 1][nt] = ldstr(p, p + 2048); }
    };
    auto ldo = [&](int k) {
      const unsigned so = (2 * seq(k >> 1) + (k & 1)) * 1024;
      of[2 * k] = __builtin_amdgcn_raw_buffer_load_b128(ws, ob, so, PAY_AUX);
      of[2 * k + 1] = __builtin_amdgcn_raw_buffer_load_b128(ws, ob + 16384, so, PAY_AUX);
    };
    auto mo = [&](int k) {
#pragma unroll
      for (int nt = 0; nt < 4; ++nt) {
        acc[0][nt] = mfma16(wf[k & 1][nt], asbf8(of[2 * k]), acc[0][nt]);
        acc[1][nt] = mfma16(wf[k & 1][nt], asbf8(of[2 * k + 1]), acc[1][nt]);
      }
    };
    rdo(0); rdo(1); SB();
    mo(0); rdo(2); SB();
    mo(1); SB();
    if (FD) __builtin_amdgcn_s_sleep(FD);
    wait_flags(ws, WS_FA + ((tile * 2 + th) * 16 + l15) * 128, tag);
    if (DLA) __builtin_amdgcn_s_sleep(DLA);
    ST(8); CT(14);
#pragma unroll
    for (int k = 2; k < 2 + OD; ++k) ldo(k);
    SB();
#pragma unroll
    for (int k = 2; k < 16; ++k) {
      if (k + OD < 16) ldo(k + OD);
      if (k + 1 < 16) rdo(k + 1);
      mo(k);
#pragma unroll
      for (int i_ = 0; i_ < 8; ++i_) { SG(0x008, 1); SG(0x100, 1); if (i_ == 1 || i_ == 5) SG(0x020, 1); }
      SB();
    }
    ST(9); CT(15);
#pragma unroll
    for (int mt = 0; mt < 2; ++mt)
#pragma unroll
      for (int nt = 0; nt < 4; ++nt) {
        const f4 v = acc[mt][nt];
        const unsigned p0 = pk2(v[0], v[1]), p1 = pk2(v[2], v[3]);
        *(LAS f4*)(L + tw[nt] + mt * 4096) = (f4){bflo(p0), bfhi(p0), bflo(p1), bfhi(p1)};
      }
    u4 ov[8];
#pragma unroll
    for (int i = 0; i < 8; ++i) ov[i] = *(const LAS u4*)(L + trd[i & 3] + (i >> 2) * 4096);
    SB();
#pragma unroll
    for (int i = 0; i < 8; ++i) __builtin_amdgcn_raw_buffer_store_b128(ov[i], ro, so_, i * 16384, OUT_AUX);
    ST(10);
  } else {
    constexpr unsigned SLOT = 32768, SCR = SLOT, PAR = 4 * SLOT;
    const int wp = w & 1, wt = w >> 1;
    const unsigned ecol = WS_EST + (cg * 64 + wp * 32 + l31) * 8;
    if (helper) {
      unsigned vx[4], vw[4], vc[4];
#pragma unroll
      for (int i = 0; i < 4; ++i) {
        const unsigned sw = (c8 ^ ((4 * i + (r8 >> 1)) & 7)) << 4;
        vx[i] = (64 * (w >> 1) + 32 * (r8 >> 2) + 16 * (r8 & 1) + 8 * (w & 1) + ((r8 >> 1) & 1) + 2 * i) * 1024 + sw - 1024 * i;
        vw[i] = (16 * (i >> 1) + 8 * (r8 >> 2) + 4 * (i & 1) + (r8 & 3)) * 1024 + sw - 1024 * i;
        vc[i] = (8 * i + r8) * 1024 + sw - 1024 * i;
      }
      const GAS char* xbase = (const GAS char*)xb + (size_t)tile * 131072;
      const GAS char* wbase = (const GAS char*)((w & 1) ? bim : bre) + (cg * 64 + (w >> 1) * 32) * 1024;
      auto issue = [&](int kk) {
        const int kc = (kk + rot) & 7;
        const unsigned s = L0 + (kk % 5) * SLOT + w * 4096;
        dmax(s, xbase + kc * 128, vx[0], vx[1], vx[2], vx[3]);
        dma4(s + 16384, wbase + kc * 128, vw[0], vw[1], vw[2], vw[3]);
      };
      ST(9); issue(0); ST(10); issue(1); ST(11);
      LOAD_EPOCH();
      const GAS char* cbase = (const GAS char*)((w & 1) ? cim : cre) + cg * 65536;
      auto issue_c = [&](int piece) {
        const unsigned s = L0 + ((piece + 3) % 5) * SLOT + w * 8192;
        const GAS char* cb = cbase + seq(2 * piece + (w >> 1)) * 128;
        dma4(s, cb, vc[0], vc[1], vc[2], vc[3]);
        dma4(s + 4096, cb + 32768, vc[0], vc[1], vc[2], vc[3]);
      };
      asm volatile("s_waitcnt vmcnt(8) lgkmcnt(0)\n\ts_barrier" : "+s"(epoch) :: "memory");
      issue(2);
      const f4 par = *(const LAS f4*)(L + PAR + w * 1024 + lane * 16);
      const f2 l64 = {par[0], par[1]}, l128 = {par[2], par[3]};
      f2 l256 = cmul(l128, l128);
      f2 wgt = hh ? l64 : (f2){1.f, 0.f};
      if (wt) wgt = cmul(wgt, l128);
      const unsigned tag = epoch + 1, t = tag & 0xffu;
      unsigned eo[8], et[8];
#pragma unroll
      for (int m = 0; m < 8; ++m) {
        const int i = 2 * tile - 1 - (2 * wt + hh) - 4 * m;
        eo[m] = i >= 0 ? ecol + i * 4096 : 0x80000000u;
        et[m] = i >= 0 ? t : 0u;
        asm volatile("" : "+v"(eo[m]), "+v"(et[m]));
      }
      asm volatile("" : "+v"(l256), "+v"(wgt));
      wait_bar<8>(); issue(3);
      wait_bar<8>(); issue(4);
      wait_bar<8>(); issue(5);
      wait_bar<8>(); issue(6);
      wait_bar<8>(); issue(7);
      wait_bar<8>(); issue_c(0);
      wait_bar<8>(); ST(1);
      if (NC_EARLY > 1) issue_c(1);
      if (NC_EARLY > 2) issue_c(2);
      ST(2);
      if (PD) { asm volatile("s_barrier" ::: "memory"); __builtin_amdgcn_s_sleep(PD); }
      f2 cin;
#pragma unroll 1
      for (unsigned sp = 0; sp < SPIN_CAP; ++sp) {
      asm volatile("" ::: "memory");
        u2 g[8];
#pragma unroll
        for (int m = 0; m < 8; ++m) g[m] = __builtin_amdgcn_raw_buffer_load_b64(ws, eo[m], 0, SC1);
        unsigned bad = 0;
        cin = (f2){0, 0};
#pragma unroll
        for (int m = 7; m >= 0; --m) {
          const unsigned cx = g[m].x ^ et[m];
          bad |= cx;
          cin = cfma(l256, cin, (f2){__uint_as_float(cx), __uint_as_float(g[m].y)});
        }
        if (__all((bad & 0xffu) == 0)) break;
      }
      ST(5);
      cin = cmul(cin, wgt);
      f2 chi;
      const f2 clo = swap_lo(cin, chi);
      if (hh == 0) *(LAS f2*)(L + SCR + (wp * 2 + wt) * 256 + l31 * 8) = clo + chi;
      ST(6);
      lbar();
      if (WOX & 2) issue_c(2);
      lbar();
      ST(7);
      if (NC_EARLY <= 1) issue_c(1);
      if (NC_EARLY <= 2 && !(WOX & 2)) issue_c(2);
      issue_c(3);
      wait_bar<0>();
      ST(8); HFLUSH();
      return;
    }
    LOAD_EPOCH();
    const int pl = cg * 64 + wp * 32 + ((l31 & 0x13) | ((l31 & 4) << 1) | ((l31 & 8) >> 1));
    const unsigned plr = lre[pl], pli = lim[pl], pdt = ldt[pl];
    u4 dv[4];
#pragma unroll
    for (int i = 0; i < 4; ++i) dv[i] = *(const GAS u4*)(dsk + cg * 64 + 16 * i + 8 * hh);
    unsigned warm0 = 0, warm1 = 0;
    if (TOUCH) {
      warm0 = __builtin_amdgcn_raw_buffer_load_b32(ws, lane < 16 ? WS_FB + ((tile * 2 + (w >> 1)) * 16 + lane) * 128 : lane == 16 ? WS_EST + tile * 8192
                                                   : lane == 17 ? WS_H + (tile * 4 + w) * 65536 : 0x80000000u, 0, SC1);
      warm1 = __builtin_amdgcn_raw_buffer_load_b32(ro, lane < 2 ? (tile * 128 + w * 32 + lane * 16) * 4096 + 2048 : 0x80000000u, 0, SC1);
    }
    SB();
    f2 lam, kap, l16, l32, l64, l128;
    {
      const float lr = bflo(plr), li = bflo(pli);
      const float dt = bflo(pk2(expf(bflo(pdt)), 0.f));
      const float mag = expf(dt * lr);
      float sn, cs;
      sincosf(dt * li, &sn, &cs);
      lam = (f2){mag * cs, mag * sn};
      const float inv = 2.f / (lr * lr + li * li), nr = lam.x - 1.f, ni = lam.y;
      kap = (f2){(nr * lr + ni * li) * inv, (ni * lr - nr * li) * inv};
      f2 t = cmul(lam, lam); t = cmul(t, t); t = cmul(t, t);
      l16 = cmul(t, t); l32 = cmul(l16, l16); l64 = cmul(l32, l32); l128 = cmul(l64, l64);
      *(LAS f4*)(L + PAR + w * 1024 + lane * 16) = (f4){l64.x, l64.y, l128.x, l128.y};
    }
    unsigned ar[4];
#pragma unroll
    for (int s = 0; s < 4; ++s) ar[s] = l31 * 128 + (((2 * s + hh) ^ ((lane >> 1) & 7)) << 4);
    f16v ur[2], ui[2];
#pragma unroll
    for (int i = 0; i < 16; ++i) { ur[0][i] = 0; ur[1][i] = 0; ui[0][i] = 0; ui[1][i] = 0; }
    bf8 F0[4], F1[4];
    auto rd = [&](bf8 (&F)[4], int kk, int s) {
      const char LAS* sx = L + (kk % 5) * SLOT + wt * 8192 + ar[s];
      const char LAS* sw = L + (kk % 5) * SLOT + 16384 + wp * 8192 + ar[s];
      F[0] = ldsr(sx); F[1] = ldsr(sw); F[2] = ldsr(sw + 4096); F[3] = ldsr(sx + 4096);
    };
    auto mm = [&](const bf8 (&F)[4]) {
      ur[0] = mfma32(F[0], F[1], ur[0]); ui[0] = mfma32(F[0], F[2], ui[0]);
      ur[1] = mfma32(F[3], F[1], ur[1]); ui[1] = mfma32(F[3], F[2], ui[1]);
    };
#define ILB() do { SG(0x008, 1); SG(0x100, 2); SG(0x008, 1); SG(0x100, 2); SG(0x008, 2); } while (0)
    asm volatile("s_waitcnt lgkmcnt(0)\n\ts_barrier" : "+s"(epoch), "+v"(lam), "+v"(kap), "+v"(l16), "+v"(l32), "+v"(l64), "+v"(dv[0]), "+v"(dv[1]), "+v"(dv[2]), "+v"(dv[3]) : "v"(warm0), "v"(warm1), "v"(cpf) : "memory");
    ST(1);
    const unsigned tag = epoch + 1, t = tag & 0xffu;
    rd(F0, 0, 0);
    SB();
#pragma unroll
    for (int kk = 0; kk < 8; ++kk) {
      rd(F1, kk, 1); mm(F0); ILB(); SB();
      rd(F0, kk, 2); mm(F1); ILB(); SB();
      rd(F1, kk, 3); mm(F0); ILB(); SB();
      if (kk < 7) { lbar(); if (kk == 6) ST(2); rd(F0, kk + 1, 0); mm(F1); ILB(); }
      else mm(F1);
      SB();
    }
    ST(3);
    f2 gr[16], gi[16];
#pragma unroll
    for (int a = 0; a < 16; ++a) {
      gr[a] = (f2){ur[a >> 3][2 * (a & 7)], ur[a >> 3][2 * (a & 7) + 1]};
      gi[a] = (f2){ui[a >> 3][2 * (a & 7)], ui[a >> 3][2 * (a & 7) + 1]};
    }
    {
      f2 LR = {lam.x, lam.x}, LI = {lam.y, lam.y}, LN = {-lam.y, -lam.y};
      asm volatile("s_nop 11" : "+v"(gr[0]), "+v"(gi[0]), "+v"(gr[8]), "+v"(gi[8]), "+v"(LR), "+v"(LI), "+v"(LN));
#pragma unroll
      for (int a = 1; a < 16; ++a)
        asm volatile("v_pk_fma_f32 %0, %6, %3, %0\n\tv_pk_fma_f32 %1, %5, %2, %1\n\tv_pk_fma_f32 %0, %4, %2, %0\n\tv_pk_fma_f32 %1, %4, %3, %1"
                     : "+v"(gr[a]), "+v"(gi[a]) : "v"(gr[a - 1]), "v"(gi[a - 1]), "v"(LR), "v"(LI), "v"(LN));
    }
    const f2 er = gr[15], ei = gi[15];
    const f2 e0 = {er.x, ei.x};
    const f2 e32 = cfma(l16, e0, (f2){er.y, ei.y});
    f2 ehi;
    const f2 elo = swap_lo(e32, ehi);
    const f2 e64 = cfma(l32, elo, ehi);
    __builtin_amdgcn_raw_buffer_store_b64((u2){tagv(e64.x, t), __float_as_uint(e64.y)}, ws, hh == 0 ? ecol + (tile * 2 + wt) * 4096 : 0x80000000u, 0, SC1);
    if (wt == 0 && hh == 0) *(LAS f2*)(L + SCR + 1024 + wp * 256 + l31 * 8) = e64;
    if (PD) asm volatile("s_barrier" ::: "memory");
    SB();
    ST(4);
    auto tokr = [&](int r) { return tile * 128 + 64 * (w >> 1) + 32 * ((r >> 2) & 1) + 16 * (r & 1) + 8 * (w & 1) + ((r >> 1) & 1) + 2 * (r >> 3); };
    u4 xd[4];
#pragma unroll
    for (int i = 0; i < 4; ++i) xd[i] = *(const GAS u4*)(xb + tokr(l31) * 512 + cg * 64 + 16 * i + 8 * hh);
    SB();
    {
      const f2 KR = {kap.x, kap.x}, KI = {kap.y, kap.y};
#pragma unroll
      for (int a = 0; a < 16; ++a) {
        const f2 xr = gr[a], xi = gi[a];
        gr[a] = fma2(KR, xr, -KI * xi); gi[a] = fma2(-KR, xi, -KI * xr);
        asm volatile("" : "+v"(gr[a]), "+v"(gi[a]));
      }
    }
    f2 pw[16];
    pw[0] = (f2){lam.x, -lam.y};
#pragma unroll
    for (int k = 1; k < 16; ++k) { pw[k] = cmul(pw[k >> 1], pw[(k - 1) >> 1]); asm volatile("" : "+v"(pw[k])); }
    u4 idn[2];
#pragma unroll
    for (int s = 0; s < 2; ++s)
#pragma unroll
      for (int jq = 0; jq < 4; ++jq) {
        const int r0 = 16 * s + 8 * (jq >> 1) + 4 * hh + 2 * (jq & 1);
        idn[s][jq] = (l31 == r0 ? 0x3f80u : 0u) | (l31 == r0 + 1 ? 0x3f800000u : 0u);
      }
    asm volatile("" : "+v"(idn[0]), "+v"(idn[1]));
#pragma unroll
    for (int i = 0; i < 4; ++i) {
#pragma unroll
      for (int j = 0; j < 4; ++j) xd[i][j] = pk2(bflo(xd[i][j]) * bflo(dv[i][j]), bfhi(xd[i][j]) * bfhi(dv[i][j]));
      asm volatile("" : "+v"(xd[i]));
    }
    f2 m0 = hh ? cmul(kap, l32) : kap, b0 = hh ? cmul(kap, elo) : (f2){0.f, 0.f};
    f2 m1 = cmul(l16, m0), b1 = cfma(l16, b0, cmul(kap, e0));
    unsigned ho[2];
    ho[0] = WS_H + (tile * 4 + wt * 2) * 65536 + (cg * 2 + wp) * 4096 + lane * 16; ho[1] = ho[0] + 65536;
    asm volatile("s_waitcnt lgkmcnt(0)\n\ts_barrier" : "+v"(ho[0]), "+v"(ho[1]), "+v"(m0), "+v"(b0), "+v"(m1), "+v"(b1) :: "memory");
    ST(5);
    f2 cin = *(const LAS f2*)(L + SCR + wp * 512 + l31 * 8) + *(const LAS f2*)(L + SCR + wp * 512 + 256 + l31 * 8);
    const f2 esib = *(const LAS f2*)(L + SCR + 1024 + wp * 256 + l31 * 8);
    if (wt) cin = cfma(l64, cin, esib);
    const f2 d0 = cfma(m0, cin, b0), d1 = cfma(m1, cin, b1);
    const f2 DR = {d0.x, d1.x}, DJ = {-d0.y, -d1.y};
    f16v zero16;
#pragma unroll
    for (int i = 0; i < 16; ++i) zero16[i] = 0;
    const unsigned ex = 2 * SLOT + wt * 16384 + wp * 4096 + lane * 16;
    auto fix = [&](int m, int c) {
      u4 a[2];
#pragma unroll
      for (int i = 0; i < 8; ++i) {
        const f2 P = pw[8 * m + i], PR = {P.x, P.x}, PI = {P.y, P.y};
        const f2 h = c ? fma2(PI, DR, fma2(PR, DJ, gi[8 * m + i])) : fma2(-PI, DJ, fma2(PR, DR, gr[8 * m + i]));
        a[i >> 2][i & 3] = pk2(h);
      }
      return mfma32(asbf8(a[1]), asbf8(idn[1]), mfma32(asbf8(a[0]), asbf8(idn[0]), zero16));
    };
    u4 ow[8];
    auto put = [&](const f16v& T, int m, int c) {
      const u4 o0 = ow[4 * m + 2 * c] = (u4){pk2(T[0], T[1]), pk2(T[2], T[3]), pk2(T[4], T[5]), pk2(T[6], T[7])};
      const u4 o1 = ow[4 * m + 2 * c + 1] = (u4){pk2(T[8], T[9]), pk2(T[10], T[11]), pk2(T[12], T[13]), pk2(T[14], T[15])};
      __builtin_amdgcn_raw_buffer_store_b128(o0, ws, ho[m] + c * 2048, 0, SC1);
      __builtin_amdgcn_raw_buffer_store_b128(o1, ws, ho[m] + c * 2048 + 1024, 0, SC1);
    };
    {
      const f16v T0 = fix(0, 0); SB();
      const f16v T1 = fix(0, 1); SB();
      put(T0, 0, 0);
      const f16v T2 = fix(1, 0); SB();
      put(T1, 0, 1);
      const f16v T3 = fix(1, 1); SB();
      put(T2, 1, 0); SB();
      put(T3, 1, 1);
    }
    SB();
#pragma unroll
    for (int i = 0; i < 8; ++i) *(LAS u4*)(L + ex + (i >> 2) * 8192 + (i & 3) * 1024) = ow[i];
    lbar();
    ST(6);
    if (!NODRAIN) drain();
    __builtin_amdgcn_raw_buffer_store_b64((u2){tag, ~tag}, ws, lane == 0 ? WS_FB + ((tile * 2 + wt) * 16 + cg * 2 + wp) * 128 : 0x80000000u, 0, SC1);
    ST(7);
    const unsigned hb = WS_H + (tile * 4 + w) * 65536 + lane * 16;
    u4 hf[64];
    bf8 cf[2][4];
    f16v acc[2];
    auto ldh = [&](int i) { hf[i] = __builtin_amdgcn_raw_buffer_load_b128(ws, hb + (i & 7) * 1024, seq(i >> 3) * 8192, PAY_AUX); };
    auto rdc = [&](int g) {
      const int tt = g >> 2, i = (g & 3) * 2;
      const char LAS* cb = L + (((tt >> 1) + 3) % 5) * SLOT + (tt & 1) * 16384 + ((i >> 1) & 1) * 8192;
      cf[g & 1][0] = ldsr(cb + ar[(i >> 2) * 2]); cf[g & 1][1] = ldsr(cb + ar[(i >> 2) * 2] + 4096);
      cf[g & 1][2] = ldsr(cb + ar[(i >> 2) * 2 + 1]); cf[g & 1][3] = ldsr(cb + ar[(i >> 2) * 2 + 1] + 4096);
    };
    auto mh = [&](int g) {
      acc[0] = mfma32(cf[g & 1][0], asbf8(hf[2 * g]), acc[0]);
      acc[1] = mfma32(cf[g & 1][1], asbf8(hf[2 * g]), acc[1]);
      acc[0] = mfma32(cf[g & 1][2], asbf8(hf[2 * g + 1]), acc[0]);
      acc[1] = mfma32(cf[g & 1][3], asbf8(hf[2 * g + 1]), acc[1]);
    };
#pragma unroll
    for (int i = 0; i < 8; ++i) hf[i] = *(const LAS u4*)(L + 2 * SLOT + w * 8192 + i * 1024 + lane * 16);
    rdc(0);
    {
      u4 ida[2];
#pragma unroll
      for (int s = 0; s < 2; ++s)
#pragma unroll
        for (int jq = 0; jq < 4; ++jq) {
          const int r0 = 16 * s + 8 * hh + 2 * jq;
          ida[s][jq] = (l31 == r0 ? 0x3f80u : 0u) | (l31 == r0 + 1 ? 0x3f800000u : 0u);
        }
      acc[0] = mfma32(asbf8(ida[1]), asbf8(xd[1]), mfma32(asbf8(ida[0]), asbf8(xd[0]), zero16));
      acc[1] = mfma32(asbf8(ida[1]), asbf8(xd[3]), mfma32(asbf8(ida[0]), asbf8(xd[2]), zero16));
    }
    SB();
#pragma unroll
    for (int g = 0; g < 4; ++g) { rdc(g + 1); mh(g); ILB(); SB(); }
    unsigned tw[8], trd[4], so_ = ((tile * 128 + 64 * (w >> 1) + 8 * (w & 1) + 16 * (q & 1) + (q >> 1)) * 1024 + 512 + cg * 64 + l15 * 4) * 4;
#pragma unroll
    for (int i = 0; i < 8; ++i) { tw[i] = 2 * SLOT + w * 8192 + l31 * 256 + (((2 * i + hh) ^ l15) << 4); asm volatile("" : "+v"(tw[i])); }
#pragma unroll
    for (int i = 0; i < 4; ++i) { trd[i] = 2 * SLOT + w * 8192 + (4 * i + q) * 256 + ((l15 ^ (4 * i + q)) << 4); asm volatile("" : "+v"(trd[i])); }
    asm volatile("s_barrier" : "+v"(so_) :: "memory");
    if (FD) __builtin_amdgcn_s_sleep(FD);
    wait_flags(ws, WS_FB + ((tile * 2 + (w >> 1)) * 16 + l15) * 128, tag);
    if (DLB) __builtin_amdgcn_s_sleep(DLB);
    ST(8); CT(14);
#pragma unroll
    for (int i = 8; i < 8 + HD; ++i) ldh(i);
    SB();
#pragma unroll
    for (int g = 4; g < 32; ++g) {
      if (2 * g + HD < 64) { ldh(2 * g + HD); ldh(2 * g + HD + 1); }
      if (g + 1 < 32) rdc(g + 1);
      mh(g);
      SG(0x008, 1); SG(0x100, 2); SG(0x008, 1); SG(0x100, 2); SG(0x008, 1); SG(0x020, 1); SG(0x008, 1); SG(0x020, 1);
      SB();
    }
    ST(9); CT(15);
#pragma unroll
    for (int i = 0; i < 8; ++i)
      *(LAS f4*)(L + tw[i]) = (f4){acc[i >> 2][4 * (i & 3)], acc[i >> 2][4 * (i & 3) + 1], acc[i >> 2][4 * (i & 3) + 2], acc[i >> 2][4 * (i & 3) + 3]};
    u4 ov[8];
#pragma unroll
    for (int i = 0; i < 8; ++i) ov[i] = *(const LAS u4*)(L + trd[i & 3] + (i >> 2) * 4096);
    SB();
#pragma unroll
    for (int i = 0; i < 8; ++i) __builtin_amdgcn_raw_buffer_store_b128(ov[i], ro, so_, (32 * (i & 1) + 2 * (i >> 1)) * 4096, OUT_AUX);
    ST(10);
  }
}

extern "C" void kernel_launch(void* const* d_in, const int* in_sizes, int n_in, void* d_out, int out_size, void* d_ws, size_t ws_size,
                              hipStream_t stream) {
  (void)hipFuncSetAttribute((const void*)fused_kernel, hipFuncAttributeMaxDynamicSharedMemorySize, LDS_BYTES);
  fused_kernel<<<256, 512, LDS_BYTES, stream>>>((const u16*)d_in[0], (const u16*)d_in[1], (const u16*)d_in[2], (const u16*)d_in[3],
                                               (const u16*)d_in[4], (const u16*)d_in[5], (const u16*)d_in[6], (const u16*)d_in[7],
                                               (const u16*)d_in[8], (const u16*)d_in[9], (const u16*)d_in[10], (const u16*)d_in[11],
                                               (float*)d_out, (char*)d_ws);
}
```

```cpp
#define BN 16
#define AN 6
#define HN 8
#include <hip/hip_runtime.h>
#include <hip/hip_bf16.h>

#define DI __device__ __forceinline__
#define LAS __attribute__((address_space(3)))
#define GAS __attribute__((address_space(1)))
typedef unsigned short u16;
typedef unsigned long long u64;
typedef __bf16 bf8 __attribute__((ext_vector_type(8)));
typedef __bf16 bf2 __attribute__((ext_vector_type(2)));
typedef short s4 __attribute__((ext_vector_type(4)));
typedef short s8 __attribute__((ext_vector_type(8)));
typedef float f16v __attribute__((ext_vector_type(16)));
typedef float f4 __attribute__((ext_vector_type(4)));
typedef float f2 __attribute__((ext_vector_type(2)));
typedef unsigned u4 __attribute__((ext_vector_type(4)));
typedef unsigned u2 __attribute__((ext_vector_type(2)));

constexpr unsigned WS_EPOCH = 0;
constexpr unsigned WS_FA = 4096;
constexpr unsigned WS_FB = WS_FA + 65536;
constexpr unsigned WS_EST = WS_FB + 65536;
constexpr unsigned WS_Z = WS_EST + 131072;
constexpr unsigned WS_KV = WS_Z + 32768;
constexpr unsigned WS_O = 1u << 20;
constexpr unsigned WS_H = 3u << 20;
constexpr unsigned WS_END = 7u << 20;
constexpr unsigned WS_STAMP = 8u << 20;
constexpr int LDS_BYTES = 160 * 1024;
constexpr unsigned SPIN_CAP = 200000;
constexpr int SC1 = 16;

#define ROT 8
#define NC_EARLY 2
#define OUT_AUX 16
#define PAY_AUX 16
#define PULL 0
#define TOUCH 1
#define CPF 10
#define HROT 1
#define XNT 0
#define PD 4
#define FD 0
#define TD 0
#define NODRAIN 0
#define WOX 2
#define DLA 0
#define DLB 0
#define QN 0
#define OD 6
#define HD 12

#define CT(i) do {} while (0)
#define ST(i) do {} while (0)
#define HFLUSH() do {} while (0)
#define SB() __builtin_amdgcn_sched_barrier(0)
#define PACE(n) do { if (n) { asm volatile("s_nop %0" :: "i"((n) ? (n) - 1 : 0)); SB(); } } while (0)
#define SG(m, n) __builtin_amdgcn_sched_group_barrier(m, n, 0)

DI unsigned pk2(float a, float b) { f2 v = {a, b}; return __builtin_bit_cast(unsigned, __builtin_convertvector(v, bf2)); }
DI unsigned pk2(f2 v) { return __builtin_bit_cast(unsigned, __builtin_convertvector(v, bf2)); }
DI float bflo(unsigned v) { return __uint_as_float(v << 16); }
DI float bfhi(unsigned v) { return __uint_as_float(v & 0xffff0000u); }
DI bf8 asbf8(u4 v) { return __builtin_bit_cast(bf8, v); }
DI f16v mfma32(bf8 a, bf8 b, f16v c) { return __builtin_amdgcn_mfma_f32_32x32x16_bf16(a, b, c, 0, 0, 0); }
DI f4 mfma16(bf8 a, bf8 b, f4 c) { return __builtin_amdgcn_mfma_f32_16x16x32_bf16(a, b, c, 0, 0, 0); }
DI f4 mfma16k16(u2 a, u2 b, f4 c) { return __builtin_amdgcn_mfma_f32_16x16x16bf16_1k(__builtin_bit_cast(s4, a), __builtin_bit_cast(s4, b), c, 0, 0, 0); }
DI bf8 ldsr(const char LAS* p) { return *(const LAS bf8*)p; }
DI bf8 ldstr(const char LAS* p0, const char LAS* p1) {
  s4 a = __builtin_amdgcn_ds_read_tr16_b64_v4i16((LAS s4*)p0), b = __builtin_amdgcn_ds_read_tr16_b64_v4i16((LAS s4*)p1);
  return __builtin_bit_cast(bf8, (s8)__builtin_shufflevector(a, b, 0, 1, 2, 3, 4, 5, 6, 7));
}
DI void dma4(unsigned lds, const GAS char* sb, unsigned v0, unsigned v1, unsigned v2, unsigned v3) {
  asm volatile("s_mov_b32 m0, %0\n\ts_nop 0\n\tglobal_load_lds_dwordx4 %1, %5\n\tglobal_load_lds_dwordx4 %2, %5 offset:1024\n\t"
               "global_load_lds_dwordx4 %3, %5 offset:2048\n\tglobal_load_lds_dwordx4 %4, %5 offset:3072"
               :: "s"(lds), "v"(v0), "v"(v1), "v"(v2), "v"(v3), "s"(sb) : "memory");
}
DI void dma4nt(unsigned lds, const GAS char* sb, unsigned v0, unsigned v1, unsigned v2, unsigned v3) {
  asm volatile("s_mov_b32 m0, %0\n\ts_nop 0\n\tglobal_load_lds_dwordx4 %1, %5 nt\n\tglobal_load_lds_dwordx4 %2, %5 offset:1024 nt\n\t"
               "global_load_lds_dwordx4 %3, %5 offset:2048 nt\n\tglobal_load_lds_dwordx4 %4, %5 offset:3072 nt"
               :: "s"(lds), "v"(v0), "v"(v1), "v"(v2), "v"(v3), "s"(sb) : "memory");
}
DI void dmax(unsigned lds, const GAS char* sb, unsigned v0, unsigned v1, unsigned v2, unsigned v3) { if (XNT) dma4nt(lds, sb, v0, v1, v2, v3); else dma4(lds, sb, v0, v1, v2, v3); }
DI void dma2(unsigned lds, const GAS char* sb, unsigned v0, unsigned v1) {
  asm volatile("s_mov_b32 m0, %0\n\ts_nop 0\n\tglobal_load_lds_dwordx4 %1, %3\n\tglobal_load_lds_dwordx4 %2, %3 offset:1024"
               :: "s"(lds), "v"(v0), "v"(v1), "s"(sb) : "memory");
}
DI void lbar() { asm volatile("s_waitcnt lgkmcnt(0)\n\ts_barrier" ::: "memory"); }
template <int N> DI void wait_bar() { asm volatile("s_waitcnt vmcnt(%0) lgkmcnt(0)\n\ts_barrier" :: "i"(N) : "memory"); }
DI void drain() { asm volatile("s_waitcnt vmcnt(0)" ::: "memory"); }
DI unsigned tagv(float x, unsigned t) { return (__float_as_uint(x) & 0xffffff00u) | t; }
DI f2 cmul(f2 a, f2 b) { return (f2){a.x * b.x - a.y * b.y, a.x * b.y + a.y * b.x}; }
DI f2 cfma(f2 a, f2 b, f2 c) { return (f2){fmaf(a.x, b.x, fmaf(-a.y, b.y, c.x)), fmaf(a.x, b.y, fmaf(a.y, b.x, c.y))}; }
DI f2 fma2(f2 a, f2 b, f2 c) { return __builtin_elementwise_fma(a, b, c); }
constexpr float EPS = 1e-6f, LOG2E = 1.44269504088896341f;
DI unsigned phi2(float s0, float s1) {
  const f2 a = fma2((f2){s0, s1}, (f2){LOG2E, LOG2E}, (f2){-EPS * LOG2E, -EPS * LOG2E});
  const f2 e = {__builtin_amdgcn_fmed3f(__builtin_amdgcn_exp2f(a.x), 0.f, 1.f), __builtin_amdgcn_fmed3f(__builtin_amdgcn_exp2f(a.y), 0.f, 1.f)};
  return pk2(e + (f2){__builtin_amdgcn_fmed3f(s0, EPS, __builtin_inff()), __builtin_amdgcn_fmed3f(s1, EPS, __builtin_inff())});
}
DI f2 swap_lo(f2 v, f2& hi) {
  const auto x = __builtin_amdgcn_permlane32_swap(__float_as_uint(v.x), __float_as_uint(v.x), false, false);
  const auto y = __builtin_amdgcn_permlane32_swap(__float_as_uint(v.y), __float_as_uint(v.y), false, false);
  hi = (f2){__uint_as_float(x[1]), __uint_as_float(y[1])};
  return (f2){__uint_as_float(x[0]), __uint_as_float(y[0])};
}
DI void wait_flags(__amdgpu_buffer_rsrc_t ws, unsigned off, unsigned tag) {
#pragma unroll 1
  for (unsigned sp = 0; sp < SPIN_CAP; ++sp) {
      asm volatile("" ::: "memory");
    const u2 f = __builtin_amdgcn_raw_buffer_load_b64(ws, off, 0, SC1);
    if (__all(f.x == tag && f.y == ~tag)) break;
    __builtin_amdgcn_s_sleep(1);
  }
  asm volatile("" ::: "memory");
}

__global__ void __launch_bounds__(512, 2)
fused_kernel(const u16* a0, const u16* a1, const u16* a2, const u16* a3, const u16* a4, const u16* a5, const u16* a6, const u16* a7,
             const u16* a8, const u16* a9, const u16* a10, const u16* a11, float* a12, char* a13) {
  extern __shared__ __attribute__((aligned(16))) char smem_[];
  char LAS* L = (char LAS*)smem_;
  const unsigned L0 = (unsigned)(size_t)L;
  ST(0);
  const int lane = threadIdx.x & 63;
  const int w8 = __builtin_amdgcn_readfirstlane(threadIdx.x >> 6), w = w8 & 3;
  unsigned cpf = 0;
  if (CPF && w8 == 0) {
    u64 pc;
    asm volatile("s_getpc_b64 %0" : "=s"(pc));
    cpf = __builtin_amdgcn_raw_buffer_load_b32(__builtin_amdgcn_make_buffer_rsrc((char*)(pc & ~127ull), 0, CPF * 4096, 0x00020000),
                                               lane < CPF ? ((blockIdx.x >> 3) * CPF + lane) * 128 : 0x80000000u, 0, 0);
  }
  typedef u64 u64x8 __attribute__((ext_vector_type(8)));
  typedef u64 u64x4 __attribute__((ext_vector_type(4)));
  typedef u64 u64x2 __attribute__((ext_vector_type(2)));
  u64x8 ka; u64x4 kb; u64x2 kc_;
  asm volatile("s_load_dwordx16 %0, %3, 0x0\n\ts_load_dwordx8 %1, %3, 0x40\n\ts_load_dwordx4 %2, %3, 0x60\n\ts_waitcnt lgkmcnt(0)"
               : "=&s"(ka), "=&s"(kb), "=&s"(kc_) : "s"(__builtin_amdgcn_kernarg_segment_ptr()) : "memory");
  const GAS u16 *xa = (const GAS u16*)ka[0], *wqkv = (const GAS u16*)ka[1], *wo = (const GAS u16*)ka[2], *xb = (const GAS u16*)ka[3];
  const GAS u16 *lre = (const GAS u16*)ka[4], *lim = (const GAS u16*)ka[5], *bre = (const GAS u16*)ka[6], *bim = (const GAS u16*)ka[7];
  const GAS u16 *cre = (const GAS u16*)kb[0], *cim = (const GAS u16*)kb[1], *ldt = (const GAS u16*)kb[2], *dsk = (const GAS u16*)kb[3];
  float* out = (float*)kc_[0]; char* wsp = (char*)kc_[1];
  const bool helper = w8 >= 4;
  const int bid = blockIdx.x, xcd = bid & 7, jj = bid >> 3;
  const int tile = (xcd >> 1) * 4 + (jj >> 3), cg = jj & 7;
  const int rot = ROT == 1 ? ((cg + 3 * (jj >> 3) + xcd) & 7) : ROT == 7 ? (((cg & 3) * 2 + (xcd >> 1)) & 7) : ROT == 12 ? ((2 * (cg & 3) + 2 * (xcd >> 1)) & 7) : ROT == 16 ? ((2 * (cg & 3) + ((jj >> 3) & 1) + 2 * (xcd >> 1)) & 7) : ((((cg + (jj >> 3)) % 3) * 3 + (xcd >> 1)) & 7);
  auto seq = [&](int t) { const int b = 2 * (jj >> 3), e = t - 1; return t == 0 ? cg : (b + e + (e >= ((cg - b) & 7) ? 1 : 0)) & 7; };
  const __amdgpu_buffer_rsrc_t ws = __builtin_amdgcn_make_buffer_rsrc(wsp, 0, WS_END, 0x00020000);
  const __amdgpu_buffer_rsrc_t ro = __builtin_amdgcn_make_buffer_rsrc(out, 0, 2048 * 1024 * 4, 0x00020000);
  const int r8 = lane >> 3, c8 = lane & 7;
  const int q = lane >> 4, l15 = lane & 15, l31 = lane & 31, hh = lane >> 5;
  unsigned epoch;
#define LOAD_EPOCH() asm volatile("s_load_dword %0, %1, 0x0" : "=s"(epoch) : "s"(wsp) : "memory")

  if ((xcd & 1) == 0) {
    constexpr unsigned WA = 131072, WB = 147456, SCR = 32768, TRB = 81920;
    constexpr unsigned QBUF[4] = {WB, 0, WA, WB}, OBUF[4] = {32768, 49152, 65536, 0};
    constexpr unsigned WBUF[8] = {WA, WB, 7 * 16384, 0, WA, WB, 0, WA};
    if (helper) {
      const int head = cg * 4 + w;
      unsigned vx[4], vw[2], vo[4];
#pragma unroll
      for (int i = 0; i < 4; ++i) vx[i] = (32 * w + 8 * i + r8) * 1024 + ((c8 ^ ((4 * i + (r8 >> 1)) & 7)) << 4) - 1024 * i;
#pragma unroll
      for (int i = 0; i < 2; ++i) vw[i] = (16 * w + 8 * i + r8) * 3072 + ((c8 ^ ((((r8 >> 1) & 1) | (i << 1)) << 1)) << 4) - 1024 * i;
      const GAS char* xbase = (const GAS char*)xa + (size_t)tile * 131072;
      const GAS char* wbase = (const GAS char*)wqkv + cg * 128;
      auto isx = [&](int kk, int slot) { dmax(L0 + slot * 16384 + w * 4096, xbase + ((kk + rot) & 7) * 128, vx[0], vx[1], vx[2], vx[3]); };
      auto isw = [&](unsigned dst, int kk, int seg) { dma2(L0 + dst + w * 2048, wbase + ((kk + rot) & 7) * (64 * 3072) + seg * 1024, vw[0], vw[1]); };
      auto issue = [&](int kk) { isx(kk, kk); isw(WBUF[kk], kk, 1); isw(WBUF[kk] + 8192, kk, 2); };
      auto isq = [&](int p) { isw(QBUF[p], 2 * p + 1, 0); isw(QBUF[p] + 8192, 2 * p + 2, 0); };
      ST(9); issue(0); ST(10); issue(1); ST(11);
      LOAD_EPOCH();
#pragma unroll
      for (int i = 0; i < 4; ++i) vo[i] = (8 * i + r8) * 1024 + ((c8 ^ (((r8 >> 1) & 3) << 1)) << 4) - 1024 * i;
      const GAS char* obase = (const GAS char*)wo + cg * 128;
      asm volatile("s_waitcnt vmcnt(8) lgkmcnt(0)\n\ts_barrier" : "+s"(epoch) :: "memory");
      issue(2);
      wait_bar<8>(); issue(3);
      wait_bar<8>(); issue(4);
      wait_bar<8>(); issue(5);
      wait_bar<8>(); issue(6);
      wait_bar<8>(); issue(7);
      wait_bar<8>();
      unsigned pfo;
      asm volatile("global_load_dword %0, %1, %2" : "=v"(pfo) : "v"(((jj >> 3) * 128 + w * 32 + l31) * 1024), "s"(obase) : "memory");
      isq(0);
      wait_bar<5>(); isq(1);
      wait_bar<4>(); isq(2);
      wait_bar<4>(); isq(3); isx(0, 1);
      asm volatile("s_waitcnt vmcnt(0)\n\ts_barrier" : "+v"(pfo) :: "memory");
      ST(1);
      const unsigned tag = epoch + 1, t = tag & 0xffu;
      const GAS char* kvb_ = (const GAS char*)wsp + WS_KV + head * 16384;
      const GAS char* zb_ = (const GAS char*)wsp + WS_Z + head * 1024;
      const unsigned qr = (jj >> 3) * 4096;
      const unsigned vk0 = lane * 16 + qr, vk1 = lane * 16 + ((qr + 4096) & 12288), vz = l15 * 64 + q * 16;
      u4 g[8], gz;
#define KVL(i, v) asm volatile("global_load_dwordx4 %0, %1, %2 offset:%3 sc1" : "=v"(g[i]) : "v"(v), "s"(kvb_), "i"((i & 3) * 1024) : "memory")
      asm volatile("global_load_dwordx4 %0, %1, %2 sc1" : "=v"(gz) : "v"(vz), "s"(zb_) : "memory");
      KVL(0, vk0); KVL(1, vk0); KVL(2, vk0); KVL(3, vk0); KVL(4, vk1); KVL(5, vk1); KVL(6, vk1); KVL(7, vk1);
      auto zsum = [&]() {
        f4 z = __builtin_bit_cast(f4, gz);
#pragma unroll
        for (int r = 0; r < 4; ++r) {
          float v = z[r];
          v += __builtin_bit_cast(float, __builtin_amdgcn_mov_dpp(__builtin_bit_cast(int, v), 0x128, 0xf, 0xf, true));
          v += __builtin_bit_cast(float, __builtin_amdgcn_mov_dpp(__builtin_bit_cast(int, v), 0x124, 0xf, 0xf, true));
          v += __builtin_bit_cast(float, __builtin_amdgcn_mov_dpp(__builtin_bit_cast(int, v), 0x122, 0xf, 0xf, true));
          v += __builtin_bit_cast(float, __builtin_amdgcn_mov_dpp(__builtin_bit_cast(int, v), 0x121, 0xf, 0xf, true));
          z[r] = v;
        }
        return z;
      };
      asm volatile("s_waitcnt vmcnt(8)" : "+v"(gz) :: "memory");
      gz.x ^= t; gz.z ^= t;
      unsigned bad = gz.x | gz.z;
      f4 zz = zsum(), kv = {0, 0, 0, 0};
      asm volatile("" : "+v"(zz));
#define KVA(i) asm volatile("s_waitcnt vmcnt(%1)" : "+v"(g[i]) : "i"(7 - i) : "memory"); g[i].x ^= t; g[i].z ^= t; bad |= g[i].x | g[i].z; kv += __builtin_bit_cast(f4, g[i]); asm volatile("" : "+v"(kv), "+v"(bad))
      KVA(0); KVA(1); KVA(2); KVA(3); KVA(4); KVA(5); KVA(6); KVA(7);
      ST(3);
      if (!__all((bad & 0xffu) == 0)) {
#pragma unroll 1
        for (unsigned rep = 0; rep < SPIN_CAP; ++rep) {
          asm volatile("" ::: "memory");
          gz = __builtin_amdgcn_raw_buffer_load_b128(ws, WS_Z + head * 1024 + vz, 0, SC1);
#pragma unroll
          for (int i = 0; i < 8; ++i) g[i] = __builtin_amdgcn_raw_buffer_load_b128(ws, WS_KV + head * 16384 + (i < 4 ? vk0 : vk1) + (i & 3) * 1024, 0, SC1);
          gz.x ^= t; gz.z ^= t;
          bad = gz.x | gz.z;
#pragma unroll
          for (int i = 0; i < 8; ++i) { g[i].x ^= t; g[i].z ^= t; bad |= g[i].x | g[i].z; }
          if (__all((bad & 0xffu) == 0)) break;
        }
        zz = zsum();
        kv = (f4){0, 0, 0, 0};
#pragma unroll
        for (int i = 0; i < 8; ++i) kv += __builtin_bit_cast(f4, g[i]);
      }
      ST(5);
      *(LAS f4*)(L + SCR + w * 1024 + lane * 16) = kv;
      *(LAS u2*)(L + SCR + 8192 + w * 512 + lane * 8) = (u2){pk2(zz[0], zz[1]), pk2(zz[2], zz[3])};
      ST(6);
      lbar();
      ST(7);
      lbar();
#pragma unroll
      for (int i = 0; i < 4; ++i)
        dma4(L0 + OBUF[i] + w * 4096, obase + (2 * seq(2 * i + (w >> 1)) + (w & 1)) * 32768, vo[0], vo[1], vo[2], vo[3]);
      wait_bar<0>();
      ST(8);
      HFLUSH();
      if (bid == 0 && w == 0) {
#pragma unroll 1
        for (unsigned sp = 0; sp < SPIN_CAP; ++sp) {
          asm volatile("" ::: "memory");
          const unsigned fo = (lane >> 3) * 4096 + (lane & 7) * 256;
          const u2 f0 = __builtin_amdgcn_raw_buffer_load_b64(ws, WS_FA + fo, 0, SC1), f1 = __builtin_amdgcn_raw_buffer_load_b64(ws, WS_FA + 32768 + fo, 0, SC1);
          const u2 f2_ = __builtin_amdgcn_raw_buffer_load_b64(ws, WS_FB + fo, 0, SC1), f3 = __builtin_amdgcn_raw_buffer_load_b64(ws, WS_FB + 32768 + fo, 0, SC1);
          if (__all(f0.x == tag && f0.y == ~tag && f1.x == tag && f1.y == ~tag && f2_.x == tag && f2_.y == ~tag && f3.x == tag && f3.y == ~tag)) break;
          __builtin_amdgcn_s_sleep(8);
        }
        if (lane == 0) __builtin_amdgcn_raw_buffer_store_b32(tag, ws, WS_EPOCH, 0, SC1);
      }
      return;
    }
    LOAD_EPOCH();
    const int hp = w & 1, th = w >> 1;
    unsigned warm0 = 0, warm1 = 0;
    if (TOUCH) {
      if (TD) __builtin_amdgcn_s_sleep(TD);
      warm0 = __builtin_amdgcn_raw_buffer_load_b32(ws, lane < 16 ? WS_FA + ((tile * 2 + th) * 16 + lane) * 128 : lane < 24 ? WS_Z + (cg * 4 + w) * 1024 + (lane - 16) * 128
                                                   : lane == 24 ? WS_KV + (cg * 4 + w) * 16384 : lane == 25 ? WS_O + tile * 131072 : 0x80000000u, 0, SC1);
      warm1 = __builtin_amdgcn_raw_buffer_load_b32(ro, lane < 2 ? (tile * 128 + w * 32 + lane * 16) * 4096 : 0x80000000u, 0, SC1);
    }
    const int qq = (lane >> 2) & 3, pp = lane & 3;
    const unsigned ax0 = l15 * 128 + ((q ^ (l15 >> 1)) << 4), ax1 = l15 * 128 + (((4 + q) ^ (l15 >> 1)) << 4);
    auto waddr = [&](int h) { return (8 * q + qq) * 128 + (((2 * h + (pp >> 1)) ^ ((((qq >> 1) & 1) | ((q & 1) << 1)) << 1)) << 4) + 8 * (pp & 1); };
    const unsigned aw = waddr(w), aq0 = waddr(2 * hp), aq1 = waddr(2 * hp + 1);
    f4 ka[8], va[8];
#pragma unroll
    for (int i = 0; i < 8; ++i) { ka[i] = (f4){EPS, EPS, EPS, EPS}; va[i] = (f4){0, 0, 0, 0}; }
    bf8 X0[4], X1[4], W0[2], W1[2];
    auto rdx = [&](bf8 (&X)[4], int kk, int ks, unsigned off) {
#pragma unroll
      for (int i = 0; i < 4; ++i) X[i] = ldsr(L + kk * 16384 + (ks ? ax1 : ax0) + off + i * 2048);
    };
    auto rdw = [&](bf8 (&W)[2], int kk, int ks) {
#pragma unroll
      for (int i = 0; i < 2; ++i) { const char LAS* p = L + WBUF[kk] + aw + ks * 4096 + i * 8192; W[i] = ldstr(p, p + 512); }
    };
    auto mm = [&](const bf8 (&X)[4], const bf8 (&W)[2], int h) {
#pragma unroll
      for (int i = 0; i < 4; ++i) {
        ka[4 * h + i] = mfma16(X[i], W[0], ka[4 * h + i]);
        va[4 * h + i] = mfma16(X[i], W[1], va[4 * h + i]);
      }
    };
#define ILV(nr, nm) do { _Pragma("unroll") for (int i_ = 0; i_ < nr; ++i_) { SG(0x008, 1); SG(0x100, 1); } if (nm > nr) SG(0x008, nm - nr); } while (0)
    asm volatile("s_waitcnt lgkmcnt(0)\n\ts_barrier" : "+s"(epoch) : "v"(warm0), "v"(warm1), "v"(cpf) : "memory");
    ST(1);
    const unsigned tag = epoch + 1, t = tag & 0xffu;
    rdw(W0, 0, 0); rdx(X0, 0, 0, 0);
    SB();
#pragma unroll
    for (int kk = 0; kk < 8; ++kk) {
      rdx(X1, kk, 0, 8192); mm(X0, W0, 0); ILV(4, 8); SB(); PACE(AN);
      rdw(W1, kk, 1); rdx(X0, kk, 1, 0); mm(X1, W0, 1); ILV(8, 8); SB(); PACE(AN);
      rdx(X1, kk, 1, 8192); mm(X0, W1, 0); ILV(4, 8); SB();
      if (kk < 7) { lbar(); rdw(W0, kk + 1, 0); rdx(X0, kk + 1, 0, 0); mm(X1, W1, 1); ILV(8, 8); }
      else mm(X1, W1, 1);
      SB();
    }
    ST(2);
    const unsigned xq = th * 8192;
    auto rdq = [&](bf8 (&X)[4], bf8 (&W)[2], int s) {
      const char LAS* p = L + QBUF[s >> 2] + ((s >> 1) & 1) * 8192 + (s & 1) * 4096;
      W[0] = ldstr(p + aq0, p + aq0 + 512); W[1] = ldstr(p + aq1, p + aq1 + 512);
      rdx(X, s < 14 ? (s >> 1) + 1 : 1, s & 1, xq);
    };
    {
      const int head = cg * 4 + w;
      f4 kv = {0, 0, 0, 0}, zz = {0, 0, 0, 0};
      const u4 ones = {0x3f803f80u, 0x3f803f80u, 0x3f803f80u, 0x3f803f80u};
#pragma unroll
      for (int m = 0; m < 4; ++m) {
        const f4 a = ka[2 * m], b = ka[2 * m + 1], c = va[2 * m], d = va[2 * m + 1];
        const u4 pa = {phi2(a[0], a[1]), phi2(a[2], a[3]), phi2(b[0], b[1]), phi2(b[2], b[3])};
        const u4 pb = {pk2(c[0], c[1]), pk2(c[2], c[3]), pk2(d[0], d[1]), pk2(d[2], d[3])};
        kv = mfma16(asbf8(pa), asbf8(pb), kv);
        zz = mfma16(asbf8(pa), asbf8(ones), zz);
      }
      const u4 skv = {tagv(kv[0], t), __float_as_uint(kv[1]), tagv(kv[2], t), __float_as_uint(kv[3])};
      __builtin_amdgcn_raw_buffer_store_b128(skv, ws, WS_KV + (head * 16 + tile) * 1024 + lane * 16, 0, SC1);
      const u4 sz = {tagv(zz[0], t), __float_as_uint(zz[1]), tagv(zz[2], t), __float_as_uint(zz[3])};
      __builtin_amdgcn_raw_buffer_store_b128(sz, ws, l15 == 0 ? WS_Z + (head * 16 + tile) * 64 + q * 16 : 0x80000000u, 0, SC1);
    }
    SB();
    ST(3);
    lbar();
    rdq(X0, W0, 0);
    SB();
    f4 qa[2][4];
#pragma unroll
    for (int i = 0; i < 8; ++i) qa[i >> 2][i & 3] = (f4){EPS, EPS, EPS, EPS};
    u4 gc[8];
    const unsigned gb = WS_KV + (cg * 4 + w) * 16384 + lane * 16, g2 = (((jj >> 3) + 2) & 3) * 4096, g3 = (((jj >> 3) + 3) & 3) * 4096;
    auto ldg = [&]() {
#pragma unroll
      for (int i = 0; i < 8; ++i) gc[i] = __builtin_amdgcn_raw_buffer_load_b128(ws, gb + (i & 3) * 1024, i < 4 ? g2 : g3, SC1);
    };
    auto mq = [&](const bf8 (&X)[4], const bf8 (&W)[2]) {
#pragma unroll
      for (int i = 0; i < 4; ++i) { qa[0][i] = mfma16(W[0], X[i], qa[0][i]); qa[1][i] = mfma16(W[1], X[i], qa[1][i]); }
    };
#pragma unroll
    for (int s = 0; s < 16; s += 2) {
      rdq(X1, W1, s + 1); mq(X0, W0); ILV(8, 8); SB();
      if (s == 2 || s == 6) lbar();
      if (s == 6) { ldg(); SB(); }
      if (s < 14) { rdq(X0, W0, s + 2); mq(X1, W1); ILV(8, 8); }
      else mq(X1, W1);
      SB();
    }
    ST(4);
    u2 pq[2][4];
#pragma unroll
    for (int i = 0; i < 8; ++i) pq[i >> 2][i & 3] = (u2){phi2(qa[i >> 2][i & 3][0], qa[i >> 2][i & 3][1]), phi2(qa[i >> 2][i & 3][2], qa[i >> 2][i & 3][3])};
    unsigned ao[4], og[4];
#pragma unroll
    for (int i = 0; i < 4; ++i) og[i] = WS_O + ((tile * 8 + th * 4 + i) * 16 + cg * 2 + hp) * 1024 + lane * 16;
#pragma unroll
    for (int nt = 0; nt < 4; ++nt) ao[nt] = (4 * q + qq) * 128 + ((((2 * nt) ^ (((2 * q + (qq >> 1)) & 3) << 1)) + (pp >> 1)) << 4) + 8 * (pp & 1);
    SB();
    {
      f4 kvp;
#pragma unroll 1
      for (unsigned rep = 0; rep < SPIN_CAP; ++rep) {
        unsigned bad = 0;
        kvp = (f4){0, 0, 0, 0};
#pragma unroll
        for (int i = 0; i < 8; ++i) { gc[i].x ^= t; gc[i].z ^= t; bad |= gc[i].x | gc[i].z; kvp += __builtin_bit_cast(f4, gc[i]); }
        if (__all((bad & 0xffu) == 0)) break;
        asm volatile("" ::: "memory");
        ldg();
      }
      *(LAS f4*)(L + SCR + 4096 + w * 1024 + lane * 16) = kvp;
    }
    asm volatile("s_waitcnt lgkmcnt(0)\n\ts_barrier" : "+v"(pq[0][0]), "+v"(pq[0][1]), "+v"(pq[0][2]), "+v"(pq[0][3]), "+v"(pq[1][0]), "+v"(pq[1][1]), "+v"(pq[1][2]), "+v"(pq[1][3]),
                 "+v"(og[0]), "+v"(og[1]), "+v"(og[2]), "+v"(og[3]) :: "memory");
    ST(5);
    const unsigned ex = TRB + th * 16384 + hp * 1024 + lane * 16;
    const f4 zero4 = {0, 0, 0, 0}, eps4 = {EPS, EPS, EPS, EPS};
    f4 num[2][4], den[2][4];
#pragma unroll
    for (int h = 0; h < 2; ++h) {
      const f4 kv = *(const LAS f4*)(L + SCR + (2 * hp + h) * 1024 + lane * 16) + *(const LAS f4*)(L + SCR + 4096 + (2 * hp + h) * 1024 + lane * 16);
      const u2 zb = *(const LAS u2*)(L + SCR + 8192 + (2 * hp + h) * 512 + lane * 8), kb2 = {pk2(kv[0], kv[1]), pk2(kv[2], kv[3])};
#pragma unroll
      for (int i = 0; i < 4; ++i) { den[h][i] = mfma16k16(zb, pq[h][i], eps4); num[h][i] = mfma16k16(kb2, pq[h][i], zero4); }
    }
    SB();
#pragma unroll
    for (int i = 0; i < 4; ++i) {
      u4 so;
#pragma unroll
      for (int h = 0; h < 2; ++h) {
        const float rc = __builtin_amdgcn_rcpf(den[h][i][0]);
        const f4 n = num[h][i];
        so[2 * h] = pk2((f2){n[0], n[1]} * rc);
        so[2 * h + 1] = pk2((f2){n[2], n[3]} * rc);
      }
      __builtin_amdgcn_raw_buffer_store_b128(so, ws, og[i], 0, SC1);
      *(LAS u4*)(L + ex + (i >> 1) * 8192 + (i & 1) * 2048) = so;
    }
    lbar();
    ST(6);
    u4 of[32];
#pragma unroll
    for (int i = 0; i < 4; ++i) of[i] = *(const LAS u4*)(L + TRB + w * 8192 + ((i & 1) * 2 + (i >> 1)) * 1024 + lane * 16);
    if (!NODRAIN) drain();
    __builtin_amdgcn_raw_buffer_store_b64((u2){tag, ~tag}, ws, lane == 0 ? WS_FA + ((tile * 2 + th) * 16 + cg * 2 + hp) * 128 : 0x80000000u, 0, SC1);
    unsigned tw[4], trd[4], so_ = ((tile * 128 + w * 32 + q) * 1024 + cg * 64 + l15 * 4) * 4;
#pragma unroll
    for (int i = 0; i < 4; ++i) {
      tw[i] = TRB + w * 8192 + l15 * 256 + (((4 * i + q) ^ l15) << 4);
      trd[i] = TRB + w * 8192 + (4 * i + q) * 256 + ((l15 ^ (4 * i + q)) << 4);
      asm volatile("" : "+v"(tw[i]), "+v"(trd[i]));
    }
    asm volatile("s_barrier" : "+v"(so_) :: "memory");
    ST(7);
    f4 acc[2][4];
#pragma unroll
    for (int i = 0; i < 8; ++i) acc[i >> 2][i & 3] = zero4;
    const unsigned ob = WS_O + (tile * 8 + w * 2) * 16384 + lane * 16;
    bf8 wf[2][4];
    auto rdo = [&](int k) {
#pragma unroll
      for (int nt = 0; nt < 4; ++nt) { const char LAS* p = L + OBUF[k >> 2] + (k & 3) * 4096 + ao[nt]; wf[k & 1][nt] = ldstr(p, p + 2048); }
    };
    auto ldo = [&](int k) {
      const unsigned so = (2 * seq(k >> 1) + (k & 1)) * 1024;
      of[2 * k] = __builtin_amdgcn_raw_buffer_load_b128(ws, ob, so, PAY_AUX);
      of[2 * k + 1] = __builtin_amdgcn_raw_buffer_load_b128(ws, ob + 16384, so, PAY_AUX);
    };
    auto mo = [&](int k) {
#pragma unroll
      for (int nt = 0; nt < 4; ++nt) {
        acc[0][nt] = mfma16(wf[k & 1][nt], asbf8(of[2 * k]), acc[0][nt]);
        acc[1][nt] = mfma16(wf[k & 1][nt], asbf8(of[2 * k + 1]), acc[1][nt]);
      }
    };
    rdo(0); rdo(1); SB();
    mo(0); rdo(2); SB();
    mo(1); SB();
    if (FD) __builtin_amdgcn_s_sleep(FD);
    wait_flags(ws, WS_FA + ((tile * 2 + th) * 16 + l15) * 128, tag);
    if (DLA) __builtin_amdgcn_s_sleep(DLA);
    ST(8); CT(14);
#pragma unroll
    for (int k = 2; k < 2 + OD; ++k) ldo(k);
    SB();
#pragma unroll
    for (int k = 2; k < 16; ++k) {
      if (k + OD < 16) ldo(k + OD);
      if (k + 1 < 16) rdo(k + 1);
      mo(k);
#pragma unroll
      for (int i_ = 0; i_ < 8; ++i_) { SG(0x008, 1); SG(0x100, 1); if (i_ == 1 || i_ == 5) SG(0x020, 1); }
      SB();
    }
    ST(9); CT(15);
#pragma unroll
    for (int mt = 0; mt < 2; ++mt)
#pragma unroll
      for (int nt = 0; nt < 4; ++nt) {
        const f4 v = acc[mt][nt];
        const unsigned p0 = pk2(v[0], v[1]), p1 = pk2(v[2], v[3]);
        *(LAS f4*)(L + tw[nt] + mt * 4096) = (f4){bflo(p0), bfhi(p0), bflo(p1), bfhi(p1)};
      }
    u4 ov[8];
#pragma unroll
    for (int i = 0; i < 8; ++i) ov[i] = *(const LAS u4*)(L + trd[i & 3] + (i >> 2) * 4096);
    SB();
#pragma unroll
    for (int i = 0; i < 8; ++i) __builtin_amdgcn_raw_buffer_store_b128(ov[i], ro, so_, i * 16384, OUT_AUX);
    ST(10);
  } else {
    constexpr unsigned SLOT = 32768, SCR = SLOT, PAR = 4 * SLOT;
    const int wp = w & 1, wt = w >> 1;
    const unsigned ecol = WS_EST + (cg * 64 + wp * 32 + l31) * 8;
    if (helper) {
      unsigned vx[4], vw[4], vc[4];
#pragma unroll
      for (int i = 0; i < 4; ++i) {
        const unsigned sw = (c8 ^ ((4 * i + (r8 >> 1)) & 7)) << 4;
        vx[i] = (64 * (w >> 1) + 32 * (r8 >> 2) + 16 * (r8 & 1) + 8 * (w & 1) + ((r8 >> 1) & 1) + 2 * i) * 1024 + sw - 1024 * i;
        vw[i] = (16 * (i >> 1) + 8 * (r8 >> 2) + 4 * (i & 1) + (r8 & 3)) * 1024 + sw - 1024 * i;
        vc[i] = (8 * i + r8) * 1024 + sw - 1024 * i;
      }
      const GAS char* xbase = (const GAS char*)xb + (size_t)tile * 131072;
      const GAS char* wbase = (const GAS char*)((w & 1) ? bim : bre) + (cg * 64 + (w >> 1) * 32) * 1024;
      auto issue = [&](int kk) {
        const int kc = (kk + rot) & 7;
        const unsigned s = L0 + (kk % 5) * SLOT + w * 4096;
        dmax(s, xbase + kc * 128, vx[0], vx[1], vx[2], vx[3]);
        dma4(s + 16384, wbase + kc * 128, vw[0], vw[1], vw[2], vw[3]);
      };
      ST(9); issue(0); ST(10); issue(1); ST(11);
      LOAD_EPOCH();
      const GAS char* cbase = (const GAS char*)((w & 1) ? cim : cre) + cg * 65536;
      auto issue_c = [&](int piece) {
        const unsigned s = L0 + ((piece + 3) % 5) * SLOT + w * 8192;
        const GAS char* cb = cbase + seq(2 * piece + (w >> 1)) * 128;
        dma4(s, cb, vc[0], vc[1], vc[2], vc[3]);
        dma4(s + 4096, cb + 32768, vc[0], vc[1], vc[2], vc[3]);
      };
      asm volatile("s_waitcnt vmcnt(8) lgkmcnt(0)\n\ts_barrier" : "+s"(epoch) :: "memory");
      issue(2);
      const f4 par = *(const LAS f4*)(L + PAR + w * 1024 + lane * 16);
      const f2 l64 = {par[0], par[1]}, l128 = {par[2], par[3]};
      f2 l256 = cmul(l128, l128);
      f2 wgt = hh ? l64 : (f2){1.f, 0.f};
      if (wt) wgt = cmul(wgt, l128);
      const unsigned tag = epoch + 1, t = tag & 0xffu;
      unsigned eo[8], et[8];
#pragma unroll
      for (int m = 0; m < 8; ++m) {
        const int i = 2 * tile - 1 - (2 * wt + hh) - 4 * m;
        eo[m] = i >= 0 ? ecol + i * 4096 : 0x80000000u;
        et[m] = i >= 0 ? t : 0u;
        asm volatile("" : "+v"(eo[m]), "+v"(et[m]));
      }
      asm volatile("" : "+v"(l256), "+v"(wgt));
      wait_bar<8>(); issue(3);
      wait_bar<8>(); issue(4);
      wait_bar<8>(); issue(5);
      wait_bar<8>(); issue(6);
      wait_bar<8>(); issue(7);
      wait_bar<8>(); issue_c(0);
      wait_bar<8>(); ST(1);
      if (NC_EARLY > 1) issue_c(1);
      if (NC_EARLY > 2) issue_c(2);
      ST(2);
      if (PD) { asm volatile("s_barrier" ::: "memory"); __builtin_amdgcn_s_sleep(PD); }
      f2 cin;
#pragma unroll 1
      for (unsigned sp = 0; sp < SPIN_CAP; ++sp) {
      asm volatile("" ::: "memory");
        u2 g[8];
#pragma unroll
        for (int m = 0; m < 8; ++m) g[m] = __builtin_amdgcn_raw_buffer_load_b64(ws, eo[m], 0, SC1);
        unsigned bad = 0;
        cin = (f2){0, 0};
#pragma unroll
        for (int m = 7; m >= 0; --m) {
          const unsigned cx = g[m].x ^ et[m];
          bad |= cx;
          cin = cfma(l256, cin, (f2){__uint_as_float(cx), __uint_as_float(g[m].y)});
        }
        if (__all((bad & 0xffu) == 0)) break;
      }
      ST(5);
      cin = cmul(cin, wgt);
      f2 chi;
      const f2 clo = swap_lo(cin, chi);
      if (hh == 0) *(LAS f2*)(L + SCR + (wp * 2 + wt) * 256 + l31 * 8) = clo + chi;
      ST(6);
      lbar();
      if (WOX & 2) issue_c(2);
      lbar();
      ST(7);
      if (NC_EARLY <= 1) issue_c(1);
      if (NC_EARLY <= 2 && !(WOX & 2)) issue_c(2);
      issue_c(3);
      wait_bar<0>();
      ST(8); HFLUSH();
      return;
    }
    LOAD_EPOCH();
    const int pl = cg * 64 + wp * 32 + ((l31 & 0x13) | ((l31 & 4) << 1) | ((l31 & 8) >> 1));
    const unsigned plr = lre[pl], pli = lim[pl], pdt = ldt[pl];
    u4 dv[4];
#pragma unroll
    for (int i = 0; i < 4; ++i) dv[i] = *(const GAS u4*)(dsk + cg * 64 + 16 * i + 8 * hh);
    unsigned warm0 = 0, warm1 = 0;
    if (TOUCH) {
      warm0 = __builtin_amdgcn_raw_buffer_load_b32(ws, lane < 16 ? WS_FB + ((tile * 2 + (w >> 1)) * 16 + lane) * 128 : lane == 16 ? WS_EST + tile * 8192
                                                   : lane == 17 ? WS_H + (tile * 4 + w) * 65536 : 0x80000000u, 0, SC1);
      warm1 = __builtin_amdgcn_raw_buffer_load_b32(ro, lane < 2 ? (tile * 128 + w * 32 + lane * 16) * 4096 + 2048 : 0x80000000u, 0, SC1);
    }
    SB();
    f2 lam, kap, l16, l32, l64, l128;
    {
      const float lr = bflo(plr), li = bflo(pli);
      const float dt = bflo(pk2(expf(bflo(pdt)), 0.f));
      const float mag = expf(dt * lr);
      float sn, cs;
      sincosf(dt * li, &sn, &cs);
      lam = (f2){mag * cs, mag * sn};
      const float inv = 2.f / (lr * lr + li * li), nr = lam.x - 1.f, ni = lam.y;
      kap = (f2){(nr * lr + ni * li) * inv, (ni * lr - nr * li) * inv};
      f2 t = cmul(lam, lam); t = cmul(t, t); t = cmul(t, t);
      l16 = cmul(t, t); l32 = cmul(l16, l16); l64 = cmul(l32, l32); l128 = cmul(l64, l64);
      *(LAS f4*)(L + PAR + w * 1024 + lane * 16) = (f4){l64.x, l64.y, l128.x, l128.y};
    }
    unsigned ar[4];
#pragma unroll
    for (int s = 0; s < 4; ++s) ar[s] = l31 * 128 + (((2 * s + hh) ^ ((lane >> 1) & 7)) << 4);
    f16v ur[2], ui[2];
#pragma unroll
    for (int i = 0; i < 16; ++i) { ur[0][i] = 0; ur[1][i] = 0; ui[0][i] = 0; ui[1][i] = 0; }
    bf8 F0[4], F1[4];
    auto rd = [&](bf8 (&F)[4], int kk, int s) {
      const char LAS* sx = L + (kk % 5) * SLOT + wt * 8192 + ar[s];
      const char LAS* sw = L + (kk % 5) * SLOT + 16384 + wp * 8192 + ar[s];
      F[0] = ldsr(sx); F[1] = ldsr(sw); F[2] = ldsr(sw + 4096); F[3] = ldsr(sx + 4096);
    };
    auto mm = [&](const bf8 (&F)[4]) {
      ur[0] = mfma32(F[0], F[1], ur[0]); ui[0] = mfma32(F[0], F[2], ui[0]);
      ur[1] = mfma32(F[3], F[1], ur[1]); ui[1] = mfma32(F[3], F[2], ui[1]);
    };
#define ILB() do { SG(0x008, 1); SG(0x100, 2); SG(0x008, 1); SG(0x100, 2); SG(0x008, 2); } while (0)
    asm volatile("s_waitcnt lgkmcnt(0)\n\ts_barrier" : "+s"(epoch), "+v"(lam), "+v"(kap), "+v"(l16), "+v"(l32), "+v"(l64), "+v"(dv[0]), "+v"(dv[1]), "+v"(dv[2]), "+v"(dv[3]) : "v"(warm0), "v"(warm1), "v"(cpf) : "memory");
    ST(1);
    const unsigned tag = epoch + 1, t = tag & 0xffu;
    rd(F0, 0, 0);
    SB();
#pragma unroll
    for (int kk = 0; kk < 8; ++kk) {
      rd(F1, kk, 1); mm(F0); ILB(); SB(); PACE(BN);
      rd(F0, kk, 2); mm(F1); ILB(); SB(); PACE(BN);
      rd(F1, kk, 3); mm(F0); ILB(); SB();
      if (kk < 7) { lbar(); if (kk == 6) ST(2); rd(F0, kk + 1, 0); mm(F1); ILB(); }
      else mm(F1);
      SB(); if (kk < 7) PACE(BN);
    }
    ST(3);
    f2 gr[16], gi[16];
#pragma unroll
    for (int a = 0; a < 16; ++a) {
      gr[a] = (f2){ur[a >> 3][2 * (a & 7)], ur[a >> 3][2 * (a & 7) + 1]};
      gi[a] = (f2){ui[a >> 3][2 * (a & 7)], ui[a >> 3][2 * (a & 7) + 1]};
    }
    {
      f2 LR = {lam.x, lam.x}, LI = {lam.y, lam.y}, LN = {-lam.y, -lam.y};
      asm volatile("s_nop 11" : "+v"(gr[0]), "+v"(gi[0]), "+v"(gr[8]), "+v"(gi[8]), "+v"(LR), "+v"(LI), "+v"(LN));
#pragma unroll
      for (int a = 1; a < 16; ++a)
        asm volatile("v_pk_fma_f32 %0, %6, %3, %0\n\tv_pk_fma_f32 %1, %5, %2, %1\n\tv_pk_fma_f32 %0, %4, %2, %0\n\tv_pk_fma_f32 %1, %4, %3, %1"
                     : "+v"(gr[a]), "+v"(gi[a]) : "v"(gr[a - 1]), "v"(gi[a - 1]), "v"(LR), "v"(LI), "v"(LN));
    }
    const f2 er = gr[15], ei = gi[15];
    const f2 e0 = {er.x, ei.x};
    const f2 e32 = cfma(l16, e0, (f2){er.y, ei.y});
    f2 ehi;
    const f2 elo = swap_lo(e32, ehi);
    const f2 e64 = cfma(l32, elo, ehi);
    __builtin_amdgcn_raw_buffer_store_b64((u2){tagv(e64.x, t), __float_as_uint(e64.y)}, ws, hh == 0 ? ecol + (tile * 2 + wt) * 4096 : 0x80000000u, 0, SC1);
    if (wt == 0 && hh == 0) *(LAS f2*)(L + SCR + 1024 + wp * 256 + l31 * 8) = e64;
    if (PD) asm volatile("s_barrier" ::: "memory");
    SB();
    ST(4);
    auto tokr = [&](int r) { return tile * 128 + 64 * (w >> 1) + 32 * ((r >> 2) & 1) + 16 * (r & 1) + 8 * (w & 1) + ((r >> 1) & 1) + 2 * (r >> 3); };
    u4 xd[4];
#pragma unroll
    for (int i = 0; i < 4; ++i) xd[i] = *(const GAS u4*)(xb + tokr(l31) * 512 + cg * 64 + 16 * i + 8 * hh);
    SB();
    {
      const f2 KR = {kap.x, kap.x}, KI = {kap.y, kap.y};
#pragma unroll
      for (int a = 0; a < 16; ++a) {
        const f2 xr = gr[a], xi = gi[a];
        gr[a] = fma2(KR, xr, -KI * xi); gi[a] = fma2(-KR, xi, -KI * xr);
        asm volatile("" : "+v"(gr[a]), "+v"(gi[a]));
      }
    }
    f2 pw[16];
    pw[0] = (f2){lam.x, -lam.y};
#pragma unroll
    for (int k = 1; k < 16; ++k) { pw[k] = cmul(pw[k >> 1], pw[(k - 1) >> 1]); asm volatile("" : "+v"(pw[k])); }
    u4 idn[2];
#pragma unroll
    for (int s = 0; s < 2; ++s)
#pragma unroll
      for (int jq = 0; jq < 4; ++jq) {
        const int r0 = 16 * s + 8 * (jq >> 1) + 4 * hh + 2 * (jq & 1);
        idn[s][jq] = (l31 == r0 ? 0x3f80u : 0u) | (l31 == r0 + 1 ? 0x3f800000u : 0u);
      }
    asm volatile("" : "+v"(idn[0]), "+v"(idn[1]));
#pragma unroll
    for (int i = 0; i < 4; ++i) {
#pragma unroll
      for (int j = 0; j < 4; ++j) xd[i][j] = pk2(bflo(xd[i][j]) * bflo(dv[i][j]), bfhi(xd[i][j]) * bfhi(dv[i][j]));
      asm volatile("" : "+v"(xd[i]));
    }
    f2 m0 = hh ? cmul(kap, l32) : kap, b0 = hh ? cmul(kap, elo) : (f2){0.f, 0.f};
    f2 m1 = cmul(l16, m0), b1 = cfma(l16, b0, cmul(kap, e0));
    unsigned ho[2];
    ho[0] = WS_H + (tile * 4 + wt * 2) * 65536 + (cg * 2 + wp) * 4096 + lane * 16; ho[1] = ho[0] + 65536;
    asm volatile("s_waitcnt lgkmcnt(0)\n\ts_barrier" : "+v"(ho[0]), "+v"(ho[1]), "+v"(m0), "+v"(b0), "+v"(m1), "+v"(b1) :: "memory");
    ST(5);
    f2 cin = *(const LAS f2*)(L + SCR + wp * 512 + l31 * 8) + *(const LAS f2*)(L + SCR + wp * 512 + 256 + l31 * 8);
    const f2 esib = *(const LAS f2*)(L + SCR + 1024 + wp * 256 + l31 * 8);
    if (wt) cin = cfma(l64, cin, esib);
    const f2 d0 = cfma(m0, cin, b0), d1 = cfma(m1, cin, b1);
    const f2 DR = {d0.x, d1.x}, DJ = {-d0.y, -d1.y};
    f16v zero16;
#pragma unroll
    for (int i = 0; i < 16; ++i) zero16[i] = 0;
    const unsigned ex = 2 * SLOT + wt * 16384 + wp * 4096 + lane * 16;
    auto fix = [&](int m, int c) {
      u4 a[2];
#pragma unroll
      for (int i = 0; i < 8; ++i) {
        const f2 P = pw[8 * m + i], PR = {P.x, P.x}, PI = {P.y, P.y};
        const f2 h = c ? fma2(PI, DR, fma2(PR, DJ, gi[8 * m + i])) : fma2(-PI, DJ, fma2(PR, DR, gr[8 * m + i]));
        a[i >> 2][i & 3] = pk2(h);
      }
      return mfma32(asbf8(a[1]), asbf8(idn[1]), mfma32(asbf8(a[0]), asbf8(idn[0]), zero16));
    };
    u4 ow[8];
    auto put = [&](const f16v& T, int m, int c) {
      const u4 o0 = ow[4 * m + 2 * c] = (u4){pk2(T[0], T[1]), pk2(T[2], T[3]), pk2(T[4], T[5]), pk2(T[6], T[7])};
      const u4 o1 = ow[4 * m + 2 * c + 1] = (u4){pk2(T[8], T[9]), pk2(T[10], T[11]), pk2(T[12], T[13]), pk2(T[14], T[15])};
      __builtin_amdgcn_raw_buffer_store_b128(o0, ws, ho[m] + c * 2048, 0, SC1);
      __builtin_amdgcn_raw_buffer_store_b128(o1, ws, ho[m] + c * 2048 + 1024, 0, SC1);
    };
    {
      const f16v T0 = fix(0, 0); SB();
      const f16v T1 = fix(0, 1); SB();
      put(T0, 0, 0);
      const f16v T2 = fix(1, 0); SB();
      put(T1, 0, 1);
      const f16v T3 = fix(1, 1); SB();
      put(T2, 1, 0); SB();
      put(T3, 1, 1);
    }
    SB();
#pragma unroll
    for (int i = 0; i < 8; ++i) *(LAS u4*)(L + ex + (i >> 2) * 8192 + (i & 3) * 1024) = ow[i];
    lbar();
    ST(6);
    if (!NODRAIN) drain();
    __builtin_amdgcn_raw_buffer_store_b64((u2){tag, ~tag}, ws, lane == 0 ? WS_FB + ((tile * 2 + wt) * 16 + cg * 2 + wp) * 128 : 0x80000000u, 0, SC1);
    ST(7);
    const unsigned hb = WS_H + (tile * 4 + w) * 65536 + lane * 16;
    u4 hf[64];
    bf8 cf[2][4];
    f16v acc[2];
    auto ldh = [&](int i) { hf[i] = __builtin_amdgcn_raw_buffer_load_b128(ws, hb + (i & 7) * 1024, seq(i >> 3) * 8192, PAY_AUX); };
    auto rdc = [&](int g) {
      const int tt = g >> 2, i = (g & 3) * 2;
      const char LAS* cb = L + (((tt >> 1) + 3) % 5) * SLOT + (tt & 1) * 16384 + ((i >> 1) & 1) * 8192;
      cf[g & 1][0] = ldsr(cb + ar[(i >> 2) * 2]); cf[g & 1][1] = ldsr(cb + ar[(i >> 2) * 2] + 4096);
      cf[g & 1][2] = ldsr(cb + ar[(i >> 2) * 2 + 1]); cf[g & 1][3] = ldsr(cb + ar[(i >> 2) * 2 + 1] + 4096);
    };
    auto mh = [&](int g) {
      acc[0] = mfma32(cf[g & 1][0], asbf8(hf[2 * g]), acc[0]);
      acc[1] = mfma32(cf[g & 1][1], asbf8(hf[2 * g]), acc[1]);
      acc[0] = mfma32(cf[g & 1][2], asbf8(hf[2 * g + 1]), acc[0]);
      acc[1] = mfma32(cf[g & 1][3], asbf8(hf[2 * g + 1]), acc[1]);
    };
#pragma unroll
    for (int i = 0; i < 8; ++i) hf[i] = *(const LAS u4*)(L + 2 * SLOT + w * 8192 + i * 1024 + lane * 16);
    rdc(0);
    {
      u4 ida[2];
#pragma unroll
      for (int s = 0; s < 2; ++s)
#pragma unroll
        for (int jq = 0; jq < 4; ++jq) {
          const int r0 = 16 * s + 8 * hh + 2 * jq;
          ida[s][jq] = (l31 == r0 ? 0x3f80u : 0u) | (l31 == r0 + 1 ? 0x3f800000u : 0u);
        }
      acc[0] = mfma32(asbf8(ida[1]), asbf8(xd[1]), mfma32(asbf8(ida[0]), asbf8(xd[0]), zero16));
      acc[1] = mfma32(asbf8(ida[1]), asbf8(xd[3]), mfma32(asbf8(ida[0]), asbf8(xd[2]), zero16));
    }
    SB();
#pragma unroll
    for (int g = 0; g < 4; ++g) { rdc(g + 1); mh(g); ILB(); SB(); }
    unsigned tw[8], trd[4], so_ = ((tile * 128 + 64 * (w >> 1) + 8 * (w & 1) + 16 * (q & 1) + (q >> 1)) * 1024 + 512 + cg * 64 + l15 * 4) * 4;
#pragma unroll
    for (int i = 0; i < 8; ++i) { tw[i] = 2 * SLOT + w * 8192 + l31 * 256 + (((2 * i + hh) ^ l15) << 4); asm volatile("" : "+v"(tw[i])); }
#pragma unroll
    for (int i = 0; i < 4; ++i) { trd[i] = 2 * SLOT + w * 8192 + (4 * i + q) * 256 + ((l15 ^ (4 * i + q)) << 4); asm volatile("" : "+v"(trd[i])); }
    asm volatile("s_barrier" : "+v"(so_) :: "memory");
    if (FD) __builtin_amdgcn_s_sleep(FD);
    wait_flags(ws, WS_FB + ((tile * 2 + (w >> 1)) * 16 + l15) * 128, tag);
    if (DLB) __builtin_amdgcn_s_sleep(DLB);
    ST(8); CT(14);
#pragma unroll
    for (int i = 8; i < 8 + HD; ++i) ldh(i);
    SB();
#pragma unroll
    for (int g = 4; g < 32; ++g) {
      if (2 * g + HD < 64) { ldh(2 * g + HD); ldh(2 * g + HD + 1); }
      if (g + 1 < 32) rdc(g + 1);
      mh(g);
      SG(0x008, 1); SG(0x100, 2); SG(0x008, 1); SG(0x100, 2); SG(0x008, 1); SG(0x020, 1); SG(0x008, 1); SG(0x020, 1);
      SB(); if (g < 30) PACE(HN);
    }
    ST(9); CT(15);
#pragma unroll
    for (int i = 0; i < 8; ++i)
      *(LAS f4*)(L + tw[i]) = (f4){acc[i >> 2][4 * (i & 3)], acc[i >> 2][4 * (i & 3) + 1], acc[i >> 2][4 * (i & 3) + 2], acc[i >> 2][4 * (i & 3) + 3]};
    u4 ov[8];
#pragma unroll
    for (int i = 0; i < 8; ++i) ov[i] = *(const LAS u4*)(L + trd[i & 3] + (i >> 2) * 4096);
    SB();
#pragma unroll
    for (int i = 0; i < 8; ++i) __builtin_amdgcn_raw_buffer_store_b128(ov[i], ro, so_, (32 * (i & 1) + 2 * (i >> 1)) * 4096, OUT_AUX);
    ST(10);
  }
}

extern "C" void kernel_launch(void* const* d_in, const int* in_sizes, int n_in, void* d_out, int out_size, void* d_ws, size_t ws_size,
                              hipStream_t stream) {
  (void)hipFuncSetAttribute((const void*)fused_kernel, hipFuncAttributeMaxDynamicSharedMemorySize, LDS_BYTES);
  fused_kernel<<<256, 512, LDS_BYTES, stream>>>((const u16*)d_in[0], (const u16*)d_in[1], (const u16*)d_in[2], (const u16*)d_in[3],
                                               (const u16*)d_in[4], (const u16*)d_in[5], (const u16*)d_in[6], (const u16*)d_in[7],
                                               (const u16*)d_in[8], (const u16*)d_in[9], (const u16*)d_in[10], (const u16*)d_in[11],
                                               (float*)d_out, (char*)d_ws);
}
```

```cpp
#define BN 10
#define AN 8
#include <hip/hip_runtime.h>
#include <hip/hip_bf16.h>

#define DI __device__ __forceinline__
#define LAS __attribute__((address_space(3)))
#define GAS __attribute__((address_space(1)))
typedef unsigned short u16;
typedef unsigned long long u64;
typedef __bf16 bf8 __attribute__((ext_vector_type(8)));
typedef __bf16 bf2 __attribute__((ext_vector_type(2)));
typedef short s4 __attribute__((ext_vector_type(4)));
typedef short s8 __attribute__((ext_vector_type(8)));
typedef float f16v __attribute__((ext_vector_type(16)));
typedef float f4 __attribute__((ext_vector_type(4)));
typedef float f2 __attribute__((ext_vector_type(2)));
typedef unsigned u4 __attribute__((ext_vector_type(4)));
typedef unsigned u2 __attribute__((ext_vector_type(2)));

constexpr unsigned WS_EPOCH = 0;
constexpr unsigned WS_FA = 4096;
constexpr unsigned WS_FB = WS_FA + 65536;
constexpr unsigned WS_EST = WS_FB + 65536;
constexpr unsigned WS_Z = WS_EST + 131072;
constexpr unsigned WS_KV = WS_Z + 32768;
constexpr unsigned WS_O = 1u << 20;
constexpr unsigned WS_H = 3u << 20;
constexpr unsigned WS_END = 7u << 20;
constexpr unsigned WS_STAMP = 8u << 20;
constexpr int LDS_BYTES = 160 * 1024;
constexpr unsigned SPIN_CAP = 200000;
constexpr int SC1 = 16;

#define ROT 8
#define NC_EARLY 2
#define OUT_AUX 16
#define PAY_AUX 16
#define PULL 0
#define TOUCH 1
#define CPF 11
#define HROT 1
#define XNT 0
#define PD 4
#define FD 0
#define TD 0
#define NODRAIN 0
#define WOX 2
#define DLA 0
#define DLB 0
#define QN 0
#define HN 0
#define PRI 2
#define OD 6
#define HD 12

#define CT(i) do {} while (0)
#define ST(i) do {} while (0)
#define HFLUSH() do {} while (0)
#define SB() __builtin_amdgcn_sched_barrier(0)
#define PACE(n) do { if (n) { asm volatile("s_nop %0" :: "i"((n) > 16 ? 15 : ((n) ? (n) - 1 : 0))); if ((n) > 16) asm volatile("s_nop %0" :: "i"((n) > 16 ? (n) - 17 : 0)); SB(); } } while (0)
#define SG(m, n) __builtin_amdgcn_sched_group_barrier(m, n, 0)

DI unsigned pk2(float a, float b) { f2 v = {a, b}; return __builtin_bit_cast(unsigned, __builtin_convertvector(v, bf2)); }
DI unsigned pk2(f2 v) { return __builtin_bit_cast(unsigned, __builtin_convertvector(v, bf2)); }
DI float bflo(unsigned v) { return __uint_as_float(v << 16); }
DI float bfhi(unsigned v) { return __uint_as_float(v & 0xffff0000u); }
DI bf8 asbf8(u4 v) { return __builtin_bit_cast(bf8, v); }
DI f16v mfma32(bf8 a, bf8 b, f16v c) { return __builtin_amdgcn_mfma_f32_32x32x16_bf16(a, b, c, 0, 0, 0); }
DI f4 mfma16(bf8 a, bf8 b, f4 c) { return __builtin_amdgcn_mfma_f32_16x16x32_bf16(a, b, c, 0, 0, 0); }
DI f4 mfma16k16(u2 a, u2 b, f4 c) { return __builtin_amdgcn_mfma_f32_16x16x16bf16_1k(__builtin_bit_cast(s4, a), __builtin_bit_cast(s4, b), c, 0, 0, 0); }
DI bf8 ldsr(const char LAS* p) { return *(const LAS bf8*)p; }
DI bf8 ldstr(const char LAS* p0, const char LAS* p1) {
  s4 a = __builtin_amdgcn_ds_read_tr16_b64_v4i16((LAS s4*)p0), b = __builtin_amdgcn_ds_read_tr16_b64_v4i16((LAS s4*)p1);
  return __builtin_bit_cast(bf8, (s8)__builtin_shufflevector(a, b, 0, 1, 2, 3, 4, 5, 6, 7));
}
DI void dma4(unsigned lds, const GAS char* sb, unsigned v0, unsigned v1, unsigned v2, unsigned v3) {
  asm volatile("s_mov_b32 m0, %0\n\ts_nop 0\n\tglobal_load_lds_dwordx4 %1, %5\n\tglobal_load_lds_dwordx4 %2, %5 offset:1024\n\t"
               "global_load_lds_dwordx4 %3, %5 offset:2048\n\tglobal_load_lds_dwordx4 %4, %5 offset:3072"
               :: "s"(lds), "v"(v0), "v"(v1), "v"(v2), "v"(v3), "s"(sb) : "memory");
}
DI void dma4nt(unsigned lds, const GAS char* sb, unsigned v0, unsigned v1, unsigned v2, unsigned v3) {
  asm volatile("s_mov_b32 m0, %0\n\ts_nop 0\n\tglobal_load_lds_dwordx4 %1, %5 nt\n\tglobal_load_lds_dwordx4 %2, %5 offset:1024 nt\n\t"
               "global_load_lds_dwordx4 %3, %5 offset:2048 nt\n\tglobal_load_lds_dwordx4 %4, %5 offset:3072 nt"
               :: "s"(lds), "v"(v0), "v"(v1), "v"(v2), "v"(v3), "s"(sb) : "memory");
}
DI void dmax(unsigned lds, const GAS char* sb, unsigned v0, unsigned v1, unsigned v2, unsigned v3) { if (XNT) dma4nt(lds, sb, v0, v1, v2, v3); else dma4(lds, sb, v0, v1, v2, v3); }
DI void dma2(unsigned lds, const GAS char* sb, unsigned v0, unsigned v1) {
  asm volatile("s_mov_b32 m0, %0\n\ts_nop 0\n\tglobal_load_lds_dwordx4 %1, %3\n\tglobal_load_lds_dwordx4 %2, %3 offset:1024"
               :: "s"(lds), "v"(v0), "v"(v1), "s"(sb) : "memory");
}
DI void lbar() { asm volatile("s_waitcnt lgkmcnt(0)\n\ts_barrier" ::: "memory"); }
template <int N> DI void wait_bar() { asm volatile("s_waitcnt vmcnt(%0) lgkmcnt(0)\n\ts_barrier" :: "i"(N) : "memory"); }
DI void drain() { asm volatile("s_waitcnt vmcnt(0)" ::: "memory"); }
DI unsigned tagv(float x, unsigned t) { return (__float_as_uint(x) & 0xffffff00u) | t; }
DI f2 cmul(f2 a, f2 b) { return (f2){a.x * b.x - a.y * b.y, a.x * b.y + a.y * b.x}; }
DI f2 cfma(f2 a, f2 b, f2 c) { return (f2){fmaf(a.x, b.x, fmaf(-a.y, b.y, c.x)), fmaf(a.x, b.y, fmaf(a.y, b.x, c.y))}; }
DI f2 fma2(f2 a, f2 b, f2 c) { return __builtin_elementwise_fma(a, b, c); }
constexpr float EPS = 1e-6f, LOG2E = 1.44269504088896341f;
DI unsigned phi2(float s0, float s1) {
  const f2 a = fma2((f2){s0, s1}, (f2){LOG2E, LOG2E}, (f2){-EPS * LOG2E, -EPS * LOG2E});
  const f2 e = {__builtin_amdgcn_fmed3f(__builtin_amdgcn_exp2f(a.x), 0.f, 1.f), __builtin_amdgcn_fmed3f(__builtin_amdgcn_exp2f(a.y), 0.f, 1.f)};
  return pk2(e + (f2){__builtin_amdgcn_fmed3f(s0, EPS, __builtin_inff()), __builtin_amdgcn_fmed3f(s1, EPS, __builtin_inff())});
}
DI f2 swap_lo(f2 v, f2& hi) {
  const auto x = __builtin_amdgcn_permlane32_swap(__float_as_uint(v.x), __float_as_uint(v.x), false, false);
  const auto y = __builtin_amdgcn_permlane32_swap(__float_as_uint(v.y), __float_as_uint(v.y), false, false);
  hi = (f2){__uint_as_float(x[1]), __uint_as_float(y[1])};
  return (f2){__uint_as_float(x[0]), __uint_as_float(y[0])};
}
DI f2 swap16(f2 a, f2 b, f2& rb) {
  const auto x = __builtin_amdgcn_permlane16_swap(__float_as_uint(a.x), __float_as_uint(b.x), false, false);
  const auto y = __builtin_amdgcn_permlane16_swap(__float_as_uint(a.y), __float_as_uint(b.y), false, false);
  rb = (f2){__uint_as_float(x[1]), __uint_as_float(y[1])};
  return (f2){__uint_as_float(x[0]), __uint_as_float(y[0])};
}
DI void wait_flags(__amdgpu_buffer_rsrc_t ws, unsigned off, unsigned tag) {
#pragma unroll 1
  for (unsigned sp = 0; sp < SPIN_CAP; ++sp) {
      asm volatile("" ::: "memory");
    const u2 f = __builtin_amdgcn_raw_buffer_load_b64(ws, off, 0, SC1);
    if (__all(f.x == tag && f.y == ~tag)) break;
    __builtin_amdgcn_s_sleep(1);
  }
  asm volatile("" ::: "memory");
}

__global__ void __launch_bounds__(512, 2)
fused_kernel(const u16* a0, const u16* a1, const u16* a2, const u16* a3, const u16* a4, const u16* a5, const u16* a6, const u16* a7,
             const u16* a8, const u16* a9, const u16* a10, const u16* a11, float* a12, char* a13) {
  extern __shared__ __attribute__((aligned(16))) char smem_[];
  char LAS* L = (char LAS*)smem_;
  const unsigned L0 = (unsigned)(size_t)L;
  ST(0);
  const int lane = threadIdx.x & 63;
  const int w8 = __builtin_amdgcn_readfirstlane(threadIdx.x >> 6), w = w8 & 3;
  unsigned cpf = 0;
  if (CPF && w8 == 0) {
    u64 pc;
    asm volatile("s_getpc_b64 %0" : "=s"(pc));
    cpf = __builtin_amdgcn_raw_buffer_load_b32(__builtin_amdgcn_make_buffer_rsrc((char*)(pc & ~127ull), 0, CPF * 4096, 0x00020000),
                                               lane < CPF ? ((blockIdx.x >> 3) * CPF + lane) * 128 : 0x80000000u, 0, 0);
  }
  typedef u64 u64x8 __attribute__((ext_vector_type(8)));
  typedef u64 u64x4 __attribute__((ext_vector_type(4)));
  typedef u64 u64x2 __attribute__((ext_vector_type(2)));
  u64x8 ka; u64x4 kb; u64x2 kc_;
  asm volatile("s_load_dwordx16 %0, %3, 0x0\n\ts_load_dwordx8 %1, %3, 0x40\n\ts_load_dwordx4 %2, %3, 0x60\n\ts_waitcnt lgkmcnt(0)"
               : "=&s"(ka), "=&s"(kb), "=&s"(kc_) : "s"(__builtin_amdgcn_kernarg_segment_ptr()) : "memory");
  const GAS u16 *xa = (const GAS u16*)ka[0], *wqkv = (const GAS u16*)ka[1], *wo = (const GAS u16*)ka[2], *xb = (const GAS u16*)ka[3];
  const GAS u16 *lre = (const GAS u16*)ka[4], *lim = (const GAS u16*)ka[5], *bre = (const GAS u16*)ka[6], *bim = (const GAS u16*)ka[7];
  const GAS u16 *cre = (const GAS u16*)kb[0], *cim = (const GAS u16*)kb[1], *ldt = (const GAS u16*)kb[2], *dsk = (const GAS u16*)kb[3];
  float* out = (float*)kc_[0]; char* wsp = (char*)kc_[1];
  const bool helper = w8 >= 4;
  const int bid = blockIdx.x, xcd = bid & 7, jj = bid >> 3;
  const int tile = (xcd >> 1) * 4 + (jj >> 3), cg = jj & 7;
  const int rot = ROT == 1 ? ((cg + 3 * (jj >> 3) + xcd) & 7) : ROT == 7 ? (((cg & 3) * 2 + (xcd >> 1)) & 7) : ROT == 12 ? ((2 * (cg & 3) + 2 * (xcd >> 1)) & 7) : ROT == 16 ? ((2 * (cg & 3) + ((jj >> 3) & 1) + 2 * (xcd >> 1)) & 7) : ((((cg + (jj >> 3)) % 3) * 3 + (xcd >> 1)) & 7);
  auto seq = [&](int t) { const int b = 2 * (jj >> 3), e = t - 1; return t == 0 ? cg : (b + e + (e >= ((cg - b) & 7) ? 1 : 0)) & 7; };
  const __amdgpu_buffer_rsrc_t ws = __builtin_amdgcn_make_buffer_rsrc(wsp, 0, WS_END, 0x00020000);
  const __amdgpu_buffer_rsrc_t ro = __builtin_amdgcn_make_buffer_rsrc(out, 0, 2048 * 1024 * 4, 0x00020000);
  const int r8 = lane >> 3, c8 = lane & 7;
  const int q = lane >> 4, l15 = lane & 15, l31 = lane & 31, hh = lane >> 5;
  unsigned epoch;
#define LOAD_EPOCH() asm volatile("s_load_dword %0, %1, 0x0" : "=s"(epoch) : "s"(wsp) : "memory")

  if ((xcd & 1) == 0) {
    constexpr unsigned WA = 131072, WB = 147456, SCR = 32768, TRB = 81920;
    constexpr unsigned QBUF[4] = {WB, 0, WA, WB}, OBUF[4] = {32768, 49152, 65536, 0};
    constexpr unsigned WBUF[8] = {WA, WB, 7 * 16384, 0, WA, WB, 0, WA};
    if (helper) {
      const int head = cg * 4 + w;
      unsigned vx[4], vw[2], vo[4];
#pragma unroll
      for (int i = 0; i < 4; ++i) vx[i] = (32 * w + 8 * i + r8) * 1024 + ((c8 ^ ((4 * i + (r8 >> 1)) & 7)) << 4) - 1024 * i;
#pragma unroll
      for (int i = 0; i < 2; ++i) vw[i] = (16 * w + 8 * i + r8) * 3072 + ((c8 ^ ((((r8 >> 1) & 1) | (i << 1)) << 1)) << 4) - 1024 * i;
      const GAS char* xbase = (const GAS char*)xa + (size_t)tile * 131072;
      const GAS char* wbase = (const GAS char*)wqkv + cg * 128;
      auto isx = [&](int kk, int slot) { dmax(L0 + slot * 16384 + w * 4096, xbase + ((kk + rot) & 7) * 128, vx[0], vx[1], vx[2], vx[3]); };
      auto isw = [&](unsigned dst, int kk, int seg) { dma2(L0 + dst + w * 2048, wbase + ((kk + rot) & 7) * (64 * 3072) + seg * 1024, vw[0], vw[1]); };
      auto issue = [&](int kk) { isx(kk, kk); isw(WBUF[kk], kk, 1); isw(WBUF[kk] + 8192, kk, 2); };
      auto isq = [&](int p) { isw(QBUF[p], 2 * p + 1, 0); isw(QBUF[p] + 8192, 2 * p + 2, 0); };
      ST(9); issue(0); ST(10); issue(1); ST(11);
      LOAD_EPOCH();
#pragma unroll
      for (int i = 0; i < 4; ++i) vo[i] = (8 * i + r8) * 1024 + ((c8 ^ (((r8 >> 1) & 3) << 1)) << 4) - 1024 * i;
      const GAS char* obase = (const GAS char*)wo + cg * 128;
      asm volatile("s_waitcnt vmcnt(8) lgkmcnt(0)\n\ts_barrier" : "+s"(epoch) :: "memory");
      issue(2);
      wait_bar<8>(); issue(3);
      wait_bar<8>(); issue(4);
      wait_bar<8>(); issue(5);
      wait_bar<8>(); issue(6);
      wait_bar<8>(); issue(7);
      wait_bar<8>();
      unsigned pfo;
      asm volatile("global_load_dword %0, %1, %2" : "=v"(pfo) : "v"(((jj >> 3) * 128 + w * 32 + l31) * 1024), "s"(obase) : "memory");
      isq(0);
      wait_bar<5>(); isq(1);
      wait_bar<4>(); isq(2);
      wait_bar<4>(); isq(3); isx(0, 1);
      asm volatile("s_waitcnt vmcnt(0)\n\ts_barrier" : "+v"(pfo) :: "memory");
      if (PRI & 1) __builtin_amdgcn_s_setprio(3);
      ST(1);
      const unsigned tag = epoch + 1, t = tag & 0xffu;
      const GAS char* kvb_ = (const GAS char*)wsp + WS_KV + head * 16384;
      const GAS char* zb_ = (const GAS char*)wsp + WS_Z + head * 1024;
      const unsigned qr = (jj >> 3) * 4096;
      const unsigned vk0 = lane * 16 + qr, vk1 = lane * 16 + ((qr + 4096) & 12288), vz = l15 * 64 + q * 16;
      u4 g[8], gz;
#define KVL(i, v) asm volatile("global_load_dwordx4 %0, %1, %2 offset:%3 sc1" : "=v"(g[i]) : "v"(v), "s"(kvb_), "i"((i & 3) * 1024) : "memory")
      asm volatile("global_load_dwordx4 %0, %1, %2 sc1" : "=v"(gz) : "v"(vz), "s"(zb_) : "memory");
      KVL(0, vk0); KVL(1, vk0); KVL(2, vk0); KVL(3, vk0); KVL(4, vk1); KVL(5, vk1); KVL(6, vk1); KVL(7, vk1);
      auto zsum = [&]() {
        f4 z = __builtin_bit_cast(f4, gz);
#pragma unroll
        for (int r = 0; r < 4; ++r) {
          float v = z[r];
          v += __builtin_bit_cast(float, __builtin_amdgcn_mov_dpp(__builtin_bit_cast(int, v), 0x128, 0xf, 0xf, true));
          v += __builtin_bit_cast(float, __builtin_amdgcn_mov_dpp(__builtin_bit_cast(int, v), 0x124, 0xf, 0xf, true));
          v += __builtin_bit_cast(float, __builtin_amdgcn_mov_dpp(__builtin_bit_cast(int, v), 0x122, 0xf, 0xf, true));
          v += __builtin_bit_cast(float, __builtin_amdgcn_mov_dpp(__builtin_bit_cast(int, v), 0x121, 0xf, 0xf, true));
          z[r] = v;
        }
        return z;
      };
      asm volatile("s_waitcnt vmcnt(8)" : "+v"(gz) :: "memory");
      gz.x ^= t; gz.z ^= t;
      unsigned bad = gz.x | gz.z;
      f4 zz = zsum(), kv = {0, 0, 0, 0};
      asm volatile("" : "+v"(zz));
#define KVA(i) asm volatile("s_waitcnt vmcnt(%1)" : "+v"(g[i]) : "i"(7 - i) : "memory"); g[i].x ^= t; g[i].z ^= t; bad |= g[i].x | g[i].z; kv += __builtin_bit_cast(f4, g[i]); asm volatile("" : "+v"(kv), "+v"(bad))
      KVA(0); KVA(1); KVA(2); KVA(3); KVA(4); KVA(5); KVA(6); KVA(7);
      ST(3);
      if (!__all((bad & 0xffu) == 0)) {
#pragma unroll 1
        for (unsigned rep = 0; rep < SPIN_CAP; ++rep) {
          asm volatile("" ::: "memory");
          gz = __builtin_amdgcn_raw_buffer_load_b128(ws, WS_Z + head * 1024 + vz, 0, SC1);
#pragma unroll
          for (int i = 0; i < 8; ++i) g[i] = __builtin_amdgcn_raw_buffer_load_b128(ws, WS_KV + head * 16384 + (i < 4 ? vk0 : vk1) + (i & 3) * 1024, 0, SC1);
          gz.x ^= t; gz.z ^= t;
          bad = gz.x | gz.z;
#pragma unroll
          for (int i = 0; i < 8; ++i) { g[i].x ^= t; g[i].z ^= t; bad |= g[i].x | g[i].z; }
          if (__all((bad & 0xffu) == 0)) break;
        }
        zz = zsum();
        kv = (f4){0, 0, 0, 0};
#pragma unroll
        for (int i = 0; i < 8; ++i) kv += __builtin_bit_cast(f4, g[i]);
      }
      ST(5);
      *(LAS f4*)(L + SCR + w * 1024 + lane * 16) = kv;
      *(LAS u2*)(L + SCR + 8192 + w * 512 + lane * 8) = (u2){pk2(zz[0], zz[1]), pk2(zz[2], zz[3])};
      ST(6);
      lbar();
      ST(7);
      lbar();
#pragma unroll
      for (int i = 0; i < 4; ++i)
        dma4(L0 + OBUF[i] + w * 4096, obase + (2 * seq(2 * i + (w >> 1)) + (w & 1)) * 32768, vo[0], vo[1], vo[2], vo[3]);
      wait_bar<0>();
      ST(8);
      HFLUSH();
      if (bid == 0 && w == 0) {
#pragma unroll 1
        for (unsigned sp = 0; sp < SPIN_CAP; ++sp) {
          asm volatile("" ::: "memory");
          const unsigned fo = (lane >> 3) * 4096 + (lane & 7) * 256;
          const u2 f0 = __builtin_amdgcn_raw_buffer_load_b64(ws, WS_FA + fo, 0, SC1), f1 = __builtin_amdgcn_raw_buffer_load_b64(ws, WS_FA + 32768 + fo, 0, SC1);
          const u2 f2_ = __builtin_amdgcn_raw_buffer_load_b64(ws, WS_FB + fo, 0, SC1), f3 = __builtin_amdgcn_raw_buffer_load_b64(ws, WS_FB + 32768 + fo, 0, SC1);
          if (__all(f0.x == tag && f0.y == ~tag && f1.x == tag && f1.y == ~tag && f2_.x == tag && f2_.y == ~tag && f3.x == tag && f3.y == ~tag)) break;
          __builtin_amdgcn_s_sleep(8);
        }
        if (lane == 0) __builtin_amdgcn_raw_buffer_store_b32(tag, ws, WS_EPOCH, 0, SC1);
      }
      return;
    }
    LOAD_EPOCH();
    const int hp = w & 1, th = w >> 1;
    unsigned warm0 = 0, warm1 = 0;
    if (TOUCH) {
      if (TD) __builtin_amdgcn_s_sleep(TD);
      warm0 = __builtin_amdgcn_raw_buffer_load_b32(ws, lane < 16 ? WS_FA + ((tile * 2 + th) * 16 + lane) * 128 : lane < 24 ? WS_Z + (cg * 4 + w) * 1024 + (lane - 16) * 128
                                                   : lane == 24 ? WS_KV + (cg * 4 + w) * 16384 : lane == 25 ? WS_O + tile * 131072 : 0x80000000u, 0, SC1);
      warm1 = __builtin_amdgcn_raw_buffer_load_b32(ro, lane < 2 ? (tile * 128 + w * 32 + lane * 16) * 4096 : 0x80000000u, 0, SC1);
    }
    const int qq = (lane >> 2) & 3, pp = lane & 3;
    const unsigned ax0 = l15 * 128 + ((q ^ (l15 >> 1)) << 4), ax1 = l15 * 128 + (((4 + q) ^ (l15 >> 1)) << 4);
    auto waddr = [&](int h) { return (8 * q + qq) * 128 + (((2 * h + (pp >> 1)) ^ ((((qq >> 1) & 1) | ((q & 1) << 1)) << 1)) << 4) + 8 * (pp & 1); };
    const unsigned aw = waddr(w), aq0 = waddr(2 * hp), aq1 = waddr(2 * hp + 1);
    f4 ka[8], va[8];
#pragma unroll
    for (int i = 0; i < 8; ++i) { ka[i] = (f4){EPS, EPS, EPS, EPS}; va[i] = (f4){0, 0, 0, 0}; }
    bf8 X0[4], X1[4], W0[2], W1[2];
    auto rdx = [&](bf8 (&X)[4], int kk, int ks, unsigned off) {
#pragma unroll
      for (int i = 0; i < 4; ++i) X[i] = ldsr(L + kk * 16384 + (ks ? ax1 : ax0) + off + i * 2048);
    };
    auto rdw = [&](bf8 (&W)[2], int kk, int ks) {
#pragma unroll
      for (int i = 0; i < 2; ++i) { const char LAS* p = L + WBUF[kk] + aw + ks * 4096 + i * 8192; W[i] = ldstr(p, p + 512); }
    };
    auto mm = [&](const bf8 (&X)[4], const bf8 (&W)[2], int h) {
#pragma unroll
      for (int i = 0; i < 4; ++i) {
        ka[4 * h + i] = mfma16(X[i], W[0], ka[4 * h + i]);
        va[4 * h + i] = mfma16(X[i], W[1], va[4 * h + i]);
      }
    };
#define ILV(nr, nm) do { _Pragma("unroll") for (int i_ = 0; i_ < nr; ++i_) { SG(0x008, 1); SG(0x100, 1); } if (nm > nr) SG(0x008, nm - nr); } while (0)
    asm volatile("s_waitcnt lgkmcnt(0)\n\ts_barrier" : "+s"(epoch) : "v"(warm0), "v"(warm1), "v"(cpf) : "memory");
    ST(1);
    const unsigned tag = epoch + 1, t = tag & 0xffu;
    rdw(W0, 0, 0); rdx(X0, 0, 0, 0);
    SB();
#pragma unroll
    for (int kk = 0; kk < 8; ++kk) {
      rdx(X1, kk, 0, 8192); mm(X0, W0, 0); ILV(4, 8); SB(); PACE(AN);
      rdw(W1, kk, 1); rdx(X0, kk, 1, 0); mm(X1, W0, 1); ILV(8, 8); SB(); PACE(AN);
      rdx(X1, kk, 1, 8192); mm(X0, W1, 0); ILV(4, 8); SB();
      if (kk < 7) { lbar(); rdw(W0, kk + 1, 0); rdx(X0, kk + 1, 0, 0); mm(X1, W1, 1); ILV(8, 8); }
      else mm(X1, W1, 1);
      SB();
    }
    ST(2);
    const unsigned xq = th * 8192;
    auto rdq = [&](bf8 (&X)[4], bf8 (&W)[2], int s) {
      const char LAS* p = L + QBUF[s >> 2] + ((s >> 1) & 1) * 8192 + (s & 1) * 4096;
      W[0] = ldstr(p + aq0, p + aq0 + 512); W[1] = ldstr(p + aq1, p + aq1 + 512);
      rdx(X, s < 14 ? (s >> 1) + 1 : 1, s & 1, xq);
    };
    {
      const int head = cg * 4 + w;
      f4 kv = {0, 0, 0, 0}, zz = {0, 0, 0, 0};
      const u4 ones = {0x3f803f80u, 0x3f803f80u, 0x3f803f80u, 0x3f803f80u};
#pragma unroll
      for (int m = 0; m < 4; ++m) {
        const f4 a = ka[2 * m], b = ka[2 * m + 1], c = va[2 * m], d = va[2 * m + 1];
        const u4 pa = {phi2(a[0], a[1]), phi2(a[2], a[3]), phi2(b[0], b[1]), phi2(b[2], b[3])};
        const u4 pb = {pk2(c[0], c[1]), pk2(c[2], c[3]), pk2(d[0], d[1]), pk2(d[2], d[3])};
        kv = mfma16(asbf8(pa), asbf8(pb), kv);
        zz = mfma16(asbf8(pa), asbf8(ones), zz);
      }
      const u4 skv = {tagv(kv[0], t), __float_as_uint(kv[1]), tagv(kv[2], t), __float_as_uint(kv[3])};
      __builtin_amdgcn_raw_buffer_store_b128(skv, ws, WS_KV + (head * 16 + tile) * 1024 + lane * 16, 0, SC1);
      const u4 sz = {tagv(zz[0], t), __float_as_uint(zz[1]), tagv(zz[2], t), __float_as_uint(zz[3])};
      __builtin_amdgcn_raw_buffer_store_b128(sz, ws, l15 == 0 ? WS_Z + (head * 16 + tile) * 64 + q * 16 : 0x80000000u, 0, SC1);
    }
    SB();
    ST(3);
    lbar();
    rdq(X0, W0, 0);
    SB();
    f4 qa[2][4];
#pragma unroll
    for (int i = 0; i < 8; ++i) qa[i >> 2][i & 3] = (f4){EPS, EPS, EPS, EPS};
    u4 gc[8];
    const unsigned gb = WS_KV + (cg * 4 + w) * 16384 + lane * 16, g2 = (((jj >> 3) + 2) & 3) * 4096, g3 = (((jj >> 3) + 3) & 3) * 4096;
    auto ldg = [&]() {
#pragma unroll
      for (int i = 0; i < 8; ++i) gc[i] = __builtin_amdgcn_raw_buffer_load_b128(ws, gb + (i & 3) * 1024, i < 4 ? g2 : g3, SC1);
    };
    auto mq = [&](const bf8 (&X)[4], const bf8 (&W)[2]) {
#pragma unroll
      for (int i = 0; i < 4; ++i) { qa[0][i] = mfma16(W[0], X[i], qa[0][i]); qa[1][i] = mfma16(W[1], X[i], qa[1][i]); }
    };
#pragma unroll
    for (int s = 0; s < 16; s += 2) {
      rdq(X1, W1, s + 1); mq(X0, W0); ILV(8, 8); SB();
      if (s == 2 || s == 6) lbar();
      if (s == 6) { ldg(); SB(); }
      if (s < 14) { rdq(X0, W0, s + 2); mq(X1, W1); ILV(8, 8); }
      else mq(X1, W1);
      SB();
    }
    ST(4);
    u2 pq[2][4];
#pragma unroll
    for (int i = 0; i < 8; ++i) pq[i >> 2][i & 3] = (u2){phi2(qa[i >> 2][i & 3][0], qa[i >> 2][i & 3][1]), phi2(qa[i >> 2][i & 3][2], qa[i >> 2][i & 3][3])};
    unsigned ao[4], og[4];
#pragma unroll
    for (int i = 0; i < 4; ++i) og[i] = WS_O + ((tile * 8 + th * 4 + i) * 16 + cg * 2 + hp) * 1024 + lane * 16;
#pragma unroll
    for (int nt = 0; nt < 4; ++nt) ao[nt] = (4 * q + qq) * 128 + ((((2 * nt) ^ (((2 * q + (qq >> 1)) & 3) << 1)) + (pp >> 1)) << 4) + 8 * (pp & 1);
    SB();
    {
      f4 kvp;
#pragma unroll 1
      for (unsigned rep = 0; rep < SPIN_CAP; ++rep) {
        unsigned bad = 0;
        kvp = (f4){0, 0, 0, 0};
#pragma unroll
        for (int i = 0; i < 8; ++i) { gc[i].x ^= t; gc[i].z ^= t; bad |= gc[i].x | gc[i].z; kvp += __builtin_bit_cast(f4, gc[i]); }
        if (__all((bad & 0xffu) == 0)) break;
        asm volatile("" ::: "memory");
        ldg();
      }
      *(LAS f4*)(L + SCR + 4096 + w * 1024 + lane * 16) = kvp;
    }
    asm volatile("s_waitcnt lgkmcnt(0)\n\ts_barrier" : "+v"(pq[0][0]), "+v"(pq[0][1]), "+v"(pq[0][2]), "+v"(pq[0][3]), "+v"(pq[1][0]), "+v"(pq[1][1]), "+v"(pq[1][2]), "+v"(pq[1][3]),
                 "+v"(og[0]), "+v"(og[1]), "+v"(og[2]), "+v"(og[3]) :: "memory");
    ST(5);
    const unsigned ex = TRB + th * 16384 + hp * 1024 + lane * 16;
    const f4 zero4 = {0, 0, 0, 0}, eps4 = {EPS, EPS, EPS, EPS};
    f4 num[2][4], den[2][4];
#pragma unroll
    for (int h = 0; h < 2; ++h) {
      const f4 kv = *(const LAS f4*)(L + SCR + (2 * hp + h) * 1024 + lane * 16) + *(const LAS f4*)(L + SCR + 4096 + (2 * hp + h) * 1024 + lane * 16);
      const u2 zb = *(const LAS u2*)(L + SCR + 8192 + (2 * hp + h) * 512 + lane * 8), kb2 = {pk2(kv[0], kv[1]), pk2(kv[2], kv[3])};
#pragma unroll
      for (int i = 0; i < 4; ++i) { den[h][i] = mfma16k16(zb, pq[h][i], eps4); num[h][i] = mfma16k16(kb2, pq[h][i], zero4); }
    }
    SB();
#pragma unroll
    for (int i = 0; i < 4; ++i) {
      u4 so;
#pragma unroll
      for (int h = 0; h < 2; ++h) {
        const float rc = __builtin_amdgcn_rcpf(den[h][i][0]);
        const f4 n = num[h][i];
        so[2 * h] = pk2((f2){n[0], n[1]} * rc);
        so[2 * h + 1] = pk2((f2){n[2], n[3]} * rc);
      }
      __builtin_amdgcn_raw_buffer_store_b128(so, ws, og[i], 0, SC1);
      *(LAS u4*)(L + ex + (i >> 1) * 8192 + (i & 1) * 2048) = so;
    }
    lbar();
    ST(6);
    u4 of[32];
#pragma unroll
    for (int i = 0; i < 4; ++i) of[i] = *(const LAS u4*)(L + TRB + w * 8192 + ((i & 1) * 2 + (i >> 1)) * 1024 + lane * 16);
    if (!NODRAIN) drain();
    __builtin_amdgcn_raw_buffer_store_b64((u2){tag, ~tag}, ws, lane == 0 ? WS_FA + ((tile * 2 + th) * 16 + cg * 2 + hp) * 128 : 0x80000000u, 0, SC1);
    unsigned tw[4], trd[4], so_ = ((tile * 128 + w * 32 + q) * 1024 + cg * 64 + l15 * 4) * 4;
#pragma unroll
    for (int i = 0; i < 4; ++i) {
      tw[i] = TRB + w * 8192 + l15 * 256 + (((4 * i + q) ^ l15) << 4);
      trd[i] = TRB + w * 8192 + (4 * i + q) * 256 + ((l15 ^ (4 * i + q)) << 4);
      asm volatile("" : "+v"(tw[i]), "+v"(trd[i]));
    }
    asm volatile("s_barrier" : "+v"(so_) :: "memory");
    ST(7);
    f4 acc[2][4];
#pragma unroll
    for (int i = 0; i < 8; ++i) acc[i >> 2][i & 3] = zero4;
    const unsigned ob = WS_O + (tile * 8 + w * 2) * 16384 + lane * 16;
    bf8 wf[2][4];
    auto rdo = [&](int k) {
#pragma unroll
      for (int nt = 0; nt < 4; ++nt) { const char LAS* p = L + OBUF[k >> 2] + (k & 3) * 4096 + ao[nt]; wf[k & 1][nt] = ldstr(p, p + 2048); }
    };
    auto ldo = [&](int k) {
      const unsigned so = (2 * seq(k >> 1) + (k & 1)) * 1024;
      of[2 * k] = __builtin_amdgcn_raw_buffer_load_b128(ws, ob, so, PAY_AUX);
      of[2 * k + 1] = __builtin_amdgcn_raw_buffer_load_b128(ws, ob + 16384, so, PAY_AUX);
    };
    auto mo = [&](int k) {
#pragma unroll
      for (int nt = 0; nt < 4; ++nt) {
        acc[0][nt] = mfma16(wf[k & 1][nt], asbf8(of[2 * k]), acc[0][nt]);
        acc[1][nt] = mfma16(wf[k & 1][nt], asbf8(of[2 * k + 1]), acc[1][nt]);
      }
    };
    rdo(0); rdo(1); SB();
    mo(0); rdo(2); SB();
    mo(1); SB();
    if (FD) __builtin_amdgcn_s_sleep(FD);
    wait_flags(ws, WS_FA + ((tile * 2 + th) * 16 + l15) * 128, tag);
    if (DLA) __builtin_amdgcn_s_sleep(DLA);
    ST(8); CT(14);
#pragma unroll
    for (int k = 2; k < 2 + OD; ++k) ldo(k);
    SB();
#pragma unroll
    for (int k = 2; k < 16; ++k) {
      if (k + OD < 16) ldo(k + OD);
      if (k + 1 < 16) rdo(k + 1);
      mo(k);
#pragma unroll
      for (int i_ = 0; i_ < 8; ++i_) { SG(0x008, 1); SG(0x100, 1); if (i_ == 1 || i_ == 5) SG(0x020, 1); }
      SB();
    }
    ST(9); CT(15);
#pragma unroll
    for (int mt = 0; mt < 2; ++mt)
#pragma unroll
      for (int nt = 0; nt < 4; ++nt) {
        const f4 v = acc[mt][nt];
        const unsigned p0 = pk2(v[0], v[1]), p1 = pk2(v[2], v[3]);
        *(LAS f4*)(L + tw[nt] + mt * 4096) = (f4){bflo(p0), bfhi(p0), bflo(p1), bfhi(p1)};
      }
    u4 ov[8];
#pragma unroll
    for (int i = 0; i < 8; ++i) ov[i] = *(const LAS u4*)(L + trd[i & 3] + (i >> 2) * 4096);
    SB();
#pragma unroll
    for (int i = 0; i < 8; ++i) __builtin_amdgcn_raw_buffer_store_b128(ov[i], ro, so_, i * 16384, OUT_AUX);
    ST(10);
  } else {
    constexpr unsigned SLOT = 32768, SCR = SLOT, PAR = 4 * SLOT;
    const int wp = w & 1, wt = w >> 1;
    const unsigned ecol = WS_EST + (cg * 64 + wp * 32 + l31) * 8;
    if (helper) {
      unsigned vx[4], vw[4], vc[4];
#pragma unroll
      for (int i = 0; i < 4; ++i) {
        const unsigned sw = (c8 ^ ((4 * i + (r8 >> 1)) & 7)) << 4;
        vx[i] = (64 * (w >> 1) + 32 * (i & 1) + 16 * (r8 >> 2) + 8 * (r8 & 1) + 4 * (w & 1) + 2 * (i >> 1) + ((r8 >> 1) & 1)) * 1024 + sw - 1024 * i;
        vw[i] = (16 * (i & 1) + 8 * (r8 >> 2) + 4 * (i >> 1) + (r8 & 3)) * 1024 + sw - 1024 * i;
        vc[i] = (8 * i + r8) * 1024 + sw - 1024 * i;
      }
      const GAS char* xbase = (const GAS char*)xb + (size_t)tile * 131072;
      const GAS char* wbase = (const GAS char*)((w & 1) ? bim : bre) + (cg * 64 + (w >> 1) * 32) * 1024;
      auto issue = [&](int kk) {
        const int kc = (kk + rot) & 7;
        const unsigned s = L0 + (kk % 5) * SLOT + w * 4096;
        dmax(s, xbase + kc * 128, vx[0], vx[1], vx[2], vx[3]);
        dma4(s + 16384, wbase + kc * 128, vw[0], vw[1], vw[2], vw[3]);
      };
      ST(9); issue(0); ST(10); issue(1); ST(11);
      LOAD_EPOCH();
      const GAS char* cbase = (const GAS char*)((w & 1) ? cim : cre) + cg * 65536;
      auto issue_c = [&](int piece) {
        const unsigned s = L0 + ((piece + 3) % 5) * SLOT + w * 8192;
        const GAS char* cb = cbase + seq(2 * piece + (w >> 1)) * 128;
        dma4(s, cb, vc[0], vc[1], vc[2], vc[3]);
        dma4(s + 4096, cb + 32768, vc[0], vc[1], vc[2], vc[3]);
      };
      asm volatile("s_waitcnt vmcnt(8) lgkmcnt(0)\n\ts_barrier" : "+s"(epoch) :: "memory");
      issue(2);
      const f4 par = *(const LAS f4*)(L + PAR + w * 1024 + lane * 16);
      const f2 l64 = {par[0], par[1]}, l128 = {par[2], par[3]};
      f2 l256 = cmul(l128, l128);
      f2 wgt = hh ? l64 : (f2){1.f, 0.f};
      if (wt) wgt = cmul(wgt, l128);
      const unsigned tag = epoch + 1, t = tag & 0xffu;
      unsigned eo[8], et[8];
#pragma unroll
      for (int m = 0; m < 8; ++m) {
        const int i = 2 * tile - 1 - (2 * wt + hh) - 4 * m;
        eo[m] = i >= 0 ? ecol + i * 4096 : 0x80000000u;
        et[m] = i >= 0 ? t : 0u;
        asm volatile("" : "+v"(eo[m]), "+v"(et[m]));
      }
      asm volatile("" : "+v"(l256), "+v"(wgt));
      wait_bar<8>(); issue(3);
      wait_bar<8>(); issue(4);
      wait_bar<8>(); issue(5);
      wait_bar<8>(); issue(6);
      wait_bar<8>(); issue(7);
      wait_bar<8>(); issue_c(0);
      wait_bar<8>(); ST(1);
      if (NC_EARLY > 1) issue_c(1);
      if (NC_EARLY > 2) issue_c(2);
      ST(2);
      if (PD) { asm volatile("s_barrier" ::: "memory"); __builtin_amdgcn_s_sleep(PD); }
      if (PRI & 2) __builtin_amdgcn_s_setprio(3);
      f2 cin;
#pragma unroll 1
      for (unsigned sp = 0; sp < SPIN_CAP; ++sp) {
      asm volatile("" ::: "memory");
        u2 g[8];
#pragma unroll
        for (int m = 0; m < 8; ++m) g[m] = __builtin_amdgcn_raw_buffer_load_b64(ws, eo[m], 0, SC1);
        unsigned bad = 0;
        cin = (f2){0, 0};
#pragma unroll
        for (int m = 7; m >= 0; --m) {
          const unsigned cx = g[m].x ^ et[m];
          bad |= cx;
          cin = cfma(l256, cin, (f2){__uint_as_float(cx), __uint_as_float(g[m].y)});
        }
        if (__all((bad & 0xffu) == 0)) break;
      }
      ST(5);
      cin = cmul(cin, wgt);
      f2 chi;
      const f2 clo = swap_lo(cin, chi);
      if (hh == 0) *(LAS f2*)(L + SCR + (wp * 2 + wt) * 256 + l31 * 8) = clo + chi;
      ST(6);
      lbar();
      if (WOX & 2) issue_c(2);
      lbar();
      ST(7);
      if (NC_EARLY <= 1) issue_c(1);
      if (NC_EARLY <= 2 && !(WOX & 2)) issue_c(2);
      issue_c(3);
      wait_bar<0>();
      ST(8); HFLUSH();
      return;
    }
    LOAD_EPOCH();
    const int pl = cg * 64 + wp * 32 + 8 * (l15 >> 2) + 4 * (q & 1) + (l15 & 3);
    const unsigned plr = lre[pl], pli = lim[pl], pdt = ldt[pl];
    u4 dv[2];
#pragma unroll
    for (int i = 0; i < 2; ++i) dv[i] = *(const GAS u4*)(dsk + cg * 64 + 32 * i + 8 * q);
    unsigned warm0 = 0, warm1 = 0;
    if (TOUCH) {
      warm0 = __builtin_amdgcn_raw_buffer_load_b32(ws, lane < 16 ? WS_FB + ((tile * 2 + wt) * 16 + lane) * 128 : lane == 16 ? WS_EST + tile * 8192
                                                   : lane == 17 ? WS_H + (tile * 4 + w) * 65536 : 0x80000000u, 0, SC1);
      warm1 = __builtin_amdgcn_raw_buffer_load_b32(ro, lane < 2 ? (tile * 128 + w * 32 + lane * 16) * 4096 + 2048 : 0x80000000u, 0, SC1);
    }
    SB();
    f2 lam[2], l8[2], kap, l16, l32, l64;
    {
      const float lr = bflo(plr), li = bflo(pli);
      const float dt = bflo(pk2(expf(bflo(pdt)), 0.f));
      const float mag = expf(dt * lr);
      float sn, cs;
      sincosf(dt * li, &sn, &cs);
      const f2 l1 = {mag * cs, mag * sn};
      const float inv = 2.f / (lr * lr + li * li), nr = l1.x - 1.f, ni = l1.y;
      kap = (f2){(nr * lr + ni * li) * inv, (ni * lr - nr * li) * inv};
      f2 t = cmul(l1, l1); t = cmul(t, t); t = cmul(t, t);
      l16 = cmul(t, t); l32 = cmul(l16, l16); l64 = cmul(l32, l32);
      const f2 l128 = cmul(l64, l64);
      *(LAS f4*)(L + PAR + w * 1024 + lane * 16) = (f4){l64.x, l64.y, l128.x, l128.y};
      lam[0] = swap16(l1, l1, lam[1]);
      l8[0] = swap16(t, t, l8[1]);
    }
    const unsigned ax[2] = {(unsigned)(l15 * 128 + ((q ^ (l15 >> 1)) << 4)), (unsigned)(l15 * 128 + (((4 + q) ^ (l15 >> 1)) << 4))};
    f4 u[2][2][4];
#pragma unroll
    for (int i = 0; i < 16; ++i) u[i >> 3][(i >> 2) & 1][i & 3] = (f4){0, 0, 0, 0};
    bf8 Xa[2], Xb[2], W0[4], W1[4];
    auto rdx = [&](bf8 (&X)[2], int kk, int ks, int h) {
#pragma unroll
      for (int i = 0; i < 2; ++i) X[i] = ldsr(L + (kk % 5) * SLOT + wt * 8192 + (2 * h + i) * 2048 + ax[ks]);
    };
    auto rdw = [&](bf8 (&W)[4], int kk, int ks) {
#pragma unroll
      for (int i = 0; i < 4; ++i) W[i] = ldsr(L + (kk % 5) * SLOT + 16384 + wp * 8192 + (i & 1) * 4096 + (i >> 1) * 2048 + ax[ks]);
    };
    auto mm = [&](const bf8 (&X)[2], const bf8 (&W)[4], int h) {
#pragma unroll
      for (int i = 0; i < 2; ++i)
#pragma unroll
        for (int j = 0; j < 4; ++j) u[j >> 1][j & 1][2 * h + i] = mfma16(X[i], W[j], u[j >> 1][j & 1][2 * h + i]);
    };
    asm volatile("s_waitcnt lgkmcnt(0)\n\ts_barrier" : "+s"(epoch), "+v"(lam[0]), "+v"(lam[1]), "+v"(l8[0]), "+v"(l8[1]), "+v"(kap), "+v"(l16), "+v"(l32), "+v"(l64),
                 "+v"(dv[0]), "+v"(dv[1]) : "v"(warm0), "v"(warm1), "v"(cpf) : "memory");
    ST(1);
    const unsigned tag = epoch + 1, t = tag & 0xffu;
    rdw(W0, 0, 0); rdx(Xa, 0, 0, 0);
    SB();
#pragma unroll
    for (int kk = 0; kk < 8; ++kk) {
      rdx(Xb, kk, 0, 1); mm(Xa, W0, 0); ILV(2, 8); SB(); PACE(BN);
      rdw(W1, kk, 1); rdx(Xa, kk, 1, 0); mm(Xb, W0, 1); ILV(6, 8); SB(); PACE(BN);
      rdx(Xb, kk, 1, 1); mm(Xa, W1, 0); ILV(2, 8); SB();
      if (kk < 7) { lbar(); if (kk == 6) ST(2); rdw(W0, kk + 1, 0); rdx(Xa, kk + 1, 0, 0); mm(Xb, W1, 1); ILV(6, 8); }
      else mm(Xb, W1, 1);
      SB(); if (kk < 7) PACE(BN);
    }
    ST(3);
    f2 gr[2][8], gi[2][8];
#pragma unroll
    for (int i = 0; i < 16; ++i) {
      const int pt = i >> 3, a = i & 7;
      gr[pt][a] = (f2){u[pt][0][a >> 1][2 * (a & 1)], u[pt][0][a >> 1][2 * (a & 1) + 1]};
      gi[pt][a] = (f2){u[pt][1][a >> 1][2 * (a & 1)], u[pt][1][a >> 1][2 * (a & 1) + 1]};
    }
    {
      f2 R0 = {lam[0].x, lam[0].x}, I0 = {lam[0].y, lam[0].y}, N0 = -I0, R1 = {lam[1].x, lam[1].x}, I1 = {lam[1].y, lam[1].y}, N1 = -I1;
      asm volatile("s_nop 7" : "+v"(gr[0][7]), "+v"(gi[0][7]), "+v"(gr[1][7]), "+v"(gi[1][7]), "+v"(gr[0][5]), "+v"(gi[0][5]), "+v"(gr[1][5]), "+v"(gi[1][5]),
                   "+v"(R0), "+v"(I0), "+v"(N0), "+v"(R1), "+v"(I1), "+v"(N1));
#pragma unroll
      for (int a = 1; a < 8; ++a)
        asm volatile("v_pk_fma_f32 %0, %10, %5, %0\n\tv_pk_fma_f32 %1, %9, %4, %1\n\tv_pk_fma_f32 %2, %13, %7, %2\n\tv_pk_fma_f32 %3, %12, %6, %3\n\t"
                     "v_pk_fma_f32 %0, %8, %4, %0\n\tv_pk_fma_f32 %1, %8, %5, %1\n\tv_pk_fma_f32 %2, %11, %6, %2\n\tv_pk_fma_f32 %3, %11, %7, %3"
                     : "+v"(gr[0][a]), "+v"(gi[0][a]), "+v"(gr[1][a]), "+v"(gi[1][a])
                     : "v"(gr[0][a - 1]), "v"(gi[0][a - 1]), "v"(gr[1][a - 1]), "v"(gi[1][a - 1]), "v"(R0), "v"(I0), "v"(N0), "v"(R1), "v"(I1), "v"(N1));
    }
    const f2 e0[2] = {{gr[0][7].x, gi[0][7].x}, {gr[1][7].x, gi[1][7].x}};
    f2 vodd;
    const f2 veven = swap16(cfma(l8[0], e0[0], (f2){gr[0][7].y, gi[0][7].y}), cfma(l8[1], e0[1], (f2){gr[1][7].y, gi[1][7].y}), vodd);
    f2 ehi;
    const f2 elo = swap_lo(cfma(l16, veven, vodd), ehi);
    const f2 e64 = cfma(l32, elo, ehi);
    __builtin_amdgcn_raw_buffer_store_b64((u2){tagv(e64.x, t), __float_as_uint(e64.y)}, ws, hh == 0 ? ecol + (tile * 2 + wt) * 4096 : 0x80000000u, 0, SC1);
    if (wt == 0 && hh == 0) *(LAS f2*)(L + SCR + 1024 + wp * 256 + l31 * 8) = e64;
    if (PD) asm volatile("s_barrier" ::: "memory");
    SB();
    ST(4);
    u4 xd[2][2];
#pragma unroll
    for (int i = 0; i < 4; ++i)
      xd[i >> 1][i & 1] = *(const GAS u4*)(xb + (tile * 128 + 64 * wt + 16 * (l15 >> 2) + 8 * (l15 & 1) + 4 * (w & 1) + 2 * (i >> 1) + ((l15 >> 1) & 1)) * 512
                                           + cg * 64 + 32 * (i & 1) + 8 * q);
    SB();
    f2 kp[2], ke[2], pw[2][8];
    kp[0] = swap16(kap, kap, kp[1]);
#pragma unroll
    for (int pt = 0; pt < 2; ++pt) {
      const f2 KR = {kp[pt].x, kp[pt].x}, KI = {kp[pt].y, kp[pt].y};
      ke[pt] = cmul(kp[pt], e0[pt]);
#pragma unroll
      for (int a = 0; a < 8; ++a) {
        const f2 xr = gr[pt][a], xi = gi[pt][a];
        gr[pt][a] = fma2(KR, xr, -KI * xi); gi[pt][a] = fma2(-KR, xi, -KI * xr);
        asm volatile("" : "+v"(gr[pt][a]), "+v"(gi[pt][a]));
      }
      pw[pt][0] = (f2){lam[pt].x, -lam[pt].y};
#pragma unroll
      for (int k = 1; k < 8; ++k) { pw[pt][k] = cmul(pw[pt][k >> 1], pw[pt][(k - 1) >> 1]); asm volatile("" : "+v"(pw[pt][k])); }
    }
    u2 idn = {(l15 == 4 * q ? 0x3f80u : 0u) | (l15 == 4 * q + 1 ? 0x3f800000u : 0u), (l15 == 4 * q + 2 ? 0x3f80u : 0u) | (l15 == 4 * q + 3 ? 0x3f800000u : 0u)};
#pragma unroll
    for (int i = 0; i < 4; ++i) {
#pragma unroll
      for (int j = 0; j < 4; ++j) xd[i >> 1][i & 1][j] = pk2(bflo(xd[i >> 1][i & 1][j]) * bflo(dv[i & 1][j]), bfhi(xd[i >> 1][i & 1][j]) * bfhi(dv[i & 1][j]));
      asm volatile("" : "+v"(xd[i >> 1][i & 1]));
    }
    f2 me = hh ? cmul(kap, l32) : kap, be = hh ? cmul(kap, elo) : (f2){0.f, 0.f};
    f2 mo = cmul(l16, me), bo = cfma(l16, be, cmul(kap, veven));
    unsigned ho[4];
#pragma unroll
    for (int i = 0; i < 4; ++i) ho[i] = WS_H + (tile * 8 + wt * 4 + i) * 32768 + (cg * 2 + wp) * 2048 + lane * 16;
    asm volatile("s_waitcnt lgkmcnt(0)\n\ts_barrier" : "+v"(ho[0]), "+v"(ho[1]), "+v"(ho[2]), "+v"(ho[3]), "+v"(me), "+v"(be), "+v"(mo), "+v"(bo),
                 "+v"(ke[0]), "+v"(ke[1]), "+v"(idn) :: "memory");
    ST(5);
    f2 cin = *(const LAS f2*)(L + SCR + wp * 512 + l31 * 8) + *(const LAS f2*)(L + SCR + wp * 512 + 256 + l31 * 8);
    const f2 esib = *(const LAS f2*)(L + SCR + 1024 + wp * 256 + l31 * 8);
    if (wt) cin = cfma(l64, cin, esib);
    f2 d0[2], DR[2], DJ[2];
    d0[0] = swap16(cfma(me, cin, be), cfma(mo, cin, bo), d0[1]);
#pragma unroll
    for (int pt = 0; pt < 2; ++pt) {
      const f2 d1 = cfma(l8[pt], d0[pt], ke[pt]);
      DR[pt] = (f2){d0[pt].x, d1.x}; DJ[pt] = (f2){-d0[pt].y, -d1.y};
    }
    const f4 zero4 = {0, 0, 0, 0};
    f4 T[8][2];
    u4 ow[8];
    auto fix = [&](int i) {
      const int mt = i >> 1, c = i & 1;
#pragma unroll
      for (int pt = 0; pt < 2; ++pt) {
        u2 a;
#pragma unroll
        for (int h = 0; h < 2; ++h) {
          const f2 P = pw[pt][2 * mt + h], PR = {P.x, P.x}, PI = {P.y, P.y};
          a[h] = pk2(c ? fma2(PI, DR[pt], fma2(PR, DJ[pt], gi[pt][2 * mt + h])) : fma2(-PI, DJ[pt], fma2(PR, DR[pt], gr[pt][2 * mt + h])));
        }
        T[i][pt] = mfma16k16(a, idn, zero4);
      }
    };
    auto put = [&](int i) {
      ow[i] = (u4){pk2(T[i][0][0], T[i][0][1]), pk2(T[i][0][2], T[i][0][3]), pk2(T[i][1][0], T[i][1][1]), pk2(T[i][1][2], T[i][1][3])};
      __builtin_amdgcn_raw_buffer_store_b128(ow[i], ws, ho[i >> 1] + (i & 1) * 1024, 0, SC1);
    };
    fix(0); SB();
#pragma unroll
    for (int i = 1; i < 8; ++i) { fix(i); SB(); put(i - 1); SB(); }
    put(7);
    SB();
    const unsigned ex = 2 * SLOT + wt * 16384 + wp * 4096 + lane * 16;
#pragma unroll
    for (int i = 0; i < 8; ++i) *(LAS u4*)(L + ex + (i >> 2) * 8192 + (i & 1) * 2048 + ((i >> 1) & 1) * 1024) = ow[i];
    lbar();
    ST(6);
    if (!NODRAIN) drain();
    __builtin_amdgcn_raw_buffer_store_b64((u2){tag, ~tag}, ws, lane == 0 ? WS_FB + ((tile * 2 + wt) * 16 + cg * 2 + wp) * 128 : 0x80000000u, 0, SC1);
    ST(7);
    const unsigned hb = WS_H + (tile * 8 + w * 2) * 32768 + lane * 16;
    u4 hf[64];
    bf8 cf[2][4];
    f4 acc[2][4];
    auto ldh = [&](int g) {
      hf[2 * g] = __builtin_amdgcn_raw_buffer_load_b128(ws, hb + (g & 3) * 1024, seq(g >> 2) * 4096, PAY_AUX);
      hf[2 * g + 1] = __builtin_amdgcn_raw_buffer_load_b128(ws, hb + 32768 + (g & 3) * 1024, seq(g >> 2) * 4096, PAY_AUX);
    };
    auto rdc = [&](int g) {
      const int tt = g >> 2;
#pragma unroll
      for (int nt = 0; nt < 4; ++nt)
        cf[g & 1][nt] = ldsr(L + (((tt >> 1) + 3) % 5) * SLOT + (tt & 1) * 16384 + (g & 1) * 8192 + nt * 2048 + ax[(g >> 1) & 1]);
    };
    auto mh = [&](int g) {
#pragma unroll
      for (int nt = 0; nt < 4; ++nt) {
        acc[0][nt] = mfma16(cf[g & 1][nt], asbf8(hf[2 * g]), acc[0][nt]);
        acc[1][nt] = mfma16(cf[g & 1][nt], asbf8(hf[2 * g + 1]), acc[1][nt]);
      }
    };
#pragma unroll
    for (int i = 0; i < 8; ++i) hf[i] = *(const LAS u4*)(L + 2 * SLOT + w * 8192 + i * 1024 + lane * 16);
    rdc(0);
    {
      u4 ida[2];
#pragma unroll
      for (int hf_ = 0; hf_ < 2; ++hf_)
#pragma unroll
        for (int jq = 0; jq < 4; ++jq) {
          const int k0 = 8 * q + 2 * jq - 16 * hf_;
          ida[hf_][jq] = (l15 == k0 ? 0x3f80u : 0u) | (l15 == k0 + 1 ? 0x3f800000u : 0u);
        }
#pragma unroll
      for (int i = 0; i < 8; ++i) acc[i >> 2][i & 3] = mfma16(asbf8(ida[i & 1]), asbf8(xd[i >> 2][(i >> 1) & 1]), zero4);
    }
    SB();
#pragma unroll
    for (int g = 0; g < 4; ++g) { rdc(g + 1); mh(g); ILV(4, 8); SB(); }
    unsigned tw[4], trd[4], so_ = ((tile * 128 + 64 * wt + 4 * (w & 1) + 8 * (q & 1) + (q >> 1)) * 1024 + 512 + cg * 64 + l15 * 4) * 4;
#pragma unroll
    for (int i = 0; i < 4; ++i) {
      tw[i] = 2 * SLOT + w * 8192 + l15 * 256 + (((4 * i + q) ^ l15) << 4);
      trd[i] = 2 * SLOT + w * 8192 + (4 * i + q) * 256 + ((l15 ^ (4 * i + q)) << 4);
      asm volatile("" : "+v"(tw[i]), "+v"(trd[i]));
    }
    asm volatile("s_barrier" : "+v"(so_) :: "memory");
    if (FD) __builtin_amdgcn_s_sleep(FD);
    wait_flags(ws, WS_FB + ((tile * 2 + wt) * 16 + l15) * 128, tag);
    if (DLB) __builtin_amdgcn_s_sleep(DLB);
    ST(8); CT(14);
#pragma unroll
    for (int g = 4; g < 4 + HD / 2; ++g) ldh(g);
    SB();
#pragma unroll
    for (int g = 4; g < 32; ++g) {
      if (g + HD / 2 < 32) ldh(g + HD / 2);
      if (g + 1 < 32) rdc(g + 1);
      mh(g);
#pragma unroll
      for (int i_ = 0; i_ < 4; ++i_) { SG(0x008, 1); SG(0x100, 1); }
      SG(0x008, 1); SG(0x020, 1); SG(0x008, 1); SG(0x020, 1); SG(0x008, 2);
      SB(); if (g < 30) PACE(HN);
    }
    ST(9); CT(15);
#pragma unroll
    for (int i = 0; i < 8; ++i) *(LAS f4*)(L + tw[i & 3] + (i >> 2) * 4096) = acc[i >> 2][i & 3];
    u4 ov[8];
#pragma unroll
    for (int i = 0; i < 8; ++i) ov[i] = *(const LAS u4*)(L + trd[i & 3] + (i >> 2) * 4096);
    SB();
#pragma unroll
    for (int i = 0; i < 8; ++i) __builtin_amdgcn_raw_buffer_store_b128(ov[i], ro, so_, (16 * (i & 3) + 2 * (i >> 2)) * 4096, OUT_AUX);
    ST(10);
  }
}

extern "C" void kernel_launch(void* const* d_in, const int* in_sizes, int n_in, void* d_out, int out_size, void* d_ws, size_t ws_size,
                              hipStream_t stream) {
  (void)hipFuncSetAttribute((const void*)fused_kernel, hipFuncAttributeMaxDynamicSharedMemorySize, LDS_BYTES);
  fused_kernel<<<256, 512, LDS_BYTES, stream>>>((const u16*)d_in[0], (const u16*)d_in[1], (const u16*)d_in[2], (const u16*)d_in[3],
                                               (const u16*)d_in[4], (const u16*)d_in[5], (const u16*)d_in[6], (const u16*)d_in[7],
                                               (const u16*)d_in[8], (const u16*)d_in[9], (const u16*)d_in[10], (const u16*)d_in[11],
                                               (float*)d_out, (char*)d_ws);
}
```

```cpp
#define AMP_A 0
#define AMP_B 0
#define AMP_BID 254
#define AMP_K 0
#include <hip/hip_runtime.h>
#include <hip/hip_bf16.h>

#define DI __device__ __forceinline__
#define LAS __attribute__((address_space(3)))
#define GAS __attribute__((address_space(1)))
typedef unsigned short u16;
typedef unsigned long long u64;
typedef __bf16 bf8 __attribute__((ext_vector_type(8)));
typedef __bf16 bf2 __attribute__((ext_vector_type(2)));
typedef short s4 __attribute__((ext_vector_type(4)));
typedef short s8 __attribute__((ext_vector_type(8)));
typedef float f16v __attribute__((ext_vector_type(16)));
typedef float f4 __attribute__((ext_vector_type(4)));
typedef float f2 __attribute__((ext_vector_type(2)));
typedef unsigned u4 __attribute__((ext_vector_type(4)));
typedef unsigned u2 __attribute__((ext_vector_type(2)));

constexpr unsigned WS_EPOCH = 0;
constexpr unsigned WS_FA = 4096;
constexpr unsigned WS_FB = WS_FA + 65536;
constexpr unsigned WS_EST = WS_FB + 65536;
constexpr unsigned WS_Z = WS_EST + 131072;
constexpr unsigned WS_KV = WS_Z + 32768;
constexpr unsigned WS_O = 1u << 20;
constexpr unsigned WS_H = 3u << 20;
constexpr unsigned WS_END = 7u << 20;
constexpr unsigned WS_STAMP = 8u << 20;
constexpr int LDS_BYTES = 160 * 1024;
constexpr unsigned SPIN_CAP = 200000;
constexpr int SC1 = 16;

#define ROT 8
#define NC_EARLY 2
#define OUT_AUX 16
#define PAY_AUX 16
#define PULL 0
#define TOUCH 1
#define CPF 11
#define HROT 1
#define XNT 0
#define PD 4
#define FD 0
#define TD 0
#define NODRAIN 0
#define WOX 2
#define DLA 0
#define DLB 0
#define BN 10
#define AN 8
#define QN 0
#define HN 0
#define PRI 2
#define OD 6
#define HD 12

#define ST(i) do { __builtin_amdgcn_sched_barrier(0); asm volatile("s_memrealtime %0\n\ts_waitcnt lgkmcnt(0)" : "=s"(st[i]) :: "memory"); __builtin_amdgcn_sched_barrier(0); } while (0)
#define CT(i) do { __builtin_amdgcn_sched_barrier(0); asm volatile("s_memtime %0\n\ts_waitcnt lgkmcnt(0)" : "=s"(st[i]) :: "memory"); __builtin_amdgcn_sched_barrier(0); } while (0)
#define HFLUSH() do {} while (0)
#define SB() __builtin_amdgcn_sched_barrier(0)
#define PACE(n) do { if (n) { asm volatile("s_nop %0" :: "i"((n) > 16 ? 15 : ((n) ? (n) - 1 : 0))); if ((n) > 16) asm volatile("s_nop %0" :: "i"((n) > 16 ? (n) - 17 : 0)); SB(); } } while (0)
#define SG(m, n) __builtin_amdgcn_sched_group_barrier(m, n, 0)

DI unsigned pk2(float a, float b) { f2 v = {a, b}; return __builtin_bit_cast(unsigned, __builtin_convertvector(v, bf2)); }
DI unsigned pk2(f2 v) { return __builtin_bit_cast(unsigned, __builtin_convertvector(v, bf2)); }
DI float bflo(unsigned v) { return __uint_as_float(v << 16); }
DI float bfhi(unsigned v) { return __uint_as_float(v & 0xffff0000u); }
DI bf8 asbf8(u4 v) { return __builtin_bit_cast(bf8, v); }
DI f16v mfma32(bf8 a, bf8 b, f16v c) { return __builtin_amdgcn_mfma_f32_32x32x16_bf16(a, b, c, 0, 0, 0); }
DI f4 mfma16(bf8 a, bf8 b, f4 c) { return __builtin_amdgcn_mfma_f32_16x16x32_bf16(a, b, c, 0, 0, 0); }
DI f4 mfma16k16(u2 a, u2 b, f4 c) { return __builtin_amdgcn_mfma_f32_16x16x16bf16_1k(__builtin_bit_cast(s4, a), __builtin_bit_cast(s4, b), c, 0, 0, 0); }
DI bf8 ldsr(const char LAS* p) { return *(const LAS bf8*)p; }
DI bf8 ldstr(const char LAS* p0, const char LAS* p1) {
  s4 a = __builtin_amdgcn_ds_read_tr16_b64_v4i16((LAS s4*)p0), b = __builtin_amdgcn_ds_read_tr16_b64_v4i16((LAS s4*)p1);
  return __builtin_bit_cast(bf8, (s8)__builtin_shufflevector(a, b, 0, 1, 2, 3, 4, 5, 6, 7));
}
DI void dma4(unsigned lds, const GAS char* sb, unsigned v0, unsigned v1, unsigned v2, unsigned v3) {
  asm volatile("s_mov_b32 m0, %0\n\ts_nop 0\n\tglobal_load_lds_dwordx4 %1, %5\n\tglobal_load_lds_dwordx4 %2, %5 offset:1024\n\t"
               "global_load_lds_dwordx4 %3, %5 offset:2048\n\tglobal_load_lds_dwordx4 %4, %5 offset:3072"
               :: "s"(lds), "v"(v0), "v"(v1), "v"(v2), "v"(v3), "s"(sb) : "memory");
}
DI void dma4nt(unsigned lds, const GAS char* sb, unsigned v0, unsigned v1, unsigned v2, unsigned v3) {
  asm volatile("s_mov_b32 m0, %0\n\ts_nop 0\n\tglobal_load_lds_dwordx4 %1, %5 nt\n\tglobal_load_lds_dwordx4 %2, %5 offset:1024 nt\n\t"
               "global_load_lds_dwordx4 %3, %5 offset:2048 nt\n\tglobal_load_lds_dwordx4 %4, %5 offset:3072 nt"
               :: "s"(lds), "v"(v0), "v"(v1), "v"(v2), "v"(v3), "s"(sb) : "memory");
}
DI void dmax(unsigned lds, const GAS char* sb, unsigned v0, unsigned v1, unsigned v2, unsigned v3) { if (XNT) dma4nt(lds, sb, v0, v1, v2, v3); else dma4(lds, sb, v0, v1, v2, v3); }
DI void dma2(unsigned lds, const GAS char* sb, unsigned v0, unsigned v1) {
  asm volatile("s_mov_b32 m0, %0\n\ts_nop 0\n\tglobal_load_lds_dwordx4 %1, %3\n\tglobal_load_lds_dwordx4 %2, %3 offset:1024"
               :: "s"(lds), "v"(v0), "v"(v1), "s"(sb) : "memory");
}
DI void lbar() { asm volatile("s_waitcnt lgkmcnt(0)\n\ts_barrier" ::: "memory"); }
template <int N> DI void wait_bar() { asm volatile("s_waitcnt vmcnt(%0) lgkmcnt(0)\n\ts_barrier" :: "i"(N) : "memory"); }
DI void drain() { asm volatile("s_waitcnt vmcnt(0)" ::: "memory"); }
DI unsigned tagv(float x, unsigned t) { return (__float_as_uint(x) & 0xffffff00u) | t; }
DI f2 cmul(f2 a, f2 b) { return (f2){a.x * b.x - a.y * b.y, a.x * b.y + a.y * b.x}; }
DI f2 cfma(f2 a, f2 b, f2 c) { return (f2){fmaf(a.x, b.x, fmaf(-a.y, b.y, c.x)), fmaf(a.x, b.y, fmaf(a.y, b.x, c.y))}; }
DI f2 fma2(f2 a, f2 b, f2 c) { return __builtin_elementwise_fma(a, b, c); }
constexpr float EPS = 1e-6f, LOG2E = 1.44269504088896341f;
DI unsigned phi2(float s0, float s1) {
  const f2 a = fma2((f2){s0, s1}, (f2){LOG2E, LOG2E}, (f2){-EPS * LOG2E, -EPS * LOG2E});
  const f2 e = {__builtin_amdgcn_fmed3f(__builtin_amdgcn_exp2f(a.x), 0.f, 1.f), __builtin_amdgcn_fmed3f(__builtin_amdgcn_exp2f(a.y), 0.f, 1.f)};
  return pk2(e + (f2){__builtin_amdgcn_fmed3f(s0, EPS, __builtin_inff()), __builtin_amdgcn_fmed3f(s1, EPS, __builtin_inff())});
}
DI f2 swap_lo(f2 v, f2& hi) {
  const auto x = __builtin_amdgcn_permlane32_swap(__float_as_uint(v.x), __float_as_uint(v.x), false, false);
  const auto y = __builtin_amdgcn_permlane32_swap(__float_as_uint(v.y), __float_as_uint(v.y), false, false);
  hi = (f2){__uint_as_float(x[1]), __uint_as_float(y[1])};
  return (f2){__uint_as_float(x[0]), __uint_as_float(y[0])};
}
DI f2 swap16(f2 a, f2 b, f2& rb) {
  const auto x = __builtin_amdgcn_permlane16_swap(__float_as_uint(a.x), __float_as_uint(b.x), false, false);
  const auto y = __builtin_amdgcn_permlane16_swap(__float_as_uint(a.y), __float_as_uint(b.y), false, false);
  rb = (f2){__uint_as_float(x[1]), __uint_as_float(y[1])};
  return (f2){__uint_as_float(x[0]), __uint_as_float(y[0])};
}
DI void wait_flags(__amdgpu_buffer_rsrc_t ws, unsigned off, unsigned tag) {
#pragma unroll 1
  for (unsigned sp = 0; sp < SPIN_CAP; ++sp) {
      asm volatile("" ::: "memory");
    const u2 f = __builtin_amdgcn_raw_buffer_load_b64(ws, off, 0, SC1);
    if (__all(f.x == tag && f.y == ~tag)) break;
    __builtin_amdgcn_s_sleep(1);
  }
  asm volatile("" ::: "memory");
}

__global__ void __launch_bounds__(512, 2)
fused_kernel(const u16* a0, const u16* a1, const u16* a2, const u16* a3, const u16* a4, const u16* a5, const u16* a6, const u16* a7,
             const u16* a8, const u16* a9, const u16* a10, const u16* a11, float* a12, char* a13) {
  extern __shared__ __attribute__((aligned(16))) char smem_[];
  char LAS* L = (char LAS*)smem_;
  const unsigned L0 = (unsigned)(size_t)L;
  u64 st[16];
  for (int i = 0; i < 16; ++i) st[i] = 0;
  ST(0);
  const int lane = threadIdx.x & 63;
  const int w8 = __builtin_amdgcn_readfirstlane(threadIdx.x >> 6), w = w8 & 3;
  unsigned cpf = 0;
  if (CPF && w8 == 0) {
    u64 pc;
    asm volatile("s_getpc_b64 %0" : "=s"(pc));
    cpf = __builtin_amdgcn_raw_buffer_load_b32(__builtin_amdgcn_make_buffer_rsrc((char*)(pc & ~127ull), 0, CPF * 4096, 0x00020000),
                                               lane < CPF ? ((blockIdx.x >> 3) * CPF + lane) * 128 : 0x80000000u, 0, 0);
  }
  typedef u64 u64x8 __attribute__((ext_vector_type(8)));
  typedef u64 u64x4 __attribute__((ext_vector_type(4)));
  typedef u64 u64x2 __attribute__((ext_vector_type(2)));
  u64x8 ka; u64x4 kb; u64x2 kc_;
  asm volatile("s_load_dwordx16 %0, %3, 0x0\n\ts_load_dwordx8 %1, %3, 0x40\n\ts_load_dwordx4 %2, %3, 0x60\n\ts_waitcnt lgkmcnt(0)"
               : "=&s"(ka), "=&s"(kb), "=&s"(kc_) : "s"(__builtin_amdgcn_kernarg_segment_ptr()) : "memory");
  const GAS u16 *xa = (const GAS u16*)ka[0], *wqkv = (const GAS u16*)ka[1], *wo = (const GAS u16*)ka[2], *xb = (const GAS u16*)ka[3];
  const GAS u16 *lre = (const GAS u16*)ka[4], *lim = (const GAS u16*)ka[5], *bre = (const GAS u16*)ka[6], *bim = (const GAS u16*)ka[7];
  const GAS u16 *cre = (const GAS u16*)kb[0], *cim = (const GAS u16*)kb[1], *ldt = (const GAS u16*)kb[2], *dsk = (const GAS u16*)kb[3];
  float* out = (float*)kc_[0]; char* wsp = (char*)kc_[1];
  const bool helper = w8 >= 4;
  const int bid = blockIdx.x, xcd = bid & 7, jj = bid >> 3;
  const int tile = (xcd >> 1) * 4 + (jj >> 3), cg = jj & 7;
  const int rot = ROT == 1 ? ((cg + 3 * (jj >> 3) + xcd) & 7) : ROT == 7 ? (((cg & 3) * 2 + (xcd >> 1)) & 7) : ROT == 12 ? ((2 * (cg & 3) + 2 * (xcd >> 1)) & 7) : ROT == 16 ? ((2 * (cg & 3) + ((jj >> 3) & 1) + 2 * (xcd >> 1)) & 7) : ((((cg + (jj >> 3)) % 3) * 3 + (xcd >> 1)) & 7);
  auto seq = [&](int t) { const int b = 2 * (jj >> 3), e = t - 1; return t == 0 ? cg : (b + e + (e >= ((cg - b) & 7) ? 1 : 0)) & 7; };
  const __amdgpu_buffer_rsrc_t ws = __builtin_amdgcn_make_buffer_rsrc(wsp, 0, WS_END, 0x00020000);
  const __amdgpu_buffer_rsrc_t ro = __builtin_amdgcn_make_buffer_rsrc(out, 0, 2048 * 1024 * 4, 0x00020000);
  const int r8 = lane >> 3, c8 = lane & 7;
  const int q = lane >> 4, l15 = lane & 15, l31 = lane & 31, hh = lane >> 5;
  unsigned epoch;
#define LOAD_EPOCH() asm volatile("s_load_dword %0, %1, 0x0" : "=s"(epoch) : "s"(wsp) : "memory")

  if ((xcd & 1) == 0) {
    constexpr unsigned WA = 131072, WB = 147456, SCR = 32768, TRB = 81920;
    constexpr unsigned QBUF[4] = {WB, 0, WA, WB}, OBUF[4] = {32768, 49152, 65536, 0};
    constexpr unsigned WBUF[8] = {WA, WB, 7 * 16384, 0, WA, WB, 0, WA};
    if (helper) {
      const int head = cg * 4 + w;
      unsigned vx[4], vw[2], vo[4];
#pragma unroll
      for (int i = 0; i < 4; ++i) vx[i] = (32 * w + 8 * i + r8) * 1024 + ((c8 ^ ((4 * i + (r8 >> 1)) & 7)) << 4) - 1024 * i;
#pragma unroll
      for (int i = 0; i < 2; ++i) vw[i] = (16 * w + 8 * i + r8) * 3072 + ((c8 ^ ((((r8 >> 1) & 1) | (i << 1)) << 1)) << 4) - 1024 * i;
      const GAS char* xbase = (const GAS char*)xa + (size_t)tile * 131072;
      const GAS char* wbase = (const GAS char*)wqkv + cg * 128;
      auto isx = [&](int kk, int slot) { dmax(L0 + slot * 16384 + w * 4096, xbase + ((kk + rot) & 7) * 128, vx[0], vx[1], vx[2], vx[3]); };
      auto isw = [&](unsigned dst, int kk, int seg) { dma2(L0 + dst + w * 2048, wbase + ((kk + rot) & 7) * (64 * 3072) + seg * 1024, vw[0], vw[1]); };
      auto issue = [&](int kk) { isx(kk, kk); isw(WBUF[kk], kk, 1); isw(WBUF[kk] + 8192, kk, 2); };
      auto isq = [&](int p) { isw(QBUF[p], 2 * p + 1, 0); isw(QBUF[p] + 8192, 2 * p + 2, 0); };
      ST(9); issue(0); ST(10); issue(1); ST(11);
      LOAD_EPOCH();
#pragma unroll
      for (int i = 0; i < 4; ++i) vo[i] = (8 * i + r8) * 1024 + ((c8 ^ (((r8 >> 1) & 3) << 1)) << 4) - 1024 * i;
      const GAS char* obase = (const GAS char*)wo + cg * 128;
      asm volatile("s_waitcnt vmcnt(8) lgkmcnt(0)\n\ts_barrier" : "+s"(epoch) :: "memory");
      issue(2);
      wait_bar<8>(); issue(3);
      wait_bar<8>(); issue(4);
      wait_bar<8>(); issue(5);
      wait_bar<8>(); issue(6);
      wait_bar<8>(); issue(7);
      wait_bar<8>();
      unsigned pfo;
      asm volatile("global_load_dword %0, %1, %2" : "=v"(pfo) : "v"(((jj >> 3) * 128 + w * 32 + l31) * 1024), "s"(obase) : "memory");
      isq(0);
      wait_bar<5>(); isq(1);
      wait_bar<4>(); isq(2);
      wait_bar<4>(); isq(3); isx(0, 1);
      asm volatile("s_waitcnt vmcnt(0)\n\ts_barrier" : "+v"(pfo) :: "memory");
      if (PRI & 1) __builtin_amdgcn_s_setprio(3);
      ST(1);
      const unsigned tag = epoch + 1, t = tag & 0xffu;
      const GAS char* kvb_ = (const GAS char*)wsp + WS_KV + head * 16384;
      const GAS char* zb_ = (const GAS char*)wsp + WS_Z + head * 1024;
      const unsigned qr = (jj >> 3) * 4096;
      const unsigned vk0 = lane * 16 + qr, vk1 = lane * 16 + ((qr + 4096) & 12288), vz = l15 * 64 + q * 16;
      u4 g[8], gz;
#define KVL(i, v) asm volatile("global_load_dwordx4 %0, %1, %2 offset:%3 sc1" : "=v"(g[i]) : "v"(v), "s"(kvb_), "i"((i & 3) * 1024) : "memory")
      asm volatile("global_load_dwordx4 %0, %1, %2 sc1" : "=v"(gz) : "v"(vz), "s"(zb_) : "memory");
      KVL(0, vk0); KVL(1, vk0); KVL(2, vk0); KVL(3, vk0); KVL(4, vk1); KVL(5, vk1); KVL(6, vk1); KVL(7, vk1);
      auto zsum = [&]() {
        f4 z = __builtin_bit_cast(f4, gz);
#pragma unroll
        for (int r = 0; r < 4; ++r) {
          float v = z[r];
          v += __builtin_bit_cast(float, __builtin_amdgcn_mov_dpp(__builtin_bit_cast(int, v), 0x128, 0xf, 0xf, true));
          v += __builtin_bit_cast(float, __builtin_amdgcn_mov_dpp(__builtin_bit_cast(int, v), 0x124, 0xf, 0xf, true));
          v += __builtin_bit_cast(float, __builtin_amdgcn_mov_dpp(__builtin_bit_cast(int, v), 0x122, 0xf, 0xf, true));
          v += __builtin_bit_cast(float, __builtin_amdgcn_mov_dpp(__builtin_bit_cast(int, v), 0x121, 0xf, 0xf, true));
          z[r] = v;
        }
        return z;
      };
      asm volatile("s_waitcnt vmcnt(8)" : "+v"(gz) :: "memory");
      gz.x ^= t; gz.z ^= t;
      unsigned bad = gz.x | gz.z;
      f4 zz = zsum(), kv = {0, 0, 0, 0};
      asm volatile("" : "+v"(zz));
#define KVA(i) asm volatile("s_waitcnt vmcnt(%1)" : "+v"(g[i]) : "i"(7 - i) : "memory"); g[i].x ^= t; g[i].z ^= t; bad |= g[i].x | g[i].z; kv += __builtin_bit_cast(f4, g[i]); asm volatile("" : "+v"(kv), "+v"(bad))
      KVA(0); KVA(1); KVA(2); KVA(3); KVA(4); KVA(5); KVA(6); KVA(7);
      ST(3);
      if (!__all((bad & 0xffu) == 0)) {
#pragma unroll 1
        for (unsigned rep = 0; rep < SPIN_CAP; ++rep) {
          asm volatile("" ::: "memory");
          gz = __builtin_amdgcn_raw_buffer_load_b128(ws, WS_Z + head * 1024 + vz, 0, SC1);
#pragma unroll
          for (int i = 0; i < 8; ++i) g[i] = __builtin_amdgcn_raw_buffer_load_b128(ws, WS_KV + head * 16384 + (i < 4 ? vk0 : vk1) + (i & 3) * 1024, 0, SC1);
          gz.x ^= t; gz.z ^= t;
          bad = gz.x | gz.z;
#pragma unroll
          for (int i = 0; i < 8; ++i) { g[i].x ^= t; g[i].z ^= t; bad |= g[i].x | g[i].z; }
          if (__all((bad & 0xffu) == 0)) break;
        }
        zz = zsum();
        kv = (f4){0, 0, 0, 0};
#pragma unroll
        for (int i = 0; i < 8; ++i) kv += __builtin_bit_cast(f4, g[i]);
      }
      ST(5);
      *(LAS f4*)(L + SCR + w * 1024 + lane * 16) = kv;
      *(LAS u2*)(L + SCR + 8192 + w * 512 + lane * 8) = (u2){pk2(zz[0], zz[1]), pk2(zz[2], zz[3])};
      ST(6);
      lbar();
      ST(7);
      lbar();
#pragma unroll
      for (int i = 0; i < 4; ++i)
        dma4(L0 + OBUF[i] + w * 4096, obase + (2 * seq(2 * i + (w >> 1)) + (w & 1)) * 32768, vo[0], vo[1], vo[2], vo[3]);
      wait_bar<0>();
      ST(8);
      HFLUSH();
      if (bid == 0 && w == 0) {
#pragma unroll 1
        for (unsigned sp = 0; sp < SPIN_CAP; ++sp) {
          asm volatile("" ::: "memory");
          const unsigned fo = (lane >> 3) * 4096 + (lane & 7) * 256;
          const u2 f0 = __builtin_amdgcn_raw_buffer_load_b64(ws, WS_FA + fo, 0, SC1), f1 = __builtin_amdgcn_raw_buffer_load_b64(ws, WS_FA + 32768 + fo, 0, SC1);
          const u2 f2_ = __builtin_amdgcn_raw_buffer_load_b64(ws, WS_FB + fo, 0, SC1), f3 = __builtin_amdgcn_raw_buffer_load_b64(ws, WS_FB + 32768 + fo, 0, SC1);
          if (__all(f0.x == tag && f0.y == ~tag && f1.x == tag && f1.y == ~tag && f2_.x == tag && f2_.y == ~tag && f3.x == tag && f3.y == ~tag)) break;
          __builtin_amdgcn_s_sleep(8);
        }
        if (lane == 0) __builtin_amdgcn_raw_buffer_store_b32(tag, ws, WS_EPOCH, 0, SC1);
      }
      return;
    }
    LOAD_EPOCH();
    const int hp = w & 1, th = w >> 1;
    unsigned warm0 = 0, warm1 = 0;
    if (TOUCH) {
      if (TD) __builtin_amdgcn_s_sleep(TD);
      warm0 = __builtin_amdgcn_raw_buffer_load_b32(ws, lane < 16 ? WS_FA + ((tile * 2 + th) * 16 + lane) * 128 : lane < 24 ? WS_Z + (cg * 4 + w) * 1024 + (lane - 16) * 128
                                                   : lane == 24 ? WS_KV + (cg * 4 + w) * 16384 : lane == 25 ? WS_O + tile * 131072 : 0x80000000u, 0, SC1);
      warm1 = __builtin_amdgcn_raw_buffer_load_b32(ro, lane < 2 ? (tile * 128 + w * 32 + lane * 16) * 4096 : 0x80000000u, 0, SC1);
    }
    const int qq = (lane >> 2) & 3, pp = lane & 3;
    const unsigned ax0 = l15 * 128 + ((q ^ (l15 >> 1)) << 4), ax1 = l15 * 128 + (((4 + q) ^ (l15 >> 1)) << 4);
    auto waddr = [&](int h) { return (8 * q + qq) * 128 + (((2 * h + (pp >> 1)) ^ ((((qq >> 1) & 1) | ((q & 1) << 1)) << 1)) << 4) + 8 * (pp & 1); };
    const unsigned aw = waddr(w), aq0 = waddr(2 * hp), aq1 = waddr(2 * hp + 1);
    f4 ka[8], va[8];
#pragma unroll
    for (int i = 0; i < 8; ++i) { ka[i] = (f4){EPS, EPS, EPS, EPS}; va[i] = (f4){0, 0, 0, 0}; }
    bf8 X0[4], X1[4], W0[2], W1[2];
    auto rdx = [&](bf8 (&X)[4], int kk, int ks, unsigned off) {
#pragma unroll
      for (int i = 0; i < 4; ++i) X[i] = ldsr(L + kk * 16384 + (ks ? ax1 : ax0) + off + i * 2048);
    };
    auto rdw = [&](bf8 (&W)[2], int kk, int ks) {
#pragma unroll
      for (int i = 0; i < 2; ++i) { const char LAS* p = L + WBUF[kk] + aw + ks * 4096 + i * 8192; W[i] = ldstr(p, p + 512); }
    };
    auto mm = [&](const bf8 (&X)[4], const bf8 (&W)[2], int h) {
#pragma unroll
      for (int i = 0; i < 4; ++i) {
        ka[4 * h + i] = mfma16(X[i], W[0], ka[4 * h + i]);
        va[4 * h + i] = mfma16(X[i], W[1], va[4 * h + i]);
      }
    };
#define ILV(nr, nm) do { _Pragma("unroll") for (int i_ = 0; i_ < nr; ++i_) { SG(0x008, 1); SG(0x100, 1); } if (nm > nr) SG(0x008, nm - nr); } while (0)
    asm volatile("s_waitcnt lgkmcnt(0)\n\ts_barrier" : "+s"(epoch) : "v"(warm0), "v"(warm1), "v"(cpf) : "memory");
    ST(1);
    const unsigned tag = epoch + 1, t = tag & 0xffu;
    rdw(W0, 0, 0); rdx(X0, 0, 0, 0);
    SB();
#pragma unroll
    for (int kk = 0; kk < 8; ++kk) {
      rdx(X1, kk, 0, 8192); mm(X0, W0, 0); ILV(4, 8); SB(); PACE(AN);
      rdw(W1, kk, 1); rdx(X0, kk, 1, 0); mm(X1, W0, 1); ILV(8, 8); SB(); PACE(AN);
      rdx(X1, kk, 1, 8192); mm(X0, W1, 0); ILV(4, 8); SB();
      if (kk < 7) { lbar(); rdw(W0, kk + 1, 0); rdx(X0, kk + 1, 0, 0); mm(X1, W1, 1); ILV(8, 8); }
      else mm(X1, W1, 1);
      SB();
    }
    ST(2);
    const unsigned xq = th * 8192;
    auto rdq = [&](bf8 (&X)[4], bf8 (&W)[2], int s) {
      const char LAS* p = L + QBUF[s >> 2] + ((s >> 1) & 1) * 8192 + (s & 1) * 4096;
      W[0] = ldstr(p + aq0, p + aq0 + 512); W[1] = ldstr(p + aq1, p + aq1 + 512);
      rdx(X, s < 14 ? (s >> 1) + 1 : 1, s & 1, xq);
    };
    {
      const int head = cg * 4 + w;
      f4 kv = {0, 0, 0, 0}, zz = {0, 0, 0, 0};
      const u4 ones = {0x3f803f80u, 0x3f803f80u, 0x3f803f80u, 0x3f803f80u};
#pragma unroll
      for (int m = 0; m < 4; ++m) {
        const f4 a = ka[2 * m], b = ka[2 * m + 1], c = va[2 * m], d = va[2 * m + 1];
        const u4 pa = {phi2(a[0], a[1]), phi2(a[2], a[3]), phi2(b[0], b[1]), phi2(b[2], b[3])};
        const u4 pb = {pk2(c[0], c[1]), pk2(c[2], c[3]), pk2(d[0], d[1]), pk2(d[2], d[3])};
        kv = mfma16(asbf8(pa), asbf8(pb), kv);
        zz = mfma16(asbf8(pa), asbf8(ones), zz);
      }
      const u4 skv = {tagv(kv[0], t), __float_as_uint(kv[1]), tagv(kv[2], t), __float_as_uint(kv[3])};
      __builtin_amdgcn_raw_buffer_store_b128(skv, ws, WS_KV + (head * 16 + tile) * 1024 + lane * 16, 0, SC1);
      const u4 sz = {tagv(zz[0], t), __float_as_uint(zz[1]), tagv(zz[2], t), __float_as_uint(zz[3])};
      __builtin_amdgcn_raw_buffer_store_b128(sz, ws, l15 == 0 ? WS_Z + (head * 16 + tile) * 64 + q * 16 : 0x80000000u, 0, SC1);
    }
    SB();
    ST(3);
    lbar();
    rdq(X0, W0, 0);
    SB();
    f4 qa[2][4];
#pragma unroll
    for (int i = 0; i < 8; ++i) qa[i >> 2][i & 3] = (f4){EPS, EPS, EPS, EPS};
    u4 gc[8];
    const unsigned gb = WS_KV + (cg * 4 + w) * 16384 + lane * 16, g2 = (((jj >> 3) + 2) & 3) * 4096, g3 = (((jj >> 3) + 3) & 3) * 4096;
    auto ldg = [&]() {
#pragma unroll
      for (int i = 0; i < 8; ++i) gc[i] = __builtin_amdgcn_raw_buffer_load_b128(ws, gb + (i & 3) * 1024, i < 4 ? g2 : g3, SC1);
    };
    auto mq = [&](const bf8 (&X)[4], const bf8 (&W)[2]) {
#pragma unroll
      for (int i = 0; i < 4; ++i) { qa[0][i] = mfma16(W[0], X[i], qa[0][i]); qa[1][i] = mfma16(W[1], X[i], qa[1][i]); }
    };
#pragma unroll
    for (int s = 0; s < 16; s += 2) {
      rdq(X1, W1, s + 1); mq(X0, W0); ILV(8, 8); SB();
      if (s == 2 || s == 6) lbar();
      if (s == 6) { ldg(); SB(); }
      if (s < 14) { rdq(X0, W0, s + 2); mq(X1, W1); ILV(8, 8); }
      else mq(X1, W1);
      SB();
    }
    ST(4);
    u2 pq[2][4];
#pragma unroll
    for (int i = 0; i < 8; ++i) pq[i >> 2][i & 3] = (u2){phi2(qa[i >> 2][i & 3][0], qa[i >> 2][i & 3][1]), phi2(qa[i >> 2][i & 3][2], qa[i >> 2][i & 3][3])};
    unsigned ao[4], og[4];
#pragma unroll
    for (int i = 0; i < 4; ++i) og[i] = WS_O + ((tile * 8 + th * 4 + i) * 16 + cg * 2 + hp) * 1024 + lane * 16;
#pragma unroll
    for (int nt = 0; nt < 4; ++nt) ao[nt] = (4 * q + qq) * 128 + ((((2 * nt) ^ (((2 * q + (qq >> 1)) & 3) << 1)) + (pp >> 1)) << 4) + 8 * (pp & 1);
    SB();
    {
      f4 kvp;
#pragma unroll 1
      for (unsigned rep = 0; rep < SPIN_CAP; ++rep) {
        unsigned bad = 0;
        kvp = (f4){0, 0, 0, 0};
#pragma unroll
        for (int i = 0; i < 8; ++i) { gc[i].x ^= t; gc[i].z ^= t; bad |= gc[i].x | gc[i].z; kvp += __builtin_bit_cast(f4, gc[i]); }
        if (__all((bad & 0xffu) == 0)) break;
        asm volatile("" ::: "memory");
        ldg();
      }
      *(LAS f4*)(L + SCR + 4096 + w * 1024 + lane * 16) = kvp;
    }
    asm volatile("s_waitcnt lgkmcnt(0)\n\ts_barrier" : "+v"(pq[0][0]), "+v"(pq[0][1]), "+v"(pq[0][2]), "+v"(pq[0][3]), "+v"(pq[1][0]), "+v"(pq[1][1]), "+v"(pq[1][2]), "+v"(pq[1][3]),
                 "+v"(og[0]), "+v"(og[1]), "+v"(og[2]), "+v"(og[3]) :: "memory");
    ST(5);
    const unsigned ex = TRB + th * 16384 + hp * 1024 + lane * 16;
    const f4 zero4 = {0, 0, 0, 0}, eps4 = {EPS, EPS, EPS, EPS};
    f4 num[2][4], den[2][4];
#pragma unroll
    for (int h = 0; h < 2; ++h) {
      const f4 kv = *(const LAS f4*)(L + SCR + (2 * hp + h) * 1024 + lane * 16) + *(const LAS f4*)(L + SCR + 4096 + (2 * hp + h) * 1024 + lane * 16);
      const u2 zb = *(const LAS u2*)(L + SCR + 8192 + (2 * hp + h) * 512 + lane * 8), kb2 = {pk2(kv[0], kv[1]), pk2(kv[2], kv[3])};
#pragma unroll
      for (int i = 0; i < 4; ++i) { den[h][i] = mfma16k16(zb, pq[h][i], eps4); num[h][i] = mfma16k16(kb2, pq[h][i], zero4); }
    }
    SB();
#pragma unroll
    for (int i = 0; i < 4; ++i) {
      u4 so;
#pragma unroll
      for (int h = 0; h < 2; ++h) {
        const float rc = __builtin_amdgcn_rcpf(den[h][i][0]);
        const f4 n = num[h][i];
        so[2 * h] = pk2((f2){n[0], n[1]} * rc);
        so[2 * h + 1] = pk2((f2){n[2], n[3]} * rc);
      }
      __builtin_amdgcn_raw_buffer_store_b128(so, ws, og[i], 0, SC1);
      *(LAS u4*)(L + ex + (i >> 1) * 8192 + (i & 1) * 2048) = so;
    }
    lbar();
    ST(6);
    u4 of[32];
#pragma unroll
    for (int i = 0; i < 4; ++i) of[i] = *(const LAS u4*)(L + TRB + w * 8192 + ((i & 1) * 2 + (i >> 1)) * 1024 + lane * 16);
    if (!NODRAIN) drain();
    __builtin_amdgcn_raw_buffer_store_b64((u2){tag, ~tag}, ws, lane == 0 ? WS_FA + ((tile * 2 + th) * 16 + cg * 2 + hp) * 128 : 0x80000000u, 0, SC1);
    unsigned tw[4], trd[4], so_ = ((tile * 128 + w * 32 + q) * 1024 + cg * 64 + l15 * 4) * 4;
#pragma unroll
    for (int i = 0; i < 4; ++i) {
      tw[i] = TRB + w * 8192 + l15 * 256 + (((4 * i + q) ^ l15) << 4);
      trd[i] = TRB + w * 8192 + (4 * i + q) * 256 + ((l15 ^ (4 * i + q)) << 4);
      asm volatile("" : "+v"(tw[i]), "+v"(trd[i]));
    }
    asm volatile("s_barrier" : "+v"(so_) :: "memory");
    ST(7);
    f4 acc[2][4];
#pragma unroll
    for (int i = 0; i < 8; ++i) acc[i >> 2][i & 3] = zero4;
    const unsigned ob = WS_O + (tile * 8 + w * 2) * 16384 + lane * 16;
    bf8 wf[2][4];
    auto rdo = [&](int k) {
#pragma unroll
      for (int nt = 0; nt < 4; ++nt) { const char LAS* p = L + OBUF[k >> 2] + (k & 3) * 4096 + ao[nt]; wf[k & 1][nt] = ldstr(p, p + 2048); }
    };
    auto ldo = [&](int k) {
      const unsigned so = (2 * seq(k >> 1) + (k & 1)) * 1024;
      of[2 * k] = __builtin_amdgcn_raw_buffer_load_b128(ws, ob, so, PAY_AUX);
      of[2 * k + 1] = __builtin_amdgcn_raw_buffer_load_b128(ws, ob + 16384, so, PAY_AUX);
    };
    auto mo = [&](int k) {
#pragma unroll
      for (int nt = 0; nt < 4; ++nt) {
        acc[0][nt] = mfma16(wf[k & 1][nt], asbf8(of[2 * k]), acc[0][nt]);
        acc[1][nt] = mfma16(wf[k & 1][nt], asbf8(of[2 * k + 1]), acc[1][nt]);
      }
    };
    rdo(0); rdo(1); SB();
    mo(0); rdo(2); SB();
    mo(1); SB();
    if (FD) __builtin_amdgcn_s_sleep(FD);
    wait_flags(ws, WS_FA + ((tile * 2 + th) * 16 + l15) * 128, tag);
    if (DLA) __builtin_amdgcn_s_sleep(DLA);
    ST(8); CT(14);
#pragma unroll
    for (int k = 2; k < 2 + OD; ++k) ldo(k);
    SB();
#pragma unroll
    for (int k = 2; k < 16; ++k) {
      if (k + OD < 16) ldo(k + OD);
      if (k + 1 < 16) rdo(k + 1);
      mo(k);
#pragma unroll
      for (int i_ = 0; i_ < 8; ++i_) { SG(0x008, 1); SG(0x100, 1); if (i_ == 1 || i_ == 5) SG(0x020, 1); }
      SB();
    }
    ST(9); CT(15);
#pragma unroll
    for (int mt = 0; mt < 2; ++mt)
#pragma unroll
      for (int nt = 0; nt < 4; ++nt) {
        const f4 v = acc[mt][nt];
        const unsigned p0 = pk2(v[0], v[1]), p1 = pk2(v[2], v[3]);
        *(LAS f4*)(L + tw[nt] + mt * 4096) = (f4){bflo(p0), bfhi(p0), bflo(p1), bfhi(p1)};
      }
    u4 ov[8];
#pragma unroll
    for (int i = 0; i < 8; ++i) ov[i] = *(const LAS u4*)(L + trd[i & 3] + (i >> 2) * 4096);
    SB();
#pragma unroll
    for (int i = 0; i < 8; ++i) __builtin_amdgcn_raw_buffer_store_b128(ov[i], ro, so_, i * 16384, OUT_AUX);
    ST(10);
  } else {
    constexpr unsigned SLOT = 32768, SCR = SLOT, PAR = 4 * SLOT;
    const int wp = w & 1, wt = w >> 1;
    const unsigned ecol = WS_EST + (cg * 64 + wp * 32 + l31) * 8;
    if (helper) {
      unsigned vx[4], vw[4], vc[4];
#pragma unroll
      for (int i = 0; i < 4; ++i) {
        const unsigned sw = (c8 ^ ((4 * i + (r8 >> 1)) & 7)) << 4;
        vx[i] = (64 * (w >> 1) + 32 * (i & 1) + 16 * (r8 >> 2) + 8 * (r8 & 1) + 4 * (w & 1) + 2 * (i >> 1) + ((r8 >> 1) & 1)) * 1024 + sw - 1024 * i;
        vw[i] = (16 * (i & 1) + 8 * (r8 >> 2) + 4 * (i >> 1) + (r8 & 3)) * 1024 + sw - 1024 * i;
        vc[i] = (8 * i + r8) * 1024 + sw - 1024 * i;
      }
      const GAS char* xbase = (const GAS char*)xb + (size_t)tile * 131072;
      const GAS char* wbase = (const GAS char*)((w & 1) ? bim : bre) + (cg * 64 + (w >> 1) * 32) * 1024;
      auto issue = [&](int kk) {
        const int kc = (kk + rot) & 7;
        const unsigned s = L0 + (kk % 5) * SLOT + w * 4096;
        dmax(s, xbase + kc * 128, vx[0], vx[1], vx[2], vx[3]);
        dma4(s + 16384, wbase + kc * 128, vw[0], vw[1], vw[2], vw[3]);
      };
      ST(9); issue(0); ST(10); issue(1); ST(11);
      LOAD_EPOCH();
      const GAS char* cbase = (const GAS char*)((w & 1) ? cim : cre) + cg * 65536;
      auto issue_c = [&](int piece) {
        const unsigned s = L0 + ((piece + 3) % 5) * SLOT + w * 8192;
        const GAS char* cb = cbase + seq(2 * piece + (w >> 1)) * 128;
        dma4(s, cb, vc[0], vc[1], vc[2], vc[3]);
        dma4(s + 4096, cb + 32768, vc[0], vc[1], vc[2], vc[3]);
      };
      asm volatile("s_waitcnt vmcnt(8) lgkmcnt(0)\n\ts_barrier" : "+s"(epoch) :: "memory");
      issue(2);
      const f4 par = *(const LAS f4*)(L + PAR + w * 1024 + lane * 16);
      const f2 l64 = {par[0], par[1]}, l128 = {par[2], par[3]};
      f2 l256 = cmul(l128, l128);
      f2 wgt = hh ? l64 : (f2){1.f, 0.f};
      if (wt) wgt = cmul(wgt, l128);
      const unsigned tag = epoch + 1, t = tag & 0xffu;
      unsigned eo[8], et[8];
#pragma unroll
      for (int m = 0; m < 8; ++m) {
        const int i = 2 * tile - 1 - (2 * wt + hh) - 4 * m;
        eo[m] = i >= 0 ? ecol + i * 4096 : 0x80000000u;
        et[m] = i >= 0 ? t : 0u;
        asm volatile("" : "+v"(eo[m]), "+v"(et[m]));
      }
      asm volatile("" : "+v"(l256), "+v"(wgt));
      wait_bar<8>(); issue(3);
      wait_bar<8>(); issue(4);
      wait_bar<8>(); issue(5);
      wait_bar<8>(); issue(6);
      wait_bar<8>(); issue(7);
      wait_bar<8>(); issue_c(0);
      wait_bar<8>(); ST(1);
      if (NC_EARLY > 1) issue_c(1);
      if (NC_EARLY > 2) issue_c(2);
      ST(2);
      if (PD) { asm volatile("s_barrier" ::: "memory"); __builtin_amdgcn_s_sleep(PD); }
      if (PRI & 2) __builtin_amdgcn_s_setprio(3);
      f2 cin;
#pragma unroll 1
      for (unsigned sp = 0; sp < SPIN_CAP; ++sp) {
      asm volatile("" ::: "memory");
        u2 g[8];
#pragma unroll
        for (int m = 0; m < 8; ++m) g[m] = __builtin_amdgcn_raw_buffer_load_b64(ws, eo[m], 0, SC1);
        unsigned bad = 0;
        cin = (f2){0, 0};
#pragma unroll
        for (int m = 7; m >= 0; --m) {
          const unsigned cx = g[m].x ^ et[m];
          bad |= cx;
          cin = cfma(l256, cin, (f2){__uint_as_float(cx), __uint_as_float(g[m].y)});
        }
        if (__all((bad & 0xffu) == 0)) break;
      }
      ST(5);
      cin = cmul(cin, wgt);
      f2 chi;
      const f2 clo = swap_lo(cin, chi);
      if (hh == 0) *(LAS f2*)(L + SCR + (wp * 2 + wt) * 256 + l31 * 8) = clo + chi;
      ST(6);
      lbar();
      if (WOX & 2) issue_c(2);
      lbar();
      ST(7);
      if (NC_EARLY <= 1) issue_c(1);
      if (NC_EARLY <= 2 && !(WOX & 2)) issue_c(2);
      issue_c(3);
      wait_bar<0>();
      ST(8); HFLUSH();
      return;
    }
    LOAD_EPOCH();
    const int pl = cg * 64 + wp * 32 + 8 * (l15 >> 2) + 4 * (q & 1) + (l15 & 3);
    const unsigned plr = lre[pl], pli = lim[pl], pdt = ldt[pl];
    u4 dv[2];
#pragma unroll
    for (int i = 0; i < 2; ++i) dv[i] = *(const GAS u4*)(dsk + cg * 64 + 32 * i + 8 * q);
    unsigned warm0 = 0, warm1 = 0;
    if (TOUCH) {
      warm0 = __builtin_amdgcn_raw_buffer_load_b32(ws, lane < 16 ? WS_FB + ((tile * 2 + wt) * 16 + lane) * 128 : lane == 16 ? WS_EST + tile * 8192
                                                   : lane == 17 ? WS_H + (tile * 4 + w) * 65536 : 0x80000000u, 0, SC1);
      warm1 = __builtin_amdgcn_raw_buffer_load_b32(ro, lane < 2 ? (tile * 128 + w * 32 + lane * 16) * 4096 + 2048 : 0x80000000u, 0, SC1);
    }
    SB();
    f2 lam[2], l8[2], kap, l16, l32, l64;
    {
      const float lr = bflo(plr), li = bflo(pli);
      const float dt = bflo(pk2(expf(bflo(pdt)), 0.f));
      const float mag = expf(dt * lr);
      float sn, cs;
      sincosf(dt * li, &sn, &cs);
      const f2 l1 = {mag * cs, mag * sn};
      const float inv = 2.f / (lr * lr + li * li), nr = l1.x - 1.f, ni = l1.y;
      kap = (f2){(nr * lr + ni * li) * inv, (ni * lr - nr * li) * inv};
      f2 t = cmul(l1, l1); t = cmul(t, t); t = cmul(t, t);
      l16 = cmul(t, t); l32 = cmul(l16, l16); l64 = cmul(l32, l32);
      const f2 l128 = cmul(l64, l64);
      *(LAS f4*)(L + PAR + w * 1024 + lane * 16) = (f4){l64.x, l64.y, l128.x, l128.y};
      lam[0] = swap16(l1, l1, lam[1]);
      l8[0] = swap16(t, t, l8[1]);
    }
    const unsigned ax[2] = {(unsigned)(l15 * 128 + ((q ^ (l15 >> 1)) << 4)), (unsigned)(l15 * 128 + (((4 + q) ^ (l15 >> 1)) << 4))};
    f4 u[2][2][4];
#pragma unroll
    for (int i = 0; i < 16; ++i) u[i >> 3][(i >> 2) & 1][i & 3] = (f4){0, 0, 0, 0};
    bf8 Xa[2], Xb[2], W0[4], W1[4];
    auto rdx = [&](bf8 (&X)[2], int kk, int ks, int h) {
#pragma unroll
      for (int i = 0; i < 2; ++i) X[i] = ldsr(L + (kk % 5) * SLOT + wt * 8192 + (2 * h + i) * 2048 + ax[ks]);
    };
    auto rdw = [&](bf8 (&W)[4], int kk, int ks) {
#pragma unroll
      for (int i = 0; i < 4; ++i) W[i] = ldsr(L + (kk % 5) * SLOT + 16384 + wp * 8192 + (i & 1) * 4096 + (i >> 1) * 2048 + ax[ks]);
    };
    auto mm = [&](const bf8 (&X)[2], const bf8 (&W)[4], int h) {
#pragma unroll
      for (int i = 0; i < 2; ++i)
#pragma unroll
        for (int j = 0; j < 4; ++j) u[j >> 1][j & 1][2 * h + i] = mfma16(X[i], W[j], u[j >> 1][j & 1][2 * h + i]);
    };
    asm volatile("s_waitcnt lgkmcnt(0)\n\ts_barrier" : "+s"(epoch), "+v"(lam[0]), "+v"(lam[1]), "+v"(l8[0]), "+v"(l8[1]), "+v"(kap), "+v"(l16), "+v"(l32), "+v"(l64),
                 "+v"(dv[0]), "+v"(dv[1]) : "v"(warm0), "v"(warm1), "v"(cpf) : "memory");
    ST(1);
    const unsigned tag = epoch + 1, t = tag & 0xffu;
    rdw(W0, 0, 0); rdx(Xa, 0, 0, 0);
    SB();
#pragma unroll
    for (int kk = 0; kk < 8; ++kk) {
      rdx(Xb, kk, 0, 1); mm(Xa, W0, 0); ILV(2, 8); SB(); PACE(BN);
      rdw(W1, kk, 1); rdx(Xa, kk, 1, 0); mm(Xb, W0, 1); ILV(6, 8); SB(); PACE(BN);
      rdx(Xb, kk, 1, 1); mm(Xa, W1, 0); ILV(2, 8); SB();
      if (kk < 7) { lbar(); if (kk == 6) ST(2); rdw(W0, kk + 1, 0); rdx(Xa, kk + 1, 0, 0); mm(Xb, W1, 1); ILV(6, 8); }
      else mm(Xb, W1, 1);
      SB(); if (kk < 7) PACE(BN);
    }
    ST(3);
    f2 gr[2][8], gi[2][8];
#pragma unroll
    for (int i = 0; i < 16; ++i) {
      const int pt = i >> 3, a = i & 7;
      gr[pt][a] = (f2){u[pt][0][a >> 1][2 * (a & 1)], u[pt][0][a >> 1][2 * (a & 1) + 1]};
      gi[pt][a] = (f2){u[pt][1][a >> 1][2 * (a & 1)], u[pt][1][a >> 1][2 * (a & 1) + 1]};
    }
    {
      f2 R0 = {lam[0].x, lam[0].x}, I0 = {lam[0].y, lam[0].y}, N0 = -I0, R1 = {lam[1].x, lam[1].x}, I1 = {lam[1].y, lam[1].y}, N1 = -I1;
      asm volatile("s_nop 7" : "+v"(gr[0][7]), "+v"(gi[0][7]), "+v"(gr[1][7]), "+v"(gi[1][7]), "+v"(gr[0][5]), "+v"(gi[0][5]), "+v"(gr[1][5]), "+v"(gi[1][5]),
                   "+v"(R0), "+v"(I0), "+v"(N0), "+v"(R1), "+v"(I1), "+v"(N1));
#pragma unroll
      for (int a = 1; a < 8; ++a)
        asm volatile("v_pk_fma_f32 %0, %10, %5, %0\n\tv_pk_fma_f32 %1, %9, %4, %1\n\tv_pk_fma_f32 %2, %13, %7, %2\n\tv_pk_fma_f32 %3, %12, %6, %3\n\t"
                     "v_pk_fma_f32 %0, %8, %4, %0\n\tv_pk_fma_f32 %1, %8, %5, %1\n\tv_pk_fma_f32 %2, %11, %6, %2\n\tv_pk_fma_f32 %3, %11, %7, %3"
                     : "+v"(gr[0][a]), "+v"(gi[0][a]), "+v"(gr[1][a]), "+v"(gi[1][a])
                     : "v"(gr[0][a - 1]), "v"(gi[0][a - 1]), "v"(gr[1][a - 1]), "v"(gi[1][a - 1]), "v"(R0), "v"(I0), "v"(N0), "v"(R1), "v"(I1), "v"(N1));
    }
    const f2 e0[2] = {{gr[0][7].x, gi[0][7].x}, {gr[1][7].x, gi[1][7].x}};
    f2 vodd;
    const f2 veven = swap16(cfma(l8[0], e0[0], (f2){gr[0][7].y, gi[0][7].y}), cfma(l8[1], e0[1], (f2){gr[1][7].y, gi[1][7].y}), vodd);
    f2 ehi;
    const f2 elo = swap_lo(cfma(l16, veven, vodd), ehi);
    const f2 e64 = cfma(l32, elo, ehi);
    __builtin_amdgcn_raw_buffer_store_b64((u2){tagv(e64.x, t), __float_as_uint(e64.y)}, ws, hh == 0 ? ecol + (tile * 2 + wt) * 4096 : 0x80000000u, 0, SC1);
    if (wt == 0 && hh == 0) *(LAS f2*)(L + SCR + 1024 + wp * 256 + l31 * 8) = e64;
    if (PD) asm volatile("s_barrier" ::: "memory");
    SB();
    ST(4);
    u4 xd[2][2];
#pragma unroll
    for (int i = 0; i < 4; ++i)
      xd[i >> 1][i & 1] = *(const GAS u4*)(xb + (tile * 128 + 64 * wt + 16 * (l15 >> 2) + 8 * (l15 & 1) + 4 * (w & 1) + 2 * (i >> 1) + ((l15 >> 1) & 1)) * 512
                                           + cg * 64 + 32 * (i & 1) + 8 * q);
    SB();
    f2 kp[2], ke[2], pw[2][8];
    kp[0] = swap16(kap, kap, kp[1]);
#pragma unroll
    for (int pt = 0; pt < 2; ++pt) {
      const f2 KR = {kp[pt].x, kp[pt].x}, KI = {kp[pt].y, kp[pt].y};
      ke[pt] = cmul(kp[pt], e0[pt]);
#pragma unroll
      for (int a = 0; a < 8; ++a) {
        const f2 xr = gr[pt][a], xi = gi[pt][a];
        gr[pt][a] = fma2(KR, xr, -KI * xi); gi[pt][a] = fma2(-KR, xi, -KI * xr);
        asm volatile("" : "+v"(gr[pt][a]), "+v"(gi[pt][a]));
      }
      pw[pt][0] = (f2){lam[pt].x, -lam[pt].y};
#pragma unroll
      for (int k = 1; k < 8; ++k) { pw[pt][k] = cmul(pw[pt][k >> 1], pw[pt][(k - 1) >> 1]); asm volatile("" : "+v"(pw[pt][k])); }
    }
    u2 idn = {(l15 == 4 * q ? 0x3f80u : 0u) | (l15 == 4 * q + 1 ? 0x3f800000u : 0u), (l15 == 4 * q + 2 ? 0x3f80u : 0u) | (l15 == 4 * q + 3 ? 0x3f800000u : 0u)};
#pragma unroll
    for (int i = 0; i < 4; ++i) {
#pragma unroll
      for (int j = 0; j < 4; ++j) xd[i >> 1][i & 1][j] = pk2(bflo(xd[i >> 1][i & 1][j]) * bflo(dv[i & 1][j]), bfhi(xd[i >> 1][i & 1][j]) * bfhi(dv[i & 1][j]));
      asm volatile("" : "+v"(xd[i >> 1][i & 1]));
    }
    f2 me = hh ? cmul(kap, l32) : kap, be = hh ? cmul(kap, elo) : (f2){0.f, 0.f};
    f2 mo = cmul(l16, me), bo = cfma(l16, be, cmul(kap, veven));
    unsigned ho[4];
#pragma unroll
    for (int i = 0; i < 4; ++i) ho[i] = WS_H + (tile * 8 + wt * 4 + i) * 32768 + (cg * 2 + wp) * 2048 + lane * 16;
    asm volatile("s_waitcnt lgkmcnt(0)\n\ts_barrier" : "+v"(ho[0]), "+v"(ho[1]), "+v"(ho[2]), "+v"(ho[3]), "+v"(me), "+v"(be), "+v"(mo), "+v"(bo),
                 "+v"(ke[0]), "+v"(ke[1]), "+v"(idn) :: "memory");
    ST(5);
    f2 cin = *(const LAS f2*)(L + SCR + wp * 512 + l31 * 8) + *(const LAS f2*)(L + SCR + wp * 512 + 256 + l31 * 8);
    const f2 esib = *(const LAS f2*)(L + SCR + 1024 + wp * 256 + l31 * 8);
    if (wt) cin = cfma(l64, cin, esib);
    f2 d0[2], DR[2], DJ[2];
    d0[0] = swap16(cfma(me, cin, be), cfma(mo, cin, bo), d0[1]);
#pragma unroll
    for (int pt = 0; pt < 2; ++pt) {
      const f2 d1 = cfma(l8[pt], d0[pt], ke[pt]);
      DR[pt] = (f2){d0[pt].x, d1.x}; DJ[pt] = (f2){-d0[pt].y, -d1.y};
    }
    const f4 zero4 = {0, 0, 0, 0};
    f4 T[8][2];
    u4 ow[8];
    auto fix = [&](int i) {
      const int mt = i >> 1, c = i & 1;
#pragma unroll
      for (int pt = 0; pt < 2; ++pt) {
        u2 a;
#pragma unroll
        for (int h = 0; h < 2; ++h) {
          const f2 P = pw[pt][2 * mt + h], PR = {P.x, P.x}, PI = {P.y, P.y};
          a[h] = pk2(c ? fma2(PI, DR[pt], fma2(PR, DJ[pt], gi[pt][2 * mt + h])) : fma2(-PI, DJ[pt], fma2(PR, DR[pt], gr[pt][2 * mt + h])));
        }
        T[i][pt] = mfma16k16(a, idn, zero4);
      }
    };
    auto put = [&](int i) {
      ow[i] = (u4){pk2(T[i][0][0], T[i][0][1]), pk2(T[i][0][2], T[i][0][3]), pk2(T[i][1][0], T[i][1][1]), pk2(T[i][1][2], T[i][1][3])};
      __builtin_amdgcn_raw_buffer_store_b128(ow[i], ws, ho[i >> 1] + (i & 1) * 1024, 0, SC1);
    };
    fix(0); SB();
#pragma unroll
    for (int i = 1; i < 8; ++i) { fix(i); SB(); put(i - 1); SB(); }
    put(7);
    SB();
    const unsigned ex = 2 * SLOT + wt * 16384 + wp * 4096 + lane * 16;
#pragma unroll
    for (int i = 0; i < 8; ++i) *(LAS u4*)(L + ex + (i >> 2) * 8192 + (i & 1) * 2048 + ((i >> 1) & 1) * 1024) = ow[i];
    lbar();
    ST(6);
    if (!NODRAIN) drain();
    __builtin_amdgcn_raw_buffer_store_b64((u2){tag, ~tag}, ws, lane == 0 ? WS_FB + ((tile * 2 + wt) * 16 + cg * 2 + wp) * 128 : 0x80000000u, 0, SC1);
    ST(7);
    const unsigned hb = WS_H + (tile * 8 + w * 2) * 32768 + lane * 16;
    u4 hf[64];
    bf8 cf[2][4];
    f4 acc[2][4];
    auto ldh = [&](int g) {
      hf[2 * g] = __builtin_amdgcn_raw_buffer_load_b128(ws, hb + (g & 3) * 1024, seq(g >> 2) * 4096, PAY_AUX);
      hf[2 * g + 1] = __builtin_amdgcn_raw_buffer_load_b128(ws, hb + 32768 + (g & 3) * 1024, seq(g >> 2) * 4096, PAY_AUX);
    };
    auto rdc = [&](int g) {
      const int tt = g >> 2;
#pragma unroll
      for (int nt = 0; nt < 4; ++nt)
        cf[g & 1][nt] = ldsr(L + (((tt >> 1) + 3) % 5) * SLOT + (tt & 1) * 16384 + (g & 1) * 8192 + nt * 2048 + ax[(g >> 1) & 1]);
    };
    auto mh = [&](int g) {
#pragma unroll
      for (int nt = 0; nt < 4; ++nt) {
        acc[0][nt] = mfma16(cf[g & 1][nt], asbf8(hf[2 * g]), acc[0][nt]);
        acc[1][nt] = mfma16(cf[g & 1][nt], asbf8(hf[2 * g + 1]), acc[1][nt]);
      }
    };
#pragma unroll
    for (int i = 0; i < 8; ++i) hf[i] = *(const LAS u4*)(L + 2 * SLOT + w * 8192 + i * 1024 + lane * 16);
    rdc(0);
    {
      u4 ida[2];
#pragma unroll
      for (int hf_ = 0; hf_ < 2; ++hf_)
#pragma unroll
        for (int jq = 0; jq < 4; ++jq) {
          const int k0 = 8 * q + 2 * jq - 16 * hf_;
          ida[hf_][jq] = (l15 == k0 ? 0x3f80u : 0u) | (l15 == k0 + 1 ? 0x3f800000u : 0u);
        }
#pragma unroll
      for (int i = 0; i < 8; ++i) acc[i >> 2][i & 3] = mfma16(asbf8(ida[i & 1]), asbf8(xd[i >> 2][(i >> 1) & 1]), zero4);
    }
    SB();
#pragma unroll
    for (int g = 0; g < 4; ++g) { rdc(g + 1); mh(g); ILV(4, 8); SB(); }
    unsigned tw[4], trd[4], so_ = ((tile * 128 + 64 * wt + 4 * (w & 1) + 8 * (q & 1) + (q >> 1)) * 1024 + 512 + cg * 64 + l15 * 4) * 4;
#pragma unroll
    for (int i = 0; i < 4; ++i) {
      tw[i] = 2 * SLOT + w * 8192 + l15 * 256 + (((4 * i + q) ^ l15) << 4);
      trd[i] = 2 * SLOT + w * 8192 + (4 * i + q) * 256 + ((l15 ^ (4 * i + q)) << 4);
      asm volatile("" : "+v"(tw[i]), "+v"(trd[i]));
    }
    asm volatile("s_barrier" : "+v"(so_) :: "memory");
    if (FD) __builtin_amdgcn_s_sleep(FD);
    wait_flags(ws, WS_FB + ((tile * 2 + wt) * 16 + l15) * 128, tag);
    if (DLB) __builtin_amdgcn_s_sleep(DLB);
    ST(8); CT(14);
#pragma unroll
    for (int g = 4; g < 4 + HD / 2; ++g) ldh(g);
    SB();
#pragma unroll
    for (int g = 4; g < 32; ++g) {
      if (g + HD / 2 < 32) ldh(g + HD / 2);
      if (g + 1 < 32) rdc(g + 1);
      mh(g);
#pragma unroll
      for (int i_ = 0; i_ < 4; ++i_) { SG(0x008, 1); SG(0x100, 1); }
      SG(0x008, 1); SG(0x020, 1); SG(0x008, 1); SG(0x020, 1); SG(0x008, 2);
      SB(); if (g < 30) PACE(HN);
    }
    ST(9); CT(15);
#pragma unroll
    for (int i = 0; i < 8; ++i) *(LAS f4*)(L + tw[i & 3] + (i >> 2) * 4096) = acc[i >> 2][i & 3];
    u4 ov[8];
#pragma unroll
    for (int i = 0; i < 8; ++i) ov[i] = *(const LAS u4*)(L + trd[i & 3] + (i >> 2) * 4096);
    SB();
#pragma unroll
    for (int i = 0; i < 8; ++i) __builtin_amdgcn_raw_buffer_store_b128(ov[i], ro, so_, (16 * (i & 3) + 2 * (i >> 2)) * 4096, OUT_AUX);
    ST(10);
  }
  drain();
  ST(11);
  if (bid == AMP_BID && w == 0) {
    const u64 until = st[11] + AMP_K * (st[AMP_B] - st[AMP_A]);
    while (__builtin_amdgcn_s_memrealtime() < until) __builtin_amdgcn_s_sleep(2);
  }
}

extern "C" void kernel_launch(void* const* d_in, const int* in_sizes, int n_in, void* d_out, int out_size, void* d_ws, size_t ws_size,
                              hipStream_t stream) {
  (void)hipFuncSetAttribute((const void*)fused_kernel, hipFuncAttributeMaxDynamicSharedMemorySize, LDS_BYTES);
  fused_kernel<<<256, 512, LDS_BYTES, stream>>>((const u16*)d_in[0], (const u16*)d_in[1], (const u16*)d_in[2], (const u16*)d_in[3],
                                               (const u16*)d_in[4], (const u16*)d_in[5], (const u16*)d_in[6], (const u16*)d_in[7],
                                               (const u16*)d_in[8], (const u16*)d_in[9], (const u16*)d_in[10], (const u16*)d_in[11],
                                               (float*)d_out, (char*)d_ws);
}
```

```cpp
#define BN 4
#define AN 12
#include <hip/hip_runtime.h>
#include <hip/hip_bf16.h>

#define DI __device__ __forceinline__
#define LAS __attribute__((address_space(3)))
#define GAS __attribute__((address_space(1)))
typedef unsigned short u16;
typedef unsigned long long u64;
typedef __bf16 bf8 __attribute__((ext_vector_type(8)));
typedef __bf16 bf2 __attribute__((ext_vector_type(2)));
typedef short s4 __attribute__((ext_vector_type(4)));
typedef short s8 __attribute__((ext_vector_type(8)));
typedef float f16v __attribute__((ext_vector_type(16)));
typedef float f4 __attribute__((ext_vector_type(4)));
typedef float f2 __attribute__((ext_vector_type(2)));
typedef unsigned u4 __attribute__((ext_vector_type(4)));
typedef unsigned u2 __attribute__((ext_vector_type(2)));

constexpr unsigned WS_EPOCH = 0;
constexpr unsigned WS_FA = 4096;
constexpr unsigned WS_FB = WS_FA + 65536;
constexpr unsigned WS_EST = WS_FB + 65536;
constexpr unsigned WS_Z = WS_EST + 131072;
constexpr unsigned WS_KV = WS_Z + 32768;
constexpr unsigned WS_O = 1u << 20;
constexpr unsigned WS_H = 3u << 20;
constexpr unsigned WS_END = 7u << 20;
constexpr unsigned WS_STAMP = 8u << 20;
constexpr int LDS_BYTES = 160 * 1024;
constexpr unsigned SPIN_CAP = 200000;
constexpr int SC1 = 16;

#define ROT 8
#define NC_EARLY 2
#define OUT_AUX 16
#define PAY_AUX 16
#define PULL 0
#define TOUCH 1
#define CPF 11
#define HROT 1
#define XNT 0
#define PD 4
#define FD 0
#define TD 0
#define NODRAIN 0
#define WOX 2
#define DLA 0
#define DLB 0
#define QN 0
#define HN 0
#define PRI 2
#define OD 6
#define HD 12

#define CT(i) do {} while (0)
#define ST(i) do {} while (0)
#define HFLUSH() do {} while (0)
#define SB() __builtin_amdgcn_sched_barrier(0)
#define PACE(n) do { if (n) { asm volatile("s_nop %0" :: "i"((n) > 16 ? 15 : ((n) ? (n) - 1 : 0))); if ((n) > 16) asm volatile("s_nop %0" :: "i"((n) > 16 ? (n) - 17 : 0)); SB(); } } while (0)
#define SG(m, n) __builtin_amdgcn_sched_group_barrier(m, n, 0)

DI unsigned pk2(float a, float b) { f2 v = {a, b}; return __builtin_bit_cast(unsigned, __builtin_convertvector(v, bf2)); }
DI unsigned pk2(f2 v) { return __builtin_bit_cast(unsigned, __builtin_convertvector(v, bf2)); }
DI float bflo(unsigned v) { return __uint_as_float(v << 16); }
DI float bfhi(unsigned v) { return __uint_as_float(v & 0xffff0000u); }
DI bf8 asbf8(u4 v) { return __builtin_bit_cast(bf8, v); }
DI f16v mfma32(bf8 a, bf8 b, f16v c) { return __builtin_amdgcn_mfma_f32_32x32x16_bf16(a, b, c, 0, 0, 0); }
DI f4 mfma16(bf8 a, bf8 b, f4 c) { return __builtin_amdgcn_mfma_f32_16x16x32_bf16(a, b, c, 0, 0, 0); }
DI f4 mfma16k16(u2 a, u2 b, f4 c) { return __builtin_amdgcn_mfma_f32_16x16x16bf16_1k(__builtin_bit_cast(s4, a), __builtin_bit_cast(s4, b), c, 0, 0, 0); }
DI bf8 ldsr(const char LAS* p) { return *(const LAS bf8*)p; }
DI bf8 ldstr(const char LAS* p0, const char LAS* p1) {
  s4 a = __builtin_amdgcn_ds_read_tr16_b64_v4i16((LAS s4*)p0), b = __builtin_amdgcn_ds_read_tr16_b64_v4i16((LAS s4*)p1);
  return __builtin_bit_cast(bf8, (s8)__builtin_shufflevector(a, b, 0, 1, 2, 3, 4, 5, 6, 7));
}
DI void dma4(unsigned lds, const GAS char* sb, unsigned v0, unsigned v1, unsigned v2, unsigned v3) {
  asm volatile("s_mov_b32 m0, %0\n\ts_nop 0\n\tglobal_load_lds_dwordx4 %1, %5\n\tglobal_load_lds_dwordx4 %2, %5 offset:1024\n\t"
               "global_load_lds_dwordx4 %3, %5 offset:2048\n\tglobal_load_lds_dwordx4 %4, %5 offset:3072"
               :: "s"(lds), "v"(v0), "v"(v1), "v"(v2), "v"(v3), "s"(sb) : "memory");
}
DI void dma4nt(unsigned lds, const GAS char* sb, unsigned v0, unsigned v1, unsigned v2, unsigned v3) {
  asm volatile("s_mov_b32 m0, %0\n\ts_nop 0\n\tglobal_load_lds_dwordx4 %1, %5 nt\n\tglobal_load_lds_dwordx4 %2, %5 offset:1024 nt\n\t"
               "global_load_lds_dwordx4 %3, %5 offset:2048 nt\n\tglobal_load_lds_dwordx4 %4, %5 offset:3072 nt"
               :: "s"(lds), "v"(v0), "v"(v1), "v"(v2), "v"(v3), "s"(sb) : "memory");
}
DI void dmax(unsigned lds, const GAS char* sb, unsigned v0, unsigned v1, unsigned v2, unsigned v3) { if (XNT) dma4nt(lds, sb, v0, v1, v2, v3); else dma4(lds, sb, v0, v1, v2, v3); }
DI void dma2(unsigned lds, const GAS char* sb, unsigned v0, unsigned v1) {
  asm volatile("s_mov_b32 m0, %0\n\ts_nop 0\n\tglobal_load_lds_dwordx4 %1, %3\n\tglobal_load_lds_dwordx4 %2, %3 offset:1024"
               :: "s"(lds), "v"(v0), "v"(v1), "s"(sb) : "memory");
}
DI void lbar() { asm volatile("s_waitcnt lgkmcnt(0)\n\ts_barrier" ::: "memory"); }
template <int N> DI void wait_bar() { asm volatile("s_waitcnt vmcnt(%0) lgkmcnt(0)\n\ts_barrier" :: "i"(N) : "memory"); }
DI void drain() { asm volatile("s_waitcnt vmcnt(0)" ::: "memory"); }
DI unsigned tagv(float x, unsigned t) { return (__float_as_uint(x) & 0xffffff00u) | t; }
DI f2 cmul(f2 a, f2 b) { return (f2){a.x * b.x - a.y * b.y, a.x * b.y + a.y * b.x}; }
DI f2 cfma(f2 a, f2 b, f2 c) { return (f2){fmaf(a.x, b.x, fmaf(-a.y, b.y, c.x)), fmaf(a.x, b.y, fmaf(a.y, b.x, c.y))}; }
DI f2 fma2(f2 a, f2 b, f2 c) { return __builtin_elementwise_fma(a, b, c); }
constexpr float EPS = 1e-6f, LOG2E = 1.44269504088896341f;
DI unsigned phi2(float s0, float s1) {
  const f2 a = fma2((f2){s0, s1}, (f2){LOG2E, LOG2E}, (f2){-EPS * LOG2E, -EPS * LOG2E});
  const f2 e = {__builtin_amdgcn_fmed3f(__builtin_amdgcn_exp2f(a.x), 0.f, 1.f), __builtin_amdgcn_fmed3f(__builtin_amdgcn_exp2f(a.y), 0.f, 1.f)};
  return pk2(e + (f2){__builtin_amdgcn_fmed3f(s0, EPS, 3.40282347e38f), __builtin_amdgcn_fmed3f(s1, EPS, 3.40282347e38f)});
}
DI f2 swap_lo(f2 v, f2& hi) {
  const auto x = __builtin_amdgcn_permlane32_swap(__float_as_uint(v.x), __float_as_uint(v.x), false, false);
  const auto y = __builtin_amdgcn_permlane32_swap(__float_as_uint(v.y), __float_as_uint(v.y), false, false);
  hi = (f2){__uint_as_float(x[1]), __uint_as_float(y[1])};
  return (f2){__uint_as_float(x[0]), __uint_as_float(y[0])};
}
DI f2 swap16(f2 a, f2 b, f2& rb) {
  const auto x = __builtin_amdgcn_permlane16_swap(__float_as_uint(a.x), __float_as_uint(b.x), false, false);
  const auto y = __builtin_amdgcn_permlane16_swap(__float_as_uint(a.y), __float_as_uint(b.y), false, false);
  rb = (f2){__uint_as_float(x[1]), __uint_as_float(y[1])};
  return (f2){__uint_as_float(x[0]), __uint_as_float(y[0])};
}
DI void wait_flags(__amdgpu_buffer_rsrc_t ws, unsigned off, unsigned tag) {
#pragma unroll 1
  for (unsigned sp = 0; sp < SPIN_CAP; ++sp) {
      asm volatile("" ::: "memory");
    const u2 f = __builtin_amdgcn_raw_buffer_load_b64(ws, off, 0, SC1);
    if (__all(f.x == tag && f.y == ~tag)) break;
    __builtin_amdgcn_s_sleep(1);
  }
  asm volatile("" ::: "memory");
}

__global__ void __launch_bounds__(512, 2)
fused_kernel(const u16* a0, const u16* a1, const u16* a2, const u16* a3, const u16* a4, const u16* a5, const u16* a6, const u16* a7,
             const u16* a8, const u16* a9, const u16* a10, const u16* a11, float* a12, char* a13) {
  extern __shared__ __attribute__((aligned(16))) char smem_[];
  char LAS* L = (char LAS*)smem_;
  const unsigned L0 = (unsigned)(size_t)L;
  ST(0);
  const int lane = threadIdx.x & 63;
  const int w8 = __builtin_amdgcn_readfirstlane(threadIdx.x >> 6), w = w8 & 3;
  unsigned cpf = 0;
  if (CPF && w8 == 0) {
    u64 pc;
    asm volatile("s_getpc_b64 %0" : "=s"(pc));
    cpf = __builtin_amdgcn_raw_buffer_load_b32(__builtin_amdgcn_make_buffer_rsrc((char*)(pc & ~127ull), 0, CPF * 4096, 0x00020000),
                                               lane < CPF ? ((blockIdx.x >> 3) * CPF + lane) * 128 : 0x80000000u, 0, 0);
  }
  typedef u64 u64x8 __attribute__((ext_vector_type(8)));
  typedef u64 u64x4 __attribute__((ext_vector_type(4)));
  typedef u64 u64x2 __attribute__((ext_vector_type(2)));
  u64x8 ka; u64x4 kb; u64x2 kc_;
  asm volatile("s_load_dwordx16 %0, %3, 0x0\n\ts_load_dwordx8 %1, %3, 0x40\n\ts_load_dwordx4 %2, %3, 0x60\n\ts_waitcnt lgkmcnt(0)"
               : "=&s"(ka), "=&s"(kb), "=&s"(kc_) : "s"(__builtin_amdgcn_kernarg_segment_ptr()) : "memory");
  const GAS u16 *xa = (const GAS u16*)ka[0], *wqkv = (const GAS u16*)ka[1], *wo = (const GAS u16*)ka[2], *xb = (const GAS u16*)ka[3];
  const GAS u16 *lre = (const GAS u16*)ka[4], *lim = (const GAS u16*)ka[5], *bre = (const GAS u16*)ka[6], *bim = (const GAS u16*)ka[7];
  const GAS u16 *cre = (const GAS u16*)kb[0], *cim = (const GAS u16*)kb[1], *ldt = (const GAS u16*)kb[2], *dsk = (const GAS u16*)kb[3];
  float* out = (float*)kc_[0]; char* wsp = (char*)kc_[1];
  const bool helper = w8 >= 4;
  const int bid = blockIdx.x, xcd = bid & 7, jj = bid >> 3;
  const int tile = (xcd >> 1) * 4 + (jj >> 3), cg = jj & 7;
  const int rot = ROT == 1 ? ((cg + 3 * (jj >> 3) + xcd) & 7) : ROT == 7 ? (((cg & 3) * 2 + (xcd >> 1)) & 7) : ROT == 12 ? ((2 * (cg & 3) + 2 * (xcd >> 1)) & 7) : ROT == 16 ? ((2 * (cg & 3) + ((jj >> 3) & 1) + 2 * (xcd >> 1)) & 7) : ((((cg + (jj >> 3)) % 3) * 3 + (xcd >> 1)) & 7);
  auto seq = [&](int t) { const int b = 2 * (jj >> 3), e = t - 1; return t == 0 ? cg : (b + e + (e >= ((cg - b) & 7) ? 1 : 0)) & 7; };
  const __amdgpu_buffer_rsrc_t ws = __builtin_amdgcn_make_buffer_rsrc(wsp, 0, WS_END, 0x00020000);
  const __amdgpu_buffer_rsrc_t ro = __builtin_amdgcn_make_buffer_rsrc(out, 0, 2048 * 1024 * 4, 0x00020000);
  const int r8 = lane >> 3, c8 = lane & 7;
  const int q = lane >> 4, l15 = lane & 15, l31 = lane & 31, hh = lane >> 5;
  unsigned epoch;
#define LOAD_EPOCH() asm volatile("s_load_dword %0, %1, 0x0" : "=s"(epoch) : "s"(wsp) : "memory")

  if ((xcd & 1) == 0) {
    constexpr unsigned WA = 131072, WB = 147456, SCR = 32768, TRB = 81920;
    constexpr unsigned QBUF[4] = {WB, 0, WA, WB}, OBUF[4] = {32768, 49152, 65536, 0};
    constexpr unsigned WBUF[8] = {WA, WB, 7 * 16384, 0, WA, WB, 0, WA};
    if (helper) {
      const int head = cg * 4 + w;
      unsigned vx[4], vw[2], vo[4];
#pragma unroll
      for (int i = 0; i < 4; ++i) vx[i] = (32 * w + 8 * i + r8) * 1024 + ((c8 ^ ((4 * i + (r8 >> 1)) & 7)) << 4) - 1024 * i;
#pragma unroll
      for (int i = 0; i < 2; ++i) vw[i] = (16 * w + 8 * i + r8) * 3072 + ((c8 ^ ((((r8 >> 1) & 1) | (i << 1)) << 1)) << 4) - 1024 * i;
      const GAS char* xbase = (const GAS char*)xa + (size_t)tile * 131072;
      const GAS char* wbase = (const GAS char*)wqkv + cg * 128;
      auto isx = [&](int kk, int slot) { dmax(L0 + slot * 16384 + w * 4096, xbase + ((kk + rot) & 7) * 128, vx[0], vx[1], vx[2], vx[3]); };
      auto isw = [&](unsigned dst, int kk, int seg) { dma2(L0 + dst + w * 2048, wbase + ((kk + rot) & 7) * (64 * 3072) + seg * 1024, vw[0], vw[1]); };
      auto issue = [&](int kk) { isx(kk, kk); isw(WBUF[kk], kk, 1); isw(WBUF[kk] + 8192, kk, 2); };
      auto isq = [&](int p) { isw(QBUF[p], 2 * p + 1, 0); isw(QBUF[p] + 8192, 2 * p + 2, 0); };
      ST(9); issue(0); ST(10); issue(1); ST(11);
      LOAD_EPOCH();
#pragma unroll
      for (int i = 0; i < 4; ++i) vo[i] = (8 * i + r8) * 1024 + ((c8 ^ (((r8 >> 1) & 3) << 1)) << 4) - 1024 * i;
      const GAS char* obase = (const GAS char*)wo + cg * 128;
      asm volatile("s_waitcnt vmcnt(8) lgkmcnt(0)\n\ts_barrier" : "+s"(epoch) :: "memory");
      issue(2);
      wait_bar<8>(); issue(3);
      wait_bar<8>(); issue(4);
      wait_bar<8>(); issue(5);
      wait_bar<8>(); issue(6);
      wait_bar<8>(); issue(7);
      wait_bar<8>();
      unsigned pfo;
      asm volatile("global_load_dword %0, %1, %2" : "=v"(pfo) : "v"(((jj >> 3) * 128 + w * 32 + l31) * 1024), "s"(obase) : "memory");
      isq(0);
      wait_bar<5>(); isq(1);
      wait_bar<4>(); isq(2);
      wait_bar<4>(); isq(3); isx(0, 1);
      asm volatile("s_waitcnt vmcnt(0)\n\ts_barrier" : "+v"(pfo) :: "memory");
      if (PRI & 1) __builtin_amdgcn_s_setprio(3);
      ST(1);
      const unsigned tag = epoch + 1, t = tag & 0xffu;
      const GAS char* kvb_ = (const GAS char*)wsp + WS_KV + head * 16384;
      const GAS char* zb_ = (const GAS char*)wsp + WS_Z + head * 1024;
      const unsigned qr = (jj >> 3) * 4096;
      const unsigned vk0 = lane * 16 + qr, vk1 = lane * 16 + ((qr + 4096) & 12288), vz = l15 * 64 + q * 16;
      u4 g[8], gz;
#define KVL(i, v) asm volatile("global_load_dwordx4 %0, %1, %2 offset:%3 sc1" : "=v"(g[i]) : "v"(v), "s"(kvb_), "i"((i & 3) * 1024) : "memory")
      asm volatile("global_load_dwordx4 %0, %1, %2 sc1" : "=v"(gz) : "v"(vz), "s"(zb_) : "memory");
      KVL(0, vk0); KVL(1, vk0); KVL(2, vk0); KVL(3, vk0); KVL(4, vk1); KVL(5, vk1); KVL(6, vk1); KVL(7, vk1);
      auto zsum = [&]() {
        float z0 = __uint_as_float(gz.x), z1 = __uint_as_float(gz.y), z2 = __uint_as_float(gz.z), z3 = __uint_as_float(gz.w);
#define ZS(r) "v_add_f32_dpp %0, %0, %0 row_ror:" #r " row_mask:0xf bank_mask:0xf\n\tv_add_f32_dpp %1, %1, %1 row_ror:" #r " row_mask:0xf bank_mask:0xf\n\t" \
              "v_add_f32_dpp %2, %2, %2 row_ror:" #r " row_mask:0xf bank_mask:0xf\n\tv_add_f32_dpp %3, %3, %3 row_ror:" #r " row_mask:0xf bank_mask:0xf\n\t"
        asm volatile("s_nop 1\n\t" ZS(8) ZS(4) ZS(2) ZS(1) "s_nop 0" : "+v"(z0), "+v"(z1), "+v"(z2), "+v"(z3));
        return (f4){z0, z1, z2, z3};
      };
      asm volatile("s_waitcnt vmcnt(8)" : "+v"(gz) :: "memory");
      gz.x ^= t; gz.z ^= t;
      unsigned bad = gz.x | gz.z;
      f4 zz = zsum(), kv = {0, 0, 0, 0};
      asm volatile("" : "+v"(zz));
#define KVA(i) asm volatile("s_waitcnt vmcnt(%1)" : "+v"(g[i]) : "i"(7 - i) : "memory"); g[i].x ^= t; g[i].z ^= t; bad |= g[i].x | g[i].z; kv += __builtin_bit_cast(f4, g[i]); asm volatile("" : "+v"(kv), "+v"(bad))
      KVA(0); KVA(1); KVA(2); KVA(3); KVA(4); KVA(5); KVA(6); KVA(7);
      ST(3);
      if (!__all((bad & 0xffu) == 0)) {
#pragma unroll 1
        for (unsigned rep = 0; rep < SPIN_CAP; ++rep) {
          asm volatile("" ::: "memory");
          gz = __builtin_amdgcn_raw_buffer_load_b128(ws, WS_Z + head * 1024 + vz, 0, SC1);
#pragma unroll
          for (int i = 0; i < 8; ++i) g[i] = __builtin_amdgcn_raw_buffer_load_b128(ws, WS_KV + head * 16384 + (i < 4 ? vk0 : vk1) + (i & 3) * 1024, 0, SC1);
          gz.x ^= t; gz.z ^= t;
          bad = gz.x | gz.z;
#pragma unroll
          for (int i = 0; i < 8; ++i) { g[i].x ^= t; g[i].z ^= t; bad |= g[i].x | g[i].z; }
          if (__all((bad & 0xffu) == 0)) break;
        }
        zz = zsum();
        kv = (f4){0, 0, 0, 0};
#pragma unroll
        for (int i = 0; i < 8; ++i) kv += __builtin_bit_cast(f4, g[i]);
      }
      ST(5);
      *(LAS f4*)(L + SCR + w * 1024 + lane * 16) = kv;
      *(LAS u2*)(L + SCR + 8192 + w * 512 + lane * 8) = (u2){pk2(zz[0], zz[1]), pk2(zz[2], zz[3])};
      ST(6);
      lbar();
      ST(7);
      lbar();
#pragma unroll
      for (int i = 0; i < 4; ++i)
        dma4(L0 + OBUF[i] + w * 4096, obase + (2 * seq(2 * i + (w >> 1)) + (w & 1)) * 32768, vo[0], vo[1], vo[2], vo[3]);
      wait_bar<0>();
      ST(8);
      HFLUSH();
      if (bid == 0 && w == 0) {
#pragma unroll 1
        for (unsigned sp = 0; sp < SPIN_CAP; ++sp) {
          asm volatile("" ::: "memory");
          const unsigned fo = (lane >> 3) * 4096 + (lane & 7) * 256;
          const u2 f0 = __builtin_amdgcn_raw_buffer_load_b64(ws, WS_FA + fo, 0, SC1), f1 = __builtin_amdgcn_raw_buffer_load_b64(ws, WS_FA + 32768 + fo, 0, SC1);
          const u2 f2_ = __builtin_amdgcn_raw_buffer_load_b64(ws, WS_FB + fo, 0, SC1), f3 = __builtin_amdgcn_raw_buffer_load_b64(ws, WS_FB + 32768 + fo, 0, SC1);
          if (__all(f0.x == tag && f0.y == ~tag && f1.x == tag && f1.y == ~tag && f2_.x == tag && f2_.y == ~tag && f3.x == tag && f3.y == ~tag)) break;
          __builtin_amdgcn_s_sleep(8);
        }
        if (lane == 0) __builtin_amdgcn_raw_buffer_store_b32(tag, ws, WS_EPOCH, 0, SC1);
      }
      return;
    }
    LOAD_EPOCH();
    const int hp = w & 1, th = w >> 1;
    unsigned warm0 = 0, warm1 = 0;
    if (TOUCH) {
      if (TD) __builtin_amdgcn_s_sleep(TD);
      warm0 = __builtin_amdgcn_raw_buffer_load_b32(ws, lane < 16 ? WS_FA + ((tile * 2 + th) * 16 + lane) * 128 : lane < 24 ? WS_Z + (cg * 4 + w) * 1024 + (lane - 16) * 128
                                                   : lane == 24 ? WS_KV + (cg * 4 + w) * 16384 : lane == 25 ? WS_O + tile * 131072 : 0x80000000u, 0, SC1);
      warm1 = __builtin_amdgcn_raw_buffer_load_b32(ro, lane < 2 ? (tile * 128 + w * 32 + lane * 16) * 4096 : 0x80000000u, 0, SC1);
    }
    const int qq = (lane >> 2) & 3, pp = lane & 3;
    const unsigned ax0 = l15 * 128 + ((q ^ (l15 >> 1)) << 4), ax1 = l15 * 128 + (((4 + q) ^ (l15 >> 1)) << 4);
    auto waddr = [&](int h) { return (8 * q + qq) * 128 + (((2 * h + (pp >> 1)) ^ ((((qq >> 1) & 1) | ((q & 1) << 1)) << 1)) << 4) + 8 * (pp & 1); };
    const unsigned aw = waddr(w), aq0 = waddr(2 * hp), aq1 = waddr(2 * hp + 1);
    unsigned ao[4], og[4], tw[4], trd[4];
#pragma unroll
    for (int i = 0; i < 4; ++i) {
      og[i] = WS_O + ((tile * 8 + th * 4 + i) * 16 + cg * 2 + hp) * 1024 + lane * 16;
      ao[i] = (4 * q + qq) * 128 + ((((2 * i) ^ (((2 * q + (qq >> 1)) & 3) << 1)) + (pp >> 1)) << 4) + 8 * (pp & 1);
      tw[i] = TRB + w * 8192 + l15 * 256 + (((4 * i + q) ^ l15) << 4);
      trd[i] = TRB + w * 8192 + (4 * i + q) * 256 + ((l15 ^ (4 * i + q)) << 4);
      asm volatile("" : "+v"(og[i]), "+v"(ao[i]), "+v"(tw[i]), "+v"(trd[i]));
    }
    unsigned so_ = ((tile * 128 + w * 32 + q) * 1024 + cg * 64 + l15 * 4) * 4, ex = TRB + th * 16384 + hp * 1024 + lane * 16;
    unsigned sk = SCR + hp * 2048 + lane * 16, sz_ = SCR + 8192 + hp * 1024 + lane * 8, sw_ = SCR + 4096 + w * 1024 + lane * 16;
    unsigned pk_ = WS_KV + ((cg * 4 + w) * 16 + tile) * 1024 + lane * 16, pz_ = l15 == 0 ? WS_Z + ((cg * 4 + w) * 16 + tile) * 64 + q * 16 : 0x80000000u;
    unsigned pf_ = lane == 0 ? WS_FA + ((tile * 2 + th) * 16 + cg * 2 + hp) * 128 : 0x80000000u;
    asm volatile("" : "+v"(so_), "+v"(ex), "+v"(sk), "+v"(sz_), "+v"(sw_), "+v"(pk_), "+v"(pz_), "+v"(pf_));
    f4 ka[8], va[8];
#pragma unroll
    for (int i = 0; i < 8; ++i) { ka[i] = (f4){EPS, EPS, EPS, EPS}; va[i] = (f4){0, 0, 0, 0}; }
    bf8 X0[4], X1[4], W0[2], W1[2];
    auto rdx = [&](bf8 (&X)[4], int kk, int ks, unsigned off) {
#pragma unroll
      for (int i = 0; i < 4; ++i) X[i] = ldsr(L + kk * 16384 + (ks ? ax1 : ax0) + off + i * 2048);
    };
    auto rdw = [&](bf8 (&W)[2], int kk, int ks) {
#pragma unroll
      for (int i = 0; i < 2; ++i) { const char LAS* p = L + WBUF[kk] + aw + ks * 4096 + i * 8192; W[i] = ldstr(p, p + 512); }
    };
    auto mm = [&](const bf8 (&X)[4], const bf8 (&W)[2], int h) {
#pragma unroll
      for (int i = 0; i < 4; ++i) {
        ka[4 * h + i] = mfma16(X[i], W[0], ka[4 * h + i]);
        va[4 * h + i] = mfma16(X[i], W[1], va[4 * h + i]);
      }
    };
#define ILV(nr, nm) do { _Pragma("unroll") for (int i_ = 0; i_ < nr; ++i_) { SG(0x008, 1); SG(0x100, 1); } if (nm > nr) SG(0x008, nm - nr); } while (0)
    asm volatile("s_waitcnt lgkmcnt(0)\n\ts_barrier" : "+s"(epoch) : "v"(warm0), "v"(warm1), "v"(cpf) : "memory");
    ST(1);
    const unsigned tag = epoch + 1, t = tag & 0xffu;
    rdw(W0, 0, 0); rdx(X0, 0, 0, 0);
    SB();
#pragma unroll
    for (int kk = 0; kk < 8; ++kk) {
      rdx(X1, kk, 0, 8192); mm(X0, W0, 0); ILV(4, 8); SB(); PACE(AN);
      rdw(W1, kk, 1); rdx(X0, kk, 1, 0); mm(X1, W0, 1); ILV(8, 8); SB(); PACE(AN);
      rdx(X1, kk, 1, 8192); mm(X0, W1, 0); ILV(4, 8); SB();
      if (kk < 7) { lbar(); rdw(W0, kk + 1, 0); rdx(X0, kk + 1, 0, 0); mm(X1, W1, 1); ILV(8, 8); }
      else mm(X1, W1, 1);
      SB();
    }
    ST(2);
    const unsigned xq = th * 8192;
    auto rdq = [&](bf8 (&X)[4], bf8 (&W)[2], int s) {
      const char LAS* p = L + QBUF[s >> 2] + ((s >> 1) & 1) * 8192 + (s & 1) * 4096;
      W[0] = ldstr(p + aq0, p + aq0 + 512); W[1] = ldstr(p + aq1, p + aq1 + 512);
      rdx(X, s < 14 ? (s >> 1) + 1 : 1, s & 1, xq);
    };
    {
      f4 kv = {0, 0, 0, 0}, zz = {0, 0, 0, 0};
      const u4 ones = {0x3f803f80u, 0x3f803f80u, 0x3f803f80u, 0x3f803f80u};
#pragma unroll
      for (int m = 0; m < 4; ++m) {
        const f4 a = ka[2 * m], b = ka[2 * m + 1], c = va[2 * m], d = va[2 * m + 1];
        const u4 pa = {phi2(a[0], a[1]), phi2(a[2], a[3]), phi2(b[0], b[1]), phi2(b[2], b[3])};
        const u4 pb = {pk2(c[0], c[1]), pk2(c[2], c[3]), pk2(d[0], d[1]), pk2(d[2], d[3])};
        kv = mfma16(asbf8(pa), asbf8(pb), kv);
        zz = mfma16(asbf8(pa), asbf8(ones), zz);
      }
      const u4 skv = {tagv(kv[0], t), __float_as_uint(kv[1]), tagv(kv[2], t), __float_as_uint(kv[3])};
      __builtin_amdgcn_raw_buffer_store_b128(skv, ws, pk_, 0, SC1);
      const u4 sz = {tagv(zz[0], t), __float_as_uint(zz[1]), tagv(zz[2], t), __float_as_uint(zz[3])};
      __builtin_amdgcn_raw_buffer_store_b128(sz, ws, pz_, 0, SC1);
    }
    SB();
    ST(3);
    lbar();
    rdq(X0, W0, 0);
    SB();
    f4 qa[2][4];
#pragma unroll
    for (int i = 0; i < 8; ++i) qa[i >> 2][i & 3] = (f4){EPS, EPS, EPS, EPS};
    u4 gc[8];
    const unsigned gb = WS_KV + (cg * 4 + w) * 16384 + lane * 16, g2 = (((jj >> 3) + 2) & 3) * 4096, g3 = (((jj >> 3) + 3) & 3) * 4096;
    auto ldg = [&](int i) { gc[i] = __builtin_amdgcn_raw_buffer_load_b128(ws, gb + (i & 3) * 1024, i < 4 ? g2 : g3, SC1); };
    auto mq = [&](const bf8 (&X)[4], const bf8 (&W)[2]) {
#pragma unroll
      for (int i = 0; i < 4; ++i) { qa[0][i] = mfma16(W[0], X[i], qa[0][i]); qa[1][i] = mfma16(W[1], X[i], qa[1][i]); }
    };
#define ILQ() do { _Pragma("unroll") for (int i_ = 0; i_ < 8; ++i_) { SG(0x008, 1); SG(0x100, 1); if (i_ == 2 || i_ == 6) SG(0x020, 1); } } while (0)
#pragma unroll
    for (int s = 0; s < 16; s += 2) {
      if (s == 8 || s == 10) { ldg(2 * s - 14); ldg(2 * s - 13); }
      rdq(X1, W1, s + 1); mq(X0, W0); ILQ(); SB();
      if (s == 2 || s == 6) lbar();
      if (s == 6 || s == 8) { ldg(2 * s - 12); ldg(2 * s - 11); }
      if (s < 14) { rdq(X0, W0, s + 2); mq(X1, W1); ILQ(); }
      else mq(X1, W1);
      SB();
    }
    ST(4);
    u2 pq[2][4];
#pragma unroll
    for (int i = 0; i < 8; ++i) {
      pq[i >> 2][i & 3] = (u2){phi2(qa[i >> 2][i & 3][0], qa[i >> 2][i & 3][1]), phi2(qa[i >> 2][i & 3][2], qa[i >> 2][i & 3][3])};
      asm volatile("" : "+v"(pq[i >> 2][i & 3]));
    }
    SB();
    {
      unsigned bad = 0;
      f4 kvp = {0, 0, 0, 0};
#pragma unroll
      for (int i = 0; i < 8; ++i) { gc[i].x ^= t; gc[i].z ^= t; bad |= gc[i].x | gc[i].z; kvp += __builtin_bit_cast(f4, gc[i]); }
      if (__builtin_expect(!__all((bad & 0xffu) == 0), 0)) {
#pragma unroll 1
        for (unsigned rep = 0; rep < SPIN_CAP; ++rep) {
          asm volatile("" ::: "memory");
          bad = 0;
          kvp = (f4){0, 0, 0, 0};
#pragma unroll
          for (int i = 0; i < 8; ++i) ldg(i);
#pragma unroll
          for (int i = 0; i < 8; ++i) { gc[i].x ^= t; gc[i].z ^= t; bad |= gc[i].x | gc[i].z; kvp += __builtin_bit_cast(f4, gc[i]); }
          asm volatile("" : "+v"(kvp));
          if (__all((bad & 0xffu) == 0)) break;
        }
      }
      *(LAS f4*)(L + sw_) = kvp;
    }
    lbar();
    ST(5);
    const f4 zero4 = {0, 0, 0, 0}, eps4 = {EPS, EPS, EPS, EPS};
    f4 num[2][4], den[2][4];
    const f4 ka0 = *(const LAS f4*)(L + sk), kb0 = *(const LAS f4*)(L + sk + 4096), ka1 = *(const LAS f4*)(L + sk + 1024), kb1 = *(const LAS f4*)(L + sk + 5120);
    const u2 zb[2] = {*(const LAS u2*)(L + sz_), *(const LAS u2*)(L + sz_ + 512)};
    SB();
    const f4 k0 = ka0 + kb0, k1 = ka1 + kb1;
    const u2 kb2[2] = {{pk2(k0[0], k0[1]), pk2(k0[2], k0[3])}, {pk2(k1[0], k1[1]), pk2(k1[2], k1[3])}};
#pragma unroll
    for (int i = 0; i < 8; ++i) { den[i & 1][i >> 1] = mfma16k16(zb[i & 1], pq[i & 1][i >> 1], eps4); num[i & 1][i >> 1] = mfma16k16(kb2[i & 1], pq[i & 1][i >> 1], zero4); }
    SB();
#pragma unroll
    for (int i = 0; i < 4; ++i) {
      u4 so;
#pragma unroll
      for (int h = 0; h < 2; ++h) {
        const float rc = __builtin_amdgcn_rcpf(den[h][i][0]);
        const f4 n = num[h][i];
        so[2 * h] = pk2((f2){n[0], n[1]} * rc);
        so[2 * h + 1] = pk2((f2){n[2], n[3]} * rc);
      }
      __builtin_amdgcn_raw_buffer_store_b128(so, ws, og[i], 0, SC1);
      *(LAS u4*)(L + ex + (i >> 1) * 8192 + (i & 1) * 2048) = so;
    }
    lbar();
    ST(6);
    u4 of[32];
#pragma unroll
    for (int i = 0; i < 4; ++i) of[i] = *(const LAS u4*)(L + TRB + w * 8192 + ((i & 1) * 2 + (i >> 1)) * 1024 + lane * 16);
    if (!NODRAIN) drain();
    __builtin_amdgcn_raw_buffer_store_b64((u2){tag, ~tag}, ws, pf_, 0, SC1);
    asm volatile("s_barrier" ::: "memory");
    ST(7);
    f4 acc[2][4];
#pragma unroll
    for (int i = 0; i < 8; ++i) acc[i >> 2][i & 3] = zero4;
    const unsigned ob = WS_O + (tile * 8 + w * 2) * 16384 + lane * 16;
    bf8 wf[2][4];
    auto rdo = [&](int k) {
#pragma unroll
      for (int nt = 0; nt < 4; ++nt) { const char LAS* p = L + OBUF[k >> 2] + (k & 3) * 4096 + ao[nt]; wf[k & 1][nt] = ldstr(p, p + 2048); }
    };
    auto ldo = [&](int k) {
      const unsigned so = (2 * seq(k >> 1) + (k & 1)) * 1024;
      of[2 * k] = __builtin_amdgcn_raw_buffer_load_b128(ws, ob, so, PAY_AUX);
      of[2 * k + 1] = __builtin_amdgcn_raw_buffer_load_b128(ws, ob + 16384, so, PAY_AUX);
    };
    auto mo = [&](int k) {
#pragma unroll
      for (int nt = 0; nt < 4; ++nt) {
        acc[0][nt] = mfma16(wf[k & 1][nt], asbf8(of[2 * k]), acc[0][nt]);
        acc[1][nt] = mfma16(wf[k & 1][nt], asbf8(of[2 * k + 1]), acc[1][nt]);
      }
    };
    rdo(0); rdo(1); SB();
    mo(0); rdo(2); SB();
    mo(1); SB();
    if (FD) __builtin_amdgcn_s_sleep(FD);
    wait_flags(ws, WS_FA + ((tile * 2 + th) * 16 + l15) * 128, tag);
    if (DLA) __builtin_amdgcn_s_sleep(DLA);
    ST(8); CT(14);
#pragma unroll
    for (int k = 2; k < 2 + OD; ++k) ldo(k);
    SB();
#pragma unroll
    for (int k = 2; k < 16; ++k) {
      if (k + OD < 16) ldo(k + OD);
      if (k + 1 < 16) rdo(k + 1);
      mo(k);
#pragma unroll
      for (int i_ = 0; i_ < 8; ++i_) { SG(0x008, 1); SG(0x100, 1); if (i_ == 1 || i_ == 5) SG(0x020, 1); }
      SB();
    }
    ST(9); CT(15);
#pragma unroll
    for (int mt = 0; mt < 2; ++mt)
#pragma unroll
      for (int nt = 0; nt < 4; ++nt) {
        const f4 v = acc[mt][nt];
        const unsigned p0 = pk2(v[0], v[1]), p1 = pk2(v[2], v[3]);
        *(LAS f4*)(L + tw[nt] + mt * 4096) = (f4){bflo(p0), bfhi(p0), bflo(p1), bfhi(p1)};
      }
    u4 ov[8];
#pragma unroll
    for (int i = 0; i < 8; ++i) ov[i] = *(const LAS u4*)(L + trd[i & 3] + (i >> 2) * 4096);
    SB();
#pragma unroll
    for (int i = 0; i < 8; ++i) __builtin_amdgcn_raw_buffer_store_b128(ov[i], ro, so_, i * 16384, OUT_AUX);
    ST(10);
  } else {
    constexpr unsigned SLOT = 32768, SCR = SLOT, PAR = 4 * SLOT;
    const int wp = w & 1, wt = w >> 1;
    const unsigned ecol = WS_EST + (cg * 64 + wp * 32 + l31) * 8;
    if (helper) {
      unsigned vx[4], vw[4], vc[4];
#pragma unroll
      for (int i = 0; i < 4; ++i) {
        const unsigned sw = (c8 ^ ((4 * i + (r8 >> 1)) & 7)) << 4;
        vx[i] = (64 * (w >> 1) + 32 * (i & 1) + 16 * (r8 >> 2) + 8 * (r8 & 1) + 4 * (w & 1) + 2 * (i >> 1) + ((r8 >> 1) & 1)) * 1024 + sw - 1024 * i;
        vw[i] = (16 * (i & 1) + 8 * (r8 >> 2) + 4 * (i >> 1) + (r8 & 3)) * 1024 + sw - 1024 * i;
        vc[i] = (8 * i + r8) * 1024 + sw - 1024 * i;
      }
      const GAS char* xbase = (const GAS char*)xb + (size_t)tile * 131072;
      const GAS char* wbase = (const GAS char*)((w & 1) ? bim : bre) + (cg * 64 + (w >> 1) * 32) * 1024;
      auto issue = [&](int kk) {
        const int kc = (kk + rot) & 7;
        const unsigned s = L0 + (kk % 5) * SLOT + w * 4096;
        dmax(s, xbase + kc * 128, vx[0], vx[1], vx[2], vx[3]);
        dma4(s + 16384, wbase + kc * 128, vw[0], vw[1], vw[2], vw[3]);
      };
      ST(9); issue(0); ST(10); issue(1); ST(11);
      LOAD_EPOCH();
      const GAS char* cbase = (const GAS char*)((w & 1) ? cim : cre) + cg * 65536;
      auto issue_c = [&](int piece) {
        const unsigned s = L0 + ((piece + 3) % 5) * SLOT + w * 8192;
        const GAS char* cb = cbase + seq(2 * piece + (w >> 1)) * 128;
        dma4(s, cb, vc[0], vc[1], vc[2], vc[3]);
        dma4(s + 4096, cb + 32768, vc[0], vc[1], vc[2], vc[3]);
      };
      asm volatile("s_waitcnt vmcnt(8) lgkmcnt(0)\n\ts_barrier" : "+s"(epoch) :: "memory");
      issue(2);
      const f4 par = *(const LAS f4*)(L + PAR + w * 1024 + lane * 16);
      const f2 l64 = {par[0], par[1]}, l128 = {par[2], par[3]};
      f2 l256 = cmul(l128, l128);
      f2 wgt = hh ? l64 : (f2){1.f, 0.f};
      if (wt) wgt = cmul(wgt, l128);
      const unsigned tag = epoch + 1, t = tag & 0xffu;
      unsigned eo[8], et[8];
#pragma unroll
      for (int m = 0; m < 8; ++m) {
        const int i = 2 * tile - 1 - (2 * wt + hh) - 4 * m;
        eo[m] = i >= 0 ? ecol + i * 4096 : 0x80000000u;
        et[m] = i >= 0 ? t : 0u;
        asm volatile("" : "+v"(eo[m]), "+v"(et[m]));
      }
      asm volatile("" : "+v"(l256), "+v"(wgt));
      wait_bar<8>(); issue(3);
      wait_bar<8>(); issue(4);
      wait_bar<8>(); issue(5);
      wait_bar<8>(); issue(6);
      wait_bar<8>(); issue(7);
      wait_bar<8>(); issue_c(0);
      wait_bar<8>(); ST(1);
      if (NC_EARLY > 1) issue_c(1);
      if (NC_EARLY > 2) issue_c(2);
      ST(2);
      asm volatile("s_barrier" ::: "memory");
      if (PD) __builtin_amdgcn_s_sleep(PD);
      if (PRI & 2) __builtin_amdgcn_s_setprio(3);
      f2 cin;
#pragma unroll 1
      for (unsigned sp = 0; sp < SPIN_CAP; ++sp) {
      asm volatile("" ::: "memory");
        u2 g[8];
#pragma unroll
        for (int m = 0; m < 8; ++m) g[m] = __builtin_amdgcn_raw_buffer_load_b64(ws, eo[m], 0, SC1);
        unsigned bad = 0;
        cin = (f2){0, 0};
#pragma unroll
        for (int m = 7; m >= 0; --m) {
          const unsigned cx = g[m].x ^ et[m];
          bad |= cx;
          cin = cfma(l256, cin, (f2){__uint_as_float(cx), __uint_as_float(g[m].y)});
        }
        if (__all((bad & 0xffu) == 0)) break;
      }
      ST(5);
      cin = cmul(cin, wgt);
      f2 chi;
      const f2 clo = swap_lo(cin, chi);
      if (hh == 0) *(LAS f2*)(L + SCR + (wp * 2 + wt) * 256 + l31 * 8) = clo + chi;
      ST(6);
      lbar();
      if (WOX & 2) issue_c(2);
      lbar();
      ST(7);
      if (NC_EARLY <= 1) issue_c(1);
      if (NC_EARLY <= 2 && !(WOX & 2)) issue_c(2);
      issue_c(3);
      wait_bar<0>();
      ST(8); HFLUSH();
      return;
    }
    LOAD_EPOCH();
    const int pl = cg * 64 + wp * 32 + 8 * (l15 >> 2) + 4 * (q & 1) + (l15 & 3);
    const unsigned plr = lre[pl], pli = lim[pl], pdt = ldt[pl];
    u4 dv[2];
#pragma unroll
    for (int i = 0; i < 2; ++i) dv[i] = *(const GAS u4*)(dsk + cg * 64 + 32 * i + 8 * q);
    unsigned warm0 = 0, warm1 = 0;
    if (TOUCH) {
      warm0 = __builtin_amdgcn_raw_buffer_load_b32(ws, lane < 16 ? WS_FB + ((tile * 2 + wt) * 16 + lane) * 128 : lane == 16 ? WS_EST + tile * 8192
                                                   : lane == 17 ? WS_H + (tile * 4 + w) * 65536 : 0x80000000u, 0, SC1);
      warm1 = __builtin_amdgcn_raw_buffer_load_b32(ro, lane < 2 ? (tile * 128 + w * 32 + lane * 16) * 4096 + 2048 : 0x80000000u, 0, SC1);
    }
    SB();
    f2 lam[2], l8[2], kap, l16, l32, l64;
    {
      const float lr = bflo(plr), li = bflo(pli);
      const float dt = bflo(pk2(expf(bflo(pdt)), 0.f));
      const float mag = expf(dt * lr);
      float sn, cs;
      sincosf(dt * li, &sn, &cs);
      const f2 l1 = {mag * cs, mag * sn};
      const float inv = 2.f / (lr * lr + li * li), nr = l1.x - 1.f, ni = l1.y;
      kap = (f2){(nr * lr + ni * li) * inv, (ni * lr - nr * li) * inv};
      f2 t = cmul(l1, l1); t = cmul(t, t); t = cmul(t, t);
      l16 = cmul(t, t); l32 = cmul(l16, l16); l64 = cmul(l32, l32);
      const f2 l128 = cmul(l64, l64);
      *(LAS f4*)(L + PAR + w * 1024 + lane * 16) = (f4){l64.x, l64.y, l128.x, l128.y};
      lam[0] = swap16(l1, l1, lam[1]);
      l8[0] = swap16(t, t, l8[1]);
    }
    f2 R0 = {lam[0].x, lam[0].x}, I0 = {lam[0].y, lam[0].y}, N0 = -I0, R1 = {lam[1].x, lam[1].x}, I1 = {lam[1].y, lam[1].y}, N1 = -I1;
    unsigned pe_ = hh == 0 ? ecol + (tile * 2 + wt) * 4096 : 0x80000000u, se_ = SCR + 1024 + (wt * 2 + wp) * 256 + hh * 1024 + l31 * 8;
    asm volatile("" : "+v"(R0), "+v"(I0), "+v"(N0), "+v"(R1), "+v"(I1), "+v"(N1), "+v"(pe_), "+v"(se_));
    const unsigned ax[2] = {(unsigned)(l15 * 128 + ((q ^ (l15 >> 1)) << 4)), (unsigned)(l15 * 128 + (((4 + q) ^ (l15 >> 1)) << 4))};
    f4 u[2][2][4];
#pragma unroll
    for (int i = 0; i < 16; ++i) u[i >> 3][(i >> 2) & 1][i & 3] = (f4){0, 0, 0, 0};
    bf8 Xa[2], Xb[2], W0[4], W1[4];
    auto rdx = [&](bf8 (&X)[2], int kk, int ks, int h) {
#pragma unroll
      for (int i = 0; i < 2; ++i) X[i] = ldsr(L + (kk % 5) * SLOT + wt * 8192 + (2 * h + i) * 2048 + ax[ks]);
    };
    auto rdw = [&](bf8 (&W)[4], int kk, int ks) {
#pragma unroll
      for (int i = 0; i < 4; ++i) W[i] = ldsr(L + (kk % 5) * SLOT + 16384 + wp * 8192 + (i & 1) * 4096 + (i >> 1) * 2048 + ax[ks]);
    };
    auto mm = [&](const bf8 (&X)[2], const bf8 (&W)[4], int h) {
#pragma unroll
      for (int i = 0; i < 2; ++i)
#pragma unroll
        for (int j = 0; j < 4; ++j) u[j >> 1][j & 1][2 * h + i] = mfma16(X[i], W[j], u[j >> 1][j & 1][2 * h + i]);
    };
    asm volatile("s_waitcnt lgkmcnt(0)\n\ts_barrier" : "+s"(epoch), "+v"(lam[0]), "+v"(lam[1]), "+v"(l8[0]), "+v"(l8[1]), "+v"(kap), "+v"(l16), "+v"(l32), "+v"(l64),
                 "+v"(dv[0]), "+v"(dv[1]) : "v"(warm0), "v"(warm1), "v"(cpf) : "memory");
    ST(1);
    const unsigned tag = epoch + 1, t = tag & 0xffu;
    rdw(W0, 0, 0); rdx(Xa, 0, 0, 0);
    SB();
#pragma unroll
    for (int kk = 0; kk < 8; ++kk) {
      rdx(Xb, kk, 0, 1); mm(Xa, W0, 0); ILV(2, 8); SB(); PACE(BN);
      rdw(W1, kk, 1); rdx(Xa, kk, 1, 0); mm(Xb, W0, 1); ILV(6, 8); SB(); PACE(BN);
      rdx(Xb, kk, 1, 1); mm(Xa, W1, 0); ILV(2, 8); SB();
      if (kk < 7) { lbar(); if (kk == 6) ST(2); rdw(W0, kk + 1, 0); rdx(Xa, kk + 1, 0, 0); mm(Xb, W1, 1); ILV(6, 8); }
      else mm(Xb, W1, 1);
      SB(); if (kk < 7) PACE(BN);
    }
    ST(3);
    f2 gr[2][8], gi[2][8];
#pragma unroll
    for (int i = 0; i < 16; ++i) {
      const int pt = i >> 3, a = i & 7;
      gr[pt][a] = (f2){u[pt][0][a >> 1][2 * (a & 1)], u[pt][0][a >> 1][2 * (a & 1) + 1]};
      gi[pt][a] = (f2){u[pt][1][a >> 1][2 * (a & 1)], u[pt][1][a >> 1][2 * (a & 1) + 1]};
    }
    {
#pragma unroll
      for (int a = 1; a < 8; ++a)
        asm volatile("v_pk_fma_f32 %0, %10, %5, %0\n\tv_pk_fma_f32 %1, %9, %4, %1\n\tv_pk_fma_f32 %2, %13, %7, %2\n\tv_pk_fma_f32 %3, %12, %6, %3\n\t"
                     "v_pk_fma_f32 %0, %8, %4, %0\n\tv_pk_fma_f32 %1, %8, %5, %1\n\tv_pk_fma_f32 %2, %11, %6, %2\n\tv_pk_fma_f32 %3, %11, %7, %3"
                     : "+v"(gr[0][a]), "+v"(gi[0][a]), "+v"(gr[1][a]), "+v"(gi[1][a])
                     : "v"(gr[0][a - 1]), "v"(gi[0][a - 1]), "v"(gr[1][a - 1]), "v"(gi[1][a - 1]), "v"(R0), "v"(I0), "v"(N0), "v"(R1), "v"(I1), "v"(N1));
    }
    const f2 e0[2] = {{gr[0][7].x, gi[0][7].x}, {gr[1][7].x, gi[1][7].x}};
    f2 vodd;
    const f2 veven = swap16(cfma(l8[0], e0[0], (f2){gr[0][7].y, gi[0][7].y}), cfma(l8[1], e0[1], (f2){gr[1][7].y, gi[1][7].y}), vodd);
    f2 ehi;
    const f2 elo = swap_lo(cfma(l16, veven, vodd), ehi);
    const f2 e64 = cfma(l32, elo, ehi);
    __builtin_amdgcn_raw_buffer_store_b64((u2){tagv(e64.x, t), __float_as_uint(e64.y)}, ws, pe_, 0, SC1);
    *(LAS f2*)(L + se_) = e64;
    lbar();
    const f2 esib = *(const LAS f2*)(L + SCR + 1024 + wp * 256 + l31 * 8);
    SB();
    ST(4);
    u4 xd[2][2];
#pragma unroll
    for (int i = 0; i < 4; ++i)
      xd[i >> 1][i & 1] = *(const GAS u4*)(xb + (tile * 128 + 64 * wt + 16 * (l15 >> 2) + 8 * (l15 & 1) + 4 * (w & 1) + 2 * (i >> 1) + ((l15 >> 1) & 1)) * 512
                                           + cg * 64 + 32 * (i & 1) + 8 * q);
    SB();
    f2 kp[2], ke[2], pw[2][8];
    kp[0] = swap16(kap, kap, kp[1]);
#pragma unroll
    for (int pt = 0; pt < 2; ++pt) {
      const f2 KR = {kp[pt].x, kp[pt].x}, KI = {kp[pt].y, kp[pt].y};
      ke[pt] = cmul(kp[pt], e0[pt]);
#pragma unroll
      for (int a = 0; a < 8; ++a) {
        const f2 xr = gr[pt][a], xi = gi[pt][a];
        gr[pt][a] = fma2(KR, xr, -KI * xi); gi[pt][a] = fma2(-KR, xi, -KI * xr);
        asm volatile("" : "+v"(gr[pt][a]), "+v"(gi[pt][a]));
      }
      pw[pt][0] = (f2){lam[pt].x, -lam[pt].y};
#pragma unroll
      for (int k = 1; k < 8; ++k) { pw[pt][k] = cmul(pw[pt][k >> 1], pw[pt][(k - 1) >> 1]); asm volatile("" : "+v"(pw[pt][k])); }
    }
    u2 idn = {(l15 == 4 * q ? 0x3f80u : 0u) | (l15 == 4 * q + 1 ? 0x3f800000u : 0u), (l15 == 4 * q + 2 ? 0x3f80u : 0u) | (l15 == 4 * q + 3 ? 0x3f800000u : 0u)};
#pragma unroll
    for (int i = 0; i < 4; ++i) {
#pragma unroll
      for (int j = 0; j < 4; ++j) xd[i >> 1][i & 1][j] = pk2(bflo(xd[i >> 1][i & 1][j]) * bflo(dv[i & 1][j]), bfhi(xd[i >> 1][i & 1][j]) * bfhi(dv[i & 1][j]));
      asm volatile("" : "+v"(xd[i >> 1][i & 1]));
    }
    f2 me = hh ? cmul(kap, l32) : kap, be = hh ? cmul(kap, elo) : (f2){0.f, 0.f};
    f2 mo = cmul(l16, me), bo = cfma(l16, be, cmul(kap, veven));
    if (wt) { be = cfma(me, esib, be); bo = cfma(mo, esib, bo); me = cmul(me, l64); mo = cmul(mo, l64); }
    unsigned sc_ = SCR + wp * 512 + l31 * 8, ho[4];
#pragma unroll
    for (int i = 0; i < 4; ++i) ho[i] = WS_H + (tile * 8 + wt * 4 + i) * 32768 + (cg * 2 + wp) * 2048 + lane * 16;
    asm volatile("s_waitcnt lgkmcnt(0)\n\ts_barrier" : "+v"(ho[0]), "+v"(ho[1]), "+v"(ho[2]), "+v"(ho[3]), "+v"(me), "+v"(be), "+v"(mo), "+v"(bo),
                 "+v"(ke[0]), "+v"(ke[1]), "+v"(idn), "+v"(sc_) :: "memory");
    ST(5);
    const f2 cin = *(const LAS f2*)(L + sc_) + *(const LAS f2*)(L + sc_ + 256);
    f2 d0[2], DR[2], DJ[2];
    d0[0] = swap16(cfma(me, cin, be), cfma(mo, cin, bo), d0[1]);
#pragma unroll
    for (int pt = 0; pt < 2; ++pt) {
      const f2 d1 = cfma(l8[pt], d0[pt], ke[pt]);
      DR[pt] = (f2){d0[pt].x, d1.x}; DJ[pt] = (f2){-d0[pt].y, -d1.y};
    }
    const f4 zero4 = {0, 0, 0, 0};
    f4 T[8][2];
    u4 ow[8];
    auto fix = [&](int i) {
      const int mt = i >> 1, c = i & 1;
#pragma unroll
      for (int pt = 0; pt < 2; ++pt) {
        u2 a;
#pragma unroll
        for (int h = 0; h < 2; ++h) {
          const f2 P = pw[pt][2 * mt + h], PR = {P.x, P.x}, PI = {P.y, P.y};
          a[h] = pk2(c ? fma2(PI, DR[pt], fma2(PR, DJ[pt], gi[pt][2 * mt + h])) : fma2(-PI, DJ[pt], fma2(PR, DR[pt], gr[pt][2 * mt + h])));
        }
        T[i][pt] = mfma16k16(a, idn, zero4);
      }
    };
    auto put = [&](int i) {
      ow[i] = (u4){pk2(T[i][0][0], T[i][0][1]), pk2(T[i][0][2], T[i][0][3]), pk2(T[i][1][0], T[i][1][1]), pk2(T[i][1][2], T[i][1][3])};
      __builtin_amdgcn_raw_buffer_store_b128(ow[i], ws, ho[i >> 1] + (i & 1) * 1024, 0, SC1);
    };
    fix(0); SB();
#pragma unroll
    for (int i = 1; i < 8; ++i) { fix(i); SB(); put(i - 1); SB(); }
    put(7);
    SB();
    const unsigned ex = 2 * SLOT + wt * 16384 + wp * 4096 + lane * 16;
#pragma unroll
    for (int i = 0; i < 8; ++i) *(LAS u4*)(L + ex + (i >> 2) * 8192 + (i & 1) * 2048 + ((i >> 1) & 1) * 1024) = ow[i];
    lbar();
    ST(6);
    if (!NODRAIN) drain();
    __builtin_amdgcn_raw_buffer_store_b64((u2){tag, ~tag}, ws, lane == 0 ? WS_FB + ((tile * 2 + wt) * 16 + cg * 2 + wp) * 128 : 0x80000000u, 0, SC1);
    ST(7);
    const unsigned hb = WS_H + (tile * 8 + w * 2) * 32768 + lane * 16;
    u4 hf[64];
    bf8 cf[2][4];
    f4 acc[2][4];
    auto ldh = [&](int g) {
      hf[2 * g] = __builtin_amdgcn_raw_buffer_load_b128(ws, hb + (g & 3) * 1024, seq(g >> 2) * 4096, PAY_AUX);
      hf[2 * g + 1] = __builtin_amdgcn_raw_buffer_load_b128(ws, hb + 32768 + (g & 3) * 1024, seq(g >> 2) * 4096, PAY_AUX);
    };
    auto rdc = [&](int g) {
      const int tt = g >> 2;
#pragma unroll
      for (int nt = 0; nt < 4; ++nt)
        cf[g & 1][nt] = ldsr(L + (((tt >> 1) + 3) % 5) * SLOT + (tt & 1) * 16384 + (g & 1) * 8192 + nt * 2048 + ax[(g >> 1) & 1]);
    };
    auto mh = [&](int g) {
#pragma unroll
      for (int nt = 0; nt < 4; ++nt) {
        acc[0][nt] = mfma16(cf[g & 1][nt], asbf8(hf[2 * g]), acc[0][nt]);
        acc[1][nt] = mfma16(cf[g & 1][nt], asbf8(hf[2 * g + 1]), acc[1][nt]);
      }
    };
#pragma unroll
    for (int i = 0; i < 8; ++i) hf[i] = *(const LAS u4*)(L + 2 * SLOT + w * 8192 + i * 1024 + lane * 16);
    rdc(0);
    {
      u4 ida[2];
#pragma unroll
      for (int hf_ = 0; hf_ < 2; ++hf_)
#pragma unroll
        for (int jq = 0; jq < 4; ++jq) {
          const int k0 = 8 * q + 2 * jq - 16 * hf_;
          ida[hf_][jq] = (l15 == k0 ? 0x3f80u : 0u) | (l15 == k0 + 1 ? 0x3f800000u : 0u);
        }
#pragma unroll
      for (int i = 0; i < 8; ++i) acc[i >> 2][i & 3] = mfma16(asbf8(ida[i & 1]), asbf8(xd[i >> 2][(i >> 1) & 1]), zero4);
    }
    SB();
#pragma unroll
    for (int g = 0; g < 4; ++g) { rdc(g + 1); mh(g); ILV(4, 8); SB(); }
    unsigned tw[4], trd[4], so_ = ((tile * 128 + 64 * wt + 4 * (w & 1) + 8 * (q & 1) + (q >> 1)) * 1024 + 512 + cg * 64 + l15 * 4) * 4;
#pragma unroll
    for (int i = 0; i < 4; ++i) {
      tw[i] = 2 * SLOT + w * 8192 + l15 * 256 + (((4 * i + q) ^ l15) << 4);
      trd[i] = 2 * SLOT + w * 8192 + (4 * i + q) * 256 + ((l15 ^ (4 * i + q)) << 4);
      asm volatile("" : "+v"(tw[i]), "+v"(trd[i]));
    }
    asm volatile("s_barrier" : "+v"(so_) :: "memory");
    if (FD) __builtin_amdgcn_s_sleep(FD);
    wait_flags(ws, WS_FB + ((tile * 2 + wt) * 16 + l15) * 128, tag);
    if (DLB) __builtin_amdgcn_s_sleep(DLB);
    ST(8); CT(14);
#pragma unroll
    for (int g = 4; g < 4 + HD / 2; ++g) ldh(g);
    SB();
#pragma unroll
    for (int g = 4; g < 32; ++g) {
      if (g + HD / 2 < 32) ldh(g + HD / 2);
      if (g + 1 < 32) rdc(g + 1);
      mh(g);
#pragma unroll
      for (int i_ = 0; i_ < 4; ++i_) { SG(0x008, 1); SG(0x100, 1); }
      SG(0x008, 1); SG(0x020, 1); SG(0x008, 1); SG(0x020, 1); SG(0x008, 2);
      SB(); if (g < 30) PACE(HN);
    }
    ST(9); CT(15);
#pragma unroll
    for (int i = 0; i < 8; ++i) *(LAS f4*)(L + tw[i & 3] + (i >> 2) * 4096) = acc[i >> 2][i & 3];
    u4 ov[8];
#pragma unroll
    for (int i = 0; i < 8; ++i) ov[i] = *(const LAS u4*)(L + trd[i & 3] + (i >> 2) * 4096);
    SB();
#pragma unroll
    for (int i = 0; i < 8; ++i) __builtin_amdgcn_raw_buffer_store_b128(ov[i], ro, so_, (16 * (i & 3) + 2 * (i >> 2)) * 4096, OUT_AUX);
    ST(10);
  }
}

extern "C" void kernel_launch(void* const* d_in, const int* in_sizes, int n_in, void* d_out, int out_size, void* d_ws, size_t ws_size,
                              hipStream_t stream) {
  (void)hipFuncSetAttribute((const void*)fused_kernel, hipFuncAttributeMaxDynamicSharedMemorySize, LDS_BYTES);
  fused_kernel<<<256, 512, LDS_BYTES, stream>>>((const u16*)d_in[0], (const u16*)d_in[1], (const u16*)d_in[2], (const u16*)d_in[3],
                                               (const u16*)d_in[4], (const u16*)d_in[5], (const u16*)d_in[6], (const u16*)d_in[7],
                                               (const u16*)d_in[8], (const u16*)d_in[9], (const u16*)d_in[10], (const u16*)d_in[11],
                                               (float*)d_out, (char*)d_ws);
}
```

```cpp
#define BN 0
#define AN 0
#include <hip/hip_runtime.h>
#include <hip/hip_bf16.h>

#define DI __device__ __forceinline__
#define LAS __attribute__((address_space(3)))
#define GAS __attribute__((address_space(1)))
typedef unsigned short u16;
typedef unsigned long long u64;
typedef __bf16 bf8 __attribute__((ext_vector_type(8)));
typedef __bf16 bf2 __attribute__((ext_vector_type(2)));
typedef short s4 __attribute__((ext_vector_type(4)));
typedef short s8 __attribute__((ext_vector_type(8)));
typedef float f16v __attribute__((ext_vector_type(16)));
typedef float f4 __attribute__((ext_vector_type(4)));
typedef float f2 __attribute__((ext_vector_type(2)));
typedef unsigned u4 __attribute__((ext_vector_type(4)));
typedef unsigned u2 __attribute__((ext_vector_type(2)));

constexpr unsigned WS_EPOCH = 0;
constexpr unsigned WS_FA = 4096;
constexpr unsigned WS_FB = WS_FA + 65536;
constexpr unsigned WS_EST = WS_FB + 65536;
constexpr unsigned WS_Z = WS_EST + 131072;
constexpr unsigned WS_KV = WS_Z + 65536;
constexpr unsigned WS_O = 1u << 20;
constexpr unsigned WS_H = 3u << 20;
constexpr unsigned WS_END = 7u << 20;
constexpr unsigned WS_STAMP = 8u << 20;
constexpr int LDS_BYTES = 160 * 1024;
constexpr unsigned SPIN_CAP = 200000;
constexpr int SC1 = 16;

#define ROT 8
#define NC_EARLY 2
#define OUT_AUX 16
#define PAY_AUX 16
#define PULL 0
#define TOUCH 1
#define CPF 10
#define HROT 1
#define XNT 0
#define PD 14
#define FD 0
#define TD 0
#define NODRAIN 0
#define WOX 2
#define DLA 0
#define DLB 0
#define QN 0
#define HN 0
#define PRI 2
#define PROG 0
#define DIRECT 0
#define FW 0
#define OD 6
#define HD 12

#define CT(i) do {} while (0)
#define ST(i) do {} while (0)
#define HFLUSH() do {} while (0)
#define SB() __builtin_amdgcn_sched_barrier(0)
#define PACE(n) do { if (n) { asm volatile("s_nop %0" :: "i"((n) > 16 ? 15 : ((n) ? (n) - 1 : 0))); if ((n) > 16) asm volatile("s_nop %0" :: "i"((n) > 16 ? (n) - 17 : 0)); SB(); } } while (0)
#define SG(m, n) __builtin_amdgcn_sched_group_barrier(m, n, 0)

DI unsigned pk2(float a, float b) { f2 v = {a, b}; return __builtin_bit_cast(unsigned, __builtin_convertvector(v, bf2)); }
DI unsigned pk2(f2 v) { return __builtin_bit_cast(unsigned, __builtin_convertvector(v, bf2)); }
DI float bflo(unsigned v) { return __uint_as_float(v << 16); }
DI float bfhi(unsigned v) { return __uint_as_float(v & 0xffff0000u); }
DI bf8 asbf8(u4 v) { return __builtin_bit_cast(bf8, v); }
DI f16v mfma32(bf8 a, bf8 b, f16v c) { return __builtin_amdgcn_mfma_f32_32x32x16_bf16(a, b, c, 0, 0, 0); }
DI f4 mfma16(bf8 a, bf8 b, f4 c) { return __builtin_amdgcn_mfma_f32_16x16x32_bf16(a, b, c, 0, 0, 0); }
DI f4 mfma16k16(u2 a, u2 b, f4 c) { return __builtin_amdgcn_mfma_f32_16x16x16bf16_1k(__builtin_bit_cast(s4, a), __builtin_bit_cast(s4, b), c, 0, 0, 0); }
DI bf8 ldsr(const char LAS* p) { return *(const LAS bf8*)p; }
DI bf8 ldstr(const char LAS* p0, const char LAS* p1) {
  s4 a = __builtin_amdgcn_ds_read_tr16_b64_v4i16((LAS s4*)p0), b = __builtin_amdgcn_ds_read_tr16_b64_v4i16((LAS s4*)p1);
  return __builtin_bit_cast(bf8, (s8)__builtin_shufflevector(a, b, 0, 1, 2, 3, 4, 5, 6, 7));
}
DI void dma4(unsigned lds, const GAS char* sb, unsigned v0, unsigned v1, unsigned v2, unsigned v3) {
  asm volatile("s_mov_b32 m0, %0\n\ts_nop 0\n\tglobal_load_lds_dwordx4 %1, %5\n\tglobal_load_lds_dwordx4 %2, %5 offset:1024\n\t"
               "global_load_lds_dwordx4 %3, %5 offset:2048\n\tglobal_load_lds_dwordx4 %4, %5 offset:3072"
               :: "s"(lds), "v"(v0), "v"(v1), "v"(v2), "v"(v3), "s"(sb) : "memory");
}
DI void dma4nt(unsigned lds, const GAS char* sb, unsigned v0, unsigned v1, unsigned v2, unsigned v3) {
  asm volatile("s_mov_b32 m0, %0\n\ts_nop 0\n\tglobal_load_lds_dwordx4 %1, %5 nt\n\tglobal_load_lds_dwordx4 %2, %5 offset:1024 nt\n\t"
               "global_load_lds_dwordx4 %3, %5 offset:2048 nt\n\tglobal_load_lds_dwordx4 %4, %5 offset:3072 nt"
               :: "s"(lds), "v"(v0), "v"(v1), "v"(v2), "v"(v3), "s"(sb) : "memory");
}
DI void dmax(unsigned lds, const GAS char* sb, unsigned v0, unsigned v1, unsigned v2, unsigned v3) { if (XNT) dma4nt(lds, sb, v0, v1, v2, v3); else dma4(lds, sb, v0, v1, v2, v3); }
DI void dma2(unsigned lds, const GAS char* sb, unsigned v0, unsigned v1) {
  asm volatile("s_mov_b32 m0, %0\n\ts_nop 0\n\tglobal_load_lds_dwordx4 %1, %3\n\tglobal_load_lds_dwordx4 %2, %3 offset:1024"
               :: "s"(lds), "v"(v0), "v"(v1), "s"(sb) : "memory");
}
struct Win { unsigned b[3]; };
DI Win mkwin(unsigned a) { Win r = {{a, a + 65536, a + 131072}}; asm volatile("" : "+v"(r.b[0]), "+v"(r.b[1]), "+v"(r.b[2])); return r; }
DI const char LAS* at(const char LAS* L, const Win& a, unsigned off) { return L + a.b[off >> 16] + (off & 65535); }
DI void lbar() { asm volatile("s_waitcnt lgkmcnt(0)\n\ts_barrier" ::: "memory"); }
template <int N> DI void wait_bar() { asm volatile("s_waitcnt vmcnt(%0) lgkmcnt(0)\n\ts_barrier" :: "i"(N) : "memory"); }
DI void drain() { asm volatile("s_waitcnt vmcnt(0)" ::: "memory"); }
DI unsigned tagv(float x, unsigned t) { return (__float_as_uint(x) & 0xffffff00u) | t; }
DI f2 cmul(f2 a, f2 b) { return (f2){a.x * b.x - a.y * b.y, a.x * b.y + a.y * b.x}; }
DI f2 cfma(f2 a, f2 b, f2 c) { return (f2){fmaf(a.x, b.x, fmaf(-a.y, b.y, c.x)), fmaf(a.x, b.y, fmaf(a.y, b.x, c.y))}; }
DI f2 fma2(f2 a, f2 b, f2 c) { return __builtin_elementwise_fma(a, b, c); }
constexpr float EPS = 1e-6f, LOG2E = 1.44269504088896341f;
DI unsigned phi2(float s0, float s1) {
  const f2 a = fma2((f2){s0, s1}, (f2){LOG2E, LOG2E}, (f2){-EPS * LOG2E, -EPS * LOG2E});
  const f2 e = {__builtin_amdgcn_fmed3f(__builtin_amdgcn_exp2f(a.x), 0.f, 1.f), __builtin_amdgcn_fmed3f(__builtin_amdgcn_exp2f(a.y), 0.f, 1.f)};
  return pk2(e + (f2){__builtin_amdgcn_fmed3f(s0, EPS, 3.40282347e38f), __builtin_amdgcn_fmed3f(s1, EPS, 3.40282347e38f)});
}
DI f2 swap_lo(f2 v, f2& hi) {
  const auto x = __builtin_amdgcn_permlane32_swap(__float_as_uint(v.x), __float_as_uint(v.x), false, false);
  const auto y = __builtin_amdgcn_permlane32_swap(__float_as_uint(v.y), __float_as_uint(v.y), false, false);
  hi = (f2){__uint_as_float(x[1]), __uint_as_float(y[1])};
  return (f2){__uint_as_float(x[0]), __uint_as_float(y[0])};
}
DI void wait_pair(__amdgpu_buffer_rsrc_t ws, unsigned off, unsigned tag, unsigned& have, int p) {
  if (__builtin_expect(((have >> (2 * p)) & 3u) != 3u, 0)) {
#pragma unroll 1
    for (unsigned sp = 0; sp < SPIN_CAP; ++sp) {
      asm volatile("" ::: "memory");
      const u2 f = __builtin_amdgcn_raw_buffer_load_b64(ws, off, 0, SC1);
      have = (unsigned)__ballot(f.x == tag && f.y == ~tag);
      if (((have >> (2 * p)) & 3u) == 3u) break;
    }
    asm volatile("" ::: "memory");
  }
}
DI f2 swap16(f2 a, f2 b, f2& rb) {
  const auto x = __builtin_amdgcn_permlane16_swap(__float_as_uint(a.x), __float_as_uint(b.x), false, false);
  const auto y = __builtin_amdgcn_permlane16_swap(__float_as_uint(a.y), __float_as_uint(b.y), false, false);
  rb = (f2){__uint_as_float(x[1]), __uint_as_float(y[1])};
  return (f2){__uint_as_float(x[0]), __uint_as_float(y[0])};
}
DI void wait_flags(__amdgpu_buffer_rsrc_t ws, unsigned off, unsigned tag) {
#pragma unroll 1
  for (unsigned sp = 0; sp < SPIN_CAP; ++sp) {
      asm volatile("" ::: "memory");
    const u2 f = __builtin_amdgcn_raw_buffer_load_b64(ws, off, 0, SC1);
    if (__all(f.x == tag && f.y == ~tag)) break;
    __builtin_amdgcn_s_sleep(1);
  }
  asm volatile("" ::: "memory");
}

__global__ void __launch_bounds__(512, 2)
fused_kernel(const u16* a0, const u16* a1, const u16* a2, const u16* a3, const u16* a4, const u16* a5, const u16* a6, const u16* a7,
             const u16* a8, const u16* a9, const u16* a10, const u16* a11, float* a12, char* a13) {
  extern __shared__ __attribute__((aligned(16))) char smem_[];
  char LAS* L = (char LAS*)smem_;
  const unsigned L0 = (unsigned)(size_t)L;
  ST(0);
  const int lane = threadIdx.x & 63;
  const int w8 = __builtin_amdgcn_readfirstlane(threadIdx.x >> 6), w = w8 & 3;
  unsigned cpf = 0;
  if (CPF && w8 == 0) {
    u64 pc;
    asm volatile("s_getpc_b64 %0" : "=s"(pc));
    cpf = __builtin_amdgcn_raw_buffer_load_b32(__builtin_amdgcn_make_buffer_rsrc((char*)(pc & ~127ull), 0, CPF * 4096, 0x00020000),
                                               lane < CPF ? ((blockIdx.x >> 3) * CPF + lane) * 128 : 0x80000000u, 0, 0);
  }
  typedef u64 u64x8 __attribute__((ext_vector_type(8)));
  typedef u64 u64x4 __attribute__((ext_vector_type(4)));
  typedef u64 u64x2 __attribute__((ext_vector_type(2)));
  u64x8 ka; u64x4 kb; u64x2 kc_;
  asm volatile("s_load_dwordx16 %0, %3, 0x0\n\ts_load_dwordx8 %1, %3, 0x40\n\ts_load_dwordx4 %2, %3, 0x60\n\ts_waitcnt lgkmcnt(0)"
               : "=&s"(ka), "=&s"(kb), "=&s"(kc_) : "s"(__builtin_amdgcn_kernarg_segment_ptr()) : "memory");
  const GAS u16 *xa = (const GAS u16*)ka[0], *wqkv = (const GAS u16*)ka[1], *wo = (const GAS u16*)ka[2], *xb = (const GAS u16*)ka[3];
  const GAS u16 *lre = (const GAS u16*)ka[4], *lim = (const GAS u16*)ka[5], *bre = (const GAS u16*)ka[6], *bim = (const GAS u16*)ka[7];
  const GAS u16 *cre = (const GAS u16*)kb[0], *cim = (const GAS u16*)kb[1], *ldt = (const GAS u16*)kb[2], *dsk = (const GAS u16*)kb[3];
  float* out = (float*)kc_[0]; char* wsp = (char*)kc_[1];
  const bool helper = w8 >= 4;
  const int bid = blockIdx.x, xcd = bid & 7, jj = bid >> 3;
  const int tile = (xcd >> 1) * 4 + (jj >> 3), cg = jj & 7;
  const int rot = ROT == 1 ? ((cg + 3 * (jj >> 3) + xcd) & 7) : ROT == 7 ? (((cg & 3) * 2 + (xcd >> 1)) & 7) : ROT == 12 ? ((2 * (cg & 3) + 2 * (xcd >> 1)) & 7) : ROT == 16 ? ((2 * (cg & 3) + ((jj >> 3) & 1) + 2 * (xcd >> 1)) & 7) : (((int)(030630630630ull >> (3 * (cg + (jj >> 3)))) + (xcd >> 1)) & 7);
  auto seq = [&](int t) { const int b = 2 * (jj >> 3), e = t - 1; return t == 0 ? cg : (b + e + (e >= ((cg - b) & 7) ? 1 : 0)) & 7; };
#define MKSQ() int sq[8]; _Pragma("unroll") for (int t_ = 1; t_ < 8; ++t_) { sq[t_] = __builtin_amdgcn_readfirstlane(seq(t_)); asm volatile("" : "+s"(sq[t_])); }
  const __amdgpu_buffer_rsrc_t ws = __builtin_amdgcn_make_buffer_rsrc(wsp, 0, WS_END, 0x00020000);
  const __amdgpu_buffer_rsrc_t ro = __builtin_amdgcn_make_buffer_rsrc(out, 0, 2048 * 1024 * 4, 0x00020000);
  const int r8 = lane >> 3, c8 = lane & 7;
  const int q = lane >> 4, l15 = lane & 15, l31 = lane & 31, hh = lane >> 5;
  unsigned epoch;
#define LOAD_EPOCH() asm volatile("s_load_dword %0, %1, 0x0" : "=s"(epoch) : "s"(wsp) : "memory")

  if ((xcd & 1) == 0) {
    constexpr unsigned WA = 131072, WB = 147456, SCR = 32768, TRB = 81920;
    constexpr unsigned QBUF[4] = {WB, 0, WA, WB}, OBUF[4] = {32768, 49152, 65536, 0};
    constexpr unsigned WBUF[8] = {WA, WB, 7 * 16384, 0, WA, WB, 0, WA};
    if (helper) {
      const int head = cg * 4 + w;
      unsigned vx[4], vw[2], vo[4];
#pragma unroll
      for (int i = 0; i < 4; ++i) vx[i] = (32 * w + 8 * i + r8) * 1024 + ((c8 ^ ((4 * i + (r8 >> 1)) & 7)) << 4) - 1024 * i;
#pragma unroll
      for (int i = 0; i < 2; ++i) vw[i] = (16 * w + 8 * i + r8) * 3072 + ((c8 ^ ((((r8 >> 1) & 1) | (i << 1)) << 1)) << 4) - 1024 * i;
      const GAS char* xbase = (const GAS char*)xa + (size_t)tile * 131072;
      const GAS char* wbase = (const GAS char*)wqkv + cg * 128;
      auto isx = [&](int kk, int slot) { dmax(L0 + slot * 16384 + w * 4096, xbase + ((kk + rot) & 7) * 128, vx[0], vx[1], vx[2], vx[3]); };
      auto isw = [&](unsigned dst, int kk, int seg) { dma2(L0 + dst + w * 2048, wbase + ((kk + rot) & 7) * (64 * 3072) + seg * 1024, vw[0], vw[1]); };
      auto issue = [&](int kk) { isx(kk, kk); isw(WBUF[kk], kk, 1); isw(WBUF[kk] + 8192, kk, 2); };
      auto isq = [&](int p) { isw(QBUF[p], 2 * p + 1, 0); isw(QBUF[p] + 8192, 2 * p + 2, 0); };
      ST(9); issue(0); ST(10); issue(1); ST(11);
      LOAD_EPOCH();
#pragma unroll
      for (int i = 0; i < 4; ++i) vo[i] = (8 * i + r8) * 1024 + ((c8 ^ (((r8 >> 1) & 3) << 1)) << 4) - 1024 * i;
      const GAS char* obase = (const GAS char*)wo + cg * 128;
      asm volatile("s_waitcnt vmcnt(8) lgkmcnt(0)\n\ts_barrier" : "+s"(epoch) :: "memory");
      issue(2);
      wait_bar<8>(); issue(3);
      wait_bar<8>(); issue(4);
      wait_bar<8>(); issue(5);
      wait_bar<8>(); issue(6);
      wait_bar<8>(); issue(7);
      wait_bar<8>();
      unsigned pfo;
      asm volatile("global_load_dword %0, %1, %2" : "=v"(pfo) : "v"(((jj >> 3) * 128 + w * 32 + l31) * 1024), "s"(obase) : "memory");
      isq(0);
      wait_bar<5>(); isq(1);
      wait_bar<4>(); isq(2);
      wait_bar<4>(); isq(3); isx(0, 1);
      asm volatile("s_waitcnt vmcnt(0)\n\ts_barrier" : "+v"(pfo) :: "memory");
      if (PRI & 1) __builtin_amdgcn_s_setprio(3);
      ST(1);
      const unsigned tag = epoch + 1, t = tag & 0xffu;
      const GAS char* kvb_ = (const GAS char*)wsp + WS_KV + head * 16384;
      const GAS char* zb_ = (const GAS char*)wsp + WS_Z + head * 2048;
      const unsigned qr = (jj >> 3) * 4096;
      const unsigned vk0 = lane * 16 + qr, vk1 = lane * 16 + ((qr + 4096) & 12288), vz = l15 * 128 + q * 16;
      u4 g[8], gz;
#define KVL(i, v) asm volatile("global_load_dwordx4 %0, %1, %2 offset:%3 sc1" : "=v"(g[i]) : "v"(v), "s"(kvb_), "i"((i & 3) * 1024) : "memory")
      asm volatile("global_load_dwordx4 %0, %1, %2 sc1" : "=v"(gz) : "v"(vz), "s"(zb_) : "memory");
      KVL(0, vk0); KVL(1, vk0); KVL(2, vk0); KVL(3, vk0); KVL(4, vk1); KVL(5, vk1); KVL(6, vk1); KVL(7, vk1);
      auto zsum = [&]() {
        float z0 = __uint_as_float(gz.x), z1 = __uint_as_float(gz.y), z2 = __uint_as_float(gz.z), z3 = __uint_as_float(gz.w);
#define ZS(r) "v_add_f32_dpp %0, %0, %0 row_ror:" #r " row_mask:0xf bank_mask:0xf\n\tv_add_f32_dpp %1, %1, %1 row_ror:" #r " row_mask:0xf bank_mask:0xf\n\t" \
              "v_add_f32_dpp %2, %2, %2 row_ror:" #r " row_mask:0xf bank_mask:0xf\n\tv_add_f32_dpp %3, %3, %3 row_ror:" #r " row_mask:0xf bank_mask:0xf\n\t"
        asm volatile("s_nop 1\n\t" ZS(8) ZS(4) ZS(2) ZS(1) "s_nop 0" : "+v"(z0), "+v"(z1), "+v"(z2), "+v"(z3));
        return (f4){z0, z1, z2, z3};
      };
      asm volatile("s_waitcnt vmcnt(8)" : "+v"(gz) :: "memory");
      gz.x ^= t; gz.z ^= t;
      unsigned bad = gz.x | gz.z;
      f4 zz = zsum(), kv = {0, 0, 0, 0};
      asm volatile("" : "+v"(zz));
#define KVA(i) asm volatile("s_waitcnt vmcnt(%1)" : "+v"(g[i]) : "i"(7 - i) : "memory"); g[i].x ^= t; g[i].z ^= t; bad |= g[i].x | g[i].z; kv += __builtin_bit_cast(f4, g[i]); asm volatile("" : "+v"(kv), "+v"(bad))
      KVA(0); KVA(1); KVA(2); KVA(3); KVA(4); KVA(5); KVA(6); KVA(7);
      ST(3);
      if (!__all((bad & 0xffu) == 0)) {
#pragma unroll 1
        for (unsigned rep = 0; rep < SPIN_CAP; ++rep) {
          asm volatile("" ::: "memory");
          gz = __builtin_amdgcn_raw_buffer_load_b128(ws, WS_Z + head * 2048 + vz, 0, SC1);
#pragma unroll
          for (int i = 0; i < 8; ++i) g[i] = __builtin_amdgcn_raw_buffer_load_b128(ws, WS_KV + head * 16384 + (i < 4 ? vk0 : vk1) + (i & 3) * 1024, 0, SC1);
          gz.x ^= t; gz.z ^= t;
          bad = gz.x | gz.z;
#pragma unroll
          for (int i = 0; i < 8; ++i) { g[i].x ^= t; g[i].z ^= t; bad |= g[i].x | g[i].z; }
          if (__all((bad & 0xffu) == 0)) break;
        }
        zz = zsum();
        kv = (f4){0, 0, 0, 0};
#pragma unroll
        for (int i = 0; i < 8; ++i) kv += __builtin_bit_cast(f4, g[i]);
      }
      ST(5);
      *(LAS f4*)(L + SCR + w * 1024 + lane * 16) = kv;
      *(LAS u2*)(L + SCR + 8192 + w * 512 + lane * 8) = (u2){pk2(zz[0], zz[1]), pk2(zz[2], zz[3])};
      ST(6);
      lbar();
      ST(7);
      lbar();
#pragma unroll
      for (int i = 0; i < 4; ++i)
        dma4(L0 + OBUF[i] + w * 4096, obase + (2 * seq(2 * i + (w >> 1)) + (w & 1)) * 32768, vo[0], vo[1], vo[2], vo[3]);
      wait_bar<0>();
      ST(8);
      HFLUSH();
      if (bid == 0 && w == 0) {
#pragma unroll 1
        for (unsigned sp = 0; sp < SPIN_CAP; ++sp) {
          asm volatile("" ::: "memory");
          const unsigned fo = (lane >> 3) * 4096 + (lane & 7) * 256;
          const u2 f0 = __builtin_amdgcn_raw_buffer_load_b64(ws, WS_FA + fo, 0, SC1), f1 = __builtin_amdgcn_raw_buffer_load_b64(ws, WS_FA + 32768 + fo, 0, SC1);
          const u2 f2_ = __builtin_amdgcn_raw_buffer_load_b64(ws, WS_FB + fo, 0, SC1), f3 = __builtin_amdgcn_raw_buffer_load_b64(ws, WS_FB + 32768 + fo, 0, SC1);
          if (__all(f0.x == tag && f0.y == ~tag && f1.x == tag && f1.y == ~tag && f2_.x == tag && f2_.y == ~tag && f3.x == tag && f3.y == ~tag)) break;
          __builtin_amdgcn_s_sleep(8);
        }
        if (lane == 0) __builtin_amdgcn_raw_buffer_store_b32(tag, ws, WS_EPOCH, 0, SC1);
      }
      return;
    }
    LOAD_EPOCH();
    const int hp = w & 1, th = w >> 1;
    unsigned warm0 = 0, warm1 = 0;
    if (TOUCH) {
      if (TD) __builtin_amdgcn_s_sleep(TD);
      warm0 = __builtin_amdgcn_raw_buffer_load_b32(ws, (!FW && lane < 16) ? WS_FA + ((tile * 2 + th) * 16 + lane) * 128 : (!FW && lane < 32) ? WS_Z + (cg * 4 + w) * 2048 + (lane - 16) * 128
                                                   : lane == 32 ? WS_END - 4096 * 256 * 6 - 128 : lane == 33 ? WS_O + tile * 131072 : 0x80000000u, 0, SC1);
      warm1 = __builtin_amdgcn_raw_buffer_load_b32(ro, lane < 2 ? (tile * 128 + w * 32 + lane * 16) * 4096 : 0x80000000u, 0, SC1);
    }
    const int qq = (lane >> 2) & 3, pp = lane & 3;
    const unsigned ax0 = l15 * 128 + ((q ^ (l15 >> 1)) << 4), ax1 = l15 * 128 + (((4 + q) ^ (l15 >> 1)) << 4);
    const Win wx[2] = {mkwin(ax0), mkwin(ax1)}, wq[2] = {mkwin(ax0 + th * 8192), mkwin(ax1 + th * 8192)};
    auto waddr = [&](int h) { return (8 * q + qq) * 128 + (((2 * h + (pp >> 1)) ^ ((((qq >> 1) & 1) | ((q & 1) << 1)) << 1)) << 4) + 8 * (pp & 1); };
    const Win aw = mkwin(waddr(w)), aq0 = mkwin(waddr(2 * hp)), aq1 = mkwin(waddr(2 * hp + 1));
    MKSQ();
    unsigned ao[4], og[4], tw[4], trd[4];
#pragma unroll
    for (int i = 0; i < 4; ++i) {
      og[i] = WS_O + ((tile * 8 + th * 4 + i) * 16 + cg * 2 + hp) * 1024 + lane * 16;
      ao[i] = (4 * q + qq) * 128 + ((((2 * i) ^ (((2 * q + (qq >> 1)) & 3) << 1)) + (pp >> 1)) << 4) + 8 * (pp & 1);
      tw[i] = TRB + w * 8192 + l15 * 256 + (((4 * i + q) ^ l15) << 4);
      trd[i] = TRB + w * 8192 + (4 * i + q) * 256 + ((l15 ^ (4 * i + q)) << 4);
      asm volatile("" : "+v"(og[i]), "+v"(ao[i]), "+v"(tw[i]), "+v"(trd[i]));
    }
    unsigned so_ = ((tile * 128 + w * 32 + q) * 1024 + cg * 64 + l15 * 4) * 4, ex = TRB + th * 16384 + hp * 1024 + lane * 16;
    unsigned sk = SCR + hp * 2048 + lane * 16, sz_ = SCR + 8192 + hp * 1024 + lane * 8, sw_ = SCR + 4096 + w * 1024 + lane * 16;
    unsigned pk_ = WS_KV + ((cg * 4 + w) * 16 + tile) * 1024 + lane * 16, pz_ = l15 < (FW ? 2 : 1) ? WS_Z + ((cg * 4 + w) * 16 + tile) * 128 + l15 * 64 + q * 16 : 0x80000000u;
    unsigned pf_ = lane < (FW ? 16 : 1) ? WS_FA + ((tile * 2 + th) * 16 + cg * 2 + hp) * 128 + lane * 8 : 0x80000000u;
    asm volatile("" : "+v"(so_), "+v"(ex), "+v"(sk), "+v"(sz_), "+v"(sw_), "+v"(pk_), "+v"(pz_), "+v"(pf_));
    f4 ka[8], va[8];
#pragma unroll
    for (int i = 0; i < 8; ++i) { ka[i] = (f4){EPS, EPS, EPS, EPS}; va[i] = (f4){0, 0, 0, 0}; }
    bf8 X0[4], X1[4], W0[2], W1[2];
    auto rdx = [&](bf8 (&X)[4], int kk, int ks, unsigned off) {
#pragma unroll
      for (int i = 0; i < 4; ++i) X[i] = ldsr(at(L, wx[ks], kk * 16384 + off + i * 2048));
    };
    auto rdw = [&](bf8 (&W)[2], int kk, int ks) {
#pragma unroll
      for (int i = 0; i < 2; ++i) { const unsigned o = WBUF[kk] + ks * 4096 + i * 8192; W[i] = ldstr(at(L, aw, o), at(L, aw, o + 512)); }
    };
    auto mm = [&](const bf8 (&X)[4], const bf8 (&W)[2], int h) {
#pragma unroll
      for (int i = 0; i < 4; ++i) {
        ka[4 * h + i] = mfma16(X[i], W[0], ka[4 * h + i]);
        va[4 * h + i] = mfma16(X[i], W[1], va[4 * h + i]);
      }
    };
#define ILV(nr, nm) do { _Pragma("unroll") for (int i_ = 0; i_ < nr; ++i_) { SG(0x008, 1); SG(0x100, 1); } if (nm > nr) SG(0x008, nm - nr); } while (0)
    asm volatile("s_waitcnt lgkmcnt(0)\n\ts_barrier" : "+s"(epoch) : "v"(warm0), "v"(warm1), "v"(cpf) : "memory");
    ST(1);
    const unsigned tag = epoch + 1, t = tag & 0xffu;
    rdw(W0, 0, 0); rdx(X0, 0, 0, 0);
    SB();
#pragma unroll
    for (int kk = 0; kk < 8; ++kk) {
      rdx(X1, kk, 0, 8192); mm(X0, W0, 0); ILV(4, 8); SB(); PACE(AN);
      rdw(W1, kk, 1); rdx(X0, kk, 1, 0); mm(X1, W0, 1); ILV(8, 8); SB(); PACE(AN);
      rdx(X1, kk, 1, 8192); mm(X0, W1, 0); ILV(4, 8); SB();
      if (kk < 7) { lbar(); rdw(W0, kk + 1, 0); rdx(X0, kk + 1, 0, 0); mm(X1, W1, 1); ILV(8, 8); }
      else mm(X1, W1, 1);
      SB();
    }
    ST(2);
    auto rdq = [&](bf8 (&X)[4], bf8 (&W)[2], int s) {
      const unsigned o = QBUF[s >> 2] + ((s >> 1) & 1) * 8192 + (s & 1) * 4096;
      W[0] = ldstr(at(L, aq0, o), at(L, aq0, o + 512)); W[1] = ldstr(at(L, aq1, o), at(L, aq1, o + 512));
#pragma unroll
      for (int i = 0; i < 4; ++i) X[i] = ldsr(at(L, wq[s & 1], (s < 14 ? (s >> 1) + 1 : 1) * 16384 + i * 2048));
    };
    {
      f4 kv = {0, 0, 0, 0}, zz = {0, 0, 0, 0};
      const u4 ones = {0x3f803f80u, 0x3f803f80u, 0x3f803f80u, 0x3f803f80u};
#pragma unroll
      for (int m = 0; m < 4; ++m) {
        const f4 a = ka[2 * m], b = ka[2 * m + 1], c = va[2 * m], d = va[2 * m + 1];
        const u4 pa = {phi2(a[0], a[1]), phi2(a[2], a[3]), phi2(b[0], b[1]), phi2(b[2], b[3])};
        const u4 pb = {pk2(c[0], c[1]), pk2(c[2], c[3]), pk2(d[0], d[1]), pk2(d[2], d[3])};
        kv = mfma16(asbf8(pa), asbf8(pb), kv);
        zz = mfma16(asbf8(pa), asbf8(ones), zz);
      }
      const u4 skv = {tagv(kv[0], t), __float_as_uint(kv[1]), tagv(kv[2], t), __float_as_uint(kv[3])};
      __builtin_amdgcn_raw_buffer_store_b128(skv, ws, pk_, 0, SC1);
      const u4 sz = {tagv(zz[0], t), __float_as_uint(zz[1]), tagv(zz[2], t), __float_as_uint(zz[3])};
      __builtin_amdgcn_raw_buffer_store_b128(sz, ws, pz_, 0, SC1);
    }
    SB();
    ST(3);
    lbar();
    rdq(X0, W0, 0);
    SB();
    f4 qa[2][4];
#pragma unroll
    for (int i = 0; i < 8; ++i) qa[i >> 2][i & 3] = (f4){EPS, EPS, EPS, EPS};
    u4 gc[8];
    const unsigned gb = WS_KV + (cg * 4 + w) * 16384 + lane * 16, g2 = (((jj >> 3) + 2) & 3) * 4096, g3 = (((jj >> 3) + 3) & 3) * 4096;
    auto ldg = [&](int i) { gc[i] = __builtin_amdgcn_raw_buffer_load_b128(ws, gb + (i & 3) * 1024, i < 4 ? g2 : g3, SC1); };
    auto mq = [&](const bf8 (&X)[4], const bf8 (&W)[2]) {
#pragma unroll
      for (int i = 0; i < 4; ++i) { qa[0][i] = mfma16(W[0], X[i], qa[0][i]); qa[1][i] = mfma16(W[1], X[i], qa[1][i]); }
    };
#define ILQ() do { _Pragma("unroll") for (int i_ = 0; i_ < 8; ++i_) { SG(0x008, 1); SG(0x100, 1); if (i_ == 2 || i_ == 6) SG(0x020, 1); } } while (0)
#pragma unroll
    for (int s = 0; s < 16; s += 2) {
      if (s == 8 || s == 10) { ldg(2 * s - 14); ldg(2 * s - 13); }
      rdq(X1, W1, s + 1); mq(X0, W0); ILQ(); SB();
      if (s == 2 || s == 6) lbar();
      if (s == 6 || s == 8) { ldg(2 * s - 12); ldg(2 * s - 11); }
      if (s < 14) { rdq(X0, W0, s + 2); mq(X1, W1); ILQ(); }
      else mq(X1, W1);
      SB();
    }
    ST(4);
    u2 pq[2][4];
#pragma unroll
    for (int i = 0; i < 8; ++i) {
      pq[i >> 2][i & 3] = (u2){phi2(qa[i >> 2][i & 3][0], qa[i >> 2][i & 3][1]), phi2(qa[i >> 2][i & 3][2], qa[i >> 2][i & 3][3])};
      asm volatile("" : "+v"(pq[i >> 2][i & 3]));
    }
    SB();
    {
      unsigned bad = 0;
      f4 kvp = {0, 0, 0, 0};
#pragma unroll
      for (int i = 0; i < 8; ++i) { gc[i].x ^= t; gc[i].z ^= t; bad |= gc[i].x | gc[i].z; kvp += __builtin_bit_cast(f4, gc[i]); }
      if (__builtin_expect(!__all((bad & 0xffu) == 0), 0)) {
#pragma unroll 1
        for (unsigned rep = 0; rep < SPIN_CAP; ++rep) {
          asm volatile("" ::: "memory");
          bad = 0;
          kvp = (f4){0, 0, 0, 0};
#pragma unroll
          for (int i = 0; i < 8; ++i) ldg(i);
#pragma unroll
          for (int i = 0; i < 8; ++i) { gc[i].x ^= t; gc[i].z ^= t; bad |= gc[i].x | gc[i].z; kvp += __builtin_bit_cast(f4, gc[i]); }
          asm volatile("" : "+v"(kvp));
          if (__all((bad & 0xffu) == 0)) break;
        }
      }
      *(LAS f4*)(L + sw_) = kvp;
    }
    lbar();
    ST(5);
    const f4 zero4 = {0, 0, 0, 0}, eps4 = {EPS, EPS, EPS, EPS};
    f4 num[2][4], den[2][4];
    const f4 ka0 = *(const LAS f4*)(L + sk), kb0 = *(const LAS f4*)(L + sk + 4096), ka1 = *(const LAS f4*)(L + sk + 1024), kb1 = *(const LAS f4*)(L + sk + 5120);
    const u2 zb[2] = {*(const LAS u2*)(L + sz_), *(const LAS u2*)(L + sz_ + 512)};
    SB();
    const f4 k0 = ka0 + kb0, k1 = ka1 + kb1;
    const u2 kb2[2] = {{pk2(k0[0], k0[1]), pk2(k0[2], k0[3])}, {pk2(k1[0], k1[1]), pk2(k1[2], k1[3])}};
#pragma unroll
    for (int i = 0; i < 8; ++i) { den[i & 1][i >> 1] = mfma16k16(zb[i & 1], pq[i & 1][i >> 1], eps4); num[i & 1][i >> 1] = mfma16k16(kb2[i & 1], pq[i & 1][i >> 1], zero4); }
    SB();
#pragma unroll
    for (int i = 0; i < 4; ++i) {
      u4 so;
#pragma unroll
      for (int h = 0; h < 2; ++h) {
        const float rc = __builtin_amdgcn_rcpf(den[h][i][0]);
        const f4 n = num[h][i];
        so[2 * h] = pk2((f2){n[0], n[1]} * rc);
        so[2 * h + 1] = pk2((f2){n[2], n[3]} * rc);
      }
      __builtin_amdgcn_raw_buffer_store_b128(so, ws, og[i], 0, SC1);
      *(LAS u4*)(L + ex + (i >> 1) * 8192 + (i & 1) * 2048) = so;
    }
    lbar();
    ST(6);
    u4 of[32];
    unsigned ah[4];
#pragma unroll
    for (int i = 0; i < 4; ++i) { ah[i] = ao[i] + 65536; asm volatile("" : "+v"(ah[i])); }
#pragma unroll
    for (int i = 0; i < 4; ++i) of[i] = *(const LAS u4*)(L + TRB + w * 8192 + ((i & 1) * 2 + (i >> 1)) * 1024 + lane * 16);
    if (!NODRAIN) drain();
    __builtin_amdgcn_raw_buffer_store_b64((u2){tag, ~tag}, ws, pf_, 0, SC1);
    asm volatile("s_barrier" ::: "memory");
    ST(7);
    f4 acc[2][4];
#pragma unroll
    for (int i = 0; i < 8; ++i) acc[i >> 2][i & 3] = zero4;
    unsigned ob = WS_O + (tile * 8 + w * 2) * 16384 + lane * 16, ob2 = ob + 16384;
    asm volatile("" : "+v"(ob), "+v"(ob2));
    bf8 wf[2][4];
    auto rdo = [&](int k) {
#pragma unroll
      for (int nt = 0; nt < 4; ++nt) { const unsigned o = OBUF[k >> 2] + (k & 3) * 4096; const char LAS* p = L + (o < 65536 ? ao[nt] : ah[nt]) + (o & 65535); wf[k & 1][nt] = ldstr(p, p + 2048); }
    };
    auto ldo = [&](int k) {
      const unsigned so = sq[k >> 1] * 2048;
      of[2 * k] = __builtin_amdgcn_raw_buffer_load_b128(ws, ob + (k & 1) * 1024, so, PAY_AUX);
      of[2 * k + 1] = __builtin_amdgcn_raw_buffer_load_b128(ws, ob2 + (k & 1) * 1024, so, PAY_AUX);
    };
    auto mo = [&](int k) {
#pragma unroll
      for (int nt = 0; nt < 4; ++nt) {
        acc[0][nt] = mfma16(wf[k & 1][nt], asbf8(of[2 * k]), acc[0][nt]);
        acc[1][nt] = mfma16(wf[k & 1][nt], asbf8(of[2 * k + 1]), acc[1][nt]);
      }
    };
    rdo(0); rdo(1); SB();
    mo(0); rdo(2); SB();
    mo(1); SB();
    if (FD) __builtin_amdgcn_s_sleep(FD);
    const unsigned fo = WS_FA + ((tile * 2 + th) * 16 + l15) * 128;
    unsigned have = 0;
    if (!PROG) wait_flags(ws, fo, tag);
    if (DLA) __builtin_amdgcn_s_sleep(DLA);
#pragma unroll
    for (int k = 2; k < 2 + OD; ++k) { if (PROG && (k & 1) == 0) wait_pair(ws, fo, tag, have, sq[k >> 1]); ldo(k); }
    ST(8); CT(14);
    SB();
#pragma unroll
    for (int k = 2; k < 16; ++k) {
      if (PROG && k + OD < 16 && ((k + OD) & 1) == 0) wait_pair(ws, fo, tag, have, sq[(k + OD) >> 1]);
      if (k + OD < 16) ldo(k + OD);
      if (k + 1 < 16) rdo(k + 1);
      mo(k);
#pragma unroll
      for (int i_ = 0; i_ < 8; ++i_) { SG(0x008, 1); SG(0x100, 1); if (i_ == 1 || i_ == 5) SG(0x020, 1); }
      SB();
    }
    ST(9); CT(15);
    if (DIRECT) {
      const unsigned sd = ((tile * 128 + w * 32 + l15) * 1024 + cg * 64 + q * 4) * 4;
      u4 dvv[8];
#pragma unroll
      for (int i = 0; i < 8; ++i) {
        const f4 v = acc[i >> 2][i & 3];
        const unsigned p0 = pk2(v[0], v[1]), p1 = pk2(v[2], v[3]);
        dvv[i] = (u4){p0 << 16, p0 & 0xffff0000u, p1 << 16, p1 & 0xffff0000u};
        __builtin_amdgcn_raw_buffer_store_b128(dvv[i], ro, sd + (i >> 2) * 65536 + (i & 3) * 64, 0, OUT_AUX);
      }
      return;
    }
#pragma unroll
    for (int mt = 0; mt < 2; ++mt)
#pragma unroll
      for (int nt = 0; nt < 4; ++nt) {
        const f4 v = acc[mt][nt];
        const unsigned p0 = pk2(v[0], v[1]), p1 = pk2(v[2], v[3]);
        *(LAS f4*)(L + tw[nt] + mt * 4096) = (f4){bflo(p0), bfhi(p0), bflo(p1), bfhi(p1)};
      }
    u4 ov[8];
#pragma unroll
    for (int i = 0; i < 8; ++i) ov[i] = *(const LAS u4*)(L + trd[i & 3] + (i >> 2) * 4096);
    SB();
#pragma unroll
    for (int i = 0; i < 8; ++i) __builtin_amdgcn_raw_buffer_store_b128(ov[i], ro, so_, i * 16384, OUT_AUX);
    ST(10);
  } else {
    constexpr unsigned SLOT = 32768, SCR = SLOT, PAR = 4 * SLOT;
    const int wp = w & 1, wt = w >> 1;
    const unsigned ecol = WS_EST + (cg * 64 + wp * 32 + l31) * 8;
    if (helper) {
      unsigned vx[4], vw[4], vc[4];
#pragma unroll
      for (int i = 0; i < 4; ++i) {
        const unsigned sw = (c8 ^ ((4 * i + (r8 >> 1)) & 7)) << 4;
        vx[i] = (64 * (w >> 1) + 32 * (i & 1) + 16 * (r8 >> 2) + 8 * (r8 & 1) + 4 * (w & 1) + 2 * (i >> 1) + ((r8 >> 1) & 1)) * 1024 + sw - 1024 * i;
        vw[i] = (16 * (i & 1) + 8 * (r8 >> 2) + 4 * (i >> 1) + (r8 & 3)) * 1024 + sw - 1024 * i;
        vc[i] = (8 * i + r8) * 1024 + sw - 1024 * i;
      }
      const GAS char* xbase = (const GAS char*)xb + (size_t)tile * 131072;
      const GAS char* wbase = (const GAS char*)((w & 1) ? bim : bre) + (cg * 64 + (w >> 1) * 32) * 1024;
      auto issue = [&](int kk) {
        const int kc = (kk + rot) & 7;
        const unsigned s = L0 + (kk % 5) * SLOT + w * 4096;
        dmax(s, xbase + kc * 128, vx[0], vx[1], vx[2], vx[3]);
        dma4(s + 16384, wbase + kc * 128, vw[0], vw[1], vw[2], vw[3]);
      };
      ST(9); issue(0); ST(10); issue(1); ST(11);
      LOAD_EPOCH();
      const GAS char* cbase = (const GAS char*)((w & 1) ? cim : cre) + cg * 65536;
      auto issue_c = [&](int piece) {
        const unsigned s = L0 + ((piece + 3) % 5) * SLOT + w * 8192;
        const GAS char* cb = cbase + seq(2 * piece + (w >> 1)) * 128;
        dma4(s, cb, vc[0], vc[1], vc[2], vc[3]);
        dma4(s + 4096, cb + 32768, vc[0], vc[1], vc[2], vc[3]);
      };
      asm volatile("s_waitcnt vmcnt(8) lgkmcnt(0)\n\ts_barrier" : "+s"(epoch) :: "memory");
      issue(2);
      const f4 par = *(const LAS f4*)(L + PAR + w * 1024 + lane * 16);
      const f2 l64 = {par[0], par[1]}, l128 = {par[2], par[3]};
      f2 l256 = cmul(l128, l128);
      f2 wgt = hh ? l64 : (f2){1.f, 0.f};
      if (wt) wgt = cmul(wgt, l128);
      const unsigned tag = epoch + 1, t = tag & 0xffu;
      unsigned eo[8], et[8];
#pragma unroll
      for (int m = 0; m < 8; ++m) {
        const int i = 2 * tile - 1 - (2 * wt + hh) - 4 * m;
        eo[m] = i >= 0 ? ecol + i * 4096 : 0x80000000u;
        et[m] = i >= 0 ? t : 0u;
        asm volatile("" : "+v"(eo[m]), "+v"(et[m]));
      }
      asm volatile("" : "+v"(l256), "+v"(wgt));
      wait_bar<8>(); issue(3);
      wait_bar<8>(); issue(4);
      wait_bar<8>(); issue(5);
      wait_bar<8>(); issue(6);
      wait_bar<8>(); issue(7);
      wait_bar<8>(); issue_c(0);
      wait_bar<8>(); ST(1);
      if (NC_EARLY > 1) issue_c(1);
      if (NC_EARLY > 2) issue_c(2);
      ST(2);
      asm volatile("s_barrier" ::: "memory");
      if (PD) __builtin_amdgcn_s_sleep(PD);
      if (PRI & 2) __builtin_amdgcn_s_setprio(3);
      f2 cin;
#pragma unroll 1
      for (unsigned sp = 0; sp < SPIN_CAP; ++sp) {
      asm volatile("" ::: "memory");
        u2 g[8];
#pragma unroll
        for (int m = 0; m < 8; ++m) g[m] = __builtin_amdgcn_raw_buffer_load_b64(ws, eo[m], 0, SC1);
        unsigned bad = 0;
        cin = (f2){0, 0};
#pragma unroll
        for (int m = 7; m >= 0; --m) {
          const unsigned cx = g[m].x ^ et[m];
          bad |= cx;
          cin = cfma(l256, cin, (f2){__uint_as_float(cx), __uint_as_float(g[m].y)});
        }
        if (__all((bad & 0xffu) == 0)) break;
      }
      ST(5);
      cin = cmul(cin, wgt);
      f2 chi;
      const f2 clo = swap_lo(cin, chi);
      if (hh == 0) *(LAS f2*)(L + SCR + (wp * 2 + wt) * 256 + l31 * 8) = clo + chi;
      ST(6);
      lbar();
      if (WOX & 2) issue_c(2);
      lbar();
      ST(7);
      if (NC_EARLY <= 1) issue_c(1);
      if (NC_EARLY <= 2 && !(WOX & 2)) issue_c(2);
      issue_c(3);
      wait_bar<0>();
      ST(8); HFLUSH();
      return;
    }
    LOAD_EPOCH();
    const int pl = cg * 64 + wp * 32 + 8 * (l15 >> 2) + 4 * (q & 1) + (l15 & 3);
    const unsigned plr = lre[pl], pli = lim[pl], pdt = ldt[pl];
    u4 dv[2];
#pragma unroll
    for (int i = 0; i < 2; ++i) dv[i] = *(const GAS u4*)(dsk + cg * 64 + 32 * i + 8 * q);
    unsigned warm0 = 0, warm1 = 0;
    if (TOUCH) {
      warm0 = __builtin_amdgcn_raw_buffer_load_b32(ws, (!FW && lane < 16) ? WS_FB + ((tile * 2 + wt) * 16 + lane) * 128 : lane == 16 ? WS_END - 4096 * 256 * 6 - 128
                                                   : lane == 17 ? WS_H + (tile * 4 + w) * 65536 : 0x80000000u, 0, SC1);
      warm1 = __builtin_amdgcn_raw_buffer_load_b32(ro, lane < 2 ? (tile * 128 + w * 32 + lane * 16) * 4096 + 2048 : 0x80000000u, 0, SC1);
    }
    SB();
    f2 lam[2], l8[2], kap, l16, l32, l64;
    {
      const float lr = bflo(plr), li = bflo(pli);
      const float dt = bflo(pk2(expf(bflo(pdt)), 0.f));
      const float mag = expf(dt * lr);
      float sn, cs;
      sincosf(dt * li, &sn, &cs);
      const f2 l1 = {mag * cs, mag * sn};
      const float inv = 2.f / (lr * lr + li * li), nr = l1.x - 1.f, ni = l1.y;
      kap = (f2){(nr * lr + ni * li) * inv, (ni * lr - nr * li) * inv};
      f2 t = cmul(l1, l1); t = cmul(t, t); t = cmul(t, t);
      l16 = cmul(t, t); l32 = cmul(l16, l16); l64 = cmul(l32, l32);
      const f2 l128 = cmul(l64, l64);
      *(LAS f4*)(L + PAR + w * 1024 + lane * 16) = (f4){l64.x, l64.y, l128.x, l128.y};
      lam[0] = swap16(l1, l1, lam[1]);
      l8[0] = swap16(t, t, l8[1]);
    }
    f2 R0 = {lam[0].x, lam[0].x}, I0 = {lam[0].y, lam[0].y}, N0 = -I0, R1 = {lam[1].x, lam[1].x}, I1 = {lam[1].y, lam[1].y}, N1 = -I1;
    unsigned pe_ = hh == 0 ? ecol + (tile * 2 + wt) * 4096 : 0x80000000u, se_ = SCR + 1024 + (wt * 2 + wp) * 256 + hh * 1024 + l31 * 8;
    asm volatile("" : "+v"(R0), "+v"(I0), "+v"(N0), "+v"(R1), "+v"(I1), "+v"(N1), "+v"(pe_), "+v"(se_));
    const unsigned ax[2] = {(unsigned)(l15 * 128 + ((q ^ (l15 >> 1)) << 4)), (unsigned)(l15 * 128 + (((4 + q) ^ (l15 >> 1)) << 4))};
    const Win wx[2] = {mkwin(ax[0] + wt * 8192), mkwin(ax[1] + wt * 8192)}, ww[2] = {mkwin(ax[0] + wp * 8192), mkwin(ax[1] + wp * 8192)};
    MKSQ();
    f4 u[2][2][4];
#pragma unroll
    for (int i = 0; i < 16; ++i) u[i >> 3][(i >> 2) & 1][i & 3] = (f4){0, 0, 0, 0};
    u4 xd[2][2];
#pragma unroll
    for (int i = 0; i < 4; ++i) xd[i >> 1][i & 1] = (u4){0, 0, 0, 0};
    const int kx = (cg - rot) & 7;
    bf8 Xa[2], Xb[2], W0[4], W1[4];
    auto rdx = [&](bf8 (&X)[2], int kk, int ks, int h) {
#pragma unroll
      for (int i = 0; i < 2; ++i) X[i] = ldsr(at(L, wx[ks], (kk % 5) * SLOT + (2 * h + i) * 2048));
    };
    auto rdw = [&](bf8 (&W)[4], int kk, int ks) {
#pragma unroll
      for (int i = 0; i < 4; ++i) W[i] = ldsr(at(L, ww[ks], (kk % 5) * SLOT + 16384 + (i & 1) * 4096 + (i >> 1) * 2048));
    };
    auto mm = [&](const bf8 (&X)[2], const bf8 (&W)[4], int h) {
#pragma unroll
      for (int i = 0; i < 2; ++i)
#pragma unroll
        for (int j = 0; j < 4; ++j) u[j >> 1][j & 1][2 * h + i] = mfma16(X[i], W[j], u[j >> 1][j & 1][2 * h + i]);
    };
    asm volatile("s_waitcnt lgkmcnt(0)\n\ts_barrier" : "+s"(epoch), "+v"(lam[0]), "+v"(lam[1]), "+v"(l8[0]), "+v"(l8[1]), "+v"(kap), "+v"(l16), "+v"(l32), "+v"(l64),
                 "+v"(dv[0]), "+v"(dv[1]) : "v"(warm0), "v"(warm1), "v"(cpf) : "memory");
    ST(1);
    const unsigned tag = epoch + 1, t = tag & 0xffu;
    rdw(W0, 0, 0); rdx(Xa, 0, 0, 0);
    SB();
#pragma unroll
    for (int kk = 0; kk < 8; ++kk) {
      rdx(Xb, kk, 0, 1); mm(Xa, W0, 0); ILV(2, 8); SB(); PACE(BN);
      rdw(W1, kk, 1); rdx(Xa, kk, 1, 0); mm(Xb, W0, 1); ILV(6, 8); SB(); PACE(BN);
      if (kk == kx) {
#pragma unroll
        for (int i = 0; i < 4; ++i) xd[i >> 1][i & 1] = *(const LAS u4*)(at(L, wx[i & 1], (kk % 5) * SLOT + (i >> 1) * 2048) + (w & 1) * 4096);
      }
      SB();
      rdx(Xb, kk, 1, 1); mm(Xa, W1, 0); ILV(2, 8); SB();
      if (kk < 7) { lbar(); if (kk == 6) ST(2); rdw(W0, kk + 1, 0); rdx(Xa, kk + 1, 0, 0); mm(Xb, W1, 1); ILV(6, 8); }
      else mm(Xb, W1, 1);
      SB(); if (kk < 7) PACE(BN);
    }
    ST(3);
    f2 gr[2][8], gi[2][8];
#pragma unroll
    for (int i = 0; i < 16; ++i) {
      const int pt = i >> 3, a = i & 7;
      gr[pt][a] = (f2){u[pt][0][a >> 1][2 * (a & 1)], u[pt][0][a >> 1][2 * (a & 1) + 1]};
      gi[pt][a] = (f2){u[pt][1][a >> 1][2 * (a & 1)], u[pt][1][a >> 1][2 * (a & 1) + 1]};
    }
    {
#pragma unroll
      for (int a = 1; a < 8; ++a)
        asm volatile("v_pk_fma_f32 %0, %10, %5, %0\n\tv_pk_fma_f32 %1, %9, %4, %1\n\tv_pk_fma_f32 %2, %13, %7, %2\n\tv_pk_fma_f32 %3, %12, %6, %3\n\t"
                     "v_pk_fma_f32 %0, %8, %4, %0\n\tv_pk_fma_f32 %1, %8, %5, %1\n\tv_pk_fma_f32 %2, %11, %6, %2\n\tv_pk_fma_f32 %3, %11, %7, %3"
                     : "+v"(gr[0][a]), "+v"(gi[0][a]), "+v"(gr[1][a]), "+v"(gi[1][a])
                     : "v"(gr[0][a - 1]), "v"(gi[0][a - 1]), "v"(gr[1][a - 1]), "v"(gi[1][a - 1]), "v"(R0), "v"(I0), "v"(N0), "v"(R1), "v"(I1), "v"(N1));
    }
    const f2 e0[2] = {{gr[0][7].x, gi[0][7].x}, {gr[1][7].x, gi[1][7].x}};
    f2 vodd;
    const f2 veven = swap16(cfma(l8[0], e0[0], (f2){gr[0][7].y, gi[0][7].y}), cfma(l8[1], e0[1], (f2){gr[1][7].y, gi[1][7].y}), vodd);
    f2 ehi;
    const f2 elo = swap_lo(cfma(l16, veven, vodd), ehi);
    const f2 e64 = cfma(l32, elo, ehi);
    __builtin_amdgcn_raw_buffer_store_b64((u2){tagv(e64.x, t), __float_as_uint(e64.y)}, ws, pe_, 0, SC1);
    *(LAS f2*)(L + se_) = e64;
    lbar();
    const f2 esib = *(const LAS f2*)(L + SCR + 1024 + wp * 256 + l31 * 8);
    SB();
    ST(4);
    f2 kp[2], ke[2], pw[2][8];
    kp[0] = swap16(kap, kap, kp[1]);
#pragma unroll
    for (int pt = 0; pt < 2; ++pt) {
      const f2 KR = {kp[pt].x, kp[pt].x}, KI = {kp[pt].y, kp[pt].y};
      ke[pt] = cmul(kp[pt], e0[pt]);
#pragma unroll
      for (int a = 0; a < 8; ++a) {
        const f2 xr = gr[pt][a], xi = gi[pt][a];
        gr[pt][a] = fma2(KR, xr, -KI * xi); gi[pt][a] = fma2(-KR, xi, -KI * xr);
        asm volatile("" : "+v"(gr[pt][a]), "+v"(gi[pt][a]));
      }
      pw[pt][0] = (f2){lam[pt].x, -lam[pt].y};
#pragma unroll
      for (int k = 1; k < 8; ++k) { pw[pt][k] = cmul(pw[pt][k >> 1], pw[pt][(k - 1) >> 1]); asm volatile("" : "+v"(pw[pt][k])); }
    }
    u2 idn = {(l15 == 4 * q ? 0x3f80u : 0u) | (l15 == 4 * q + 1 ? 0x3f800000u : 0u), (l15 == 4 * q + 2 ? 0x3f80u : 0u) | (l15 == 4 * q + 3 ? 0x3f800000u : 0u)};
#pragma unroll
    for (int i = 0; i < 4; ++i) {
#pragma unroll
      for (int j = 0; j < 4; ++j) xd[i >> 1][i & 1][j] = pk2(bflo(xd[i >> 1][i & 1][j]) * bflo(dv[i & 1][j]), bfhi(xd[i >> 1][i & 1][j]) * bfhi(dv[i & 1][j]));
      asm volatile("" : "+v"(xd[i >> 1][i & 1]));
    }
    f2 me = hh ? cmul(kap, l32) : kap, be = hh ? cmul(kap, elo) : (f2){0.f, 0.f};
    f2 mo = cmul(l16, me), bo = cfma(l16, be, cmul(kap, veven));
    if (wt) { be = cfma(me, esib, be); bo = cfma(mo, esib, bo); me = cmul(me, l64); mo = cmul(mo, l64); }
    unsigned sc_ = SCR + wp * 512 + l31 * 8, ho[4];
#pragma unroll
    for (int i = 0; i < 4; ++i) ho[i] = WS_H + (tile * 8 + wt * 4 + i) * 32768 + (cg * 2 + wp) * 2048 + lane * 16;
    asm volatile("s_waitcnt lgkmcnt(0)\n\ts_barrier" : "+v"(ho[0]), "+v"(ho[1]), "+v"(ho[2]), "+v"(ho[3]), "+v"(me), "+v"(be), "+v"(mo), "+v"(bo),
                 "+v"(ke[0]), "+v"(ke[1]), "+v"(idn), "+v"(sc_) :: "memory");
    ST(5);
    const f2 cin = *(const LAS f2*)(L + sc_) + *(const LAS f2*)(L + sc_ + 256);
    f2 d0[2], DR[2], DJ[2];
    d0[0] = swap16(cfma(me, cin, be), cfma(mo, cin, bo), d0[1]);
#pragma unroll
    for (int pt = 0; pt < 2; ++pt) {
      const f2 d1 = cfma(l8[pt], d0[pt], ke[pt]);
      DR[pt] = (f2){d0[pt].x, d1.x}; DJ[pt] = (f2){-d0[pt].y, -d1.y};
    }
    const f4 zero4 = {0, 0, 0, 0};
    f4 T[8][2];
    u4 ow[8];
    auto fix = [&](int i) {
      const int mt = i >> 1, c = i & 1;
#pragma unroll
      for (int pt = 0; pt < 2; ++pt) {
        u2 a;
#pragma unroll
        for (int h = 0; h < 2; ++h) {
          const f2 P = pw[pt][2 * mt + h], PR = {P.x, P.x}, PI = {P.y, P.y};
          a[h] = pk2(c ? fma2(PI, DR[pt], fma2(PR, DJ[pt], gi[pt][2 * mt + h])) : fma2(-PI, DJ[pt], fma2(PR, DR[pt], gr[pt][2 * mt + h])));
        }
        T[i][pt] = mfma16k16(a, idn, zero4);
      }
    };
    auto put = [&](int i) {
      ow[i] = (u4){pk2(T[i][0][0], T[i][0][1]), pk2(T[i][0][2], T[i][0][3]), pk2(T[i][1][0], T[i][1][1]), pk2(T[i][1][2], T[i][1][3])};
      __builtin_amdgcn_raw_buffer_store_b128(ow[i], ws, ho[i >> 1] + (i & 1) * 1024, 0, SC1);
    };
    fix(0); SB();
#pragma unroll
    for (int i = 1; i < 8; ++i) { fix(i); SB(); put(i - 1); SB(); }
    put(7);
    SB();
    const unsigned ex = 2 * SLOT + wt * 16384 + wp * 4096 + lane * 16;
#pragma unroll
    for (int i = 0; i < 8; ++i) *(LAS u4*)(L + ex + (i >> 2) * 8192 + (i & 1) * 2048 + ((i >> 1) & 1) * 1024) = ow[i];
    lbar();
    ST(6);
    if (!NODRAIN) drain();
    __builtin_amdgcn_raw_buffer_store_b64((u2){tag, ~tag}, ws, lane < (FW ? 16 : 1) ? WS_FB + ((tile * 2 + wt) * 16 + cg * 2 + wp) * 128 + lane * 8 : 0x80000000u, 0, SC1);
    ST(7);
    unsigned hb = WS_H + (tile * 8 + w * 2) * 32768 + lane * 16, hb2 = hb + 32768;
    asm volatile("" : "+v"(hb), "+v"(hb2));
    const Win wc[2] = {mkwin(ax[0]), mkwin(ax[1])};
    u4 hf[64];
    bf8 cf[2][4];
    f4 acc[2][4];
    auto ldh = [&](int g) {
      hf[2 * g] = __builtin_amdgcn_raw_buffer_load_b128(ws, hb + (g & 3) * 1024, sq[g >> 2] * 4096, PAY_AUX);
      hf[2 * g + 1] = __builtin_amdgcn_raw_buffer_load_b128(ws, hb2 + (g & 3) * 1024, sq[g >> 2] * 4096, PAY_AUX);
    };
    auto rdc = [&](int g) {
      const int tt = g >> 2;
#pragma unroll
      for (int nt = 0; nt < 4; ++nt)
        cf[g & 1][nt] = ldsr(at(L, wc[(g >> 1) & 1], (((tt >> 1) + 3) % 5) * SLOT + (tt & 1) * 16384 + (g & 1) * 8192 + nt * 2048));
    };
    auto mh = [&](int g) {
#pragma unroll
      for (int nt = 0; nt < 4; ++nt) {
        acc[0][nt] = mfma16(cf[g & 1][nt], asbf8(hf[2 * g]), acc[0][nt]);
        acc[1][nt] = mfma16(cf[g & 1][nt], asbf8(hf[2 * g + 1]), acc[1][nt]);
      }
    };
#pragma unroll
    for (int i = 0; i < 8; ++i) hf[i] = *(const LAS u4*)(L + 2 * SLOT + w * 8192 + i * 1024 + lane * 16);
    rdc(0);
    {
      u4 ida[2];
#pragma unroll
      for (int hf_ = 0; hf_ < 2; ++hf_)
#pragma unroll
        for (int jq = 0; jq < 4; ++jq) {
          const int k0 = 8 * q + 2 * jq - 16 * hf_;
          ida[hf_][jq] = (l15 == k0 ? 0x3f80u : 0u) | (l15 == k0 + 1 ? 0x3f800000u : 0u);
        }
#pragma unroll
      for (int i = 0; i < 8; ++i) acc[i >> 2][i & 3] = mfma16(asbf8(ida[i & 1]), asbf8(xd[i >> 2][(i >> 1) & 1]), zero4);
    }
    SB();
#pragma unroll
    for (int g = 0; g < 4; ++g) { rdc(g + 1); mh(g); ILV(4, 8); SB(); }
    unsigned tw[4], trd[4], so_ = ((tile * 128 + 64 * wt + 4 * (w & 1) + 8 * (q & 1) + (q >> 1)) * 1024 + 512 + cg * 64 + l15 * 4) * 4;
#pragma unroll
    for (int i = 0; i < 4; ++i) {
      tw[i] = 2 * SLOT + w * 8192 + l15 * 256 + (((4 * i + q) ^ l15) << 4);
      trd[i] = 2 * SLOT + w * 8192 + (4 * i + q) * 256 + ((l15 ^ (4 * i + q)) << 4);
      asm volatile("" : "+v"(tw[i]), "+v"(trd[i]));
    }
    asm volatile("s_barrier" : "+v"(so_) :: "memory");
    if (FD) __builtin_amdgcn_s_sleep(FD);
    const unsigned fo = WS_FB + ((tile * 2 + wt) * 16 + l15) * 128;
    unsigned have = 0;
    if (!PROG) wait_flags(ws, fo, tag);
    if (DLB) __builtin_amdgcn_s_sleep(DLB);
#pragma unroll
    for (int g = 4; g < 4 + HD / 2; ++g) { if (PROG && (g & 3) == 0) wait_pair(ws, fo, tag, have, sq[g >> 2]); ldh(g); }
    ST(8); CT(14);
    SB();
#pragma unroll
    for (int g = 4; g < 32; ++g) {
      if (PROG && g + HD / 2 < 32 && ((g + HD / 2) & 3) == 0) wait_pair(ws, fo, tag, have, sq[(g + HD / 2) >> 2]);
      if (g + HD / 2 < 32) ldh(g + HD / 2);
      if (g + 1 < 32) rdc(g + 1);
      mh(g);
#pragma unroll
      for (int i_ = 0; i_ < 4; ++i_) { SG(0x008, 1); SG(0x100, 1); }
      SG(0x008, 1); SG(0x020, 1); SG(0x008, 1); SG(0x020, 1); SG(0x008, 2);
      SB(); if (g < 30) PACE(HN);
    }
    ST(9); CT(15);
    if (DIRECT) {
      const unsigned sd = ((tile * 128 + 64 * wt + 4 * (w & 1) + 16 * (l15 >> 2) + 8 * (l15 & 1) + ((l15 >> 1) & 1)) * 1024 + 512 + cg * 64 + q * 4) * 4;
#pragma unroll
      for (int i = 0; i < 8; ++i) __builtin_amdgcn_raw_buffer_store_b128(__builtin_bit_cast(u4, acc[i >> 2][i & 3]), ro, sd + (i >> 2) * 8192 + (i & 3) * 64, 0, OUT_AUX);
      return;
    }
#pragma unroll
    for (int i = 0; i < 8; ++i) *(LAS f4*)(L + tw[i & 3] + (i >> 2) * 4096) = acc[i >> 2][i & 3];
    u4 ov[8];
#pragma unroll
    for (int i = 0; i < 8; ++i) ov[i] = *(const LAS u4*)(L + trd[i & 3] + (i >> 2) * 4096);
    SB();
#pragma unroll
    for (int i = 0; i < 8; ++i) __builtin_amdgcn_raw_buffer_store_b128(ov[i], ro, so_, (16 * (i & 3) + 2 * (i >> 2)) * 4096, OUT_AUX);
    ST(10);
  }
}

extern "C" void kernel_launch(void* const* d_in, const int* in_sizes, int n_in, void* d_out, int out_size, void* d_ws, size_t ws_size,
                              hipStream_t stream) {
  (void)hipFuncSetAttribute((const void*)fused_kernel, hipFuncAttributeMaxDynamicSharedMemorySize, LDS_BYTES);
  fused_kernel<<<256, 512, LDS_BYTES, stream>>>((const u16*)d_in[0], (const u16*)d_in[1], (const u16*)d_in[2], (const u16*)d_in[3],
                                               (const u16*)d_in[4], (const u16*)d_in[5], (const u16*)d_in[6], (const u16*)d_in[7],
                                               (const u16*)d_in[8], (const u16*)d_in[9], (const u16*)d_in[10], (const u16*)d_in[11],
                                               (float*)d_out, (char*)d_ws);
}
```

```cpp
#define DLA 0
#include <hip/hip_runtime.h>
#include <hip/hip_bf16.h>

#define DI __device__ __forceinline__
#define LAS __attribute__((address_space(3)))
#define GAS __attribute__((address_space(1)))
typedef unsigned short u16;
typedef unsigned long long u64;
typedef __bf16 bf8 __attribute__((ext_vector_type(8)));
typedef __bf16 bf2 __attribute__((ext_vector_type(2)));
typedef short s4 __attribute__((ext_vector_type(4)));
typedef short s8 __attribute__((ext_vector_type(8)));
typedef float f16v __attribute__((ext_vector_type(16)));
typedef float f4 __attribute__((ext_vector_type(4)));
typedef float f2 __attribute__((ext_vector_type(2)));
typedef unsigned u4 __attribute__((ext_vector_type(4)));
typedef unsigned u2 __attribute__((ext_vector_type(2)));

constexpr unsigned WS_EPOCH = 0;
constexpr unsigned WS_FA = 4096;
constexpr unsigned WS_FB = WS_FA + 65536;
constexpr unsigned WS_EST = WS_FB + 65536;
constexpr unsigned WS_Z = WS_EST + 131072;
constexpr unsigned WS_KV = WS_Z + 65536;
constexpr unsigned WS_O = 1u << 20;
constexpr unsigned WS_H = 3u << 20;
constexpr unsigned WS_END = 7u << 20;
constexpr unsigned WS_STAMP = 8u << 20;
constexpr int LDS_BYTES = 160 * 1024;
constexpr unsigned SPIN_CAP = 200000;
constexpr int SC1 = 16;

#define ROT 8
#define NC_EARLY 2
#define OUT_AUX 16
#define PAY_AUX 16
#define PULL 0
#define TOUCH 1
#define CPF 10
#define HROT 1
#define XNT 0
#define PD 14
#define FD 0
#define TD 0
#define NODRAIN 0
#define WOX 2
#define DLB 0
#define BN 8
#define AN 12
#define QN 0
#define HN 0
#define PRI 2
#define PROG 0
#define DIRECT 0
#define FW 0
#define GS 10
#define OD 6
#define HD 12

#define CT(i) do {} while (0)
#define ST(i) do {} while (0)
#define HFLUSH() do {} while (0)
#define SB() __builtin_amdgcn_sched_barrier(0)
#define PACE(n) do { if (n) { asm volatile("s_nop %0" :: "i"((n) > 16 ? 15 : ((n) ? (n) - 1 : 0))); if ((n) > 16) asm volatile("s_nop %0" :: "i"((n) > 16 ? (n) - 17 : 0)); SB(); } } while (0)
#define SG(m, n) __builtin_amdgcn_sched_group_barrier(m, n, 0)

DI unsigned pk2(float a, float b) { f2 v = {a, b}; return __builtin_bit_cast(unsigned, __builtin_convertvector(v, bf2)); }
DI unsigned pk2(f2 v) { return __builtin_bit_cast(unsigned, __builtin_convertvector(v, bf2)); }
DI float bflo(unsigned v) { return __uint_as_float(v << 16); }
DI float bfhi(unsigned v) { return __uint_as_float(v & 0xffff0000u); }
DI bf8 asbf8(u4 v) { return __builtin_bit_cast(bf8, v); }
DI f16v mfma32(bf8 a, bf8 b, f16v c) { return __builtin_amdgcn_mfma_f32_32x32x16_bf16(a, b, c, 0, 0, 0); }
DI f4 mfma16(bf8 a, bf8 b, f4 c) { return __builtin_amdgcn_mfma_f32_16x16x32_bf16(a, b, c, 0, 0, 0); }
DI void mfa(f4& c, bf8 a, bf8 b) { asm volatile("v_mfma_f32_16x16x32_bf16 %0, %1, %2, %0" : "+v"(c) : "v"(a), "v"(b)); }
DI f4 mfma16k16(u2 a, u2 b, f4 c) { return __builtin_amdgcn_mfma_f32_16x16x16bf16_1k(__builtin_bit_cast(s4, a), __builtin_bit_cast(s4, b), c, 0, 0, 0); }
DI bf8 ldsr(const char LAS* p) { return *(const LAS bf8*)p; }
DI bf8 ldstr(const char LAS* p0, const char LAS* p1) {
  s4 a = __builtin_amdgcn_ds_read_tr16_b64_v4i16((LAS s4*)p0), b = __builtin_amdgcn_ds_read_tr16_b64_v4i16((LAS s4*)p1);
  return __builtin_bit_cast(bf8, (s8)__builtin_shufflevector(a, b, 0, 1, 2, 3, 4, 5, 6, 7));
}
DI void dma4(unsigned lds, const GAS char* sb, unsigned v0, unsigned v1, unsigned v2, unsigned v3) {
  asm volatile("s_mov_b32 m0, %0\n\ts_nop 0\n\tglobal_load_lds_dwordx4 %1, %5\n\tglobal_load_lds_dwordx4 %2, %5 offset:1024\n\t"
               "global_load_lds_dwordx4 %3, %5 offset:2048\n\tglobal_load_lds_dwordx4 %4, %5 offset:3072"
               :: "s"(lds), "v"(v0), "v"(v1), "v"(v2), "v"(v3), "s"(sb) : "memory");
}
DI void dma4nt(unsigned lds, const GAS char* sb, unsigned v0, unsigned v1, unsigned v2, unsigned v3) {
  asm volatile("s_mov_b32 m0, %0\n\ts_nop 0\n\tglobal_load_lds_dwordx4 %1, %5 nt\n\tglobal_load_lds_dwordx4 %2, %5 offset:1024 nt\n\t"
               "global_load_lds_dwordx4 %3, %5 offset:2048 nt\n\tglobal_load_lds_dwordx4 %4, %5 offset:3072 nt"
               :: "s"(lds), "v"(v0), "v"(v1), "v"(v2), "v"(v3), "s"(sb) : "memory");
}
DI void dmax(unsigned lds, const GAS char* sb, unsigned v0, unsigned v1, unsigned v2, unsigned v3) { if (XNT) dma4nt(lds, sb, v0, v1, v2, v3); else dma4(lds, sb, v0, v1, v2, v3); }
DI void dma2(unsigned lds, const GAS char* sb, unsigned v0, unsigned v1) {
  asm volatile("s_mov_b32 m0, %0\n\ts_nop 0\n\tglobal_load_lds_dwordx4 %1, %3\n\tglobal_load_lds_dwordx4 %2, %3 offset:1024"
               :: "s"(lds), "v"(v0), "v"(v1), "s"(sb) : "memory");
}
struct Win { unsigned b[3]; };
DI Win mkwin(unsigned a) { Win r = {{a, a + 65536, a + 131072}}; asm volatile("" : "+v"(r.b[0]), "+v"(r.b[1]), "+v"(r.b[2])); return r; }
DI const char LAS* at(const char LAS* L, const Win& a, unsigned off) { return L + a.b[off >> 16] + (off & 65535); }
DI void lbar() { asm volatile("s_waitcnt lgkmcnt(0)\n\ts_barrier" ::: "memory"); }
template <int N> DI void wait_bar() { asm volatile("s_waitcnt vmcnt(%0) lgkmcnt(0)\n\ts_barrier" :: "i"(N) : "memory"); }
DI void drain() { asm volatile("s_waitcnt vmcnt(0)" ::: "memory"); }
DI unsigned tagv(float x, unsigned t) { return (__float_as_uint(x) & 0xffffff00u) | t; }
DI f2 cmul(f2 a, f2 b) { return (f2){a.x * b.x - a.y * b.y, a.x * b.y + a.y * b.x}; }
DI f2 cfma(f2 a, f2 b, f2 c) { return (f2){fmaf(a.x, b.x, fmaf(-a.y, b.y, c.x)), fmaf(a.x, b.y, fmaf(a.y, b.x, c.y))}; }
DI f2 fma2(f2 a, f2 b, f2 c) { return __builtin_elementwise_fma(a, b, c); }
constexpr float EPS = 1e-6f, LOG2E = 1.44269504088896341f;
DI unsigned phi2(float s0, float s1) {
  const f2 a = fma2((f2){s0, s1}, (f2){LOG2E, LOG2E}, (f2){-EPS * LOG2E, -EPS * LOG2E});
  const f2 e = {__builtin_amdgcn_fmed3f(__builtin_amdgcn_exp2f(a.x), 0.f, 1.f), __builtin_amdgcn_fmed3f(__builtin_amdgcn_exp2f(a.y), 0.f, 1.f)};
  return pk2(e + (f2){__builtin_amdgcn_fmed3f(s0, EPS, 3.40282347e38f), __builtin_amdgcn_fmed3f(s1, EPS, 3.40282347e38f)});
}
DI f2 swap_lo(f2 v, f2& hi) {
  const auto x = __builtin_amdgcn_permlane32_swap(__float_as_uint(v.x), __float_as_uint(v.x), false, false);
  const auto y = __builtin_amdgcn_permlane32_swap(__float_as_uint(v.y), __float_as_uint(v.y), false, false);
  hi = (f2){__uint_as_float(x[1]), __uint_as_float(y[1])};
  return (f2){__uint_as_float(x[0]), __uint_as_float(y[0])};
}
DI void wait_pair(__amdgpu_buffer_rsrc_t ws, unsigned off, unsigned tag, unsigned& have, int p) {
  if (__builtin_expect(((have >> (2 * p)) & 3u) != 3u, 0)) {
#pragma unroll 1
    for (unsigned sp = 0; sp < SPIN_CAP; ++sp) {
      asm volatile("" ::: "memory");
      const u2 f = __builtin_amdgcn_raw_buffer_load_b64(ws, off, 0, SC1);
      have = (unsigned)__ballot(f.x == tag && f.y == ~tag);
      if (((have >> (2 * p)) & 3u) == 3u) break;
    }
    asm volatile("" ::: "memory");
  }
}
DI f2 swap16(f2 a, f2 b, f2& rb) {
  const auto x = __builtin_amdgcn_permlane16_swap(__float_as_uint(a.x), __float_as_uint(b.x), false, false);
  const auto y = __builtin_amdgcn_permlane16_swap(__float_as_uint(a.y), __float_as_uint(b.y), false, false);
  rb = (f2){__uint_as_float(x[1]), __uint_as_float(y[1])};
  return (f2){__uint_as_float(x[0]), __uint_as_float(y[0])};
}
DI void wait_flags(__amdgpu_buffer_rsrc_t ws, unsigned off, unsigned tag) {
#pragma unroll 1
  for (unsigned sp = 0; sp < SPIN_CAP; ++sp) {
      asm volatile("" ::: "memory");
    const u2 f = __builtin_amdgcn_raw_buffer_load_b64(ws, off, 0, SC1);
    if (__all(f.x == tag && f.y == ~tag)) break;
    __builtin_amdgcn_s_sleep(1);
  }
  asm volatile("" ::: "memory");
}

__global__ void __launch_bounds__(512, 2)
fused_kernel(const u16* a0, const u16* a1, const u16* a2, const u16* a3, const u16* a4, const u16* a5, const u16* a6, const u16* a7,
             const u16* a8, const u16* a9, const u16* a10, const u16* a11, float* a12, char* a13) {
  extern __shared__ __attribute__((aligned(16))) char smem_[];
  char LAS* L = (char LAS*)smem_;
  const unsigned L0 = (unsigned)(size_t)L;
  ST(0);
  const int lane = threadIdx.x & 63;
  const int w8 = __builtin_amdgcn_readfirstlane(threadIdx.x >> 6), w = w8 & 3;
  unsigned cpf = 0;
  if (CPF && w8 == 0) {
    u64 pc;
    asm volatile("s_getpc_b64 %0" : "=s"(pc));
    cpf = __builtin_amdgcn_raw_buffer_load_b32(__builtin_amdgcn_make_buffer_rsrc((char*)(pc & ~127ull), 0, CPF * 4096, 0x00020000),
                                               lane < CPF ? ((blockIdx.x >> 3) * CPF + lane) * 128 : 0x80000000u, 0, 0);
  }
  typedef u64 u64x8 __attribute__((ext_vector_type(8)));
  typedef u64 u64x4 __attribute__((ext_vector_type(4)));
  typedef u64 u64x2 __attribute__((ext_vector_type(2)));
  u64x8 ka; u64x4 kb; u64x2 kc_;
  asm volatile("s_load_dwordx16 %0, %3, 0x0\n\ts_load_dwordx8 %1, %3, 0x40\n\ts_load_dwordx4 %2, %3, 0x60\n\ts_waitcnt lgkmcnt(0)"
               : "=&s"(ka), "=&s"(kb), "=&s"(kc_) : "s"(__builtin_amdgcn_kernarg_segment_ptr()) : "memory");
  const GAS u16 *xa = (const GAS u16*)ka[0], *wqkv = (const GAS u16*)ka[1], *wo = (const GAS u16*)ka[2], *xb = (const GAS u16*)ka[3];
  const GAS u16 *lre = (const GAS u16*)ka[4], *lim = (const GAS u16*)ka[5], *bre = (const GAS u16*)ka[6], *bim = (const GAS u16*)ka[7];
  const GAS u16 *cre = (const GAS u16*)kb[0], *cim = (const GAS u16*)kb[1], *ldt = (const GAS u16*)kb[2], *dsk = (const GAS u16*)kb[3];
  float* out = (float*)kc_[0]; char* wsp = (char*)kc_[1];
  const bool helper = w8 >= 4;
  const int bid = blockIdx.x, xcd = bid & 7, jj = bid >> 3;
  const int tile = (xcd >> 1) * 4 + (jj >> 3), cg = jj & 7;
  const int rot = ROT == 1 ? ((cg + 3 * (jj >> 3) + xcd) & 7) : ROT == 7 ? (((cg & 3) * 2 + (xcd >> 1)) & 7) : ROT == 12 ? ((2 * (cg & 3) + 2 * (xcd >> 1)) & 7) : ROT == 16 ? ((2 * (cg & 3) + ((jj >> 3) & 1) + 2 * (xcd >> 1)) & 7) : (((int)(030630630630ull >> (3 * (cg + (jj >> 3)))) + (xcd >> 1)) & 7);
  auto seq = [&](int t) { const int b = 2 * (jj >> 3), e = t - 1; return t == 0 ? cg : (b + e + (e >= ((cg - b) & 7) ? 1 : 0)) & 7; };
#define MKSQ() int sq[8]; _Pragma("unroll") for (int t_ = 1; t_ < 8; ++t_) { sq[t_] = __builtin_amdgcn_readfirstlane(seq(t_)); asm volatile("" : "+s"(sq[t_])); }
  const __amdgpu_buffer_rsrc_t ws = __builtin_amdgcn_make_buffer_rsrc(wsp, 0, WS_END, 0x00020000);
  const __amdgpu_buffer_rsrc_t ro = __builtin_amdgcn_make_buffer_rsrc(out, 0, 2048 * 1024 * 4, 0x00020000);
  const int r8 = lane >> 3, c8 = lane & 7;
  const int q = lane >> 4, l15 = lane & 15, l31 = lane & 31, hh = lane >> 5;
  unsigned epoch;
#define LOAD_EPOCH() asm volatile("s_load_dword %0, %1, 0x0" : "=s"(epoch) : "s"(wsp) : "memory")

  if ((xcd & 1) == 0) {
    constexpr unsigned WA = 131072, WB = 147456, SCR = 32768, TRB = 81920;
    constexpr unsigned QBUF[4] = {WB, 0, WA, WB}, OBUF[4] = {32768, 49152, 65536, 0};
    constexpr unsigned WBUF[8] = {WA, WB, 7 * 16384, 0, WA, WB, 0, WA};
    if (helper) {
      const int head = cg * 4 + w;
      unsigned vx[4], vw[2], vo[4];
#pragma unroll
      for (int i = 0; i < 4; ++i) vx[i] = (32 * w + 8 * i + r8) * 1024 + ((c8 ^ ((4 * i + (r8 >> 1)) & 7)) << 4) - 1024 * i;
#pragma unroll
      for (int i = 0; i < 2; ++i) vw[i] = (16 * w + 8 * i + r8) * 3072 + ((c8 ^ ((((r8 >> 1) & 1) | (i << 1)) << 1)) << 4) - 1024 * i;
      const GAS char* xbase = (const GAS char*)xa + (size_t)tile * 131072;
      const GAS char* wbase = (const GAS char*)wqkv + cg * 128;
      auto isx = [&](int kk, int slot) { dmax(L0 + slot * 16384 + w * 4096, xbase + ((kk + rot) & 7) * 128, vx[0], vx[1], vx[2], vx[3]); };
      auto isw = [&](unsigned dst, int kk, int seg) { dma2(L0 + dst + w * 2048, wbase + ((kk + rot) & 7) * (64 * 3072) + seg * 1024, vw[0], vw[1]); };
      auto issue = [&](int kk) { isx(kk, kk); isw(WBUF[kk], kk, 1); isw(WBUF[kk] + 8192, kk, 2); };
      auto isq = [&](int p) { isw(QBUF[p], 2 * p + 1, 0); isw(QBUF[p] + 8192, 2 * p + 2, 0); };
      ST(9); issue(0); ST(10); issue(1); ST(11);
      LOAD_EPOCH();
#pragma unroll
      for (int i = 0; i < 4; ++i) vo[i] = (8 * i + r8) * 1024 + ((c8 ^ (((r8 >> 1) & 3) << 1)) << 4) - 1024 * i;
      const GAS char* obase = (const GAS char*)wo + cg * 128;
      asm volatile("s_waitcnt vmcnt(8) lgkmcnt(0)\n\ts_barrier" : "+s"(epoch) :: "memory");
      issue(2);
      wait_bar<8>(); issue(3);
      wait_bar<8>(); issue(4);
      wait_bar<8>(); issue(5);
      wait_bar<8>(); issue(6);
      wait_bar<8>(); issue(7);
      wait_bar<8>();
      unsigned pfo;
      asm volatile("global_load_dword %0, %1, %2" : "=v"(pfo) : "v"(((jj >> 3) * 128 + w * 32 + l31) * 1024), "s"(obase) : "memory");
      isq(0);
      wait_bar<5>(); isq(1);
      wait_bar<4>(); isq(2);
      wait_bar<4>(); isq(3); isx(0, 1);
      asm volatile("s_waitcnt vmcnt(8)\n\ts_barrier\n\ts_waitcnt vmcnt(0)" : "+v"(pfo) :: "memory");
      if (PRI & 1) __builtin_amdgcn_s_setprio(3);
      ST(1);
      const unsigned tag = epoch + 1, t = tag & 0xffu;
      const GAS char* kvb_ = (const GAS char*)wsp + WS_KV + head * 16384;
      const GAS char* zb_ = (const GAS char*)wsp + WS_Z + head * 2048;
      const unsigned qr = (jj >> 3) * 4096;
      const unsigned vk0 = lane * 16 + qr, vk1 = lane * 16 + ((qr + 4096) & 12288), vz = l15 * 128 + q * 16;
      u4 g[8], gz;
#define KVL(i, v) asm volatile("global_load_dwordx4 %0, %1, %2 offset:%3 sc1" : "=v"(g[i]) : "v"(v), "s"(kvb_), "i"((i & 3) * 1024) : "memory")
      asm volatile("global_load_dwordx4 %0, %1, %2 sc1" : "=v"(gz) : "v"(vz), "s"(zb_) : "memory");
      KVL(0, vk0); KVL(1, vk0); KVL(2, vk0); KVL(3, vk0); KVL(4, vk1); KVL(5, vk1); KVL(6, vk1); KVL(7, vk1);
      asm volatile("s_barrier" ::: "memory");
      auto zsum = [&]() {
        float z0 = __uint_as_float(gz.x), z1 = __uint_as_float(gz.y), z2 = __uint_as_float(gz.z), z3 = __uint_as_float(gz.w);
#define ZS(r) "v_add_f32_dpp %0, %0, %0 row_ror:" #r " row_mask:0xf bank_mask:0xf\n\tv_add_f32_dpp %1, %1, %1 row_ror:" #r " row_mask:0xf bank_mask:0xf\n\t" \
              "v_add_f32_dpp %2, %2, %2 row_ror:" #r " row_mask:0xf bank_mask:0xf\n\tv_add_f32_dpp %3, %3, %3 row_ror:" #r " row_mask:0xf bank_mask:0xf\n\t"
        asm volatile("s_nop 1\n\t" ZS(8) ZS(4) ZS(2) ZS(1) "s_nop 0" : "+v"(z0), "+v"(z1), "+v"(z2), "+v"(z3));
        return (f4){z0, z1, z2, z3};
      };
      asm volatile("s_waitcnt vmcnt(8)" : "+v"(gz) :: "memory");
      gz.x ^= t; gz.z ^= t;
      unsigned bad = gz.x | gz.z;
      f4 zz = zsum(), kv = {0, 0, 0, 0};
      asm volatile("" : "+v"(zz));
#define KVA(i) asm volatile("s_waitcnt vmcnt(%1)" : "+v"(g[i]) : "i"(7 - i) : "memory"); g[i].x ^= t; g[i].z ^= t; bad |= g[i].x | g[i].z; kv += __builtin_bit_cast(f4, g[i]); asm volatile("" : "+v"(kv), "+v"(bad))
      KVA(0); KVA(1); KVA(2); KVA(3); KVA(4); KVA(5); KVA(6); KVA(7);
      ST(3);
      if (!__all((bad & 0xffu) == 0)) {
#pragma unroll 1
        for (unsigned rep = 0; rep < SPIN_CAP; ++rep) {
          asm volatile("" ::: "memory");
          gz = __builtin_amdgcn_raw_buffer_load_b128(ws, WS_Z + head * 2048 + vz, 0, SC1);
#pragma unroll
          for (int i = 0; i < 8; ++i) g[i] = __builtin_amdgcn_raw_buffer_load_b128(ws, WS_KV + head * 16384 + (i < 4 ? vk0 : vk1) + (i & 3) * 1024, 0, SC1);
          gz.x ^= t; gz.z ^= t;
          bad = gz.x | gz.z;
#pragma unroll
          for (int i = 0; i < 8; ++i) { g[i].x ^= t; g[i].z ^= t; bad |= g[i].x | g[i].z; }
          if (__all((bad & 0xffu) == 0)) break;
        }
        zz = zsum();
        kv = (f4){0, 0, 0, 0};
#pragma unroll
        for (int i = 0; i < 8; ++i) kv += __builtin_bit_cast(f4, g[i]);
      }
      ST(5);
      *(LAS f4*)(L + SCR + w * 1024 + lane * 16) = kv;
      *(LAS u2*)(L + SCR + 8192 + w * 512 + lane * 8) = (u2){pk2(zz[0], zz[1]), pk2(zz[2], zz[3])};
      ST(6);
      lbar();
      ST(7);
      lbar();
#pragma unroll
      for (int i = 0; i < 4; ++i)
        dma4(L0 + OBUF[i] + w * 4096, obase + (2 * seq(2 * i + (w >> 1)) + (w & 1)) * 32768, vo[0], vo[1], vo[2], vo[3]);
      wait_bar<0>();
      ST(8);
      HFLUSH();
      if (bid == 0 && w == 0) {
#pragma unroll 1
        for (unsigned sp = 0; sp < SPIN_CAP; ++sp) {
          asm volatile("" ::: "memory");
          const unsigned fo = (lane >> 3) * 4096 + (lane & 7) * 256;
          const u2 f0 = __builtin_amdgcn_raw_buffer_load_b64(ws, WS_FA + fo, 0, SC1), f1 = __builtin_amdgcn_raw_buffer_load_b64(ws, WS_FA + 32768 + fo, 0, SC1);
          const u2 f2_ = __builtin_amdgcn_raw_buffer_load_b64(ws, WS_FB + fo, 0, SC1), f3 = __builtin_amdgcn_raw_buffer_load_b64(ws, WS_FB + 32768 + fo, 0, SC1);
          if (__all(f0.x == tag && f0.y == ~tag && f1.x == tag && f1.y == ~tag && f2_.x == tag && f2_.y == ~tag && f3.x == tag && f3.y == ~tag)) break;
          __builtin_amdgcn_s_sleep(8);
        }
        if (lane == 0) __builtin_amdgcn_raw_buffer_store_b32(tag, ws, WS_EPOCH, 0, SC1);
      }
      return;
    }
    LOAD_EPOCH();
    const int hp = w & 1, th = w >> 1;
    unsigned warm0 = 0, warm1 = 0;
    if (TOUCH) {
      if (TD) __builtin_amdgcn_s_sleep(TD);
      warm0 = __builtin_amdgcn_raw_buffer_load_b32(ws, (!FW && lane < 16) ? WS_FA + ((tile * 2 + th) * 16 + lane) * 128 : (!FW && lane < 32) ? WS_Z + (cg * 4 + w) * 2048 + (lane - 16) * 128
                                                   : lane == 32 ? WS_END - 4096 * 256 * 6 - 128 : lane == 33 ? WS_O + tile * 131072 : 0x80000000u, 0, SC1);
      warm1 = __builtin_amdgcn_raw_buffer_load_b32(ro, lane < 2 ? (tile * 128 + w * 32 + lane * 16) * 4096 : 0x80000000u, 0, SC1);
    }
    const int qq = (lane >> 2) & 3, pp = lane & 3;
    const unsigned ax0 = l15 * 128 + ((q ^ (l15 >> 1)) << 4), ax1 = l15 * 128 + (((4 + q) ^ (l15 >> 1)) << 4);
    const Win wx[2] = {mkwin(ax0), mkwin(ax1)}, wq[2] = {mkwin(ax0 + th * 8192), mkwin(ax1 + th * 8192)};
    auto waddr = [&](int h) { return (8 * q + qq) * 128 + (((2 * h + (pp >> 1)) ^ ((((qq >> 1) & 1) | ((q & 1) << 1)) << 1)) << 4) + 8 * (pp & 1); };
    const Win aw = mkwin(waddr(w)), aq0 = mkwin(waddr(2 * hp)), aq1 = mkwin(waddr(2 * hp + 1));
    MKSQ();
    unsigned ao[4], og[4], tw[4], trd[4];
#pragma unroll
    for (int i = 0; i < 4; ++i) {
      og[i] = WS_O + ((tile * 8 + th * 4 + i) * 16 + cg * 2 + hp) * 1024 + lane * 16;
      ao[i] = (4 * q + qq) * 128 + ((((2 * i) ^ (((2 * q + (qq >> 1)) & 3) << 1)) + (pp >> 1)) << 4) + 8 * (pp & 1);
      tw[i] = TRB + w * 8192 + l15 * 256 + (((4 * i + q) ^ l15) << 4);
      trd[i] = TRB + w * 8192 + (4 * i + q) * 256 + ((l15 ^ (4 * i + q)) << 4);
      asm volatile("" : "+v"(og[i]), "+v"(ao[i]), "+v"(tw[i]), "+v"(trd[i]));
    }
    unsigned so_ = ((tile * 128 + w * 32 + q) * 1024 + cg * 64 + l15 * 4) * 4, ex = TRB + th * 16384 + hp * 1024 + lane * 16;
    unsigned sk = SCR + hp * 2048 + lane * 16, sz_ = SCR + 8192 + hp * 1024 + lane * 8, sw_ = SCR + 4096 + w * 1024 + lane * 16;
    unsigned pk_ = WS_KV + ((cg * 4 + w) * 16 + tile) * 1024 + lane * 16, pz_ = l15 < (FW ? 2 : 1) ? WS_Z + ((cg * 4 + w) * 16 + tile) * 128 + l15 * 64 + q * 16 : 0x80000000u;
    unsigned pf_ = lane < (FW ? 16 : 1) ? WS_FA + ((tile * 2 + th) * 16 + cg * 2 + hp) * 128 + lane * 8 : 0x80000000u;
    asm volatile("" : "+v"(so_), "+v"(ex), "+v"(sk), "+v"(sz_), "+v"(sw_), "+v"(pk_), "+v"(pz_), "+v"(pf_));
    f4 ka[8], va[8];
#pragma unroll
    for (int i = 0; i < 8; ++i) { ka[i] = (f4){EPS, EPS, EPS, EPS}; va[i] = (f4){0, 0, 0, 0}; }
    bf8 X0[4], X1[4], W0[2], W1[2];
    auto rdx = [&](bf8 (&X)[4], int kk, int ks, unsigned off) {
#pragma unroll
      for (int i = 0; i < 4; ++i) X[i] = ldsr(at(L, wx[ks], kk * 16384 + off + i * 2048));
    };
    auto rdw = [&](bf8 (&W)[2], int kk, int ks) {
#pragma unroll
      for (int i = 0; i < 2; ++i) { const unsigned o = WBUF[kk] + ks * 4096 + i * 8192; W[i] = ldstr(at(L, aw, o), at(L, aw, o + 512)); }
    };
    auto mm = [&](const bf8 (&X)[4], const bf8 (&W)[2], int h) {
#pragma unroll
      for (int i = 0; i < 4; ++i) {
        ka[4 * h + i] = mfma16(X[i], W[0], ka[4 * h + i]);
        va[4 * h + i] = mfma16(X[i], W[1], va[4 * h + i]);
      }
    };
#define ILV(nr, nm) do { _Pragma("unroll") for (int i_ = 0; i_ < nr; ++i_) { SG(0x008, 1); SG(0x100, 1); } if (nm > nr) SG(0x008, nm - nr); } while (0)
    asm volatile("s_waitcnt lgkmcnt(0)\n\ts_barrier" : "+s"(epoch) : "v"(warm0), "v"(warm1), "v"(cpf) : "memory");
    ST(1);
    const unsigned tag = epoch + 1, t = tag & 0xffu;
    rdw(W0, 0, 0); rdx(X0, 0, 0, 0);
    SB();
#pragma unroll
    for (int kk = 0; kk < 8; ++kk) {
      rdx(X1, kk, 0, 8192); mm(X0, W0, 0); ILV(4, 8); SB(); PACE(AN);
      rdw(W1, kk, 1); rdx(X0, kk, 1, 0); mm(X1, W0, 1); ILV(8, 8); SB(); PACE(AN);
      rdx(X1, kk, 1, 8192); mm(X0, W1, 0); ILV(4, 8); SB();
      if (kk < 7) { lbar(); rdw(W0, kk + 1, 0); rdx(X0, kk + 1, 0, 0); mm(X1, W1, 1); ILV(8, 8); }
      else mm(X1, W1, 1);
      SB();
    }
    ST(2);
    bf8 XQ[3][4];
    s4 WH[3][4];
    auto rdq = [&](int s, int i) {
      const unsigned o = QBUF[s >> 2] + ((s >> 1) & 1) * 8192 + (s & 1) * 4096;
      if (i < 4) WH[s % 3][i] = __builtin_amdgcn_ds_read_tr16_b64_v4i16((LAS s4*)at(L, i < 2 ? aq0 : aq1, o + (i & 1) * 512));
      else XQ[s % 3][i - 4] = ldsr(at(L, wq[s & 1], (s < 14 ? (s >> 1) + 1 : 1) * 16384 + (i - 4) * 2048));
    };
    {
      f4 kv = {0, 0, 0, 0}, zz = {0, 0, 0, 0};
      const u4 ones = {0x3f803f80u, 0x3f803f80u, 0x3f803f80u, 0x3f803f80u};
#pragma unroll
      for (int m = 0; m < 4; ++m) {
        const f4 a = ka[2 * m], b = ka[2 * m + 1], c = va[2 * m], d = va[2 * m + 1];
        const u4 pa = {phi2(a[0], a[1]), phi2(a[2], a[3]), phi2(b[0], b[1]), phi2(b[2], b[3])};
        const u4 pb = {pk2(c[0], c[1]), pk2(c[2], c[3]), pk2(d[0], d[1]), pk2(d[2], d[3])};
        kv = mfma16(asbf8(pa), asbf8(pb), kv);
        zz = mfma16(asbf8(pa), asbf8(ones), zz);
      }
      const u4 skv = {tagv(kv[0], t), __float_as_uint(kv[1]), tagv(kv[2], t), __float_as_uint(kv[3])};
      __builtin_amdgcn_raw_buffer_store_b128(skv, ws, pk_, 0, SC1);
      const u4 sz = {tagv(zz[0], t), __float_as_uint(zz[1]), tagv(zz[2], t), __float_as_uint(zz[3])};
      __builtin_amdgcn_raw_buffer_store_b128(sz, ws, pz_, 0, SC1);
    }
    SB();
    ST(3);
    lbar();
#pragma unroll
    for (int i = 0; i < 16; ++i) rdq(i >> 3, i & 7);
    SB();
    f4 qa[2][4];
#pragma unroll
    for (int i = 0; i < 8; ++i) qa[i >> 2][i & 3] = (f4){EPS, EPS, EPS, EPS};
    u4 gc[8];
    const unsigned gb = WS_KV + (cg * 4 + w) * 16384 + lane * 16, g2 = (((jj >> 3) + 2) & 3) * 4096, g3 = (((jj >> 3) + 3) & 3) * 4096;
    auto ldg = [&](int i) { gc[i] = __builtin_amdgcn_raw_buffer_load_b128(ws, gb + (i & 3) * 1024, i < 4 ? g2 : g3, SC1); };
    asm volatile("s_nop 1" : "+v"(qa[0][0]), "+v"(qa[0][1]), "+v"(qa[0][2]), "+v"(qa[0][3]), "+v"(qa[1][0]), "+v"(qa[1][1]), "+v"(qa[1][2]), "+v"(qa[1][3]));
#pragma unroll
    for (int s = 0; s < 16; ++s) {
      if (s == 2) asm volatile("s_waitcnt lgkmcnt(4)\n\ts_barrier" ::: "memory");
      if (s == 6 || s == 10) asm volatile("s_barrier" ::: "memory");
      bf8 W[2] = {__builtin_bit_cast(bf8, (s8)__builtin_shufflevector(WH[s % 3][0], WH[s % 3][1], 0, 1, 2, 3, 4, 5, 6, 7)),
                  __builtin_bit_cast(bf8, (s8)__builtin_shufflevector(WH[s % 3][2], WH[s % 3][3], 0, 1, 2, 3, 4, 5, 6, 7))};
      asm volatile("" : "+v"(W[0]), "+v"(W[1]), "+v"(XQ[s % 3][0]), "+v"(XQ[s % 3][1]), "+v"(XQ[s % 3][2]), "+v"(XQ[s % 3][3]));
#pragma unroll
      for (int i = 0; i < 8; ++i) {
        mfa(qa[i & 1][i >> 1], W[i & 1], XQ[s % 3][i >> 1]); SB();
        if (s < 14) { rdq(s + 2, i); SB(); }
        if (s >= GS && s < GS + 4 && (i == 2 || i == 6)) { ldg(2 * (s - GS) + (i >> 2)); SB(); }
      }
    }
    asm volatile("s_nop 7" : "+v"(qa[0][0]), "+v"(qa[0][1]), "+v"(qa[0][2]), "+v"(qa[0][3]), "+v"(qa[1][0]), "+v"(qa[1][1]), "+v"(qa[1][2]), "+v"(qa[1][3]));
    ST(4);
    u2 pq[2][4];
#pragma unroll
    for (int i = 0; i < 8; ++i) {
      pq[i >> 2][i & 3] = (u2){phi2(qa[i >> 2][i & 3][0], qa[i >> 2][i & 3][1]), phi2(qa[i >> 2][i & 3][2], qa[i >> 2][i & 3][3])};
      asm volatile("" : "+v"(pq[i >> 2][i & 3]));
    }
    SB();
    {
      unsigned bad = 0;
      f4 kvp = {0, 0, 0, 0};
#pragma unroll
      for (int i = 0; i < 8; ++i) { gc[i].x ^= t; gc[i].z ^= t; bad |= gc[i].x | gc[i].z; kvp += __builtin_bit_cast(f4, gc[i]); }
      if (__builtin_expect(!__all((bad & 0xffu) == 0), 0)) {
#pragma unroll 1
        for (unsigned rep = 0; rep < SPIN_CAP; ++rep) {
          asm volatile("" ::: "memory");
          bad = 0;
          kvp = (f4){0, 0, 0, 0};
#pragma unroll
          for (int i = 0; i < 8; ++i) ldg(i);
#pragma unroll
          for (int i = 0; i < 8; ++i) { gc[i].x ^= t; gc[i].z ^= t; bad |= gc[i].x | gc[i].z; kvp += __builtin_bit_cast(f4, gc[i]); }
          asm volatile("" : "+v"(kvp));
          if (__all((bad & 0xffu) == 0)) break;
        }
      }
      *(LAS f4*)(L + sw_) = kvp;
    }
    lbar();
    ST(5);
    const f4 zero4 = {0, 0, 0, 0}, eps4 = {EPS, EPS, EPS, EPS};
    f4 num[2][4], den[2][4];
    const f4 ka0 = *(const LAS f4*)(L + sk), kb0 = *(const LAS f4*)(L + sk + 4096), ka1 = *(const LAS f4*)(L + sk + 1024), kb1 = *(const LAS f4*)(L + sk + 5120);
    const u2 zb[2] = {*(const LAS u2*)(L + sz_), *(const LAS u2*)(L + sz_ + 512)};
    SB();
    const f4 k0 = ka0 + kb0, k1 = ka1 + kb1;
    const u2 kb2[2] = {{pk2(k0[0], k0[1]), pk2(k0[2], k0[3])}, {pk2(k1[0], k1[1]), pk2(k1[2], k1[3])}};
#pragma unroll
    for (int i = 0; i < 8; ++i) { den[i & 1][i >> 1] = mfma16k16(zb[i & 1], pq[i & 1][i >> 1], eps4); num[i & 1][i >> 1] = mfma16k16(kb2[i & 1], pq[i & 1][i >> 1], zero4); }
    SB();
#pragma unroll
    for (int i = 0; i < 4; ++i) {
      u4 so;
#pragma unroll
      for (int h = 0; h < 2; ++h) {
        const float rc = __builtin_amdgcn_rcpf(den[h][i][0]);
        const f4 n = num[h][i];
        so[2 * h] = pk2((f2){n[0], n[1]} * rc);
        so[2 * h + 1] = pk2((f2){n[2], n[3]} * rc);
      }
      __builtin_amdgcn_raw_buffer_store_b128(so, ws, og[i], 0, SC1);
      *(LAS u4*)(L + ex + (i >> 1) * 8192 + (i & 1) * 2048) = so;
    }
    lbar();
    ST(6);
    u4 of[32];
    unsigned ah[4];
#pragma unroll
    for (int i = 0; i < 4; ++i) { ah[i] = ao[i] + 65536; asm volatile("" : "+v"(ah[i])); }
#pragma unroll
    for (int i = 0; i < 4; ++i) of[i] = *(const LAS u4*)(L + TRB + w * 8192 + ((i & 1) * 2 + (i >> 1)) * 1024 + lane * 16);
    if (!NODRAIN) drain();
    __builtin_amdgcn_raw_buffer_store_b64((u2){tag, ~tag}, ws, pf_, 0, SC1);
    asm volatile("s_barrier" ::: "memory");
    ST(7);
    f4 acc[2][4];
#pragma unroll
    for (int i = 0; i < 8; ++i) acc[i >> 2][i & 3] = zero4;
    unsigned ob = WS_O + (tile * 8 + w * 2) * 16384 + lane * 16, ob2 = ob + 16384;
    asm volatile("" : "+v"(ob), "+v"(ob2));
    bf8 wf[2][4];
    auto rdo = [&](int k) {
#pragma unroll
      for (int nt = 0; nt < 4; ++nt) { const unsigned o = OBUF[k >> 2] + (k & 3) * 4096; const char LAS* p = L + (o < 65536 ? ao[nt] : ah[nt]) + (o & 65535); wf[k & 1][nt] = ldstr(p, p + 2048); }
    };
    auto ldo = [&](int k) {
      const unsigned so = sq[k >> 1] * 2048;
      of[2 * k] = __builtin_amdgcn_raw_buffer_load_b128(ws, ob + (k & 1) * 1024, so, PAY_AUX);
      of[2 * k + 1] = __builtin_amdgcn_raw_buffer_load_b128(ws, ob2 + (k & 1) * 1024, so, PAY_AUX);
    };
    auto mo = [&](int k) {
#pragma unroll
      for (int nt = 0; nt < 4; ++nt) {
        acc[0][nt] = mfma16(wf[k & 1][nt], asbf8(of[2 * k]), acc[0][nt]);
        acc[1][nt] = mfma16(wf[k & 1][nt], asbf8(of[2 * k + 1]), acc[1][nt]);
      }
    };
    rdo(0); rdo(1); SB();
    mo(0); rdo(2); SB();
    mo(1); SB();
    if (FD) __builtin_amdgcn_s_sleep(FD);
    const unsigned fo = WS_FA + ((tile * 2 + th) * 16 + l15) * 128;
    unsigned have = 0;
    if (!PROG) wait_flags(ws, fo, tag);
    if (DLA) __builtin_amdgcn_s_sleep(DLA);
#pragma unroll
    for (int k = 2; k < 2 + OD; ++k) { if (PROG && (k & 1) == 0) wait_pair(ws, fo, tag, have, sq[k >> 1]); ldo(k); }
    ST(8); CT(14);
    SB();
#pragma unroll
    for (int k = 2; k < 16; ++k) {
      if (PROG && k + OD < 16 && ((k + OD) & 1) == 0) wait_pair(ws, fo, tag, have, sq[(k + OD) >> 1]);
      if (k + OD < 16) ldo(k + OD);
      if (k + 1 < 16) rdo(k + 1);
      mo(k);
#pragma unroll
      for (int i_ = 0; i_ < 8; ++i_) { SG(0x008, 1); SG(0x100, 1); if (i_ == 1 || i_ == 5) SG(0x020, 1); }
      SB();
    }
    ST(9); CT(15);
    if (DIRECT) {
      const unsigned sd = ((tile * 128 + w * 32 + l15) * 1024 + cg * 64 + q * 4) * 4;
      u4 dvv[8];
#pragma unroll
      for (int i = 0; i < 8; ++i) {
        const f4 v = acc[i >> 2][i & 3];
        const unsigned p0 = pk2(v[0], v[1]), p1 = pk2(v[2], v[3]);
        dvv[i] = (u4){p0 << 16, p0 & 0xffff0000u, p1 << 16, p1 & 0xffff0000u};
        __builtin_amdgcn_raw_buffer_store_b128(dvv[i], ro, sd + (i >> 2) * 65536 + (i & 3) * 64, 0, OUT_AUX);
      }
      return;
    }
#pragma unroll
    for (int mt = 0; mt < 2; ++mt)
#pragma unroll
      for (int nt = 0; nt < 4; ++nt) {
        const f4 v = acc[mt][nt];
        const unsigned p0 = pk2(v[0], v[1]), p1 = pk2(v[2], v[3]);
        *(LAS f4*)(L + tw[nt] + mt * 4096) = (f4){bflo(p0), bfhi(p0), bflo(p1), bfhi(p1)};
      }
    u4 ov[8];
#pragma unroll
    for (int i = 0; i < 8; ++i) ov[i] = *(const LAS u4*)(L + trd[i & 3] + (i >> 2) * 4096);
    SB();
#pragma unroll
    for (int i = 0; i < 8; ++i) __builtin_amdgcn_raw_buffer_store_b128(ov[i], ro, so_, i * 16384, OUT_AUX);
    ST(10);
  } else {
    constexpr unsigned SLOT = 32768, SCR = SLOT, PAR = 4 * SLOT;
    const int wp = w & 1, wt = w >> 1;
    const unsigned ecol = WS_EST + (cg * 64 + wp * 32 + l31) * 8;
    if (helper) {
      unsigned vx[4], vw[4], vc[4];
#pragma unroll
      for (int i = 0; i < 4; ++i) {
        const unsigned sw = (c8 ^ ((4 * i + (r8 >> 1)) & 7)) << 4;
        vx[i] = (64 * (w >> 1) + 32 * (i & 1) + 16 * (r8 >> 2) + 8 * (r8 & 1) + 4 * (w & 1) + 2 * (i >> 1) + ((r8 >> 1) & 1)) * 1024 + sw - 1024 * i;
        vw[i] = (16 * (i & 1) + 8 * (r8 >> 2) + 4 * (i >> 1) + (r8 & 3)) * 1024 + sw - 1024 * i;
        vc[i] = (8 * i + r8) * 1024 + sw - 1024 * i;
      }
      const GAS char* xbase = (const GAS char*)xb + (size_t)tile * 131072;
      const GAS char* wbase = (const GAS char*)((w & 1) ? bim : bre) + (cg * 64 + (w >> 1) * 32) * 1024;
      auto issue = [&](int kk) {
        const int kc = (kk + rot) & 7;
        const unsigned s = L0 + (kk % 5) * SLOT + w * 4096;
        dmax(s, xbase + kc * 128, vx[0], vx[1], vx[2], vx[3]);
        dma4(s + 16384, wbase + kc * 128, vw[0], vw[1], vw[2], vw[3]);
      };
      ST(9); issue(0); ST(10); issue(1); ST(11);
      LOAD_EPOCH();
      const GAS char* cbase = (const GAS char*)((w & 1) ? cim : cre) + cg * 65536;
      auto issue_c = [&](int piece) {
        const unsigned s = L0 + ((piece + 3) % 5) * SLOT + w * 8192;
        const GAS char* cb = cbase + seq(2 * piece + (w >> 1)) * 128;
        dma4(s, cb, vc[0], vc[1], vc[2], vc[3]);
        dma4(s + 4096, cb + 32768, vc[0], vc[1], vc[2], vc[3]);
      };
      asm volatile("s_waitcnt vmcnt(8) lgkmcnt(0)\n\ts_barrier" : "+s"(epoch) :: "memory");
      issue(2);
      const f4 par = *(const LAS f4*)(L + PAR + w * 1024 + lane * 16);
      const f2 l64 = {par[0], par[1]}, l128 = {par[2], par[3]};
      f2 l256 = cmul(l128, l128);
      f2 wgt = hh ? l64 : (f2){1.f, 0.f};
      if (wt) wgt = cmul(wgt, l128);
      const unsigned tag = epoch + 1, t = tag & 0xffu;
      unsigned eo[8], et[8];
#pragma unroll
      for (int m = 0; m < 8; ++m) {
        const int i = 2 * tile - 1 - (2 * wt + hh) - 4 * m;
        eo[m] = i >= 0 ? ecol + i * 4096 : 0x80000000u;
        et[m] = i >= 0 ? t : 0u;
        asm volatile("" : "+v"(eo[m]), "+v"(et[m]));
      }
      asm volatile("" : "+v"(l256), "+v"(wgt));
      wait_bar<8>(); issue(3);
      wait_bar<8>(); issue(4);
      wait_bar<8>(); issue(5);
      wait_bar<8>(); issue(6);
      wait_bar<8>(); issue(7);
      wait_bar<8>(); issue_c(0);
      wait_bar<8>(); ST(1);
      if (NC_EARLY > 1) issue_c(1);
      if (NC_EARLY > 2) issue_c(2);
      ST(2);
      asm volatile("s_barrier" ::: "memory");
      if (PD) __builtin_amdgcn_s_sleep(PD);
      if (PRI & 2) __builtin_amdgcn_s_setprio(3);
      f2 cin;
#pragma unroll 1
      for (unsigned sp = 0; sp < SPIN_CAP; ++sp) {
      asm volatile("" ::: "memory");
        u2 g[8];
#pragma unroll
        for (int m = 0; m < 8; ++m) g[m] = __builtin_amdgcn_raw_buffer_load_b64(ws, eo[m], 0, SC1);
        unsigned bad = 0;
        cin = (f2){0, 0};
#pragma unroll
        for (int m = 7; m >= 0; --m) {
          const unsigned cx = g[m].x ^ et[m];
          bad |= cx;
          cin = cfma(l256, cin, (f2){__uint_as_float(cx), __uint_as_float(g[m].y)});
        }
        if (__all((bad & 0xffu) == 0)) break;
      }
      ST(5);
      cin = cmul(cin, wgt);
      f2 chi;
      const f2 clo = swap_lo(cin, chi);
      if (hh == 0) *(LAS f2*)(L + SCR + (wp * 2 + wt) * 256 + l31 * 8) = clo + chi;
      ST(6);
      lbar();
      if (WOX & 2) issue_c(2);
      lbar();
      ST(7);
      if (NC_EARLY <= 1) issue_c(1);
      if (NC_EARLY <= 2 && !(WOX & 2)) issue_c(2);
      issue_c(3);
      wait_bar<0>();
      ST(8); HFLUSH();
      return;
    }
    LOAD_EPOCH();
    const int pl = cg * 64 + wp * 32 + 8 * (l15 >> 2) + 4 * (q & 1) + (l15 & 3);
    const unsigned plr = lre[pl], pli = lim[pl], pdt = ldt[pl];
    u4 dv[2];
#pragma unroll
    for (int i = 0; i < 2; ++i) dv[i] = *(const GAS u4*)(dsk + cg * 64 + 32 * i + 8 * q);
    unsigned warm0 = 0, warm1 = 0;
    if (TOUCH) {
      warm0 = __builtin_amdgcn_raw_buffer_load_b32(ws, (!FW && lane < 16) ? WS_FB + ((tile * 2 + wt) * 16 + lane) * 128 : lane == 16 ? WS_END - 4096 * 256 * 6 - 128
                                                   : lane == 17 ? WS_H + (tile * 4 + w) * 65536 : 0x80000000u, 0, SC1);
      warm1 = __builtin_amdgcn_raw_buffer_load_b32(ro, lane < 2 ? (tile * 128 + w * 32 + lane * 16) * 4096 + 2048 : 0x80000000u, 0, SC1);
    }
    SB();
    f2 lam[2], l8[2], kap, l16, l32, l64;
    {
      const float lr = bflo(plr), li = bflo(pli);
      const float dt = bflo(pk2(expf(bflo(pdt)), 0.f));
      const float mag = expf(dt * lr);
      float sn, cs;
      sincosf(dt * li, &sn, &cs);
      const f2 l1 = {mag * cs, mag * sn};
      const float inv = 2.f / (lr * lr + li * li), nr = l1.x - 1.f, ni = l1.y;
      kap = (f2){(nr * lr + ni * li) * inv, (ni * lr - nr * li) * inv};
      f2 t = cmul(l1, l1); t = cmul(t, t); t = cmul(t, t);
      l16 = cmul(t, t); l32 = cmul(l16, l16); l64 = cmul(l32, l32);
      const f2 l128 = cmul(l64, l64);
      *(LAS f4*)(L + PAR + w * 1024 + lane * 16) = (f4){l64.x, l64.y, l128.x, l128.y};
      lam[0] = swap16(l1, l1, lam[1]);
      l8[0] = swap16(t, t, l8[1]);
    }
    f2 R0 = {lam[0].x, lam[0].x}, I0 = {lam[0].y, lam[0].y}, N0 = -I0, R1 = {lam[1].x, lam[1].x}, I1 = {lam[1].y, lam[1].y}, N1 = -I1;
    unsigned pe_ = hh == 0 ? ecol + (tile * 2 + wt) * 4096 : 0x80000000u, se_ = SCR + 1024 + (wt * 2 + wp) * 256 + hh * 1024 + l31 * 8;
    asm volatile("" : "+v"(R0), "+v"(I0), "+v"(N0), "+v"(R1), "+v"(I1), "+v"(N1), "+v"(pe_), "+v"(se_));
    const unsigned ax[2] = {(unsigned)(l15 * 128 + ((q ^ (l15 >> 1)) << 4)), (unsigned)(l15 * 128 + (((4 + q) ^ (l15 >> 1)) << 4))};
    const Win wx[2] = {mkwin(ax[0] + wt * 8192), mkwin(ax[1] + wt * 8192)}, ww[2] = {mkwin(ax[0] + wp * 8192), mkwin(ax[1] + wp * 8192)};
    MKSQ();
    f4 u[2][2][4];
#pragma unroll
    for (int i = 0; i < 16; ++i) u[i >> 3][(i >> 2) & 1][i & 3] = (f4){0, 0, 0, 0};
    u4 xd[2][2];
#pragma unroll
    for (int i = 0; i < 4; ++i) xd[i >> 1][i & 1] = (u4){0, 0, 0, 0};
    const int kx = (cg - rot) & 7;
    bf8 Xa[2], Xb[2], W0[4], W1[4];
    auto rdx = [&](bf8 (&X)[2], int kk, int ks, int h) {
#pragma unroll
      for (int i = 0; i < 2; ++i) X[i] = ldsr(at(L, wx[ks], (kk % 5) * SLOT + (2 * h + i) * 2048));
    };
    auto rdw = [&](bf8 (&W)[4], int kk, int ks) {
#pragma unroll
      for (int i = 0; i < 4; ++i) W[i] = ldsr(at(L, ww[ks], (kk % 5) * SLOT + 16384 + (i & 1) * 4096 + (i >> 1) * 2048));
    };
    auto mm = [&](const bf8 (&X)[2], const bf8 (&W)[4], int h) {
#pragma unroll
      for (int i = 0; i < 2; ++i)
#pragma unroll
        for (int j = 0; j < 4; ++j) u[j >> 1][j & 1][2 * h + i] = mfma16(X[i], W[j], u[j >> 1][j & 1][2 * h + i]);
    };
    asm volatile("s_waitcnt lgkmcnt(0)\n\ts_barrier" : "+s"(epoch), "+v"(lam[0]), "+v"(lam[1]), "+v"(l8[0]), "+v"(l8[1]), "+v"(kap), "+v"(l16), "+v"(l32), "+v"(l64),
                 "+v"(dv[0]), "+v"(dv[1]) : "v"(warm0), "v"(warm1), "v"(cpf) : "memory");
    ST(1);
    const unsigned tag = epoch + 1, t = tag & 0xffu;
    rdw(W0, 0, 0); rdx(Xa, 0, 0, 0);
    SB();
#pragma unroll
    for (int kk = 0; kk < 8; ++kk) {
      rdx(Xb, kk, 0, 1); mm(Xa, W0, 0); ILV(2, 8); SB(); PACE(BN);
      rdw(W1, kk, 1); rdx(Xa, kk, 1, 0); mm(Xb, W0, 1); ILV(6, 8); SB(); PACE(BN);
      if (kk == kx) {
#pragma unroll
        for (int i = 0; i < 4; ++i) xd[i >> 1][i & 1] = *(const LAS u4*)(at(L, wx[i & 1], (kk % 5) * SLOT + (i >> 1) * 2048) + (w & 1) * 4096);
      }
      SB();
      rdx(Xb, kk, 1, 1); mm(Xa, W1, 0); ILV(2, 8); SB();
      if (kk < 7) { lbar(); if (kk == 6) ST(2); rdw(W0, kk + 1, 0); rdx(Xa, kk + 1, 0, 0); mm(Xb, W1, 1); ILV(6, 8); }
      else mm(Xb, W1, 1);
      SB(); if (kk < 7) PACE(BN);
    }
    ST(3);
    f2 gr[2][8], gi[2][8];
#pragma unroll
    for (int i = 0; i < 16; ++i) {
      const int pt = i >> 3, a = i & 7;
      gr[pt][a] = (f2){u[pt][0][a >> 1][2 * (a & 1)], u[pt][0][a >> 1][2 * (a & 1) + 1]};
      gi[pt][a] = (f2){u[pt][1][a >> 1][2 * (a & 1)], u[pt][1][a >> 1][2 * (a & 1) + 1]};
    }
    {
#pragma unroll
      for (int a = 1; a < 8; ++a)
        asm volatile("v_pk_fma_f32 %0, %10, %5, %0\n\tv_pk_fma_f32 %1, %9, %4, %1\n\tv_pk_fma_f32 %2, %13, %7, %2\n\tv_pk_fma_f32 %3, %12, %6, %3\n\t"
                     "v_pk_fma_f32 %0, %8, %4, %0\n\tv_pk_fma_f32 %1, %8, %5, %1\n\tv_pk_fma_f32 %2, %11, %6, %2\n\tv_pk_fma_f32 %3, %11, %7, %3"
                     : "+v"(gr[0][a]), "+v"(gi[0][a]), "+v"(gr[1][a]), "+v"(gi[1][a])
                     : "v"(gr[0][a - 1]), "v"(gi[0][a - 1]), "v"(gr[1][a - 1]), "v"(gi[1][a - 1]), "v"(R0), "v"(I0), "v"(N0), "v"(R1), "v"(I1), "v"(N1));
    }
    const f2 e0[2] = {{gr[0][7].x, gi[0][7].x}, {gr[1][7].x, gi[1][7].x}};
    f2 vodd;
    const f2 veven = swap16(cfma(l8[0], e0[0], (f2){gr[0][7].y, gi[0][7].y}), cfma(l8[1], e0[1], (f2){gr[1][7].y, gi[1][7].y}), vodd);
    f2 ehi;
    const f2 elo = swap_lo(cfma(l16, veven, vodd), ehi);
    const f2 e64 = cfma(l32, elo, ehi);
    __builtin_amdgcn_raw_buffer_store_b64((u2){tagv(e64.x, t), __float_as_uint(e64.y)}, ws, pe_, 0, SC1);
    *(LAS f2*)(L + se_) = e64;
    lbar();
    const f2 esib = *(const LAS f2*)(L + SCR + 1024 + wp * 256 + l31 * 8);
    SB();
    ST(4);
    f2 kp[2], ke[2], pw[2][8];
    kp[0] = swap16(kap, kap, kp[1]);
#pragma unroll
    for (int pt = 0; pt < 2; ++pt) {
      const f2 KR = {kp[pt].x, kp[pt].x}, KI = {kp[pt].y, kp[pt].y};
      ke[pt] = cmul(kp[pt], e0[pt]);
#pragma unroll
      for (int a = 0; a < 8; ++a) {
        const f2 xr = gr[pt][a], xi = gi[pt][a];
        gr[pt][a] = fma2(KR, xr, -KI * xi); gi[pt][a] = fma2(-KR, xi, -KI * xr);
        asm volatile("" : "+v"(gr[pt][a]), "+v"(gi[pt][a]));
      }
      pw[pt][0] = (f2){lam[pt].x, -lam[pt].y};
#pragma unroll
      for (int k = 1; k < 8; ++k) { pw[pt][k] = cmul(pw[pt][k >> 1], pw[pt][(k - 1) >> 1]); asm volatile("" : "+v"(pw[pt][k])); }
    }
    u2 idn = {(l15 == 4 * q ? 0x3f80u : 0u) | (l15 == 4 * q + 1 ? 0x3f800000u : 0u), (l15 == 4 * q + 2 ? 0x3f80u : 0u) | (l15 == 4 * q + 3 ? 0x3f800000u : 0u)};
#pragma unroll
    for (int i = 0; i < 4; ++i) {
#pragma unroll
      for (int j = 0; j < 4; ++j) xd[i >> 1][i & 1][j] = pk2(bflo(xd[i >> 1][i & 1][j]) * bflo(dv[i & 1][j]), bfhi(xd[i >> 1][i & 1][j]) * bfhi(dv[i & 1][j]));
      asm volatile("" : "+v"(xd[i >> 1][i & 1]));
    }
    f2 me = hh ? cmul(kap, l32) : kap, be = hh ? cmul(kap, elo) : (f2){0.f, 0.f};
    f2 mo = cmul(l16, me), bo = cfma(l16, be, cmul(kap, veven));
    if (wt) { be = cfma(me, esib, be); bo = cfma(mo, esib, bo); me = cmul(me, l64); mo = cmul(mo, l64); }
    unsigned pf_ = lane == 0 ? WS_FB + ((tile * 2 + wt) * 16 + cg * 2 + wp) * 128 : 0x80000000u, ex = 2 * SLOT + wt * 16384 + wp * 4096 + lane * 16;
    asm volatile("" : "+v"(pf_), "+v"(ex));
    unsigned sc_ = SCR + wp * 512 + l31 * 8, ho[4];
#pragma unroll
    for (int i = 0; i < 4; ++i) ho[i] = WS_H + (tile * 8 + wt * 4 + i) * 32768 + (cg * 2 + wp) * 2048 + lane * 16;
    asm volatile("s_waitcnt lgkmcnt(0)\n\ts_barrier" : "+v"(ho[0]), "+v"(ho[1]), "+v"(ho[2]), "+v"(ho[3]), "+v"(me), "+v"(be), "+v"(mo), "+v"(bo),
                 "+v"(ke[0]), "+v"(ke[1]), "+v"(idn), "+v"(sc_) :: "memory");
    ST(5);
    const f2 cin = *(const LAS f2*)(L + sc_) + *(const LAS f2*)(L + sc_ + 256);
    f2 d0[2], DR[2], DJ[2];
    d0[0] = swap16(cfma(me, cin, be), cfma(mo, cin, bo), d0[1]);
#pragma unroll
    for (int pt = 0; pt < 2; ++pt) {
      const f2 d1 = cfma(l8[pt], d0[pt], ke[pt]);
      DR[pt] = (f2){d0[pt].x, d1.x}; DJ[pt] = (f2){-d0[pt].y, -d1.y};
    }
    const f4 zero4 = {0, 0, 0, 0};
    f4 T[8][2];
    u4 ow[8];
    auto fix = [&](int i) {
      const int mt = i >> 1, c = i & 1;
#pragma unroll
      for (int pt = 0; pt < 2; ++pt) {
        u2 a;
#pragma unroll
        for (int h = 0; h < 2; ++h) {
          const f2 P = pw[pt][2 * mt + h], PR = {P.x, P.x}, PI = {P.y, P.y};
          a[h] = pk2(c ? fma2(PI, DR[pt], fma2(PR, DJ[pt], gi[pt][2 * mt + h])) : fma2(-PI, DJ[pt], fma2(PR, DR[pt], gr[pt][2 * mt + h])));
        }
        T[i][pt] = mfma16k16(a, idn, zero4);
      }
    };
    auto put = [&](int i) {
      ow[i] = (u4){pk2(T[i][0][0], T[i][0][1]), pk2(T[i][0][2], T[i][0][3]), pk2(T[i][1][0], T[i][1][1]), pk2(T[i][1][2], T[i][1][3])};
      __builtin_amdgcn_raw_buffer_store_b128(ow[i], ws, ho[i >> 1] + (i & 1) * 1024, 0, SC1);
    };
    fix(0); SB();
#pragma unroll
    for (int i = 1; i < 8; ++i) { fix(i); SB(); put(i - 1); SB(); }
    put(7);
    SB();
#pragma unroll
    for (int i = 0; i < 8; ++i) *(LAS u4*)(L + ex + (i >> 2) * 8192 + (i & 1) * 2048 + ((i >> 1) & 1) * 1024) = ow[i];
    lbar();
    ST(6);
    if (!NODRAIN) drain();
    __builtin_amdgcn_raw_buffer_store_b64((u2){tag, ~tag}, ws, pf_, 0, SC1);
    ST(7);
    unsigned hb = WS_H + (tile * 8 + w * 2) * 32768 + lane * 16, hb2 = hb + 32768;
    asm volatile("" : "+v"(hb), "+v"(hb2));
    const Win wc[2] = {mkwin(ax[0]), mkwin(ax[1])};
    u4 hf[64];
    bf8 cf[2][4];
    f4 acc[2][4];
    auto ldh = [&](int g) {
      hf[2 * g] = __builtin_amdgcn_raw_buffer_load_b128(ws, hb + (g & 3) * 1024, sq[g >> 2] * 4096, PAY_AUX);
      hf[2 * g + 1] = __builtin_amdgcn_raw_buffer_load_b128(ws, hb2 + (g & 3) * 1024, sq[g >> 2] * 4096, PAY_AUX);
    };
    auto rdc = [&](int g) {
      const int tt = g >> 2;
#pragma unroll
      for (int nt = 0; nt < 4; ++nt)
        cf[g & 1][nt] = ldsr(at(L, wc[(g >> 1) & 1], (((tt >> 1) + 3) % 5) * SLOT + (tt & 1) * 16384 + (g & 1) * 8192 + nt * 2048));
    };
    auto mh = [&](int g) {
#pragma unroll
      for (int nt = 0; nt < 4; ++nt) {
        acc[0][nt] = mfma16(cf[g & 1][nt], asbf8(hf[2 * g]), acc[0][nt]);
        acc[1][nt] = mfma16(cf[g & 1][nt], asbf8(hf[2 * g + 1]), acc[1][nt]);
      }
    };
#pragma unroll
    for (int i = 0; i < 8; ++i) hf[i] = *(const LAS u4*)(L + 2 * SLOT + w * 8192 + i * 1024 + lane * 16);
    rdc(0);
    {
      u4 ida[2];
#pragma unroll
      for (int hf_ = 0; hf_ < 2; ++hf_)
#pragma unroll
        for (int jq = 0; jq < 4; ++jq) {
          const int k0 = 8 * q + 2 * jq - 16 * hf_;
          ida[hf_][jq] = (l15 == k0 ? 0x3f80u : 0u) | (l15 == k0 + 1 ? 0x3f800000u : 0u);
        }
#pragma unroll
      for (int i = 0; i < 8; ++i) acc[i >> 2][i & 3] = mfma16(asbf8(ida[i & 1]), asbf8(xd[i >> 2][(i >> 1) & 1]), zero4);
    }
    SB();
#pragma unroll
    for (int g = 0; g < 4; ++g) { rdc(g + 1); mh(g); ILV(4, 8); SB(); }
    unsigned tw[4], trd[4], so_ = ((tile * 128 + 64 * wt + 4 * (w & 1) + 8 * (q & 1) + (q >> 1)) * 1024 + 512 + cg * 64 + l15 * 4) * 4;
#pragma unroll
    for (int i = 0; i < 4; ++i) {
      tw[i] = 2 * SLOT + w * 8192 + l15 * 256 + (((4 * i + q) ^ l15) << 4);
      trd[i] = 2 * SLOT + w * 8192 + (4 * i + q) * 256 + ((l15 ^ (4 * i + q)) << 4);
      asm volatile("" : "+v"(tw[i]), "+v"(trd[i]));
    }
    asm volatile("s_barrier" : "+v"(so_) :: "memory");
    if (FD) __builtin_amdgcn_s_sleep(FD);
    const unsigned fo = WS_FB + ((tile * 2 + wt) * 16 + l15) * 128;
    unsigned have = 0;
    if (!PROG) wait_flags(ws, fo, tag);
    if (DLB) __builtin_amdgcn_s_sleep(DLB);
#pragma unroll
    for (int g = 4; g < 4 + HD / 2; ++g) { if (PROG && (g & 3) == 0) wait_pair(ws, fo, tag, have, sq[g >> 2]); ldh(g); }
    ST(8); CT(14);
    SB();
#pragma unroll
    for (int g = 4; g < 32; ++g) {
      if (PROG && g + HD / 2 < 32 && ((g + HD / 2) & 3) == 0) wait_pair(ws, fo, tag, have, sq[(g + HD / 2) >> 2]);
      if (g + HD / 2 < 32) ldh(g + HD / 2);
      if (g + 1 < 32) rdc(g + 1);
      mh(g);
#pragma unroll
      for (int i_ = 0; i_ < 4; ++i_) { SG(0x008, 1); SG(0x100, 1); }
      SG(0x008, 1); SG(0x020, 1); SG(0x008, 1); SG(0x020, 1); SG(0x008, 2);
      SB(); if (g < 30) PACE(HN);
    }
    ST(9); CT(15);
    if (DIRECT) {
      const unsigned sd = ((tile * 128 + 64 * wt + 4 * (w & 1) + 16 * (l15 >> 2) + 8 * (l15 & 1) + ((l15 >> 1) & 1)) * 1024 + 512 + cg * 64 + q * 4) * 4;
#pragma unroll
      for (int i = 0; i < 8; ++i) __builtin_amdgcn_raw_buffer_store_b128(__builtin_bit_cast(u4, acc[i >> 2][i & 3]), ro, sd + (i >> 2) * 8192 + (i & 3) * 64, 0, OUT_AUX);
      return;
    }
#pragma unroll
    for (int i = 0; i < 8; ++i) *(LAS f4*)(L + tw[i & 3] + (i >> 2) * 4096) = acc[i >> 2][i & 3];
    u4 ov[8];
#pragma unroll
    for (int i = 0; i < 8; ++i) ov[i] = *(const LAS u4*)(L + trd[i & 3] + (i >> 2) * 4096);
    SB();
#pragma unroll
    for (int i = 0; i < 8; ++i) __builtin_amdgcn_raw_buffer_store_b128(ov[i], ro, so_, (16 * (i & 3) + 2 * (i >> 2)) * 4096, OUT_AUX);
    ST(10);
  }
}

extern "C" void kernel_launch(void* const* d_in, const int* in_sizes, int n_in, void* d_out, int out_size, void* d_ws, size_t ws_size,
                              hipStream_t stream) {
  (void)hipFuncSetAttribute((const void*)fused_kernel, hipFuncAttributeMaxDynamicSharedMemorySize, LDS_BYTES);
  fused_kernel<<<256, 512, LDS_BYTES, stream>>>((const u16*)d_in[0], (const u16*)d_in[1], (const u16*)d_in[2], (const u16*)d_in[3],
                                               (const u16*)d_in[4], (const u16*)d_in[5], (const u16*)d_in[6], (const u16*)d_in[7],
                                               (const u16*)d_in[8], (const u16*)d_in[9], (const u16*)d_in[10], (const u16*)d_in[11],
                                               (float*)d_out, (char*)d_ws);
}
```

```cpp
#include <hip/hip_runtime.h>
#include <hip/hip_bf16.h>

#define DI __device__ __forceinline__
#define LAS __attribute__((address_space(3)))
#define GAS __attribute__((address_space(1)))
typedef unsigned short u16;
typedef unsigned long long u64;
typedef __bf16 bf8 __attribute__((ext_vector_type(8)));
typedef __bf16 bf2 __attribute__((ext_vector_type(2)));
typedef short s4 __attribute__((ext_vector_type(4)));
typedef short s8 __attribute__((ext_vector_type(8)));
typedef float f4 __attribute__((ext_vector_type(4)));
typedef float f2 __attribute__((ext_vector_type(2)));
typedef unsigned u4 __attribute__((ext_vector_type(4)));
typedef unsigned u2 __attribute__((ext_vector_type(2)));

constexpr unsigned WS_EPOCH = 0;
constexpr unsigned WS_FA = 4096;
constexpr unsigned WS_FB = WS_FA + 65536;
constexpr unsigned WS_EST = WS_FB + 65536;
constexpr unsigned WS_Z = WS_EST + 131072;
constexpr unsigned WS_KV = WS_Z + 65536;
constexpr unsigned WS_O = 1u << 20;
constexpr unsigned WS_H = 3u << 20;
constexpr unsigned WS_END = 7u << 20;
constexpr int LDS_BYTES = 160 * 1024;
constexpr unsigned SPIN_CAP = 200000;
constexpr int SC1 = 16;

constexpr unsigned OOB = 0x80000000u;
constexpr int PACE_A = 12, PACE_B = 8;
constexpr int OD = 6, HD = 6;

#define SB() __builtin_amdgcn_sched_barrier(0)
#define PACE(n) do { asm volatile("s_nop %0" :: "i"((n) - 1)); SB(); } while (0)
#define SG(m, n) __builtin_amdgcn_sched_group_barrier(m, n, 0)

DI unsigned pk2(float a, float b) { f2 v = {a, b}; return __builtin_bit_cast(unsigned, __builtin_convertvector(v, bf2)); }
DI unsigned pk2(f2 v) { return __builtin_bit_cast(unsigned, __builtin_convertvector(v, bf2)); }
DI float bflo(unsigned v) { return __uint_as_float(v << 16); }
DI float bfhi(unsigned v) { return __uint_as_float(v & 0xffff0000u); }
DI bf8 asbf8(u4 v) { return __builtin_bit_cast(bf8, v); }
DI f4 mfma16(bf8 a, bf8 b, f4 c) { return __builtin_amdgcn_mfma_f32_16x16x32_bf16(a, b, c, 0, 0, 0); }
DI void mfa(f4& c, bf8 a, bf8 b) { asm volatile("v_mfma_f32_16x16x32_bf16 %0, %1, %2, %0" : "+v"(c) : "v"(a), "v"(b)); }
DI f4 mfma16k16(u2 a, u2 b, f4 c) { return __builtin_amdgcn_mfma_f32_16x16x16bf16_1k(__builtin_bit_cast(s4, a), __builtin_bit_cast(s4, b), c, 0, 0, 0); }
DI bf8 ldsr(const char LAS* p) { return *(const LAS bf8*)p; }
DI bf8 ldstr(const char LAS* p0, const char LAS* p1) {
  s4 a = __builtin_amdgcn_ds_read_tr16_b64_v4i16((LAS s4*)p0), b = __builtin_amdgcn_ds_read_tr16_b64_v4i16((LAS s4*)p1);
  return __builtin_bit_cast(bf8, (s8)__builtin_shufflevector(a, b, 0, 1, 2, 3, 4, 5, 6, 7));
}
DI void dma4(unsigned lds, const GAS char* sb, unsigned v0, unsigned v1, unsigned v2, unsigned v3) {
  asm volatile("s_mov_b32 m0, %0\n\ts_nop 0\n\tglobal_load_lds_dwordx4 %1, %5\n\tglobal_load_lds_dwordx4 %2, %5 offset:1024\n\t"
               "global_load_lds_dwordx4 %3, %5 offset:2048\n\tglobal_load_lds_dwordx4 %4, %5 offset:3072"
               :: "s"(lds), "v"(v0), "v"(v1), "v"(v2), "v"(v3), "s"(sb) : "memory");
}
DI void dma2(unsigned lds, const GAS char* sb, unsigned v0, unsigned v1) {
  asm volatile("s_mov_b32 m0, %0\n\ts_nop 0\n\tglobal_load_lds_dwordx4 %1, %3\n\tglobal_load_lds_dwordx4 %2, %3 offset:1024"
               :: "s"(lds), "v"(v0), "v"(v1), "s"(sb) : "memory");
}
struct Win { unsigned b[3]; };
DI Win mkwin(unsigned a) { Win r = {{a, a + 65536, a + 131072}}; asm volatile("" : "+v"(r.b[0]), "+v"(r.b[1]), "+v"(r.b[2])); return r; }
DI const char LAS* at(const char LAS* L, const Win& a, unsigned off) { return L + a.b[off >> 16] + (off & 65535); }
DI void lbar() { asm volatile("s_waitcnt lgkmcnt(0)\n\ts_barrier" ::: "memory"); }
template <int N> DI void wait_bar() { asm volatile("s_waitcnt vmcnt(%0) lgkmcnt(0)\n\ts_barrier" :: "i"(N) : "memory"); }
DI void drain() { asm volatile("s_waitcnt vmcnt(0)" ::: "memory"); }
DI unsigned tagv(float x, unsigned t) { return (__float_as_uint(x) & 0xffffff00u) | t; }
DI f2 cmul(f2 a, f2 b) { return (f2){a.x * b.x - a.y * b.y, a.x * b.y + a.y * b.x}; }
DI f2 cfma(f2 a, f2 b, f2 c) { return (f2){fmaf(a.x, b.x, fmaf(-a.y, b.y, c.x)), fmaf(a.x, b.y, fmaf(a.y, b.x, c.y))}; }
DI f2 fma2(f2 a, f2 b, f2 c) { return __builtin_elementwise_fma(a, b, c); }
constexpr float EPS = 1e-6f, LOG2E = 1.44269504088896341f;
DI unsigned phi2(float s0, float s1) {
  const f2 a = fma2((f2){s0, s1}, (f2){LOG2E, LOG2E}, (f2){-EPS * LOG2E, -EPS * LOG2E});
  const f2 e = {__builtin_amdgcn_fmed3f(__builtin_amdgcn_exp2f(a.x), 0.f, 1.f), __builtin_amdgcn_fmed3f(__builtin_amdgcn_exp2f(a.y), 0.f, 1.f)};
  return pk2(e + (f2){__builtin_amdgcn_fmed3f(s0, EPS, 3.40282347e38f), __builtin_amdgcn_fmed3f(s1, EPS, 3.40282347e38f)});
}
DI f2 swap_lo(f2 v, f2& hi) {
  const auto x = __builtin_amdgcn_permlane32_swap(__float_as_uint(v.x), __float_as_uint(v.x), false, false);
  const auto y = __builtin_amdgcn_permlane32_swap(__float_as_uint(v.y), __float_as_uint(v.y), false, false);
  hi = (f2){__uint_as_float(x[1]), __uint_as_float(y[1])};
  return (f2){__uint_as_float(x[0]), __uint_as_float(y[0])};
}
DI f2 swap16(f2 a, f2 b, f2& rb) {
  const auto x = __builtin_amdgcn_permlane16_swap(__float_as_uint(a.x), __float_as_uint(b.x), false, false);
  const auto y = __builtin_amdgcn_permlane16_swap(__float_as_uint(a.y), __float_as_uint(b.y), false, false);
  rb = (f2){__uint_as_float(x[1]), __uint_as_float(y[1])};
  return (f2){__uint_as_float(x[0]), __uint_as_float(y[0])};
}
DI void wait_flags(__amdgpu_buffer_rsrc_t ws, unsigned off, unsigned tag) {
#pragma unroll 1
  for (unsigned sp = 0; sp < SPIN_CAP; ++sp) {
    asm volatile("" ::: "memory");
    const u2 f = __builtin_amdgcn_raw_buffer_load_b64(ws, off, 0, SC1);
    if (__all(f.x == tag && f.y == ~tag)) break;
    __builtin_amdgcn_s_sleep(1);
  }
  asm volatile("" ::: "memory");
}

__global__ void __launch_bounds__(512, 2)
fused_kernel(const u16* a0, const u16* a1, const u16* a2, const u16* a3, const u16* a4, const u16* a5, const u16* a6, const u16* a7,
             const u16* a8, const u16* a9, const u16* a10, const u16* a11, float* a12, char* a13) {
  extern __shared__ __attribute__((aligned(16))) char smem_[];
  char LAS* L = (char LAS*)smem_;
  const unsigned L0 = (unsigned)(size_t)L;
  typedef u64 u64x8 __attribute__((ext_vector_type(8)));
  typedef u64 u64x4 __attribute__((ext_vector_type(4)));
  typedef u64 u64x2 __attribute__((ext_vector_type(2)));
  u64x8 ka; u64x4 kb; u64x2 kc_;
  asm volatile("s_load_dwordx16 %0, %3, 0x0\n\ts_load_dwordx8 %1, %3, 0x40\n\ts_load_dwordx4 %2, %3, 0x60"
               : "=&s"(ka), "=&s"(kb), "=&s"(kc_) : "s"(__builtin_amdgcn_kernarg_segment_ptr()) : "memory");
  SB();
  const int lane = threadIdx.x & 63;
  const int w8 = __builtin_amdgcn_readfirstlane(threadIdx.x >> 6), w = w8 & 3;
  unsigned cpf = 0;
  if (w8 == 0) {
    u64 pc; unsigned len;
    asm volatile("s_getpc_b64 %0\n.Lcode_pf%=:\n\ts_mov_b32 %1, .Lcode_lastA-.Lcode_pf%=\n\ts_max_u32 %1, %1, .Lcode_lastB-.Lcode_pf%=" : "=s"(pc), "=s"(len) :: "scc");
    cpf = __builtin_amdgcn_raw_buffer_load_b32(__builtin_amdgcn_make_buffer_rsrc((char*)(pc & ~127ull), 0, len, 0x00020000),
                                               lane < 10 ? ((blockIdx.x >> 3) * 10 + lane) * 128 : OOB, 0, 0);
  }
  const bool helper = w8 >= 4;
  const int bid = blockIdx.x, xcd = bid & 7, jj = bid >> 3;
  int tile = (xcd >> 1) * 4 + (jj >> 3), cg = jj & 7;
  int rot = ((int)(030630630630ull >> (3 * (cg + (jj >> 3)))) + (xcd >> 1)) & 7;
  auto seq = [&](int t) { const int b = 2 * (jj >> 3), e = t - 1; return t == 0 ? cg : (b + e + (e >= ((cg - b) & 7) ? 1 : 0)) & 7; };
#define MKSQ() int sq[8]; _Pragma("unroll") for (int t_ = 1; t_ < 8; ++t_) { sq[t_] = __builtin_amdgcn_readfirstlane(seq(t_)); asm volatile("" : "+s"(sq[t_])); }
  int r8 = lane >> 3, c8 = lane & 7;
  const int q = lane >> 4, l15 = lane & 15, l31 = lane & 31, hh = lane >> 5;
  asm volatile("s_waitcnt lgkmcnt(0)" : "+s"(ka), "+s"(kb), "+s"(kc_), "+s"(tile), "+s"(cg), "+s"(rot), "+v"(r8), "+v"(c8) :: "memory");
  const GAS u16 *xa = (const GAS u16*)ka[0], *wqkv = (const GAS u16*)ka[1], *wo = (const GAS u16*)ka[2], *xb = (const GAS u16*)ka[3];
  const GAS u16 *lre = (const GAS u16*)ka[4], *lim = (const GAS u16*)ka[5], *bre = (const GAS u16*)ka[6], *bim = (const GAS u16*)ka[7];
  const GAS u16 *cre = (const GAS u16*)kb[0], *cim = (const GAS u16*)kb[1], *ldt = (const GAS u16*)kb[2], *dsk = (const GAS u16*)kb[3];
  float* out = (float*)kc_[0]; char* wsp = (char*)kc_[1];
  const __amdgpu_buffer_rsrc_t ws = __builtin_amdgcn_make_buffer_rsrc(wsp, 0, WS_END, 0x00020000);
  const __amdgpu_buffer_rsrc_t ro = __builtin_amdgcn_make_buffer_rsrc(out, 0, 2048 * 1024 * 4, 0x00020000);
  unsigned epoch;
#define LOAD_EPOCH() asm volatile("s_load_dword %0, %1, 0x0\n\ts_waitcnt lgkmcnt(0)" : "=&s"(epoch) : "s"(wsp) : "memory")

  if ((xcd & 1) == 0) {
    constexpr unsigned WA = 131072, WB = 147456, SCR = 32768, TRB = 81920;
    constexpr unsigned QBUF[4] = {WB, 0, WA, WB}, OBUF[4] = {32768, 49152, 65536, 0};
    constexpr unsigned WBUF[8] = {WA, WB, 7 * 16384, 0, WA, WB, 0, WA};
    if (helper) {
      const int head = cg * 4 + w;
      unsigned vx[4], vw[2], vo[4];
#pragma unroll
      for (int i = 0; i < 4; ++i) vx[i] = (32 * w + 8 * i + r8) * 1024 + ((c8 ^ ((4 * i + (r8 >> 1)) & 7)) << 4) - 1024 * i;
#pragma unroll
      for (int i = 0; i < 2; ++i) vw[i] = (16 * w + 8 * i + r8) * 3072 + ((c8 ^ ((((r8 >> 1) & 1) | (i << 1)) << 1)) << 4) - 1024 * i;
      const GAS char* xbase = (const GAS char*)xa + (size_t)tile * 131072;
      const GAS char* wbase = (const GAS char*)wqkv + cg * 128;
      auto isx = [&](int kk, int slot) { dma4(L0 + slot * 16384 + w * 4096, xbase + ((kk + rot) & 7) * 128, vx[0], vx[1], vx[2], vx[3]); };
      auto isw = [&](unsigned dst, int kk, int seg) { dma2(L0 + dst + w * 2048, wbase + ((kk + rot) & 7) * (64 * 3072) + seg * 1024, vw[0], vw[1]); };
      auto issue = [&](int kk) { isx(kk, kk); isw(WBUF[kk], kk, 1); isw(WBUF[kk] + 8192, kk, 2); };
      auto isq = [&](int p) { isw(QBUF[p], 2 * p + 1, 0); isw(QBUF[p] + 8192, 2 * p + 2, 0); };
      issue(0); issue(1);
      LOAD_EPOCH();
#pragma unroll
      for (int i = 0; i < 4; ++i) vo[i] = (8 * i + r8) * 1024 + ((c8 ^ (((r8 >> 1) & 3) << 1)) << 4) - 1024 * i;
      const GAS char* obase = (const GAS char*)wo + cg * 128;
      wait_bar<8>();
      issue(2);
      wait_bar<8>(); issue(3);
      wait_bar<8>(); issue(4);
      wait_bar<8>(); issue(5);
      wait_bar<8>(); issue(6);
      wait_bar<8>(); issue(7);
      wait_bar<8>();
      unsigned pfo;
      asm volatile("global_load_dword %0, %1, %2" : "=v"(pfo) : "v"(((jj >> 3) * 128 + w * 32 + l31) * 1024), "s"(obase) : "memory");
      isq(0);
      wait_bar<5>(); isq(1);
      wait_bar<4>(); isq(2);
      wait_bar<4>(); isq(3); isx(0, 1);
      asm volatile("s_waitcnt vmcnt(8)\n\ts_barrier\n\ts_waitcnt vmcnt(0)" : "+v"(pfo) :: "memory");
      const unsigned tag = epoch + 1, t = tag & 0xffu;
      const GAS char* kvb_ = (const GAS char*)wsp + WS_KV + head * 16384;
      const GAS char* zb_ = (const GAS char*)wsp + WS_Z + head * 2048;
      const unsigned qr = (jj >> 3) * 4096;
      const unsigned vk0 = lane * 16 + qr, vk1 = lane * 16 + ((qr + 4096) & 12288), vz = l15 * 128 + q * 16;
      u4 g[8], gz;
#define KVL(i, v) asm volatile("global_load_dwordx4 %0, %1, %2 offset:%3 sc1" : "=v"(g[i]) : "v"(v), "s"(kvb_), "i"((i & 3) * 1024) : "memory")
      asm volatile("global_load_dwordx4 %0, %1, %2 sc1" : "=v"(gz) : "v"(vz), "s"(zb_) : "memory");
      KVL(0, vk0); KVL(1, vk0); KVL(2, vk0); KVL(3, vk0); KVL(4, vk1); KVL(5, vk1); KVL(6, vk1); KVL(7, vk1);
      asm volatile("s_barrier" ::: "memory");
      auto zsum = [&]() {
        float z0 = __uint_as_float(gz.x), z1 = __uint_as_float(gz.y), z2 = __uint_as_float(gz.z), z3 = __uint_as_float(gz.w);
#define ZS(r) "v_add_f32_dpp %0, %0, %0 row_ror:" #r " row_mask:0xf bank_mask:0xf\n\tv_add_f32_dpp %1, %1, %1 row_ror:" #r " row_mask:0xf bank_mask:0xf\n\t" \
              "v_add_f32_dpp %2, %2, %2 row_ror:" #r " row_mask:0xf bank_mask:0xf\n\tv_add_f32_dpp %3, %3, %3 row_ror:" #r " row_mask:0xf bank_mask:0xf\n\t"
        asm volatile("s_nop 1\n\t" ZS(8) ZS(4) ZS(2) ZS(1) "s_nop 0" : "+v"(z0), "+v"(z1), "+v"(z2), "+v"(z3));
        return (f4){z0, z1, z2, z3};
      };
      asm volatile("s_waitcnt vmcnt(8)" : "+v"(gz) :: "memory");
      gz.x ^= t; gz.z ^= t;
      unsigned bad = gz.x | gz.z;
      f4 zz = zsum(), kv = {0, 0, 0, 0};
      asm volatile("" : "+v"(zz));
#define KVA(i) asm volatile("s_waitcnt vmcnt(%1)" : "+v"(g[i]) : "i"(7 - i) : "memory"); g[i].x ^= t; g[i].z ^= t; bad |= g[i].x | g[i].z; kv += __builtin_bit_cast(f4, g[i]); asm volatile("" : "+v"(kv), "+v"(bad))
      KVA(0); KVA(1); KVA(2); KVA(3); KVA(4); KVA(5); KVA(6); KVA(7);
      if (!__all((bad & 0xffu) == 0)) {
#pragma unroll 1
        for (unsigned rep = 0; rep < SPIN_CAP; ++rep) {
          asm volatile("" ::: "memory");
          gz = __builtin_amdgcn_raw_buffer_load_b128(ws, WS_Z + head * 2048 + vz, 0, SC1);
#pragma unroll
          for (int i = 0; i < 8; ++i) g[i] = __builtin_amdgcn_raw_buffer_load_b128(ws, WS_KV + head * 16384 + (i < 4 ? vk0 : vk1) + (i & 3) * 1024, 0, SC1);
          gz.x ^= t; gz.z ^= t;
          bad = gz.x | gz.z;
#pragma unroll
          for (int i = 0; i < 8; ++i) { g[i].x ^= t; g[i].z ^= t; bad |= g[i].x | g[i].z; }
          if (__all((bad & 0xffu) == 0)) break;
        }
        zz = zsum();
        kv = (f4){0, 0, 0, 0};
#pragma unroll
        for (int i = 0; i < 8; ++i) kv += __builtin_bit_cast(f4, g[i]);
      }
      *(LAS f4*)(L + SCR + w * 1024 + lane * 16) = kv;
      *(LAS u2*)(L + SCR + 8192 + w * 512 + lane * 8) = (u2){pk2(zz[0], zz[1]), pk2(zz[2], zz[3])};
      lbar();
      lbar();
#pragma unroll
      for (int i = 0; i < 4; ++i)
        dma4(L0 + OBUF[i] + w * 4096, obase + (2 * seq(2 * i + (w >> 1)) + (w & 1)) * 32768, vo[0], vo[1], vo[2], vo[3]);
      wait_bar<0>();
      if (bid == 0 && w == 0) {
#pragma unroll 1
        for (unsigned sp = 0; sp < SPIN_CAP; ++sp) {
          asm volatile("" ::: "memory");
          const unsigned fo = (lane >> 3) * 4096 + (lane & 7) * 256;
          const u2 f0 = __builtin_amdgcn_raw_buffer_load_b64(ws, WS_FA + fo, 0, SC1), f1 = __builtin_amdgcn_raw_buffer_load_b64(ws, WS_FA + 32768 + fo, 0, SC1);
          const u2 f2_ = __builtin_amdgcn_raw_buffer_load_b64(ws, WS_FB + fo, 0, SC1), f3 = __builtin_amdgcn_raw_buffer_load_b64(ws, WS_FB + 32768 + fo, 0, SC1);
          if (__all(f0.x == tag && f0.y == ~tag && f1.x == tag && f1.y == ~tag && f2_.x == tag && f2_.y == ~tag && f3.x == tag && f3.y == ~tag)) break;
          __builtin_amdgcn_s_sleep(8);
        }
        if (lane == 0) __builtin_amdgcn_raw_buffer_store_b32(tag, ws, WS_EPOCH, 0, SC1);
      }
      return;
    }
    const int hp = w & 1, th = w >> 1;
    unsigned warm0, warm1;
    {
      warm0 = __builtin_amdgcn_raw_buffer_load_b32(ws, lane < 16 ? WS_FA + ((tile * 2 + th) * 16 + lane) * 128 : lane < 32 ? WS_Z + (cg * 4 + w) * 2048 + (lane - 16) * 128
                                                   : lane == 32 ? WS_O - 128 : lane == 33 ? WS_O + tile * 131072 : OOB, 0, SC1);
      warm1 = __builtin_amdgcn_raw_buffer_load_b32(ro, lane < 2 ? (tile * 128 + w * 32 + lane * 16) * 4096 : OOB, 0, SC1);
    }
    LOAD_EPOCH();
    const int qq = (lane >> 2) & 3, pp = lane & 3;
    const unsigned ax0 = l15 * 128 + ((q ^ (l15 >> 1)) << 4), ax1 = l15 * 128 + (((4 + q) ^ (l15 >> 1)) << 4);
    const Win wx[2] = {mkwin(ax0), mkwin(ax1)}, wq[2] = {mkwin(ax0 + th * 8192), mkwin(ax1 + th * 8192)};
    auto waddr = [&](int h) { return (8 * q + qq) * 128 + (((2 * h + (pp >> 1)) ^ ((((qq >> 1) & 1) | ((q & 1) << 1)) << 1)) << 4) + 8 * (pp & 1); };
    const Win aw = mkwin(waddr(w)), aq0 = mkwin(waddr(2 * hp)), aq1 = mkwin(waddr(2 * hp + 1));
    MKSQ();
    unsigned ao[4], og[4], tw[4], trd[4];
#pragma unroll
    for (int i = 0; i < 4; ++i) {
      og[i] = WS_O + ((tile * 8 + th * 4 + i) * 16 + cg * 2 + hp) * 1024 + lane * 16;
      ao[i] = (4 * q + qq) * 128 + ((((2 * i) ^ (((2 * q + (qq >> 1)) & 3) << 1)) + (pp >> 1)) << 4) + 8 * (pp & 1);
      tw[i] = TRB + w * 8192 + l15 * 256 + (((4 * i + q) ^ l15) << 4);
      trd[i] = TRB + w * 8192 + (4 * i + q) * 256 + ((l15 ^ (4 * i + q)) << 4);
      asm volatile("" : "+v"(og[i]), "+v"(ao[i]), "+v"(tw[i]), "+v"(trd[i]));
    }
    unsigned so_ = ((tile * 128 + w * 32 + q) * 1024 + cg * 64 + l15 * 4) * 4, ex = TRB + th * 16384 + hp * 1024 + lane * 16;
    unsigned sk = SCR + hp * 2048 + lane * 16, sz_ = SCR + 8192 + hp * 1024 + lane * 8, sw_ = SCR + 4096 + w * 1024 + lane * 16;
    unsigned pk_ = WS_KV + ((cg * 4 + w) * 16 + tile) * 1024 + lane * 16, pz_ = l15 == 0 ? WS_Z + ((cg * 4 + w) * 16 + tile) * 128 + q * 16 : OOB;
    unsigned pf_ = lane == 0 ? WS_FA + ((tile * 2 + th) * 16 + cg * 2 + hp) * 128 : OOB;
    asm volatile("" : "+v"(so_), "+v"(ex), "+v"(sk), "+v"(sz_), "+v"(sw_), "+v"(pk_), "+v"(pz_), "+v"(pf_));
    f4 ka[8], va[8];
#pragma unroll
    for (int i = 0; i < 8; ++i) { ka[i] = (f4){EPS, EPS, EPS, EPS}; va[i] = (f4){0, 0, 0, 0}; }
    bf8 X0[4], X1[4], W0[2], W1[2];
    auto rdx = [&](bf8 (&X)[4], int kk, int ks, unsigned off) {
#pragma unroll
      for (int i = 0; i < 4; ++i) X[i] = ldsr(at(L, wx[ks], kk * 16384 + off + i * 2048));
    };
    auto rdw = [&](bf8 (&W)[2], int kk, int ks) {
#pragma unroll
      for (int i = 0; i < 2; ++i) { const unsigned o = WBUF[kk] + ks * 4096 + i * 8192; W[i] = ldstr(at(L, aw, o), at(L, aw, o + 512)); }
    };
    auto mm = [&](const bf8 (&X)[4], const bf8 (&W)[2], int h) {
#pragma unroll
      for (int i = 0; i < 4; ++i) {
        ka[4 * h + i] = mfma16(X[i], W[0], ka[4 * h + i]);
        va[4 * h + i] = mfma16(X[i], W[1], va[4 * h + i]);
      }
    };
#define ILV(nr, nm) do { _Pragma("unroll") for (int i_ = 0; i_ < nr; ++i_) { SG(0x008, 1); SG(0x100, 1); } if (nm > nr) SG(0x008, nm - nr); } while (0)
    asm volatile("s_barrier" :: "v"(warm0), "v"(warm1), "v"(cpf) : "memory");
    const unsigned tag = epoch + 1, t = tag & 0xffu;
    rdw(W0, 0, 0); rdx(X0, 0, 0, 0);
    SB();
#pragma unroll
    for (int kk = 0; kk < 8; ++kk) {
      rdx(X1, kk, 0, 8192); mm(X0, W0, 0); ILV(4, 8); SB(); PACE(PACE_A);
      rdw(W1, kk, 1); rdx(X0, kk, 1, 0); mm(X1, W0, 1); ILV(8, 8); SB(); PACE(PACE_A);
      rdx(X1, kk, 1, 8192); mm(X0, W1, 0); ILV(4, 8); SB();
      if (kk < 7) { lbar(); rdw(W0, kk + 1, 0); rdx(X0, kk + 1, 0, 0); mm(X1, W1, 1); ILV(8, 8); }
      else mm(X1, W1, 1);
      SB();
    }
    bf8 XQ[3][4];
    s4 WH[3][4];
    auto rdq = [&](int s, int i) {
      const unsigned o = QBUF[s >> 2] + ((s >> 1) & 1) * 8192 + (s & 1) * 4096;
      if (i < 4) WH[s % 3][i] = __builtin_amdgcn_ds_read_tr16_b64_v4i16((LAS s4*)at(L, i < 2 ? aq0 : aq1, o + (i & 1) * 512));
      else XQ[s % 3][i - 4] = ldsr(at(L, wq[s & 1], (s < 14 ? (s >> 1) + 1 : 1) * 16384 + (i - 4) * 2048));
    };
    {
      f4 kv = {0, 0, 0, 0}, zz = {0, 0, 0, 0};
      const u4 ones = {0x3f803f80u, 0x3f803f80u, 0x3f803f80u, 0x3f803f80u};
#pragma unroll
      for (int m = 0; m < 4; ++m) {
        const f4 a = ka[2 * m], b = ka[2 * m + 1], c = va[2 * m], d = va[2 * m + 1];
        const u4 pa = {phi2(a[0], a[1]), phi2(a[2], a[3]), phi2(b[0], b[1]), phi2(b[2], b[3])};
        const u4 pb = {pk2(c[0], c[1]), pk2(c[2], c[3]), pk2(d[0], d[1]), pk2(d[2], d[3])};
        kv = mfma16(asbf8(pa), asbf8(pb), kv);
        zz = mfma16(asbf8(pa), asbf8(ones), zz);
      }
      const u4 skv = {tagv(kv[0], t), __float_as_uint(kv[1]), tagv(kv[2], t), __float_as_uint(kv[3])};
      __builtin_amdgcn_raw_buffer_store_b128(skv, ws, pk_, 0, SC1);
      const u4 sz = {tagv(zz[0], t), __float_as_uint(zz[1]), tagv(zz[2], t), __float_as_uint(zz[3])};
      __builtin_amdgcn_raw_buffer_store_b128(sz, ws, pz_, 0, SC1);
    }
    SB();
    lbar();
#pragma unroll
    for (int i = 0; i < 16; ++i) rdq(i >> 3, i & 7);
    SB();
    f4 qa[2][4];
#pragma unroll
    for (int i = 0; i < 8; ++i) qa[i >> 2][i & 3] = (f4){EPS, EPS, EPS, EPS};
    u4 gc[8];
    const unsigned gb = WS_KV + (cg * 4 + w) * 16384 + lane * 16, g2 = (((jj >> 3) + 2) & 3) * 4096, g3 = (((jj >> 3) + 3) & 3) * 4096;
    auto ldg = [&](int i) { gc[i] = __builtin_amdgcn_raw_buffer_load_b128(ws, gb + (i & 3) * 1024, i < 4 ? g2 : g3, SC1); };
    asm volatile("s_nop 1" : "+v"(qa[0][0]), "+v"(qa[0][1]), "+v"(qa[0][2]), "+v"(qa[0][3]), "+v"(qa[1][0]), "+v"(qa[1][1]), "+v"(qa[1][2]), "+v"(qa[1][3]));
#pragma unroll
    for (int s = 0; s < 16; ++s) {
      if (s == 2) asm volatile("s_waitcnt lgkmcnt(4)\n\ts_barrier" ::: "memory");
      if (s == 6 || s == 10) asm volatile("s_barrier" ::: "memory");
      bf8 W[2] = {__builtin_bit_cast(bf8, (s8)__builtin_shufflevector(WH[s % 3][0], WH[s % 3][1], 0, 1, 2, 3, 4, 5, 6, 7)),
                  __builtin_bit_cast(bf8, (s8)__builtin_shufflevector(WH[s % 3][2], WH[s % 3][3], 0, 1, 2, 3, 4, 5, 6, 7))};
      asm volatile("" : "+v"(W[0]), "+v"(W[1]), "+v"(XQ[s % 3][0]), "+v"(XQ[s % 3][1]), "+v"(XQ[s % 3][2]), "+v"(XQ[s % 3][3]));
#pragma unroll
      for (int i = 0; i < 8; ++i) {
        mfa(qa[i & 1][i >> 1], W[i & 1], XQ[s % 3][i >> 1]); SB();
        if (s < 14) { rdq(s + 2, i); SB(); }
        if (s >= 10 && s < 14 && (i == 2 || i == 6)) { ldg(2 * (s - 10) + (i >> 2)); SB(); }
      }
    }
    asm volatile("s_nop 7" : "+v"(qa[0][0]), "+v"(qa[0][1]), "+v"(qa[0][2]), "+v"(qa[0][3]), "+v"(qa[1][0]), "+v"(qa[1][1]), "+v"(qa[1][2]), "+v"(qa[1][3]));
    u2 pq[2][4];
#pragma unroll
    for (int i = 0; i < 8; ++i) {
      pq[i >> 2][i & 3] = (u2){phi2(qa[i >> 2][i & 3][0], qa[i >> 2][i & 3][1]), phi2(qa[i >> 2][i & 3][2], qa[i >> 2][i & 3][3])};
      asm volatile("" : "+v"(pq[i >> 2][i & 3]));
    }
    SB();
    {
      unsigned bad = 0;
      f4 kvp = {0, 0, 0, 0};
#pragma unroll
      for (int i = 0; i < 8; ++i) { gc[i].x ^= t; gc[i].z ^= t; bad |= gc[i].x | gc[i].z; kvp += __builtin_bit_cast(f4, gc[i]); }
      if (__builtin_expect(!__all((bad & 0xffu) == 0), 0)) {
#pragma unroll 1
        for (unsigned rep = 0; rep < SPIN_CAP; ++rep) {
          asm volatile("" ::: "memory");
          bad = 0;
          kvp = (f4){0, 0, 0, 0};
#pragma unroll
          for (int i = 0; i < 8; ++i) ldg(i);
#pragma unroll
          for (int i = 0; i < 8; ++i) { gc[i].x ^= t; gc[i].z ^= t; bad |= gc[i].x | gc[i].z; kvp += __builtin_bit_cast(f4, gc[i]); }
          asm volatile("" : "+v"(kvp));
          if (__all((bad & 0xffu) == 0)) break;
        }
      }
      *(LAS f4*)(L + sw_) = kvp;
    }
    lbar();
    const f4 zero4 = {0, 0, 0, 0}, eps4 = {EPS, EPS, EPS, EPS};
    f4 num[2][4], den[2][4];
    const f4 ka0 = *(const LAS f4*)(L + sk), kb0 = *(const LAS f4*)(L + sk + 4096), ka1 = *(const LAS f4*)(L + sk + 1024), kb1 = *(const LAS f4*)(L + sk + 5120);
    const u2 zb[2] = {*(const LAS u2*)(L + sz_), *(const LAS u2*)(L + sz_ + 512)};
    SB();
    const f4 k0 = ka0 + kb0, k1 = ka1 + kb1;
    const u2 kb2[2] = {{pk2(k0[0], k0[1]), pk2(k0[2], k0[3])}, {pk2(k1[0], k1[1]), pk2(k1[2], k1[3])}};
#pragma unroll
    for (int i = 0; i < 8; ++i) { den[i & 1][i >> 1] = mfma16k16(zb[i & 1], pq[i & 1][i >> 1], eps4); num[i & 1][i >> 1] = mfma16k16(kb2[i & 1], pq[i & 1][i >> 1], zero4); }
    SB();
#pragma unroll
    for (int i = 0; i < 4; ++i) {
      u4 so;
#pragma unroll
      for (int h = 0; h < 2; ++h) {
        const float rc = __builtin_amdgcn_rcpf(den[h][i][0]);
        const f4 n = num[h][i];
        so[2 * h] = pk2((f2){n[0], n[1]} * rc);
        so[2 * h + 1] = pk2((f2){n[2], n[3]} * rc);
      }
      __builtin_amdgcn_raw_buffer_store_b128(so, ws, og[i], 0, SC1);
      *(LAS u4*)(L + ex + (i >> 1) * 8192 + (i & 1) * 2048) = so;
    }
    lbar();
    u4 of[32];
    unsigned ah[4];
#pragma unroll
    for (int i = 0; i < 4; ++i) { ah[i] = ao[i] + 65536; asm volatile("" : "+v"(ah[i])); }
#pragma unroll
    for (int i = 0; i < 4; ++i) of[i] = *(const LAS u4*)(L + TRB + w * 8192 + ((i & 1) * 2 + (i >> 1)) * 1024 + lane * 16);
    drain();
    __builtin_amdgcn_raw_buffer_store_b64((u2){tag, ~tag}, ws, pf_, 0, SC1);
    asm volatile("s_barrier" ::: "memory");
    f4 acc[2][4];
#pragma unroll
    for (int i = 0; i < 8; ++i) acc[i >> 2][i & 3] = zero4;
    unsigned ob = WS_O + (tile * 8 + w * 2) * 16384 + lane * 16, ob2 = ob + 16384;
    asm volatile("" : "+v"(ob), "+v"(ob2));
    bf8 wf[2][4];
    auto rdo = [&](int k) {
#pragma unroll
      for (int nt = 0; nt < 4; ++nt) { const unsigned o = OBUF[k >> 2] + (k & 3) * 4096; const char LAS* p = L + (o < 65536 ? ao[nt] : ah[nt]) + (o & 65535); wf[k & 1][nt] = ldstr(p, p + 2048); }
    };
    auto ldo = [&](int k) {
      const unsigned so = sq[k >> 1] * 2048;
      of[2 * k] = __builtin_amdgcn_raw_buffer_load_b128(ws, ob + (k & 1) * 1024, so, SC1);
      of[2 * k + 1] = __builtin_amdgcn_raw_buffer_load_b128(ws, ob2 + (k & 1) * 1024, so, SC1);
    };
    auto mo = [&](int k) {
#pragma unroll
      for (int nt = 0; nt < 4; ++nt) {
        acc[0][nt] = mfma16(wf[k & 1][nt], asbf8(of[2 * k]), acc[0][nt]);
        acc[1][nt] = mfma16(wf[k & 1][nt], asbf8(of[2 * k + 1]), acc[1][nt]);
      }
    };
    rdo(0); rdo(1); SB();
    mo(0); rdo(2); SB();
    mo(1); SB();
    wait_flags(ws, WS_FA + ((tile * 2 + th) * 16 + l15) * 128, tag);
#pragma unroll
    for (int k = 2; k < 2 + OD; ++k) ldo(k);
    SB();
#pragma unroll
    for (int k = 2; k < 16; ++k) {
      if (k + OD < 16) ldo(k + OD);
      if (k + 1 < 16) rdo(k + 1);
      mo(k);
#pragma unroll
      for (int i_ = 0; i_ < 8; ++i_) { SG(0x008, 1); SG(0x100, 1); if (i_ == 1 || i_ == 5) SG(0x020, 1); }
      SB();
    }
#pragma unroll
    for (int mt = 0; mt < 2; ++mt)
#pragma unroll
      for (int nt = 0; nt < 4; ++nt) {
        const f4 v = acc[mt][nt];
        const unsigned p0 = pk2(v[0], v[1]), p1 = pk2(v[2], v[3]);
        *(LAS f4*)(L + tw[nt] + mt * 4096) = (f4){bflo(p0), bfhi(p0), bflo(p1), bfhi(p1)};
      }
    u4 ov[8];
#pragma unroll
    for (int i = 0; i < 8; ++i) ov[i] = *(const LAS u4*)(L + trd[i & 3] + (i >> 2) * 4096);
    SB();
#pragma unroll
    for (int i = 0; i < 8; ++i) __builtin_amdgcn_raw_buffer_store_b128(ov[i], ro, so_, i * 16384, SC1);
    asm volatile(".Lcode_lastA:");
  } else {
    constexpr unsigned SLOT = 32768, SCR = SLOT, PAR = 4 * SLOT;
    const int wp = w & 1, wt = w >> 1;
    const unsigned ecol = WS_EST + (cg * 64 + wp * 32 + l31) * 8;
    if (helper) {
      unsigned vx[4], vw[4], vc[4];
#pragma unroll
      for (int i = 0; i < 4; ++i) {
        const unsigned sw = (c8 ^ ((4 * i + (r8 >> 1)) & 7)) << 4;
        vx[i] = (64 * (w >> 1) + 32 * (i & 1) + 16 * (r8 >> 2) + 8 * (r8 & 1) + 4 * (w & 1) + 2 * (i >> 1) + ((r8 >> 1) & 1)) * 1024 + sw - 1024 * i;
        vw[i] = (16 * (i & 1) + 8 * (r8 >> 2) + 4 * (i >> 1) + (r8 & 3)) * 1024 + sw - 1024 * i;
        vc[i] = (8 * i + r8) * 1024 + sw - 1024 * i;
      }
      const GAS char* xbase = (const GAS char*)xb + (size_t)tile * 131072;
      const GAS char* wbase = (const GAS char*)((w & 1) ? bim : bre) + (cg * 64 + (w >> 1) * 32) * 1024;
      auto issue = [&](int kk) {
        const int kc = (kk + rot) & 7;
        const unsigned s = L0 + (kk % 5) * SLOT + w * 4096;
        dma4(s, xbase + kc * 128, vx[0], vx[1], vx[2], vx[3]);
        dma4(s + 16384, wbase + kc * 128, vw[0], vw[1], vw[2], vw[3]);
      };
      issue(0); issue(1);
      LOAD_EPOCH();
      const GAS char* cbase = (const GAS char*)((w & 1) ? cim : cre) + cg * 65536;
      auto issue_c = [&](int piece) {
        const unsigned s = L0 + ((piece + 3) % 5) * SLOT + w * 8192;
        const GAS char* cb = cbase + seq(2 * piece + (w >> 1)) * 128;
        dma4(s, cb, vc[0], vc[1], vc[2], vc[3]);
        dma4(s + 4096, cb + 32768, vc[0], vc[1], vc[2], vc[3]);
      };
      wait_bar<8>();
      issue(2);
      const f4 par = *(const LAS f4*)(L + PAR + w * 1024 + lane * 16);
      const f2 l64 = {par[0], par[1]}, l128 = {par[2], par[3]};
      f2 l256 = cmul(l128, l128);
      f2 wgt = hh ? l64 : (f2){1.f, 0.f};
      if (wt) wgt = cmul(wgt, l128);
      const unsigned tag = epoch + 1, t = tag & 0xffu;
      unsigned eo[8], et[8];
#pragma unroll
      for (int m = 0; m < 8; ++m) {
        const int i = 2 * tile - 1 - (2 * wt + hh) - 4 * m;
        eo[m] = i >= 0 ? ecol + i * 4096 : OOB;
        et[m] = i >= 0 ? t : 0u;
        asm volatile("" : "+v"(eo[m]), "+v"(et[m]));
      }
      asm volatile("" : "+v"(l256), "+v"(wgt));
      wait_bar<8>(); issue(3);
      wait_bar<8>(); issue(4);
      wait_bar<8>(); issue(5);
      wait_bar<8>(); issue(6);
      wait_bar<8>(); issue(7);
      wait_bar<8>(); issue_c(0);
      wait_bar<8>();
      issue_c(1);
      asm volatile("s_barrier" ::: "memory");
      __builtin_amdgcn_s_sleep(14);
      __builtin_amdgcn_s_setprio(3);
      f2 cin;
#pragma unroll 1
      for (unsigned sp = 0; sp < SPIN_CAP; ++sp) {
        asm volatile("" ::: "memory");
        u2 g[8];
#pragma unroll
        for (int m = 0; m < 8; ++m) g[m] = __builtin_amdgcn_raw_buffer_load_b64(ws, eo[m], 0, SC1);
        unsigned bad = 0;
        cin = (f2){0, 0};
#pragma unroll
        for (int m = 7; m >= 0; --m) {
          const unsigned cx = g[m].x ^ et[m];
          bad |= cx;
          cin = cfma(l256, cin, (f2){__uint_as_float(cx), __uint_as_float(g[m].y)});
        }
        if (__all((bad & 0xffu) == 0)) break;
      }
      cin = cmul(cin, wgt);
      f2 chi;
      const f2 clo = swap_lo(cin, chi);
      if (hh == 0) *(LAS f2*)(L + SCR + (wp * 2 + wt) * 256 + l31 * 8) = clo + chi;
      lbar();
      issue_c(2);
      lbar();
      issue_c(3);
      wait_bar<0>();
      return;
    }
    const int pl = cg * 64 + wp * 32 + 8 * (l15 >> 2) + 4 * (q & 1) + (l15 & 3);
    const unsigned plr = lre[pl], pli = lim[pl], pdt = ldt[pl];
    u4 dv[2];
#pragma unroll
    for (int i = 0; i < 2; ++i) dv[i] = *(const GAS u4*)(dsk + cg * 64 + 32 * i + 8 * q);
    unsigned warm0, warm1;
    {
      warm0 = __builtin_amdgcn_raw_buffer_load_b32(ws, lane < 16 ? WS_FB + ((tile * 2 + wt) * 16 + lane) * 128 : lane == 16 ? WS_O - 128
                                                   : lane == 17 ? WS_H + (tile * 4 + w) * 65536 : OOB, 0, SC1);
      warm1 = __builtin_amdgcn_raw_buffer_load_b32(ro, lane < 2 ? (tile * 128 + w * 32 + lane * 16) * 4096 + 2048 : OOB, 0, SC1);
    }
    LOAD_EPOCH();
    SB();
    f2 lam[2], l8[2], kap, l16, l32, l64;
    {
      const float lr = bflo(plr), li = bflo(pli);
      const float dt = bflo(pk2(expf(bflo(pdt)), 0.f));
      const float mag = expf(dt * lr);
      float sn, cs;
      sincosf(dt * li, &sn, &cs);
      const f2 l1 = {mag * cs, mag * sn};
      const float inv = 2.f / (lr * lr + li * li), nr = l1.x - 1.f, ni = l1.y;
      kap = (f2){(nr * lr + ni * li) * inv, (ni * lr - nr * li) * inv};
      f2 t = cmul(l1, l1); t = cmul(t, t); t = cmul(t, t);
      l16 = cmul(t, t); l32 = cmul(l16, l16); l64 = cmul(l32, l32);
      const f2 l128 = cmul(l64, l64);
      *(LAS f4*)(L + PAR + w * 1024 + lane * 16) = (f4){l64.x, l64.y, l128.x, l128.y};
      lam[0] = swap16(l1, l1, lam[1]);
      l8[0] = swap16(t, t, l8[1]);
    }
    f2 R0 = {lam[0].x, lam[0].x}, I0 = {lam[0].y, lam[0].y}, N0 = -I0, R1 = {lam[1].x, lam[1].x}, I1 = {lam[1].y, lam[1].y}, N1 = -I1;
    unsigned pe_ = hh == 0 ? ecol + (tile * 2 + wt) * 4096 : OOB, se_ = SCR + 1024 + (wt * 2 + wp) * 256 + hh * 1024 + l31 * 8;
    asm volatile("" : "+v"(R0), "+v"(I0), "+v"(N0), "+v"(R1), "+v"(I1), "+v"(N1), "+v"(pe_), "+v"(se_));
    const unsigned ax[2] = {(unsigned)(l15 * 128 + ((q ^ (l15 >> 1)) << 4)), (unsigned)(l15 * 128 + (((4 + q) ^ (l15 >> 1)) << 4))};
    const Win wx[2] = {mkwin(ax[0] + wt * 8192), mkwin(ax[1] + wt * 8192)}, ww[2] = {mkwin(ax[0] + wp * 8192), mkwin(ax[1] + wp * 8192)};
    MKSQ();
    f4 u[2][2][4];
#pragma unroll
    for (int i = 0; i < 16; ++i) u[i >> 3][(i >> 2) & 1][i & 3] = (f4){0, 0, 0, 0};
    u4 xd[2][2];
#pragma unroll
    for (int i = 0; i < 4; ++i) xd[i >> 1][i & 1] = (u4){0, 0, 0, 0};
    const int kx = (cg - rot) & 7;
    bf8 Xa[2], Xb[2], W0[4], W1[4];
    auto rdx = [&](bf8 (&X)[2], int kk, int ks, int h) {
#pragma unroll
      for (int i = 0; i < 2; ++i) X[i] = ldsr(at(L, wx[ks], (kk % 5) * SLOT + (2 * h + i) * 2048));
    };
    auto rdw = [&](bf8 (&W)[4], int kk, int ks) {
#pragma unroll
      for (int i = 0; i < 4; ++i) W[i] = ldsr(at(L, ww[ks], (kk % 5) * SLOT + 16384 + (i & 1) * 4096 + (i >> 1) * 2048));
    };
    auto mm = [&](const bf8 (&X)[2], const bf8 (&W)[4], int h) {
#pragma unroll
      for (int i = 0; i < 2; ++i)
#pragma unroll
        for (int j = 0; j < 4; ++j) u[j >> 1][j & 1][2 * h + i] = mfma16(X[i], W[j], u[j >> 1][j & 1][2 * h + i]);
    };
    asm volatile("s_waitcnt lgkmcnt(0)\n\ts_barrier" : "+v"(lam[0]), "+v"(lam[1]), "+v"(l8[0]), "+v"(l8[1]), "+v"(kap), "+v"(l16), "+v"(l32), "+v"(l64),
                 "+v"(dv[0]), "+v"(dv[1]) : "v"(warm0), "v"(warm1), "v"(cpf) : "memory");
    const unsigned tag = epoch + 1, t = tag & 0xffu;
    rdw(W0, 0, 0); rdx(Xa, 0, 0, 0);
    SB();
#pragma unroll
    for (int kk = 0; kk < 8; ++kk) {
      rdx(Xb, kk, 0, 1); mm(Xa, W0, 0); ILV(2, 8); SB(); PACE(PACE_B);
      rdw(W1, kk, 1); rdx(Xa, kk, 1, 0); mm(Xb, W0, 1); ILV(6, 8); SB(); PACE(PACE_B);
      if (kk == kx) {
#pragma unroll
        for (int i = 0; i < 4; ++i) xd[i >> 1][i & 1] = *(const LAS u4*)(at(L, wx[i & 1], (kk % 5) * SLOT + (i >> 1) * 2048) + (w & 1) * 4096);
      }
      SB();
      rdx(Xb, kk, 1, 1); mm(Xa, W1, 0); ILV(2, 8); SB();
      if (kk < 7) { lbar(); rdw(W0, kk + 1, 0); rdx(Xa, kk + 1, 0, 0); mm(Xb, W1, 1); ILV(6, 8); }
      else mm(Xb, W1, 1);
      SB(); if (kk < 7) PACE(PACE_B);
    }
    f2 gr[2][8], gi[2][8];
#pragma unroll
    for (int i = 0; i < 16; ++i) {
      const int pt = i >> 3, a = i & 7;
      gr[pt][a] = (f2){u[pt][0][a >> 1][2 * (a & 1)], u[pt][0][a >> 1][2 * (a & 1) + 1]};
      gi[pt][a] = (f2){u[pt][1][a >> 1][2 * (a & 1)], u[pt][1][a >> 1][2 * (a & 1) + 1]};
    }
    {
#pragma unroll
      for (int a = 1; a < 8; ++a)
        asm volatile("v_pk_fma_f32 %0, %10, %5, %0\n\tv_pk_fma_f32 %1, %9, %4, %1\n\tv_pk_fma_f32 %2, %13, %7, %2\n\tv_pk_fma_f32 %3, %12, %6, %3\n\t"
                     "v_pk_fma_f32 %0, %8, %4, %0\n\tv_pk_fma_f32 %1, %8, %5, %1\n\tv_pk_fma_f32 %2, %11, %6, %2\n\tv_pk_fma_f32 %3, %11, %7, %3"
                     : "+v"(gr[0][a]), "+v"(gi[0][a]), "+v"(gr[1][a]), "+v"(gi[1][a])
                     : "v"(gr[0][a - 1]), "v"(gi[0][a - 1]), "v"(gr[1][a - 1]), "v"(gi[1][a - 1]), "v"(R0), "v"(I0), "v"(N0), "v"(R1), "v"(I1), "v"(N1));
    }
    const f2 e0[2] = {{gr[0][7].x, gi[0][7].x}, {gr[1][7].x, gi[1][7].x}};
    f2 vodd;
    const f2 veven = swap16(cfma(l8[0], e0[0], (f2){gr[0][7].y, gi[0][7].y}), cfma(l8[1], e0[1], (f2){gr[1][7].y, gi[1][7].y}), vodd);
    f2 ehi;
    const f2 elo = swap_lo(cfma(l16, veven, vodd), ehi);
    const f2 e64 = cfma(l32, elo, ehi);
    __builtin_amdgcn_raw_buffer_store_b64((u2){tagv(e64.x, t), __float_as_uint(e64.y)}, ws, pe_, 0, SC1);
    *(LAS f2*)(L + se_) = e64;
    lbar();
    const f2 esib = *(const LAS f2*)(L + SCR + 1024 + wp * 256 + l31 * 8);
    SB();
    f2 kp[2], ke[2], pw[2][8];
    kp[0] = swap16(kap, kap, kp[1]);
#pragma unroll
    for (int pt = 0; pt < 2; ++pt) {
      const f2 KR = {kp[pt].x, kp[pt].x}, KI = {kp[pt].y, kp[pt].y};
      ke[pt] = cmul(kp[pt], e0[pt]);
#pragma unroll
      for (int a = 0; a < 8; ++a) {
        const f2 xr = gr[pt][a], xi = gi[pt][a];
        gr[pt][a] = fma2(KR, xr, -KI * xi); gi[pt][a] = fma2(-KR, xi, -KI * xr);
        asm volatile("" : "+v"(gr[pt][a]), "+v"(gi[pt][a]));
      }
      pw[pt][0] = (f2){lam[pt].x, -lam[pt].y};
#pragma unroll
      for (int k = 1; k < 8; ++k) { pw[pt][k] = cmul(pw[pt][k >> 1], pw[pt][(k - 1) >> 1]); asm volatile("" : "+v"(pw[pt][k])); }
    }
    u2 idn = {(l15 == 4 * q ? 0x3f80u : 0u) | (l15 == 4 * q + 1 ? 0x3f800000u : 0u), (l15 == 4 * q + 2 ? 0x3f80u : 0u) | (l15 == 4 * q + 3 ? 0x3f800000u : 0u)};
#pragma unroll
    for (int i = 0; i < 4; ++i) {
#pragma unroll
      for (int j = 0; j < 4; ++j) xd[i >> 1][i & 1][j] = pk2(bflo(xd[i >> 1][i & 1][j]) * bflo(dv[i & 1][j]), bfhi(xd[i >> 1][i & 1][j]) * bfhi(dv[i & 1][j]));
      asm volatile("" : "+v"(xd[i >> 1][i & 1]));
    }
    f2 me = hh ? cmul(kap, l32) : kap, be = hh ? cmul(kap, elo) : (f2){0.f, 0.f};
    f2 mo = cmul(l16, me), bo = cfma(l16, be, cmul(kap, veven));
    if (wt) { be = cfma(me, esib, be); bo = cfma(mo, esib, bo); me = cmul(me, l64); mo = cmul(mo, l64); }
    unsigned pf_ = lane == 0 ? WS_FB + ((tile * 2 + wt) * 16 + cg * 2 + wp) * 128 : OOB, ex = 2 * SLOT + wt * 16384 + wp * 4096 + lane * 16;
    asm volatile("" : "+v"(pf_), "+v"(ex));
    unsigned sc_ = SCR + wp * 512 + l31 * 8, ho[4];
#pragma unroll
    for (int i = 0; i < 4; ++i) ho[i] = WS_H + (tile * 8 + wt * 4 + i) * 32768 + (cg * 2 + wp) * 2048 + lane * 16;
    asm volatile("s_waitcnt lgkmcnt(0)\n\ts_barrier" : "+v"(ho[0]), "+v"(ho[1]), "+v"(ho[2]), "+v"(ho[3]), "+v"(me), "+v"(be), "+v"(mo), "+v"(bo),
                 "+v"(ke[0]), "+v"(ke[1]), "+v"(idn), "+v"(sc_) :: "memory");
    const f2 cin = *(const LAS f2*)(L + sc_) + *(const LAS f2*)(L + sc_ + 256);
    f2 d0[2], DR[2], DJ[2];
    d0[0] = swap16(cfma(me, cin, be), cfma(mo, cin, bo), d0[1]);
#pragma unroll
    for (int pt = 0; pt < 2; ++pt) {
      const f2 d1 = cfma(l8[pt], d0[pt], ke[pt]);
      DR[pt] = (f2){d0[pt].x, d1.x}; DJ[pt] = (f2){-d0[pt].y, -d1.y};
    }
    const f4 zero4 = {0, 0, 0, 0};
    f4 T[8][2];
    u4 ow[8];
    auto fix = [&](int i) {
      const int mt = i >> 1, c = i & 1;
#pragma unroll
      for (int pt = 0; pt < 2; ++pt) {
        u2 a;
#pragma unroll
        for (int h = 0; h < 2; ++h) {
          const f2 P = pw[pt][2 * mt + h], PR = {P.x, P.x}, PI = {P.y, P.y};
          a[h] = pk2(c ? fma2(PI, DR[pt], fma2(PR, DJ[pt], gi[pt][2 * mt + h])) : fma2(-PI, DJ[pt], fma2(PR, DR[pt], gr[pt][2 * mt + h])));
        }
        T[i][pt] = mfma16k16(a, idn, zero4);
      }
    };
    auto put = [&](int i) {
      ow[i] = (u4){pk2(T[i][0][0], T[i][0][1]), pk2(T[i][0][2], T[i][0][3]), pk2(T[i][1][0], T[i][1][1]), pk2(T[i][1][2], T[i][1][3])};
      __builtin_amdgcn_raw_buffer_store_b128(ow[i], ws, ho[i >> 1] + (i & 1) * 1024, 0, SC1);
    };
    fix(0); SB();
#pragma unroll
    for (int i = 1; i < 8; ++i) { fix(i); SB(); put(i - 1); SB(); }
    put(7);
    SB();
#pragma unroll
    for (int i = 0; i < 8; ++i) *(LAS u4*)(L + ex + (i >> 2) * 8192 + (i & 1) * 2048 + ((i >> 1) & 1) * 1024) = ow[i];
    lbar();
    drain();
    __builtin_amdgcn_raw_buffer_store_b64((u2){tag, ~tag}, ws, pf_, 0, SC1);
    unsigned hb = WS_H + (tile * 8 + w * 2) * 32768 + lane * 16, hb2 = hb + 32768;
    asm volatile("" : "+v"(hb), "+v"(hb2));
    const Win wc[2] = {mkwin(ax[0]), mkwin(ax[1])};
    u4 hf[64];
    bf8 cf[2][4];
    f4 acc[2][4];
    auto ldh = [&](int g) {
      hf[2 * g] = __builtin_amdgcn_raw_buffer_load_b128(ws, hb + (g & 3) * 1024, sq[g >> 2] * 4096, SC1);
      hf[2 * g + 1] = __builtin_amdgcn_raw_buffer_load_b128(ws, hb2 + (g & 3) * 1024, sq[g >> 2] * 4096, SC1);
    };
    auto rdc = [&](int g) {
      const int tt = g >> 2;
#pragma unroll
      for (int nt = 0; nt < 4; ++nt)
        cf[g & 1][nt] = ldsr(at(L, wc[(g >> 1) & 1], (((tt >> 1) + 3) % 5) * SLOT + (tt & 1) * 16384 + (g & 1) * 8192 + nt * 2048));
    };
    auto mh = [&](int g) {
#pragma unroll
      for (int nt = 0; nt < 4; ++nt) {
        acc[0][nt] = mfma16(cf[g & 1][nt], asbf8(hf[2 * g]), acc[0][nt]);
        acc[1][nt] = mfma16(cf[g & 1][nt], asbf8(hf[2 * g + 1]), acc[1][nt]);
      }
    };
#pragma unroll
    for (int i = 0; i < 8; ++i) hf[i] = *(const LAS u4*)(L + 2 * SLOT + w * 8192 + i * 1024 + lane * 16);
    rdc(0);
    {
      u4 ida[2];
#pragma unroll
      for (int hf_ = 0; hf_ < 2; ++hf_)
#pragma unroll
        for (int jq = 0; jq < 4; ++jq) {
          const int k0 = 8 * q + 2 * jq - 16 * hf_;
          ida[hf_][jq] = (l15 == k0 ? 0x3f80u : 0u) | (l15 == k0 + 1 ? 0x3f800000u : 0u);
        }
#pragma unroll
      for (int i = 0; i < 8; ++i) acc[i >> 2][i & 3] = mfma16(asbf8(ida[i & 1]), asbf8(xd[i >> 2][(i >> 1) & 1]), zero4);
    }
    SB();
#pragma unroll
    for (int g = 0; g < 4; ++g) { rdc(g + 1); mh(g); ILV(4, 8); SB(); }
    unsigned tw[4], trd[4], so_ = ((tile * 128 + 64 * wt + 4 * (w & 1) + 8 * (q & 1) + (q >> 1)) * 1024 + 512 + cg * 64 + l15 * 4) * 4;
#pragma unroll
    for (int i = 0; i < 4; ++i) {
      tw[i] = 2 * SLOT + w * 8192 + l15 * 256 + (((4 * i + q) ^ l15) << 4);
      trd[i] = 2 * SLOT + w * 8192 + (4 * i + q) * 256 + ((l15 ^ (4 * i + q)) << 4);
      asm volatile("" : "+v"(tw[i]), "+v"(trd[i]));
    }
    asm volatile("s_barrier" : "+v"(so_) :: "memory");
    wait_flags(ws, WS_FB + ((tile * 2 + wt) * 16 + l15) * 128, tag);
#pragma unroll
    for (int g = 4; g < 4 + HD; ++g) ldh(g);
    SB();
#pragma unroll
    for (int g = 4; g < 32; ++g) {
      if (g + HD < 32) ldh(g + HD);
      if (g + 1 < 32) rdc(g + 1);
      mh(g);
#pragma unroll
      for (int i_ = 0; i_ < 4; ++i_) { SG(0x008, 1); SG(0x100, 1); }
      SG(0x008, 1); SG(0x020, 1); SG(0x008, 1); SG(0x020, 1); SG(0x008, 2);
      SB();
    }
#pragma unroll
    for (int i = 0; i < 8; ++i) *(LAS f4*)(L + tw[i & 3] + (i >> 2) * 4096) = acc[i >> 2][i & 3];
    u4 ov[8];
#pragma unroll
    for (int i = 0; i < 8; ++i) ov[i] = *(const LAS u4*)(L + trd[i & 3] + (i >> 2) * 4096);
    SB();
#pragma unroll
    for (int i = 0; i < 8; ++i) __builtin_amdgcn_raw_buffer_store_b128(ov[i], ro, so_, (16 * (i & 3) + 2 * (i >> 2)) * 4096, SC1);
    asm volatile(".Lcode_lastB:");
  }
}

extern "C" void kernel_launch(void* const* d_in, const int* in_sizes, int n_in, void* d_out, int out_size, void* d_ws, size_t ws_size,
                              hipStream_t stream) {
  (void)hipFuncSetAttribute((const void*)fused_kernel, hipFuncAttributeMaxDynamicSharedMemorySize, LDS_BYTES);
  fused_kernel<<<256, 512, LDS_BYTES, stream>>>((const u16*)d_in[0], (const u16*)d_in[1], (const u16*)d_in[2], (const u16*)d_in[3],
                                               (const u16*)d_in[4], (const u16*)d_in[5], (const u16*)d_in[6], (const u16*)d_in[7],
                                               (const u16*)d_in[8], (const u16*)d_in[9], (const u16*)d_in[10], (const u16*)d_in[11],
                                               (float*)d_out, (char*)d_ws);
}
```

```cpp
#define ADLY 0
#define RA {0,16,24,28,24,8,0,0}
#define RB {0,0,8,10,8,0,0,0}
#include <hip/hip_runtime.h>
#include <hip/hip_bf16.h>

#define DI __device__ __forceinline__
#define LAS __attribute__((address_space(3)))
#define GAS __attribute__((address_space(1)))
typedef unsigned short u16;
typedef unsigned long long u64;
typedef __bf16 bf8 __attribute__((ext_vector_type(8)));
typedef __bf16 bf2 __attribute__((ext_vector_type(2)));
typedef short s4 __attribute__((ext_vector_type(4)));
typedef short s8 __attribute__((ext_vector_type(8)));
typedef float f4 __attribute__((ext_vector_type(4)));
typedef float f2 __attribute__((ext_vector_type(2)));
typedef unsigned u4 __attribute__((ext_vector_type(4)));
typedef unsigned u2 __attribute__((ext_vector_type(2)));

constexpr unsigned WS_EPOCH = 0;
constexpr unsigned WS_FA = 4096;
constexpr unsigned WS_FB = WS_FA + 65536;
constexpr unsigned WS_EST = WS_FB + 65536;
constexpr unsigned WS_Z = WS_EST + 131072;
constexpr unsigned WS_KV = WS_Z + 65536;
constexpr unsigned WS_O = 1u << 20;
constexpr unsigned WS_H = 3u << 20;
constexpr unsigned WS_END = 7u << 20;
constexpr int LDS_BYTES = 160 * 1024;
constexpr unsigned SPIN_CAP = 200000;
constexpr int SC1 = 16;

constexpr unsigned OOB = 0x80000000u;
constexpr int PACE_A[8] = RA, PACE_B[8] = RB;
constexpr int OD = 6, HD = 6;

#define SB() __builtin_amdgcn_sched_barrier(0)
#define PACE(n) do { if ((n) > 0) { asm volatile(".rept %c0\n\ts_nop 15\n\t.endr\n\ts_nop %1" :: "i"(((n) - 1) >> 4), "i"(((n) - 1) & 15)); SB(); } } while (0)
#define SG(m, n) __builtin_amdgcn_sched_group_barrier(m, n, 0)

DI unsigned pk2(float a, float b) { f2 v = {a, b}; return __builtin_bit_cast(unsigned, __builtin_convertvector(v, bf2)); }
DI unsigned pk2(f2 v) { return __builtin_bit_cast(unsigned, __builtin_convertvector(v, bf2)); }
DI float bflo(unsigned v) { return __uint_as_float(v << 16); }
DI float bfhi(unsigned v) { return __uint_as_float(v & 0xffff0000u); }
DI bf8 asbf8(u4 v) { return __builtin_bit_cast(bf8, v); }
DI f4 mfma16(bf8 a, bf8 b, f4 c) { return __builtin_amdgcn_mfma_f32_16x16x32_bf16(a, b, c, 0, 0, 0); }
DI void mfa(f4& c, bf8 a, bf8 b) { asm volatile("v_mfma_f32_16x16x32_bf16 %0, %1, %2, %0" : "+v"(c) : "v"(a), "v"(b)); }
DI f4 mfma16k16(u2 a, u2 b, f4 c) { return __builtin_amdgcn_mfma_f32_16x16x16bf16_1k(__builtin_bit_cast(s4, a), __builtin_bit_cast(s4, b), c, 0, 0, 0); }
DI bf8 ldsr(const char LAS* p) { return *(const LAS bf8*)p; }
DI bf8 ldstr(const char LAS* p0, const char LAS* p1) {
  s4 a = __builtin_amdgcn_ds_read_tr16_b64_v4i16((LAS s4*)p0), b = __builtin_amdgcn_ds_read_tr16_b64_v4i16((LAS s4*)p1);
  return __builtin_bit_cast(bf8, (s8)__builtin_shufflevector(a, b, 0, 1, 2, 3, 4, 5, 6, 7));
}
DI void dma4(unsigned lds, const GAS char* sb, unsigned v0, unsigned v1, unsigned v2, unsigned v3) {
  asm volatile("s_mov_b32 m0, %0\n\ts_nop 0\n\tglobal_load_lds_dwordx4 %1, %5\n\tglobal_load_lds_dwordx4 %2, %5 offset:1024\n\t"
               "global_load_lds_dwordx4 %3, %5 offset:2048\n\tglobal_load_lds_dwordx4 %4, %5 offset:3072"
               :: "s"(lds), "v"(v0), "v"(v1), "v"(v2), "v"(v3), "s"(sb) : "memory");
}
DI void dma2(unsigned lds, const GAS char* sb, unsigned v0, unsigned v1) {
  asm volatile("s_mov_b32 m0, %0\n\ts_nop 0\n\tglobal_load_lds_dwordx4 %1, %3\n\tglobal_load_lds_dwordx4 %2, %3 offset:1024"
               :: "s"(lds), "v"(v0), "v"(v1), "s"(sb) : "memory");
}
struct Win { unsigned b[3]; };
DI Win mkwin(unsigned a) { Win r = {{a, a + 65536, a + 131072}}; asm volatile("" : "+v"(r.b[0]), "+v"(r.b[1]), "+v"(r.b[2])); return r; }
DI const char LAS* at(const char LAS* L, const Win& a, unsigned off) { return L + a.b[off >> 16] + (off & 65535); }
DI void lbar() { asm volatile("s_waitcnt lgkmcnt(0)\n\ts_barrier" ::: "memory"); }
template <int N> DI void wait_bar() { asm volatile("s_waitcnt vmcnt(%0) lgkmcnt(0)\n\ts_barrier" :: "i"(N) : "memory"); }
DI void drain() { asm volatile("s_waitcnt vmcnt(0)" ::: "memory"); }
DI unsigned tagv(float x, unsigned t) { return (__float_as_uint(x) & 0xffffff00u) | t; }
DI f2 cmul(f2 a, f2 b) { return (f2){a.x * b.x - a.y * b.y, a.x * b.y + a.y * b.x}; }
DI f2 cfma(f2 a, f2 b, f2 c) { return (f2){fmaf(a.x, b.x, fmaf(-a.y, b.y, c.x)), fmaf(a.x, b.y, fmaf(a.y, b.x, c.y))}; }
DI f2 fma2(f2 a, f2 b, f2 c) { return __builtin_elementwise_fma(a, b, c); }
constexpr float EPS = 1e-6f, LOG2E = 1.44269504088896341f;
DI unsigned phi2(float s0, float s1) {
  const f2 a = fma2((f2){s0, s1}, (f2){LOG2E, LOG2E}, (f2){-EPS * LOG2E, -EPS * LOG2E});
  const f2 e = {__builtin_amdgcn_fmed3f(__builtin_amdgcn_exp2f(a.x), 0.f, 1.f), __builtin_amdgcn_fmed3f(__builtin_amdgcn_exp2f(a.y), 0.f, 1.f)};
  return pk2(e + (f2){__builtin_amdgcn_fmed3f(s0, EPS, 3.40282347e38f), __builtin_amdgcn_fmed3f(s1, EPS, 3.40282347e38f)});
}
DI f2 swap_lo(f2 v, f2& hi) {
  const auto x = __builtin_amdgcn_permlane32_swap(__float_as_uint(v.x), __float_as_uint(v.x), false, false);
  const auto y = __builtin_amdgcn_permlane32_swap(__float_as_uint(v.y), __float_as_uint(v.y), false, false);
  hi = (f2){__uint_as_float(x[1]), __uint_as_float(y[1])};
  return (f2){__uint_as_float(x[0]), __uint_as_float(y[0])};
}
DI f2 swap16(f2 a, f2 b, f2& rb) {
  const auto x = __builtin_amdgcn_permlane16_swap(__float_as_uint(a.x), __float_as_uint(b.x), false, false);
  const auto y = __builtin_amdgcn_permlane16_swap(__float_as_uint(a.y), __float_as_uint(b.y), false, false);
  rb = (f2){__uint_as_float(x[1]), __uint_as_float(y[1])};
  return (f2){__uint_as_float(x[0]), __uint_as_float(y[0])};
}
DI void wait_flags(__amdgpu_buffer_rsrc_t ws, unsigned off, unsigned tag) {
#pragma unroll 1
  for (unsigned sp = 0; sp < SPIN_CAP; ++sp) {
    asm volatile("" ::: "memory");
    const u2 f = __builtin_amdgcn_raw_buffer_load_b64(ws, off, 0, SC1);
    if (__all(f.x == tag && f.y == ~tag)) break;
  }
  asm volatile("" ::: "memory");
}

__global__ void __launch_bounds__(512, 2)
fused_kernel(const u16* a0, const u16* a1, const u16* a2, const u16* a3, const u16* a4, const u16* a5, const u16* a6, const u16* a7,
             const u16* a8, const u16* a9, const u16* a10, const u16* a11, float* a12, char* a13) {
  extern __shared__ __attribute__((aligned(16))) char smem_[];
  char LAS* L = (char LAS*)smem_;
  const unsigned L0 = (unsigned)(size_t)L;
  typedef u64 u64x8 __attribute__((ext_vector_type(8)));
  typedef u64 u64x4 __attribute__((ext_vector_type(4)));
  typedef u64 u64x2 __attribute__((ext_vector_type(2)));
  u64x8 ka; u64x4 kb; u64x2 kc_;
  asm volatile("s_load_dwordx16 %0, %3, 0x0\n\ts_load_dwordx8 %1, %3, 0x40\n\ts_load_dwordx4 %2, %3, 0x60"
               : "=&s"(ka), "=&s"(kb), "=&s"(kc_) : "s"(__builtin_amdgcn_kernarg_segment_ptr()) : "memory");
  SB();
  const int lane = threadIdx.x & 63;
  const int w8 = __builtin_amdgcn_readfirstlane(threadIdx.x >> 6), w = w8 & 3;
  unsigned cpf = 0;
  if (w8 == 0) {
    u64 pc; unsigned len;
    asm volatile("s_getpc_b64 %0\n.Lcode_pf%=:\n\ts_mov_b32 %1, .Lcode_lastA-.Lcode_pf%=\n\ts_max_u32 %1, %1, .Lcode_lastB-.Lcode_pf%=" : "=s"(pc), "=s"(len) :: "scc");
    cpf = __builtin_amdgcn_raw_buffer_load_b32(__builtin_amdgcn_make_buffer_rsrc((char*)(pc & ~127ull), 0, len, 0x00020000),
                                               lane < 10 ? ((blockIdx.x >> 3) * 10 + lane) * 128 : OOB, 0, 0);
  }
  const bool helper = w8 >= 4;
  const int bid = blockIdx.x, xcd = bid & 7, jj = bid >> 3;
  int tile = (xcd >> 1) * 4 + (jj >> 3), cg = jj & 7;
  int rot = (((cg + (jj >> 3)) & 1) * 4 + (xcd >> 1)) & 7;
  auto seq = [&](int t) { const int b = 2 * (jj >> 3), e = t - 1; return t == 0 ? cg : (b + e + (e >= ((cg - b) & 7) ? 1 : 0)) & 7; };
#define MKSQ() int sq[8]; _Pragma("unroll") for (int t_ = 1; t_ < 8; ++t_) { sq[t_] = __builtin_amdgcn_readfirstlane(seq(t_)); asm volatile("" : "+s"(sq[t_])); }
  int r8 = lane >> 3, c8 = lane & 7;
  const int q = lane >> 4, l15 = lane & 15, l31 = lane & 31, hh = lane >> 5;
  asm volatile("s_waitcnt lgkmcnt(0)" : "+s"(ka), "+s"(kb), "+s"(kc_), "+s"(tile), "+s"(cg), "+s"(rot), "+v"(r8), "+v"(c8) :: "memory");
  const GAS u16 *xa = (const GAS u16*)ka[0], *wqkv = (const GAS u16*)ka[1], *wo = (const GAS u16*)ka[2], *xb = (const GAS u16*)ka[3];
  const GAS u16 *lre = (const GAS u16*)ka[4], *lim = (const GAS u16*)ka[5], *bre = (const GAS u16*)ka[6], *bim = (const GAS u16*)ka[7];
  const GAS u16 *cre = (const GAS u16*)kb[0], *cim = (const GAS u16*)kb[1], *ldt = (const GAS u16*)kb[2], *dsk = (const GAS u16*)kb[3];
  float* out = (float*)kc_[0]; char* wsp = (char*)kc_[1];
  const __amdgpu_buffer_rsrc_t ws = __builtin_amdgcn_make_buffer_rsrc(wsp, 0, WS_END, 0x00020000);
  const __amdgpu_buffer_rsrc_t ro = __builtin_amdgcn_make_buffer_rsrc(out, 0, 2048 * 1024 * 4, 0x00020000);
  unsigned epoch;
#define LOAD_EPOCH() asm volatile("s_load_dword %0, %1, 0x0\n\ts_waitcnt lgkmcnt(0)" : "=&s"(epoch) : "s"(wsp) : "memory")

  if ((xcd & 1) == 0) {
    constexpr unsigned WA = 131072, WB = 147456, SCR = 32768, TRB = 81920;
    constexpr unsigned QBUF[4] = {WB, 0, WA, WB}, OBUF[4] = {32768, 49152, 65536, 0};
    constexpr unsigned WBUF[8] = {WA, WB, 7 * 16384, 0, WA, WB, 0, WA};
    if (helper) {
      const int head = cg * 4 + w;
      unsigned vx[4], vw[2], vo[4];
#pragma unroll
      for (int i = 0; i < 4; ++i) vx[i] = (32 * w + 8 * i + r8) * 1024 + ((c8 ^ ((4 * i + (r8 >> 1)) & 7)) << 4) - 1024 * i;
#pragma unroll
      for (int i = 0; i < 2; ++i) vw[i] = (16 * w + 8 * i + r8) * 3072 + ((c8 ^ ((((r8 >> 1) & 1) | (i << 1)) << 1)) << 4) - 1024 * i;
      const GAS char* xbase = (const GAS char*)xa + (size_t)tile * 131072;
      const GAS char* wbase = (const GAS char*)wqkv + cg * 128;
      auto isx = [&](int kk, int slot) { dma4(L0 + slot * 16384 + w * 4096, xbase + ((kk + rot) & 7) * 128, vx[0], vx[1], vx[2], vx[3]); };
      auto isw = [&](unsigned dst, int kk, int seg) { dma2(L0 + dst + w * 2048, wbase + ((kk + rot) & 7) * (64 * 3072) + seg * 1024, vw[0], vw[1]); };
      auto issue = [&](int kk) { isx(kk, kk); isw(WBUF[kk], kk, 1); isw(WBUF[kk] + 8192, kk, 2); };
      auto isq = [&](int p) { isw(QBUF[p], 2 * p + 1, 0); isw(QBUF[p] + 8192, 2 * p + 2, 0); };
      if (ADLY) __builtin_amdgcn_s_sleep(ADLY);
      issue(0); issue(1);
      LOAD_EPOCH();
#pragma unroll
      for (int i = 0; i < 4; ++i) vo[i] = (8 * i + r8) * 1024 + ((c8 ^ (((r8 >> 1) & 3) << 1)) << 4) - 1024 * i;
      const GAS char* obase = (const GAS char*)wo + cg * 128;
      wait_bar<8>();
      issue(2);
      wait_bar<8>(); issue(3);
      wait_bar<8>(); issue(4);
      wait_bar<8>(); issue(5);
      wait_bar<8>(); issue(6);
      wait_bar<8>(); issue(7);
      wait_bar<8>();
      unsigned pfo;
      asm volatile("global_load_dword %0, %1, %2" : "=v"(pfo) : "v"(((jj >> 3) * 128 + w * 32 + l31) * 1024), "s"(obase) : "memory");
      isq(0);
      wait_bar<5>(); isq(1);
      wait_bar<4>(); isq(2);
      wait_bar<4>(); isq(3); isx(0, 1);
      asm volatile("s_waitcnt vmcnt(8)\n\ts_barrier\n\ts_waitcnt vmcnt(0)" : "+v"(pfo) :: "memory");
      const unsigned tag = epoch + 1, t = tag & 0xffu;
      const GAS char* kvb_ = (const GAS char*)wsp + WS_KV + head * 16384;
      const GAS char* zb_ = (const GAS char*)wsp + WS_Z + head * 2048;
      const unsigned qr = (jj >> 3) * 4096;
      const unsigned vk0 = lane * 16 + qr, vk1 = lane * 16 + ((qr + 4096) & 12288), vz = l15 * 128 + q * 16;
      u4 g[8], gz;
#define KVL(i, v) asm volatile("global_load_dwordx4 %0, %1, %2 offset:%3 sc1" : "=v"(g[i]) : "v"(v), "s"(kvb_), "i"((i & 3) * 1024) : "memory")
      asm volatile("global_load_dwordx4 %0, %1, %2 sc1" : "=v"(gz) : "v"(vz), "s"(zb_) : "memory");
      KVL(0, vk0); KVL(1, vk0); KVL(2, vk0); KVL(3, vk0); KVL(4, vk1); KVL(5, vk1); KVL(6, vk1); KVL(7, vk1);
      asm volatile("s_barrier" ::: "memory");
      auto zsum = [&]() {
        float z0 = __uint_as_float(gz.x), z1 = __uint_as_float(gz.y), z2 = __uint_as_float(gz.z), z3 = __uint_as_float(gz.w);
#define ZS(r) "v_add_f32_dpp %0, %0, %0 row_ror:" #r " row_mask:0xf bank_mask:0xf\n\tv_add_f32_dpp %1, %1, %1 row_ror:" #r " row_mask:0xf bank_mask:0xf\n\t" \
              "v_add_f32_dpp %2, %2, %2 row_ror:" #r " row_mask:0xf bank_mask:0xf\n\tv_add_f32_dpp %3, %3, %3 row_ror:" #r " row_mask:0xf bank_mask:0xf\n\t"
        asm volatile("s_nop 1\n\t" ZS(8) ZS(4) ZS(2) ZS(1) "s_nop 0" : "+v"(z0), "+v"(z1), "+v"(z2), "+v"(z3));
        return (f4){z0, z1, z2, z3};
      };
      asm volatile("s_waitcnt vmcnt(8)" : "+v"(gz) :: "memory");
      gz.x ^= t; gz.z ^= t;
      unsigned bad = gz.x | gz.z;
      f4 zz = zsum(), kv = {0, 0, 0, 0};
      asm volatile("" : "+v"(zz));
#define KVA(i) asm volatile("s_waitcnt vmcnt(%1)" : "+v"(g[i]) : "i"(7 - i) : "memory"); g[i].x ^= t; g[i].z ^= t; bad |= g[i].x | g[i].z; kv += __builtin_bit_cast(f4, g[i]); asm volatile("" : "+v"(kv), "+v"(bad))
      KVA(0); KVA(1); KVA(2); KVA(3); KVA(4); KVA(5); KVA(6); KVA(7);
      if (!__all((bad & 0xffu) == 0)) {
#pragma unroll 1
        for (unsigned rep = 0; rep < SPIN_CAP; ++rep) {
          asm volatile("" ::: "memory");
          gz = __builtin_amdgcn_raw_buffer_load_b128(ws, WS_Z + head * 2048 + vz, 0, SC1);
#pragma unroll
          for (int i = 0; i < 8; ++i) g[i] = __builtin_amdgcn_raw_buffer_load_b128(ws, WS_KV + head * 16384 + (i < 4 ? vk0 : vk1) + (i & 3) * 1024, 0, SC1);
          gz.x ^= t; gz.z ^= t;
          bad = gz.x | gz.z;
#pragma unroll
          for (int i = 0; i < 8; ++i) { g[i].x ^= t; g[i].z ^= t; bad |= g[i].x | g[i].z; }
          if (__all((bad & 0xffu) == 0)) break;
        }
        zz = zsum();
        kv = (f4){0, 0, 0, 0};
#pragma unroll
        for (int i = 0; i < 8; ++i) kv += __builtin_bit_cast(f4, g[i]);
      }
      *(LAS f4*)(L + SCR + w * 1024 + lane * 16) = kv;
      *(LAS u2*)(L + SCR + 8192 + w * 512 + lane * 8) = (u2){pk2(zz[0], zz[1]), pk2(zz[2], zz[3])};
      lbar();
      lbar();
#pragma unroll
      for (int i = 0; i < 4; ++i)
        dma4(L0 + OBUF[i] + w * 4096, obase + (2 * seq(2 * i + (w >> 1)) + (w & 1)) * 32768, vo[0], vo[1], vo[2], vo[3]);
      wait_bar<0>();
      if (bid == 0 && w == 0) {
#pragma unroll 1
        for (unsigned sp = 0; sp < SPIN_CAP; ++sp) {
          asm volatile("" ::: "memory");
          const unsigned fo = (lane >> 3) * 4096 + (lane & 7) * 256;
          const u2 f0 = __builtin_amdgcn_raw_buffer_load_b64(ws, WS_FA + fo, 0, SC1), f1 = __builtin_amdgcn_raw_buffer_load_b64(ws, WS_FA + 32768 + fo, 0, SC1);
          const u2 f2_ = __builtin_amdgcn_raw_buffer_load_b64(ws, WS_FB + fo, 0, SC1), f3 = __builtin_amdgcn_raw_buffer_load_b64(ws, WS_FB + 32768 + fo, 0, SC1);
          if (__all(f0.x == tag && f0.y == ~tag && f1.x == tag && f1.y == ~tag && f2_.x == tag && f2_.y == ~tag && f3.x == tag && f3.y == ~tag)) break;
          __builtin_amdgcn_s_sleep(8);
        }
        if (lane == 0) __builtin_amdgcn_raw_buffer_store_b32(tag, ws, WS_EPOCH, 0, SC1);
      }
      return;
    }
    const int hp = w & 1, th = w >> 1;
    unsigned warm0, warm1, warm2;
    {
      warm0 = __builtin_amdgcn_raw_buffer_load_b32(ws, lane < 16 ? WS_FA + ((tile * 2 + th) * 16 + lane) * 128 : lane < 32 ? WS_Z + (cg * 4 + w) * 2048 + (lane - 16) * 128
                                                   : lane == 32 ? WS_O - 128 : lane == 33 ? WS_O + tile * 131072 : OOB, 0, SC1);
      warm1 = __builtin_amdgcn_raw_buffer_load_b32(ro, lane < 2 ? (tile * 128 + w * 32 + lane * 16) * 4096 : OOB, 0, SC1);
      warm2 = *(const GAS unsigned*)(wo + (w * 4 + (lane & 3)) * 16384 + cg * 64);
    }
    LOAD_EPOCH();
    const int qq = (lane >> 2) & 3, pp = lane & 3;
    const unsigned ax0 = l15 * 128 + ((q ^ (l15 >> 1)) << 4), ax1 = l15 * 128 + (((4 + q) ^ (l15 >> 1)) << 4);
    const Win wx[2] = {mkwin(ax0), mkwin(ax1)}, wq[2] = {mkwin(ax0 + th * 8192), mkwin(ax1 + th * 8192)};
    auto waddr = [&](int h) { return (8 * q + qq) * 128 + (((2 * h + (pp >> 1)) ^ ((((qq >> 1) & 1) | ((q & 1) << 1)) << 1)) << 4) + 8 * (pp & 1); };
    const Win aw = mkwin(waddr(w)), aq0 = mkwin(waddr(2 * hp)), aq1 = mkwin(waddr(2 * hp + 1));
    MKSQ();
    unsigned ao[4], og[4], tw[4], trd[4];
#pragma unroll
    for (int i = 0; i < 4; ++i) {
      og[i] = WS_O + ((tile * 8 + th * 4 + i) * 16 + cg * 2 + hp) * 1024 + lane * 16;
      ao[i] = (4 * q + qq) * 128 + ((((2 * i) ^ (((2 * q + (qq >> 1)) & 3) << 1)) + (pp >> 1)) << 4) + 8 * (pp & 1);
      tw[i] = TRB + w * 8192 + l15 * 256 + (((4 * i + q) ^ l15) << 4);
      trd[i] = TRB + w * 8192 + (4 * i + q) * 256 + ((l15 ^ (4 * i + q)) << 4);
      asm volatile("" : "+v"(og[i]), "+v"(ao[i]), "+v"(tw[i]), "+v"(trd[i]));
    }
    unsigned so_ = ((tile * 128 + w * 32 + q) * 1024 + cg * 64 + l15 * 4) * 4, ex = TRB + th * 16384 + hp * 1024 + lane * 16;
    unsigned sk = SCR + hp * 2048 + lane * 16, sz_ = SCR + 8192 + hp * 1024 + lane * 8, sw_ = SCR + 4096 + w * 1024 + lane * 16;
    unsigned pk_ = WS_KV + ((cg * 4 + w) * 16 + tile) * 1024 + lane * 16, pz_ = l15 == 0 ? WS_Z + ((cg * 4 + w) * 16 + tile) * 128 + q * 16 : OOB;
    unsigned pf_ = lane == 0 ? WS_FA + ((tile * 2 + th) * 16 + cg * 2 + hp) * 128 : OOB;
    asm volatile("" : "+v"(so_), "+v"(ex), "+v"(sk), "+v"(sz_), "+v"(sw_), "+v"(pk_), "+v"(pz_), "+v"(pf_));
    f4 ka[8], va[8];
#pragma unroll
    for (int i = 0; i < 8; ++i) { ka[i] = (f4){EPS, EPS, EPS, EPS}; va[i] = (f4){0, 0, 0, 0}; }
    bf8 X0[4], X1[4], W0[2], W1[2];
    auto rdx = [&](bf8 (&X)[4], int kk, int ks, unsigned off) {
#pragma unroll
      for (int i = 0; i < 4; ++i) X[i] = ldsr(at(L, wx[ks], kk * 16384 + off + i * 2048));
    };
    auto rdw = [&](bf8 (&W)[2], int kk, int ks) {
#pragma unroll
      for (int i = 0; i < 2; ++i) { const unsigned o = WBUF[kk] + ks * 4096 + i * 8192; W[i] = ldstr(at(L, aw, o), at(L, aw, o + 512)); }
    };
    auto mm = [&](const bf8 (&X)[4], const bf8 (&W)[2], int h) {
#pragma unroll
      for (int i = 0; i < 4; ++i) {
        ka[4 * h + i] = mfma16(X[i], W[0], ka[4 * h + i]);
        va[4 * h + i] = mfma16(X[i], W[1], va[4 * h + i]);
      }
    };
#define ILV(nr, nm) do { _Pragma("unroll") for (int i_ = 0; i_ < nr; ++i_) { SG(0x008, 1); SG(0x100, 1); } if (nm > nr) SG(0x008, nm - nr); } while (0)
    asm volatile("s_barrier" :: "v"(warm0), "v"(warm1), "v"(warm2), "v"(cpf) : "memory");
    const unsigned tag = epoch + 1, t = tag & 0xffu;
    rdw(W0, 0, 0); rdx(X0, 0, 0, 0);
    SB();
#pragma unroll
    for (int kk = 0; kk < 8; ++kk) {
      rdx(X1, kk, 0, 8192); mm(X0, W0, 0); ILV(4, 8); SB(); PACE(PACE_A[kk]);
      rdw(W1, kk, 1); rdx(X0, kk, 1, 0); mm(X1, W0, 1); ILV(8, 8); SB(); PACE(PACE_A[kk]);
      rdx(X1, kk, 1, 8192); mm(X0, W1, 0); ILV(4, 8); SB();
      if (kk < 7) { lbar(); rdw(W0, kk + 1, 0); rdx(X0, kk + 1, 0, 0); mm(X1, W1, 1); ILV(8, 8); }
      else mm(X1, W1, 1);
      SB(); PACE(PACE_A[kk]);
    }
    bf8 XQ[3][4];
    s4 WH[3][4];
    auto rdq = [&](int s, int i) {
      const unsigned o = QBUF[s >> 2] + ((s >> 1) & 1) * 8192 + (s & 1) * 4096;
      if (i < 4) WH[s % 3][i] = __builtin_amdgcn_ds_read_tr16_b64_v4i16((LAS s4*)at(L, i < 2 ? aq0 : aq1, o + (i & 1) * 512));
      else XQ[s % 3][i - 4] = ldsr(at(L, wq[s & 1], (s < 14 ? (s >> 1) + 1 : 1) * 16384 + (i - 4) * 2048));
    };
    {
      f4 kv = {0, 0, 0, 0}, zz = {0, 0, 0, 0};
      const u4 ones = {0x3f803f80u, 0x3f803f80u, 0x3f803f80u, 0x3f803f80u};
#pragma unroll
      for (int m = 0; m < 4; ++m) {
        const f4 a = ka[2 * m], b = ka[2 * m + 1], c = va[2 * m], d = va[2 * m + 1];
        const u4 pa = {phi2(a[0], a[1]), phi2(a[2], a[3]), phi2(b[0], b[1]), phi2(b[2], b[3])};
        const u4 pb = {pk2(c[0], c[1]), pk2(c[2], c[3]), pk2(d[0], d[1]), pk2(d[2], d[3])};
        kv = mfma16(asbf8(pa), asbf8(pb), kv);
        zz = mfma16(asbf8(pa), asbf8(ones), zz);
      }
      const u4 skv = {tagv(kv[0], t), __float_as_uint(kv[1]), tagv(kv[2], t), __float_as_uint(kv[3])};
      __builtin_amdgcn_raw_buffer_store_b128(skv, ws, pk_, 0, SC1);
      const u4 sz = {tagv(zz[0], t), __float_as_uint(zz[1]), tagv(zz[2], t), __float_as_uint(zz[3])};
      __builtin_amdgcn_raw_buffer_store_b128(sz, ws, pz_, 0, SC1);
    }
    SB();
    lbar();
#pragma unroll
    for (int i = 0; i < 16; ++i) rdq(i >> 3, i & 7);
    SB();
    f4 qa[2][4];
#pragma unroll
    for (int i = 0; i < 8; ++i) qa[i >> 2][i & 3] = (f4){EPS, EPS, EPS, EPS};
    u4 gc[8];
    const unsigned gb = WS_KV + (cg * 4 + w) * 16384 + lane * 16, g2 = (((jj >> 3) + 2) & 3) * 4096, g3 = (((jj >> 3) + 3) & 3) * 4096;
    auto ldg = [&](int i) { gc[i] = __builtin_amdgcn_raw_buffer_load_b128(ws, gb + (i & 3) * 1024, i < 4 ? g2 : g3, SC1); };
    asm volatile("s_nop 1" : "+v"(qa[0][0]), "+v"(qa[0][1]), "+v"(qa[0][2]), "+v"(qa[0][3]), "+v"(qa[1][0]), "+v"(qa[1][1]), "+v"(qa[1][2]), "+v"(qa[1][3]));
#pragma unroll
    for (int s = 0; s < 16; ++s) {
      if (s == 2) asm volatile("s_waitcnt lgkmcnt(4)\n\ts_barrier" ::: "memory");
      if (s == 6 || s == 10) asm volatile("s_barrier" ::: "memory");
      bf8 W[2] = {__builtin_bit_cast(bf8, (s8)__builtin_shufflevector(WH[s % 3][0], WH[s % 3][1], 0, 1, 2, 3, 4, 5, 6, 7)),
                  __builtin_bit_cast(bf8, (s8)__builtin_shufflevector(WH[s % 3][2], WH[s % 3][3], 0, 1, 2, 3, 4, 5, 6, 7))};
      asm volatile("" : "+v"(W[0]), "+v"(W[1]), "+v"(XQ[s % 3][0]), "+v"(XQ[s % 3][1]), "+v"(XQ[s % 3][2]), "+v"(XQ[s % 3][3]));
#pragma unroll
      for (int i = 0; i < 8; ++i) {
        mfa(qa[i & 1][i >> 1], W[i & 1], XQ[s % 3][i >> 1]); SB();
        if (s < 14) { rdq(s + 2, i); SB(); }
        if (s >= 11 && s < 15 && (i == 2 || i == 6)) { ldg(2 * (s - 11) + (i >> 2)); SB(); }
      }
    }
    asm volatile("s_nop 7" : "+v"(qa[0][0]), "+v"(qa[0][1]), "+v"(qa[0][2]), "+v"(qa[0][3]), "+v"(qa[1][0]), "+v"(qa[1][1]), "+v"(qa[1][2]), "+v"(qa[1][3]));
    u2 pq[2][4];
#pragma unroll
    for (int i = 0; i < 8; ++i) {
      pq[i >> 2][i & 3] = (u2){phi2(qa[i >> 2][i & 3][0], qa[i >> 2][i & 3][1]), phi2(qa[i >> 2][i & 3][2], qa[i >> 2][i & 3][3])};
      asm volatile("" : "+v"(pq[i >> 2][i & 3]));
    }
    SB();
    {
      unsigned bad = 0;
      f4 kvp = {0, 0, 0, 0};
#pragma unroll
      for (int i = 0; i < 8; ++i) { gc[i].x ^= t; gc[i].z ^= t; bad |= gc[i].x | gc[i].z; kvp += __builtin_bit_cast(f4, gc[i]); }
      if (__builtin_expect(!__all((bad & 0xffu) == 0), 0)) {
#pragma unroll 1
        for (unsigned rep = 0; rep < SPIN_CAP; ++rep) {
          asm volatile("" ::: "memory");
          bad = 0;
          kvp = (f4){0, 0, 0, 0};
#pragma unroll
          for (int i = 0; i < 8; ++i) ldg(i);
#pragma unroll
          for (int i = 0; i < 8; ++i) { gc[i].x ^= t; gc[i].z ^= t; bad |= gc[i].x | gc[i].z; kvp += __builtin_bit_cast(f4, gc[i]); }
          asm volatile("" : "+v"(kvp));
          if (__all((bad & 0xffu) == 0)) break;
        }
      }
      *(LAS f4*)(L + sw_) = kvp;
    }
    lbar();
    const f4 zero4 = {0, 0, 0, 0}, eps4 = {EPS, EPS, EPS, EPS};
    f4 num[2][4], den[2][4];
    const f4 ka0 = *(const LAS f4*)(L + sk), kb0 = *(const LAS f4*)(L + sk + 4096), ka1 = *(const LAS f4*)(L + sk + 1024), kb1 = *(const LAS f4*)(L + sk + 5120);
    const u2 zb[2] = {*(const LAS u2*)(L + sz_), *(const LAS u2*)(L + sz_ + 512)};
    SB();
    const f4 k0 = ka0 + kb0, k1 = ka1 + kb1;
    const u2 kb2[2] = {{pk2(k0[0], k0[1]), pk2(k0[2], k0[3])}, {pk2(k1[0], k1[1]), pk2(k1[2], k1[3])}};
#pragma unroll
    for (int i = 0; i < 8; ++i) { den[i & 1][i >> 1] = mfma16k16(zb[i & 1], pq[i & 1][i >> 1], eps4); num[i & 1][i >> 1] = mfma16k16(kb2[i & 1], pq[i & 1][i >> 1], zero4); }
    SB();
#pragma unroll
    for (int i = 0; i < 4; ++i) {
      u4 so;
#pragma unroll
      for (int h = 0; h < 2; ++h) {
        const float rc = __builtin_amdgcn_rcpf(den[h][i][0]);
        const f4 n = num[h][i];
        so[2 * h] = pk2((f2){n[0], n[1]} * rc);
        so[2 * h + 1] = pk2((f2){n[2], n[3]} * rc);
      }
      __builtin_amdgcn_raw_buffer_store_b128(so, ws, og[i], 0, SC1);
      *(LAS u4*)(L + ex + (i >> 1) * 8192 + (i & 1) * 2048) = so;
    }
    lbar();
    u4 of[32];
    unsigned ah[4];
#pragma unroll
    for (int i = 0; i < 4; ++i) { ah[i] = ao[i] + 65536; asm volatile("" : "+v"(ah[i])); }
#pragma unroll
    for (int i = 0; i < 4; ++i) of[i] = *(const LAS u4*)(L + TRB + w * 8192 + ((i & 1) * 2 + (i >> 1)) * 1024 + lane * 16);
    drain();
    __builtin_amdgcn_raw_buffer_store_b64((u2){tag, ~tag}, ws, pf_, 0, SC1);
    asm volatile("s_barrier" ::: "memory");
    f4 acc[2][4];
#pragma unroll
    for (int i = 0; i < 8; ++i) acc[i >> 2][i & 3] = zero4;
    unsigned ob = WS_O + (tile * 8 + w * 2) * 16384 + lane * 16, ob2 = ob + 16384;
    asm volatile("" : "+v"(ob), "+v"(ob2));
    bf8 wf[2][4];
    auto rdo = [&](int k) {
#pragma unroll
      for (int nt = 0; nt < 4; ++nt) { const unsigned o = OBUF[k >> 2] + (k & 3) * 4096; const char LAS* p = L + (o < 65536 ? ao[nt] : ah[nt]) + (o & 65535); wf[k & 1][nt] = ldstr(p, p + 2048); }
    };
    auto ldo = [&](int k) {
      const unsigned so = sq[k >> 1] * 2048;
      of[2 * k] = __builtin_amdgcn_raw_buffer_load_b128(ws, ob + (k & 1) * 1024, so, SC1);
      of[2 * k + 1] = __builtin_amdgcn_raw_buffer_load_b128(ws, ob2 + (k & 1) * 1024, so, SC1);
    };
    auto mo = [&](int k) {
#pragma unroll
      for (int nt = 0; nt < 4; ++nt) {
        acc[0][nt] = mfma16(wf[k & 1][nt], asbf8(of[2 * k]), acc[0][nt]);
        acc[1][nt] = mfma16(wf[k & 1][nt], asbf8(of[2 * k + 1]), acc[1][nt]);
      }
    };
    rdo(0); rdo(1); SB();
    mo(0); rdo(2); SB();
    mo(1); SB();
    wait_flags(ws, WS_FA + ((tile * 2 + th) * 16 + l15) * 128, tag);
#pragma unroll
    for (int k = 2; k < 2 + OD; ++k) ldo(k);
    SB();
#pragma unroll
    for (int k = 2; k < 16; ++k) {
      if (k + OD < 16) ldo(k + OD);
      if (k + 1 < 16) rdo(k + 1);
      mo(k);
#pragma unroll
      for (int i_ = 0; i_ < 8; ++i_) { SG(0x008, 1); SG(0x100, 1); if (i_ == 1 || i_ == 5) SG(0x020, 1); }
      SB();
    }
#pragma unroll
    for (int mt = 0; mt < 2; ++mt)
#pragma unroll
      for (int nt = 0; nt < 4; ++nt) {
        const f4 v = acc[mt][nt];
        const unsigned p0 = pk2(v[0], v[1]), p1 = pk2(v[2], v[3]);
        *(LAS f4*)(L + tw[nt] + mt * 4096) = (f4){bflo(p0), bfhi(p0), bflo(p1), bfhi(p1)};
      }
    u4 ov[8];
#pragma unroll
    for (int i = 0; i < 8; ++i) ov[i] = *(const LAS u4*)(L + trd[i & 3] + (i >> 2) * 4096);
    SB();
#pragma unroll
    for (int i = 0; i < 8; ++i) __builtin_amdgcn_raw_buffer_store_b128(ov[i], ro, so_, i * 16384, SC1);
    asm volatile(".Lcode_lastA:");
  } else {
    constexpr unsigned SLOT = 32768, SCR = SLOT, PAR = 4 * SLOT;
    const int wp = w & 1, wt = w >> 1;
    const unsigned ecol = WS_EST + (cg * 64 + wp * 32 + l31) * 8;
    if (helper) {
      unsigned vx[4], vw[4], vc[4];
#pragma unroll
      for (int i = 0; i < 4; ++i) {
        const unsigned sw = (c8 ^ ((4 * i + (r8 >> 1)) & 7)) << 4;
        vx[i] = (64 * (w >> 1) + 32 * (i & 1) + 16 * (r8 >> 2) + 8 * (r8 & 1) + 4 * (w & 1) + 2 * (i >> 1) + ((r8 >> 1) & 1)) * 1024 + sw - 1024 * i;
        vw[i] = (16 * (i & 1) + 8 * (r8 >> 2) + 4 * (i >> 1) + (r8 & 3)) * 1024 + sw - 1024 * i;
        vc[i] = (8 * i + r8) * 1024 + sw - 1024 * i;
      }
      const GAS char* xbase = (const GAS char*)xb + (size_t)tile * 131072;
      const GAS char* wbase = (const GAS char*)((w & 1) ? bim : bre) + (cg * 64 + (w >> 1) * 32) * 1024;
      auto issue = [&](int kk) {
        const int kc = (kk + rot) & 7;
        const unsigned s = L0 + (kk % 5) * SLOT + w * 4096;
        dma4(s, xbase + kc * 128, vx[0], vx[1], vx[2], vx[3]);
        dma4(s + 16384, wbase + kc * 128, vw[0], vw[1], vw[2], vw[3]);
      };
      issue(0); issue(1);
      LOAD_EPOCH();
      const GAS char* cbase = (const GAS char*)((w & 1) ? cim : cre) + cg * 65536;
      auto issue_c = [&](int piece) {
        const unsigned s = L0 + ((piece + 3) % 5) * SLOT + w * 8192;
        const GAS char* cb = cbase + seq(2 * piece + (w >> 1)) * 128;
        dma4(s, cb, vc[0], vc[1], vc[2], vc[3]);
        dma4(s + 4096, cb + 32768, vc[0], vc[1], vc[2], vc[3]);
      };
      wait_bar<8>();
      issue(2);
      const f4 par = *(const LAS f4*)(L + PAR + w * 1024 + lane * 16);
      const f2 l64 = {par[0], par[1]}, l128 = {par[2], par[3]};
      f2 l256 = cmul(l128, l128);
      f2 wgt = hh ? l64 : (f2){1.f, 0.f};
      if (wt) wgt = cmul(wgt, l128);
      const unsigned tag = epoch + 1, t = tag & 0xffu;
      unsigned eo[8], et[8];
#pragma unroll
      for (int m = 0; m < 8; ++m) {
        const int i = 2 * tile - 1 - (2 * wt + hh) - 4 * m;
        eo[m] = i >= 0 ? ecol + i * 4096 : OOB;
        et[m] = i >= 0 ? t : 0u;
        asm volatile("" : "+v"(eo[m]), "+v"(et[m]));
      }
      asm volatile("" : "+v"(l256), "+v"(wgt));
      wait_bar<8>(); issue(3);
      wait_bar<8>(); issue(4);
      wait_bar<8>(); issue(5);
      wait_bar<8>(); issue(6);
      wait_bar<8>(); issue(7);
      wait_bar<8>(); issue_c(0);
      wait_bar<8>();
      issue_c(1);
      asm volatile("s_barrier" ::: "memory");
      __builtin_amdgcn_s_sleep(12);
      __builtin_amdgcn_s_setprio(3);
      f2 cin;
#pragma unroll 1
      for (unsigned sp = 0; sp < SPIN_CAP; ++sp) {
        asm volatile("" ::: "memory");
        u2 g[8];
#pragma unroll
        for (int m = 0; m < 8; ++m) g[m] = __builtin_amdgcn_raw_buffer_load_b64(ws, eo[m], 0, SC1);
        unsigned bad = 0;
        cin = (f2){0, 0};
#pragma unroll
        for (int m = 7; m >= 0; --m) {
          const unsigned cx = g[m].x ^ et[m];
          bad |= cx;
          cin = cfma(l256, cin, (f2){__uint_as_float(cx), __uint_as_float(g[m].y)});
        }
        if (__all((bad & 0xffu) == 0)) break;
      }
      cin = cmul(cin, wgt);
      f2 chi;
      const f2 clo = swap_lo(cin, chi);
      if (hh == 0) *(LAS f2*)(L + SCR + (wp * 2 + wt) * 256 + l31 * 8) = clo + chi;
      lbar();
      issue_c(2);
      lbar();
      issue_c(3);
      wait_bar<0>();
      return;
    }
    const int pl = cg * 64 + wp * 32 + 8 * (l15 >> 2) + 4 * (q & 1) + (l15 & 3);
    const unsigned plr = lre[pl], pli = lim[pl], pdt = ldt[pl];
    u4 dv[2];
#pragma unroll
    for (int i = 0; i < 2; ++i) dv[i] = *(const GAS u4*)(dsk + cg * 64 + 32 * i + 8 * q);
    unsigned warm0, warm1, warm2;
    {
      warm0 = __builtin_amdgcn_raw_buffer_load_b32(ws, lane < 16 ? WS_FB + ((tile * 2 + wt) * 16 + lane) * 128 : lane == 16 ? WS_O - 128
                                                   : lane == 17 ? WS_H + (tile * 4 + w) * 65536 : OOB, 0, SC1);
      warm1 = __builtin_amdgcn_raw_buffer_load_b32(ro, lane < 2 ? (tile * 128 + w * 32 + lane * 16) * 4096 + 2048 : OOB, 0, SC1);
      warm2 = *(const GAS unsigned*)(((w & 1) ? cim : cre) + cg * 32768 + (w >> 1) * 16384 + (lane & 1) * 15872);
    }
    LOAD_EPOCH();
    SB();
    f2 lam[2], l8[2], kap, l16, l32, l64;
    {
      const float lr = bflo(plr), li = bflo(pli);
      const float dt = bflo(pk2(expf(bflo(pdt)), 0.f));
      const float mag = expf(dt * lr);
      float sn, cs;
      sincosf(dt * li, &sn, &cs);
      const f2 l1 = {mag * cs, mag * sn};
      const float inv = 2.f / (lr * lr + li * li), nr = l1.x - 1.f, ni = l1.y;
      kap = (f2){(nr * lr + ni * li) * inv, (ni * lr - nr * li) * inv};
      f2 t = cmul(l1, l1); t = cmul(t, t); t = cmul(t, t);
      l16 = cmul(t, t); l32 = cmul(l16, l16); l64 = cmul(l32, l32);
      const f2 l128 = cmul(l64, l64);
      *(LAS f4*)(L + PAR + w * 1024 + lane * 16) = (f4){l64.x, l64.y, l128.x, l128.y};
      lam[0] = swap16(l1, l1, lam[1]);
      l8[0] = swap16(t, t, l8[1]);
    }
    f2 R0 = {lam[0].x, lam[0].x}, I0 = {lam[0].y, lam[0].y}, N0 = -I0, R1 = {lam[1].x, lam[1].x}, I1 = {lam[1].y, lam[1].y}, N1 = -I1;
    unsigned pe_ = hh == 0 ? ecol + (tile * 2 + wt) * 4096 : OOB, se_ = SCR + 1024 + (wt * 2 + wp) * 256 + hh * 1024 + l31 * 8;
    asm volatile("" : "+v"(R0), "+v"(I0), "+v"(N0), "+v"(R1), "+v"(I1), "+v"(N1), "+v"(pe_), "+v"(se_));
    const unsigned ax[2] = {(unsigned)(l15 * 128 + ((q ^ (l15 >> 1)) << 4)), (unsigned)(l15 * 128 + (((4 + q) ^ (l15 >> 1)) << 4))};
    const Win wx[2] = {mkwin(ax[0] + wt * 8192), mkwin(ax[1] + wt * 8192)}, ww[2] = {mkwin(ax[0] + wp * 8192), mkwin(ax[1] + wp * 8192)};
    MKSQ();
    f4 u[2][2][4];
#pragma unroll
    for (int i = 0; i < 16; ++i) u[i >> 3][(i >> 2) & 1][i & 3] = (f4){0, 0, 0, 0};
    u4 xd[2][2];
#pragma unroll
    for (int i = 0; i < 4; ++i) xd[i >> 1][i & 1] = (u4){0, 0, 0, 0};
    const int kx = (cg - rot) & 7;
    bf8 Xa[2], Xb[2], W0[4], W1[4];
    auto rdx = [&](bf8 (&X)[2], int kk, int ks, int h) {
#pragma unroll
      for (int i = 0; i < 2; ++i) X[i] = ldsr(at(L, wx[ks], (kk % 5) * SLOT + (2 * h + i) * 2048));
    };
    auto rdw = [&](bf8 (&W)[4], int kk, int ks) {
#pragma unroll
      for (int i = 0; i < 4; ++i) W[i] = ldsr(at(L, ww[ks], (kk % 5) * SLOT + 16384 + (i & 1) * 4096 + (i >> 1) * 2048));
    };
    auto mm = [&](const bf8 (&X)[2], const bf8 (&W)[4], int h) {
#pragma unroll
      for (int i = 0; i < 2; ++i)
#pragma unroll
        for (int j = 0; j < 4; ++j) u[j >> 1][j & 1][2 * h + i] = mfma16(X[i], W[j], u[j >> 1][j & 1][2 * h + i]);
    };
    asm volatile("s_waitcnt lgkmcnt(0)\n\ts_barrier" : "+v"(lam[0]), "+v"(lam[1]), "+v"(l8[0]), "+v"(l8[1]), "+v"(kap), "+v"(l16), "+v"(l32), "+v"(l64),
                 "+v"(dv[0]), "+v"(dv[1]) : "v"(warm0), "v"(warm1), "v"(warm2), "v"(cpf) : "memory");
    const unsigned tag = epoch + 1, t = tag & 0xffu;
    rdw(W0, 0, 0); rdx(Xa, 0, 0, 0);
    SB();
#pragma unroll
    for (int kk = 0; kk < 8; ++kk) {
      rdx(Xb, kk, 0, 1); mm(Xa, W0, 0); ILV(2, 8); SB(); PACE(PACE_B[kk]);
      rdw(W1, kk, 1); rdx(Xa, kk, 1, 0); mm(Xb, W0, 1); ILV(6, 8); SB(); PACE(PACE_B[kk]);
      if (kk == kx) {
#pragma unroll
        for (int i = 0; i < 4; ++i) xd[i >> 1][i & 1] = *(const LAS u4*)(at(L, wx[i & 1], (kk % 5) * SLOT + (i >> 1) * 2048) + (w & 1) * 4096);
      }
      SB();
      rdx(Xb, kk, 1, 1); mm(Xa, W1, 0); ILV(2, 8); SB();
      if (kk < 7) { lbar(); rdw(W0, kk + 1, 0); rdx(Xa, kk + 1, 0, 0); mm(Xb, W1, 1); ILV(6, 8); }
      else mm(Xb, W1, 1);
      SB(); if (kk < 7) PACE(PACE_B[kk]);
    }
    f2 gr[2][8], gi[2][8];
#pragma unroll
    for (int i = 0; i < 16; ++i) {
      const int pt = i >> 3, a = i & 7;
      gr[pt][a] = (f2){u[pt][0][a >> 1][2 * (a & 1)], u[pt][0][a >> 1][2 * (a & 1) + 1]};
      gi[pt][a] = (f2){u[pt][1][a >> 1][2 * (a & 1)], u[pt][1][a >> 1][2 * (a & 1) + 1]};
    }
    {
#pragma unroll
      for (int a = 1; a < 8; ++a)
        asm volatile("v_pk_fma_f32 %0, %10, %5, %0\n\tv_pk_fma_f32 %1, %9, %4, %1\n\tv_pk_fma_f32 %2, %13, %7, %2\n\tv_pk_fma_f32 %3, %12, %6, %3\n\t"
                     "v_pk_fma_f32 %0, %8, %4, %0\n\tv_pk_fma_f32 %1, %8, %5, %1\n\tv_pk_fma_f32 %2, %11, %6, %2\n\tv_pk_fma_f32 %3, %11, %7, %3"
                     : "+v"(gr[0][a]), "+v"(gi[0][a]), "+v"(gr[1][a]), "+v"(gi[1][a])
                     : "v"(gr[0][a - 1]), "v"(gi[0][a - 1]), "v"(gr[1][a - 1]), "v"(gi[1][a - 1]), "v"(R0), "v"(I0), "v"(N0), "v"(R1), "v"(I1), "v"(N1));
    }
    const f2 e0[2] = {{gr[0][7].x, gi[0][7].x}, {gr[1][7].x, gi[1][7].x}};
    f2 vodd;
    const f2 veven = swap16(cfma(l8[0], e0[0], (f2){gr[0][7].y, gi[0][7].y}), cfma(l8[1], e0[1], (f2){gr[1][7].y, gi[1][7].y}), vodd);
    f2 ehi;
    const f2 elo = swap_lo(cfma(l16, veven, vodd), ehi);
    const f2 e64 = cfma(l32, elo, ehi);
    __builtin_amdgcn_raw_buffer_store_b64((u2){tagv(e64.x, t), __float_as_uint(e64.y)}, ws, pe_, 0, SC1);
    *(LAS f2*)(L + se_) = e64;
    lbar();
    const f2 esib = *(const LAS f2*)(L + SCR + 1024 + wp * 256 + l31 * 8);
    SB();
    f2 kp[2], ke[2], pw[2][8];
    kp[0] = swap16(kap, kap, kp[1]);
#pragma unroll
    for (int pt = 0; pt < 2; ++pt) {
      const f2 KR = {kp[pt].x, kp[pt].x}, KI = {kp[pt].y, kp[pt].y};
      ke[pt] = cmul(kp[pt], e0[pt]);
#pragma unroll
      for (int a = 0; a < 8; ++a) {
        const f2 xr = gr[pt][a], xi = gi[pt][a];
        gr[pt][a] = fma2(KR, xr, -KI * xi); gi[pt][a] = fma2(-KR, xi, -KI * xr);
        asm volatile("" : "+v"(gr[pt][a]), "+v"(gi[pt][a]));
      }
      pw[pt][0] = (f2){lam[pt].x, -lam[pt].y};
#pragma unroll
      for (int k = 1; k < 8; ++k) { pw[pt][k] = cmul(pw[pt][k >> 1], pw[pt][(k - 1) >> 1]); asm volatile("" : "+v"(pw[pt][k])); }
    }
    u2 idn = {(l15 == 4 * q ? 0x3f80u : 0u) | (l15 == 4 * q + 1 ? 0x3f800000u : 0u), (l15 == 4 * q + 2 ? 0x3f80u : 0u) | (l15 == 4 * q + 3 ? 0x3f800000u : 0u)};
#pragma unroll
    for (int i = 0; i < 4; ++i) {
#pragma unroll
      for (int j = 0; j < 4; ++j) xd[i >> 1][i & 1][j] = pk2(bflo(xd[i >> 1][i & 1][j]) * bflo(dv[i & 1][j]), bfhi(xd[i >> 1][i & 1][j]) * bfhi(dv[i & 1][j]));
      asm volatile("" : "+v"(xd[i >> 1][i & 1]));
    }
    f2 me = hh ? cmul(kap, l32) : kap, be = hh ? cmul(kap, elo) : (f2){0.f, 0.f};
    f2 mo = cmul(l16, me), bo = cfma(l16, be, cmul(kap, veven));
    if (wt) { be = cfma(me, esib, be); bo = cfma(mo, esib, bo); me = cmul(me, l64); mo = cmul(mo, l64); }
    unsigned pf_ = lane == 0 ? WS_FB + ((tile * 2 + wt) * 16 + cg * 2 + wp) * 128 : OOB, ex = 2 * SLOT + wt * 16384 + wp * 4096 + lane * 16;
    asm volatile("" : "+v"(pf_), "+v"(ex));
    unsigned sc_ = SCR + wp * 512 + l31 * 8, ho[4];
#pragma unroll
    for (int i = 0; i < 4; ++i) ho[i] = WS_H + (tile * 8 + wt * 4 + i) * 32768 + (cg * 2 + wp) * 2048 + lane * 16;
    asm volatile("s_waitcnt lgkmcnt(0)\n\ts_barrier" : "+v"(ho[0]), "+v"(ho[1]), "+v"(ho[2]), "+v"(ho[3]), "+v"(me), "+v"(be), "+v"(mo), "+v"(bo),
                 "+v"(ke[0]), "+v"(ke[1]), "+v"(idn), "+v"(sc_) :: "memory");
    const f2 cin = *(const LAS f2*)(L + sc_) + *(const LAS f2*)(L + sc_ + 256);
    f2 d0[2], DR[2], DJ[2];
    d0[0] = swap16(cfma(me, cin, be), cfma(mo, cin, bo), d0[1]);
#pragma unroll
    for (int pt = 0; pt < 2; ++pt) {
      const f2 d1 = cfma(l8[pt], d0[pt], ke[pt]);
      DR[pt] = (f2){d0[pt].x, d1.x}; DJ[pt] = (f2){-d0[pt].y, -d1.y};
    }
    const f4 zero4 = {0, 0, 0, 0};
    f4 T[8][2];
    u4 ow[8];
    auto fix = [&](int i) {
      const int mt = i >> 1, c = i & 1;
#pragma unroll
      for (int pt = 0; pt < 2; ++pt) {
        u2 a;
#pragma unroll
        for (int h = 0; h < 2; ++h) {
          const f2 P = pw[pt][2 * mt + h], PR = {P.x, P.x}, PI = {P.y, P.y};
          a[h] = pk2(c ? fma2(PI, DR[pt], fma2(PR, DJ[pt], gi[pt][2 * mt + h])) : fma2(-PI, DJ[pt], fma2(PR, DR[pt], gr[pt][2 * mt + h])));
        }
        T[i][pt] = mfma16k16(a, idn, zero4);
      }
    };
    auto put = [&](int i) {
      ow[i] = (u4){pk2(T[i][0][0], T[i][0][1]), pk2(T[i][0][2], T[i][0][3]), pk2(T[i][1][0], T[i][1][1]), pk2(T[i][1][2], T[i][1][3])};
      __builtin_amdgcn_raw_buffer_store_b128(ow[i], ws, ho[i >> 1] + (i & 1) * 1024, 0, SC1);
    };
    fix(0); SB();
#pragma unroll
    for (int i = 1; i < 8; ++i) { fix(i); SB(); put(i - 1); SB(); }
    put(7);
    SB();
#pragma unroll
    for (int i = 0; i < 8; ++i) *(LAS u4*)(L + ex + (i >> 2) * 8192 + (i & 1) * 2048 + ((i >> 1) & 1) * 1024) = ow[i];
    lbar();
    drain();
    __builtin_amdgcn_raw_buffer_store_b64((u2){tag, ~tag}, ws, pf_, 0, SC1);
    unsigned hb = WS_H + (tile * 8 + w * 2) * 32768 + lane * 16, hb2 = hb + 32768;
    asm volatile("" : "+v"(hb), "+v"(hb2));
    const Win wc[2] = {mkwin(ax[0]), mkwin(ax[1])};
    u4 hf[64];
    bf8 cf[2][4];
    f4 acc[2][4];
    auto ldh = [&](int g) {
      hf[2 * g] = __builtin_amdgcn_raw_buffer_load_b128(ws, hb + (g & 3) * 1024, sq[g >> 2] * 4096, SC1);
      hf[2 * g + 1] = __builtin_amdgcn_raw_buffer_load_b128(ws, hb2 + (g & 3) * 1024, sq[g >> 2] * 4096, SC1);
    };
    auto rdc = [&](int g) {
      const int tt = g >> 2;
#pragma unroll
      for (int nt = 0; nt < 4; ++nt)
        cf[g & 1][nt] = ldsr(at(L, wc[(g >> 1) & 1], (((tt >> 1) + 3) % 5) * SLOT + (tt & 1) * 16384 + (g & 1) * 8192 + nt * 2048));
    };
    auto mh = [&](int g) {
#pragma unroll
      for (int nt = 0; nt < 4; ++nt) {
        acc[0][nt] = mfma16(cf[g & 1][nt], asbf8(hf[2 * g]), acc[0][nt]);
        acc[1][nt] = mfma16(cf[g & 1][nt], asbf8(hf[2 * g + 1]), acc[1][nt]);
      }
    };
#pragma unroll
    for (int i = 0; i < 8; ++i) hf[i] = *(const LAS u4*)(L + 2 * SLOT + w * 8192 + i * 1024 + lane * 16);
    rdc(0);
    {
      u4 ida[2];
#pragma unroll
      for (int hf_ = 0; hf_ < 2; ++hf_)
#pragma unroll
        for (int jq = 0; jq < 4; ++jq) {
          const int k0 = 8 * q + 2 * jq - 16 * hf_;
          ida[hf_][jq] = (l15 == k0 ? 0x3f80u : 0u) | (l15 == k0 + 1 ? 0x3f800000u : 0u);
        }
#pragma unroll
      for (int i = 0; i < 8; ++i) acc[i >> 2][i & 3] = mfma16(asbf8(ida[i & 1]), asbf8(xd[i >> 2][(i >> 1) & 1]), zero4);
    }
    SB();
#pragma unroll
    for (int g = 0; g < 4; ++g) { rdc(g + 1); mh(g); ILV(4, 8); SB(); }
    unsigned tw[4], trd[4], so_ = ((tile * 128 + 64 * wt + 4 * (w & 1) + 8 * (q & 1) + (q >> 1)) * 1024 + 512 + cg * 64 + l15 * 4) * 4;
#pragma unroll
    for (int i = 0; i < 4; ++i) {
      tw[i] = 2 * SLOT + w * 8192 + l15 * 256 + (((4 * i + q) ^ l15) << 4);
      trd[i] = 2 * SLOT + w * 8192 + (4 * i + q) * 256 + ((l15 ^ (4 * i + q)) << 4);
      asm volatile("" : "+v"(tw[i]), "+v"(trd[i]));
    }
    asm volatile("s_barrier" : "+v"(so_) :: "memory");
    wait_flags(ws, WS_FB + ((tile * 2 + wt) * 16 + l15) * 128, tag);
#pragma unroll
    for (int g = 4; g < 4 + HD; ++g) ldh(g);
    SB();
#pragma unroll
    for (int g = 4; g < 32; ++g) {
      if (g + HD < 32) ldh(g + HD);
      if (g + 1 < 32) rdc(g + 1);
      mh(g);
#pragma unroll
      for (int i_ = 0; i_ < 4; ++i_) { SG(0x008, 1); SG(0x100, 1); }
      SG(0x008, 1); SG(0x020, 1); SG(0x008, 1); SG(0x020, 1); SG(0x008, 2);
      SB();
    }
#pragma unroll
    for (int i = 0; i < 8; ++i) *(LAS f4*)(L + tw[i & 3] + (i >> 2) * 4096) = acc[i >> 2][i & 3];
    u4 ov[8];
#pragma unroll
    for (int i = 0; i < 8; ++i) ov[i] = *(const LAS u4*)(L + trd[i & 3] + (i >> 2) * 4096);
    SB();
#pragma unroll
    for (int i = 0; i < 8; ++i) __builtin_amdgcn_raw_buffer_store_b128(ov[i], ro, so_, (16 * (i & 3) + 2 * (i >> 2)) * 4096, SC1);
    asm volatile(".Lcode_lastB:");
  }
}

extern "C" void kernel_launch(void* const* d_in, const int* in_sizes, int n_in, void* d_out, int out_size, void* d_ws, size_t ws_size,
                              hipStream_t stream) {
  (void)hipFuncSetAttribute((const void*)fused_kernel, hipFuncAttributeMaxDynamicSharedMemorySize, LDS_BYTES);
  fused_kernel<<<256, 512, LDS_BYTES, stream>>>((const u16*)d_in[0], (const u16*)d_in[1], (const u16*)d_in[2], (const u16*)d_in[3],
                                               (const u16*)d_in[4], (const u16*)d_in[5], (const u16*)d_in[6], (const u16*)d_in[7],
                                               (const u16*)d_in[8], (const u16*)d_in[9], (const u16*)d_in[10], (const u16*)d_in[11],
                                               (float*)d_out, (char*)d_ws);
}
```
